```python
import torch

def _softmax(x, axis=-1):
    return torch.softmax(x, dim=axis)
def _gelu(x):
    return torch.nn.functional.gelu(x, approximate="tanh")
def _silu(x):
    return torch.nn.functional.silu(x)
def _sigmoid(x):
    return torch.sigmoid(x)
def _layernorm(x, axis=-1, eps=1e-5):
    m = x.mean(dim=axis, keepdim=True)
    v = x.var(dim=axis, keepdim=True, unbiased=False)
    return (x - m) / torch.sqrt(v + eps)
def _scatter_add(tgt, idx, src, axis=0):
    return tgt.clone().index_add_(axis, idx, src)
def _segment_sum(src, seg, n):
    out = torch.zeros((n,) + src.shape[1:], dtype=src.dtype)
    return out.index_add_(0, seg, src)
def _pad(x, pw):
    flat = tuple(v for pair in reversed(pw) for v in pair)
    return torch.nn.functional.pad(x, flat)

kernel_name = 'sequential_preLN__sliding_local_dilated__glu_gated_geglu__prompt'

def setup_inputs():

    _w = (lambda a: (lambda t: t.to(torch.bfloat16) if t.is_floating_point() else t)(torch.as_tensor(a)))
    def _A_setup():
        B, S, D = 1, 2048, 1024
        return {
            "x": _w((lambda *s: torch.randn(*s, dtype=torch.bfloat16) * 0.05)(B, S, D)),
            "wqkv": _w((lambda *s: torch.randn(*s, dtype=torch.bfloat16) * 0.05)(D, 3 * D)),
            "wo": _w((lambda *s: torch.randn(*s, dtype=torch.bfloat16) * 0.05)(D, D)),
        }
    _A_in = _A_setup()

    def _B_setup():
        B, S, Dm, Df = 1, 2048, 1024, 3584
        return {
            "x": _w((lambda *s: torch.randn(*s, dtype=torch.bfloat16) * 0.05)(B, S, Dm)),
            "w_gate": _w((lambda *s: torch.randn(*s, dtype=torch.bfloat16) * 0.8)(Dm, Df) * (Dm ** -0.5)),
            "w_up": _w((lambda *s: torch.randn(*s, dtype=torch.bfloat16) * 0.8)(Dm, Df) * (Dm ** -0.5)),
            "w_down": _w((lambda *s: torch.randn(*s, dtype=torch.bfloat16) * 0.8)(Df, Dm) * (Df ** -0.5)),
        }
    _B_in = _B_setup()

    return {
        "A_x": _A_in["x"],
        "A_wqkv": _A_in["wqkv"],
        "A_wo": _A_in["wo"],
        "B_x": _B_in["x"],
        "B_w_gate": _B_in["w_gate"],
        "B_w_up": _B_in["w_up"],
        "B_w_down": _B_in["w_down"],
    }

def reference(A_x, A_wqkv, A_wo, B_x, B_w_gate, B_w_up, B_w_down):
    def _A_ref(x, wqkv, wo):
        B, S, D = x.shape
        H = 32; Dh = D // H
        W, dil = 8, 2
        qkv = (lambda x, *d: x.permute(*d))((x @ wqkv).reshape(B, S, 3, H, Dh), 2, 0, 3, 1, 4)
        q, k, v = qkv[0], qkv[1], qkv[2]
        scale = Dh ** -0.5
        pos = torch.arange(S)
        off = torch.arange(W) * dil
        idx = pos[:, None] - off[None, :]
        mask = idx >= 0
        idx_c = torch.where(mask, idx, 0).reshape(-1)
        kg = k[:, :, idx_c, :].reshape(B, H, S, W, Dh)
        vg = v[:, :, idx_c, :].reshape(B, H, S, W, Dh)
        logits = torch.einsum("bhsd,bhswd->bhsw", q, kg) * scale
        logits = torch.where(mask[None, None, :, :], logits, -1e9)
        attn = _softmax(logits, axis=-1)
        o = torch.einsum("bhsw,bhswd->bhsd", attn, vg)
        return (lambda x, *d: x.permute(*d))(o, 0, 2, 1, 3).reshape(B, S, D) @ wo

    def _B_ref(x, w_gate, w_up, w_down):
        g = torch.einsum("bsd,df->bsf", x, w_gate)
        u = torch.einsum("bsd,df->bsf", x, w_up)
        h = _gelu(g) * u
        return torch.einsum("bsf,fd->bsd", h, w_down)

    x = A_x
    h = x + _A_ref(_layernorm(x, axis=-1), A_wqkv, A_wo)
    h2 = h + _B_ref(_layernorm(h, axis=-1), B_w_gate, B_w_up, B_w_down)
    return h2
```

```cpp
#include <hip/hip_runtime.h>
#include <hip/hip_bf16.h>
#include <stdint.h>
#include <type_traits>

typedef unsigned short bf16_t;
typedef short bf16x8 __attribute__((ext_vector_type(8)));
typedef short s16x4 __attribute__((ext_vector_type(4)));
typedef float f32x4 __attribute__((ext_vector_type(4)));
typedef float f32x2 __attribute__((ext_vector_type(2)));
typedef unsigned u32x2 __attribute__((ext_vector_type(2)));
typedef unsigned u32x4 __attribute__((ext_vector_type(4)));
typedef __bf16 bf16x2_t __attribute__((ext_vector_type(2)));
typedef __attribute__((address_space(3))) void* lds_void_ptr;
typedef __attribute__((address_space(3))) char* lds_char_ptr;

constexpr int S_ = 2048, D_ = 1024, DF_ = 3584;
constexpr int RBO = D_ / 8, RBH = D_ / 8, RBA = DF_ / 8;

__device__ __forceinline__ float bflo(unsigned v) { return __uint_as_float(v << 16); }
__device__ __forceinline__ float bfhi(unsigned v) { return __uint_as_float(v & 0xffff0000u); }
__device__ __forceinline__ unsigned pk_bf16(float lo, float hi) {
  f32x2 v = {lo, hi};
  bf16x2_t b = __builtin_convertvector(v, bf16x2_t);
  return __builtin_bit_cast(unsigned, b);
}
__device__ __forceinline__ int wswz(int row) { return (((row >> 1) & 1) | (((row >> 3) & 1) << 1)) << 1; }

template <int OFF> __device__ __forceinline__ s16x4 ds_tr(unsigned addr) {
  s16x4 r; asm volatile("ds_read_b64_tr_b16 %0, %1 offset:%2" : "=v"(r) : "v"(addr), "n"(OFF)); return r;
}
template <int OFF> __device__ __forceinline__ bf16x8 ds_b128(unsigned addr) {
  bf16x8 r; asm volatile("ds_read_b128 %0, %1 offset:%2" : "=v"(r) : "v"(addr), "n"(OFF)); return r;
}
template <int OFF> __device__ __forceinline__ f32x2 ds_b64(unsigned addr) {
  f32x2 r; asm volatile("ds_read_b64 %0, %1 offset:%2" : "=v"(r) : "v"(addr), "n"(OFF)); return r;
}
__device__ __forceinline__ float sum8(float v) {
  v += __int_as_float(__builtin_amdgcn_update_dpp(0, __float_as_int(v), 0x128, 0xf, 0xf, false));
  auto a = __builtin_amdgcn_permlane16_swap(__float_as_uint(v), __float_as_uint(v), false, false);
  v = __uint_as_float(a[0]) + __uint_as_float(a[1]);
  auto b = __builtin_amdgcn_permlane32_swap(__float_as_uint(v), __float_as_uint(v), false, false);
  return __uint_as_float(b[0]) + __uint_as_float(b[1]);
}
__device__ __forceinline__ float sum8l(float v) {
  v += __int_as_float(__builtin_amdgcn_update_dpp(0, __float_as_int(v), 0xb1, 0xf, 0xf, false));
  v += __int_as_float(__builtin_amdgcn_update_dpp(0, __float_as_int(v), 0x4e, 0xf, 0xf, false));
  return v + __int_as_float(__builtin_amdgcn_update_dpp(0, __float_as_int(v), 0x141, 0xf, 0xf, false));
}
__device__ __forceinline__ int ds_r32(unsigned addr) { int r; asm volatile("ds_read_b32 %0, %1" : "=v"(r) : "v"(addr)); return r; }
__device__ __forceinline__ void ds_w32(unsigned addr, int v) { asm volatile("ds_write_b32 %0, %1" :: "v"(addr), "v"(v) : "memory"); }
template <int N> __device__ __forceinline__ void wait_vm() { asm volatile("s_waitcnt vmcnt(%0)" ::"n"(N) : "memory"); }
__device__ __forceinline__ void raw_barrier() { asm volatile("" ::: "memory"); __builtin_amdgcn_s_barrier(); asm volatile("" ::: "memory"); }
__device__ __forceinline__ void lds_barrier() { asm volatile("s_waitcnt lgkmcnt(0)" ::: "memory"); __builtin_amdgcn_s_barrier(); asm volatile("" ::: "memory"); }

template <int I, int N, class F> __device__ __forceinline__ void static_for(F&& f) {
  if constexpr (I < N) { f(std::integral_constant<int, I>{}); static_for<I + 1, N>(f); }
}
template <int V> using IC = std::integral_constant<int, V>;
#define RSRC(p) __builtin_amdgcn_make_buffer_rsrc((void*)(p), 0, 0x7fffffff, 0x00020000)
#define RSRCN(p, n) __builtin_amdgcn_make_buffer_rsrc((void*)(p), 0, (n), 0x00020000)
#define LDSP(a) ((lds_void_ptr)(size_t)(a))
#define MFMA(a, b, c) __builtin_amdgcn_mfma_f32_16x16x32_bf16(a, b, c, 0, 0, 0)
#define CODE_ADDR(v, sym) asm volatile("s_getpc_b64 s[40:41]\n\ts_add_u32 s40, s40, " sym "@rel32@lo+4\n\ts_addc_u32 s41, s41, " sym "@rel32@hi+12\n\ts_mov_b64 %0, s[40:41]" : "=s"(v) :: "s40", "s41", "scc")
#define MERGED 1
constexpr int SNAP = 162752;
constexpr int STAB1 = 786432;
constexpr int SY_O = 12288;
#define SYO(t) (SY_O + (t) * 32 + (((((t) >> 3) ^ 1) & 1) << 11))
constexpr unsigned FIN = 65536u;
constexpr int KN_TRY = 6;
__device__ __forceinline__ unsigned snap_get(unsigned lds0, int i) {
  int v = ds_r32(lds0 + SNAP + i * 4);
  asm volatile("s_waitcnt lgkmcnt(0)" : "+v"(v) :: "memory");
  return (unsigned)__builtin_amdgcn_readfirstlane(v);
}
__device__ __forceinline__ bool in_time(unsigned lds0, unsigned level) { return (unsigned)__builtin_amdgcn_s_memrealtime() - snap_get(lds0, 7) < level * 5000000u; }
__device__ __forceinline__ void wait_count(unsigned lds0, const unsigned* cnt, unsigned v0, unsigned n, unsigned level) {
  while (((unsigned)__builtin_amdgcn_readfirstlane(__hip_atomic_load(cnt, __ATOMIC_RELAXED, __HIP_MEMORY_SCOPE_AGENT)) - v0) >> 16 < n && in_time(lds0, level)) {}
}
__device__ __forceinline__ void wait_field(unsigned lds0, const unsigned* cnt, unsigned v0, int sh, unsigned level) {
  while (((((unsigned)__builtin_amdgcn_readfirstlane(__hip_atomic_load(cnt, __ATOMIC_RELAXED, __HIP_MEMORY_SCOPE_AGENT)) - v0) >> sh) & 255u) < 32u && in_time(lds0, level)) {}
}
constexpr int ACNT = SNAP - 60;
__device__ __forceinline__ void lds_inc(unsigned addr) { if (__builtin_amdgcn_mbcnt_hi(~0u, __builtin_amdgcn_mbcnt_lo(~0u, 0u)) == 0) asm volatile("ds_add_u32 %0, %1" :: "v"(addr), "v"(1) : "memory"); }
constexpr int FPAD = SNAP - 704;
__device__ __forceinline__ void lds_wait8(unsigned addr, int n = 8) {
  int dv; unsigned polls = 0;
  do { dv = ds_r32(addr); asm volatile("s_waitcnt lgkmcnt(0)" : "+v"(dv) :: "memory"); } while (__builtin_amdgcn_readfirstlane(dv) < n && ++polls < 40000000u);
}
template <int N> __device__ __forceinline__ bool marked(unsigned addr) {
  bf16x8 v[N];
  static_for<0, N>([&](auto ic) { constexpr int i = decltype(ic)::value; v[i] = ds_b128<i * 4096>(addr); });
  asm volatile("s_waitcnt lgkmcnt(0)" ::: "memory");
  unsigned m = 0;
#pragma unroll
  for (int i = 0; i < N; ++i) {
    asm volatile("" : "+v"(v[i]));
    const u32x4 u = __builtin_bit_cast(u32x4, v[i]);
    m = max(max(m, u[0]), u[1]); m = max(max(m, u[2]), u[3]);
  }
  return __builtin_amdgcn_ballot_w64(m == 0xffffffffu) != 0;
}

struct GemmArgs {
  const bf16_t* X;
  const bf16_t* W0;
  const bf16_t* W1;
  const bf16_t* R;
  bf16_t* O;
  float* st;
  const bf16_t* CW0;
  const bf16_t* CW1;
  float* cs;
  const bf16_t* PF0;
  const bf16_t* PF1;
  unsigned* sync;
  float* slab;
  unsigned warm;
};
#define STAMP(i)
#define STAMPW(i, w)
template <int MF, int NF> struct Frags { bf16x8 x[MF]; s16x4 lo[NF], hi[NF]; };

constexpr int WOB = 57600;
constexpr int wo_pos(int s) { return s < 6 ? WOB + s * 16384 : (s - 6) * 16384; }
template <class HK>
__device__ __forceinline__ void k1_phase(const GemmArgs& a, HK hook) {
#if defined(__HIP_DEVICE_COMPILE__)
  constexpr int K = D_, LDW = 3 * D_, WN = 4;
  constexpr int HR = 16, STRIDE = 128;
  constexpr int XT = 144 * 128, WU = 24576;
  constexpr int XBASE = 0, WBASE = 3 * XT, RING = WBASE + 4 * WU;
  constexpr int MF = 4, NF = 3, MFX = 5;
  constexpr int NT = 16, NXI = 18, NLW = 4, XI = 5, XIMIN = 4, WI = 6;
  extern __shared__ __attribute__((aligned(16))) char smem[];
  STAMP(0);
  const unsigned lds0 = (unsigned)(size_t)(lds_char_ptr)smem;
  const int tid = threadIdx.x, lane = tid & 63;
  const int wave = __builtin_amdgcn_readfirstlane(tid >> 6);
  const int wm = wave / WN, wn = wave % WN;
  const int bid = blockIdx.x, xcd = bid & 7, idx = bid >> 3;
  const int tm = (xcd / 4) * 8 + idx / 4, tn = (xcd % 4) * 4 + idx % 4;
  const int m0 = tm * 128;
  const int fr = lane & 15, fq = lane >> 4;
  const int wq = (lane >> 2) & 3, wp = lane & 3;
  constexpr int TCH = RING + 2304;
  auto touch = [&](const void* base, unsigned voff, unsigned soff, unsigned lim = 0x7fffffffu) {
    __builtin_amdgcn_raw_ptr_buffer_load_lds(RSRCN(base, lim), LDSP(lds0 + TCH), 4, voff, soff, 0, 0);
  };
  const auto rO = RSRC(a.O);

  unsigned xsrc[XI], wsrc[WI];
  {
    const int w4 = wave & 3, lr = lane >> 3;
    const unsigned xv = (unsigned)(lr * (K * 2) + (((lane & 7) ^ ((w4 * 4 + (lr >> 1)) & 7)) << 4));
    const int r0 = m0 - HR + w4 * 8;
#pragma unroll
    for (int i = 0; i < XI; ++i) xsrc[i] = xv + (unsigned)((i == 0 ? (r0 < 0 ? 0 : r0) : r0 + 32 * (i < 4 ? i : 4)) * (K * 2));
    const unsigned wv = (unsigned)((w4 * 8 + lr) * (LDW * 2) + tn * 128 + (((lane & 7) ^ wswz(w4 * 8 + lr)) << 4));
#pragma unroll
    for (int i = 0; i < WI; ++i) wsrc[i] = wv + (unsigned)((i & 1) * 32 * (LDW * 2) + (i >> 1) * 2048);
  }
  auto ldX = [&](int i, int tile, int slot) {
    const int j = wave + NLW * i;
    if (j >= NXI) return;
    __builtin_amdgcn_raw_ptr_buffer_load_lds(RSRC(a.X), LDSP(lds0 + XBASE + slot * XT + j * 1024), 16, xsrc[i], tile * 128, 0, 0);
  };
  auto ldW = [&](int i, int unit, int slot) {
    const int j = wave + NLW * i;
    __builtin_amdgcn_raw_ptr_buffer_load_lds(RSRC(a.W0), LDSP(lds0 + WBASE + slot * WU + j * 1024), 16, wsrc[i], unit * 64 * LDW * 2, 0, 0);
  };
  auto issueX = [&](int tile, int slot) {
    if (wave >= NLW) return;
#pragma unroll
    for (int i = 0; i < XI; ++i) ldX(i, tile, slot);
  };
  auto issueW = [&](int unit, int slot) {
    if (wave >= NLW) return;
#pragma unroll
    for (int i = 0; i < WI; ++i) ldW(i, unit, slot);
  };

  f32x4 acc[NF][MF];
#pragma unroll
  for (int n = 0; n < NF; ++n)
#pragma unroll
    for (int m = 0; m < MF; ++m) acc[n][m] = (f32x4){0.f, 0.f, 0.f, 0.f};
  f32x4 acch = {0.f, 0.f, 0.f, 0.f};
  f32x4 accs = {0.f, 0.f, 0.f, 0.f};
  constexpr int ONES = RING;

  unsigned xa0, xa1, xh0, xh1;
  { const unsigned o = fr * 128 + ((fq ^ ((fr >> 1) & 7)) << 4), mo = (HR + wm * MF * 16) * 128;
    xa0 = lds0 + XBASE + mo + o; xa1 = lds0 + XBASE + mo + (o ^ 64); xh0 = lds0 + XBASE + o; xh1 = lds0 + XBASE + (o ^ 64); }
  unsigned wa;
  { const int row = 8 * fq + wq; wa = lds0 + WBASE + row * STRIDE + (((wn << 1) ^ wswz(row)) << 4) + wp * 8; }

  using I0 = IC<0>; using I1 = IC<1>;
  using T = std::true_type; using F = std::false_type;
  typedef Frags<MFX, NF> FR;
  struct RdCtx { unsigned xa, xh, wb; };
  auto mkctx = [&](bool ks1, unsigned xoffs, unsigned woffs) {
    RdCtx c; c.xa = (ks1 ? xa1 : xa0) + xoffs; c.xh = fr == 0 ? lds0 + ONES : (ks1 ? xh1 : xh0) + xoffs; c.wb = wa + woffs;
    return c;
  };
  auto rd_item = [&](FR& f, const RdCtx& c, auto KSc, auto kc) {
    constexpr int KS = decltype(KSc)::value, k = decltype(kc)::value;
    if constexpr (k < MF) f.x[k] = ds_b128<k * 2048>(c.xa);
    else if constexpr (k < MFX) f.x[k] = ds_b128<0>(c.xh);
    else {
      constexpr int n = k - MFX, o = n * 8192 + KS * 32 * STRIDE;
      f.lo[n] = ds_tr<o>(c.wb); f.hi[n] = ds_tr<o + 4 * STRIDE>(c.wb);
    }
  };
  auto wait_frags = [&](FR& f) {
    asm volatile("s_waitcnt lgkmcnt(0)" ::: "memory");
#pragma unroll
    for (int m = 0; m < MFX; ++m) asm volatile("" : "+v"(f.x[m]));
#pragma unroll
    for (int n = 0; n < NF; ++n) asm volatile("" : "+v"(f.lo[n]), "+v"(f.hi[n]));
  };
  auto step = [&](auto CSc, auto STc, FR& cur, FR& nxt, const RdCtx& c, auto KSc, auto PFc, auto NLc, auto&& ld) {
    constexpr bool PF = decltype(PFc)::value != 0; constexpr int NL = decltype(NLc)::value;
    constexpr int R = MFX + NF, RG = NF - 1;
    constexpr bool CS = decltype(CSc)::value; constexpr int ST = decltype(STc)::value;
    static_for<0, NF>([&](auto gc) {
      constexpr int g = decltype(gc)::value;
      if constexpr (PF) static_for<0, R>([&](auto kc) { constexpr int k = decltype(kc)::value; if constexpr (k * RG / R == g) rd_item(nxt, c, KSc, kc); });
      static_for<0, NL>([&](auto ic) { constexpr int i = decltype(ic)::value; if constexpr (i * NF / NL == g) ld(i); });
      const bf16x8 w = __builtin_shufflevector(cur.lo[g], cur.hi[g], 0, 1, 2, 3, 4, 5, 6, 7);
#pragma unroll
      for (int m = 0; m < MF; ++m) acc[g][m] = MFMA(w, cur.x[m], acc[g][m]);
      if constexpr (g == 1 + ST / WN) acch = MFMA(w, cur.x[MF], acch);
      if constexpr (CS && g == 0) accs = MFMA(w, cur.x[MF], accs);
      __builtin_amdgcn_sched_barrier(0);
    });
    if constexpr (PF) wait_frags(nxt);
  };
  auto step1 = [&](auto CSc, auto STc, FR& cur, FR& nxt, const RdCtx& c, auto KSc, auto R0c, auto RLc, auto IBc, auto&& sync, auto NLc, auto&& ld) {
    constexpr bool CS = decltype(CSc)::value; constexpr int ST = decltype(STc)::value, NL = decltype(NLc)::value, R0 = decltype(R0c)::value;
    constexpr int E0 = CS ? 1 : 0, E1 = 1 == 1 + ST / WN ? 1 : 0, E2 = 2 == 1 + ST / WN ? 1 : 0;
    constexpr int NM = 12 + E0 + E1 + E2, NR = MFX + 2 * NF;
    constexpr int IB = decltype(IBc)::value < 0 && decltype(IBc)::value > -100 ? NM + decltype(IBc)::value : decltype(IBc)::value;
    constexpr int RL = decltype(RLc)::value < 0 ? NM + decltype(RLc)::value : decltype(RLc)::value;
    static_for<0, NM>([&](auto ic) {
      constexpr int I = decltype(ic)::value;
      constexpr int g = I < 4 + E0 ? 0 : (I < 8 + E0 + E1 ? 1 : 2), q = I - (g == 0 ? 0 : (g == 1 ? 4 + E0 : 8 + E0 + E1));
      const bf16x8 w = __builtin_shufflevector(cur.lo[g], cur.hi[g], 0, 1, 2, 3, 4, 5, 6, 7);
      if constexpr (q < 4) acc[g][q] = MFMA(w, cur.x[q], acc[g][q]);
      else if constexpr (g == 0) accs = MFMA(w, cur.x[MF], accs);
      else acch = MFMA(w, cur.x[MF], acch);
      __builtin_amdgcn_sched_barrier(0);
      if constexpr (I == IB) sync();
      static_for<0, NR>([&](auto rc) {
        constexpr int r = decltype(rc)::value;
        if constexpr (R0 + r * (RL - R0) / (NR - 1) == I) {
          if constexpr (r < MFX) rd_item(nxt, c, KSc, rc);
          else {
            constexpr int n = (r - MFX) >> 1, o = n * 8192 + decltype(KSc)::value * 32 * STRIDE;
            if constexpr ((r - MFX) & 1) nxt.hi[n] = ds_tr<o + 4 * STRIDE>(c.wb); else nxt.lo[n] = ds_tr<o>(c.wb);
          }
        }
      });
      static_for<0, NL>([&](auto lc) { constexpr int i = decltype(lc)::value; if constexpr (R0 + i * (NM - R0) / NL == I) ld(i); });
      __builtin_amdgcn_sched_barrier(0);
    });
    wait_frags(nxt);
  };
  FR fa, fb;
  using IM1 = IC<-1>;

  asm volatile("s_waitcnt lgkmcnt(0)" :: "s"(a.warm), "v"(wsrc[WI - 1]), "v"(xsrc[XI - 1]));
  issueW(0, 0); issueX(0, 0); issueW(1, 1); issueX(1, 1);
  u32x4 sx[4];
  const unsigned t_begin = (unsigned)__builtin_amdgcn_s_memrealtime();
  constexpr int TNEW = RING + 3072;
  auto ldT = [&](int dst) {
    int lx = lane; asm volatile("" : "+v"(lx));
    const auto rT = RSRCN((const char*)a.cs + STAB1, 2048 * 16);
    const unsigned to = (unsigned)((m0 - 16) * 16 + lx * 16);
    __builtin_amdgcn_raw_ptr_buffer_load_lds(rT, LDSP(lds0 + dst), 16, to, 0, 0, 16);
    __builtin_amdgcn_raw_ptr_buffer_load_lds(rT, LDSP(lds0 + dst + 1024), 16, to + 1024u, 0, 0, 16);
    if (lx < 16) __builtin_amdgcn_raw_ptr_buffer_load_lds(rT, LDSP(lds0 + dst + 2048), 16, to + 2048u, 0, 0, 16);
  };
  auto ldS = [&]() {
    int lx = lane; asm volatile("" : "+v"(lx));
    const unsigned so = (unsigned)((m0 + tn * 8 + wave * 2 + (lx >> 5)) * (K * 2) + (lx & 31) * 16);
#pragma unroll
    for (int i = 0; i < 4; ++i) sx[i] = __builtin_amdgcn_raw_buffer_load_b128(RSRC(a.X), so, i * 512, 0);
  };
  __builtin_amdgcn_sched_barrier(0);
  if (wave == 4) {
    if (lane < 4) *(unsigned*)(smem + ONES + lane * 4) = 0x3f803f80u;
  }
  if (wave == 5) {
    const int p2 = (xcd >> 1) * 2 + (idx >> 4), mb = (xcd & 1) * 16 + (idx & 15);
#pragma unroll
    for (int i = 0; i < 3; ++i)
      __builtin_amdgcn_raw_buffer_store_b128((u32x4){~0u, ~0u, ~0u, ~0u}, RSRC(a.st), lane * 16 + i * 1024, ((p2 * 16 + (mb >> 1)) * RBH + (xcd >> 2) * 64 + (mb & 1) * 12) * 256, 16);
  }
  constexpr int HQ = 5;
  const int slc = (3 - ((unsigned)(size_t)a.W0 >> 7) - (xcd & 3) * 4) & 7;
  const int hid = (idx >> 2) * 3 + (idx & 3) - ((idx & 3) > slc ? 1 : 0);
  const bool hlp = slc < 4 && (idx & 3) != slc;
  const int hj1 = hlp ? hid / 3 - HQ : -100, hj2 = hlp ? hid / 3 + 8 - HQ : -100;
  const unsigned htv = (unsigned)(lane * (LDW * 2) + (hid % 3) * 2048 + ((xcd & 3) * 4 + slc) * 128);
  if (wave == 6 && hlp && hj1 <= 0) touch(a.W0, htv, (hid / 3) * (64 * LDW * 2));
  auto code_touch = [&]() {
    unsigned long long c0, c1; CODE_ADDR(c0, "k_all"); CODE_ADDR(c1, "k_zend");
    const unsigned len = c1 - c0 < 262144ull ? (unsigned)(c1 - c0) : 0u;
    touch((const void*)c0, (unsigned)((lane * 32 + idx) * 128), 0, len);
  };
  if (wave == 7) code_touch();
  if (wave < NLW) wait_vm<WI + XIMIN>();
  lds_barrier();
  { const RdCtx c = mkctx(false, 0, 0); static_for<0, MFX + NF>([&](auto kc) { rd_item(fa, c, I0{}, kc); }); }
  wait_frags(fa);
  STAMP(1);
  auto run = [&](auto ROLEc) __attribute__((always_inline)) {
    constexpr int ROLE = decltype(ROLEc)::value;
    constexpr bool LDR = ROLE < NLW;
    using CS0 = std::integral_constant<bool, ROLE / WN == 0>; using CS1 = std::integral_constant<bool, ROLE / WN == 1>;
    using STt = IC<ROLE>;
    auto nold = [&](int) {};
    { const RdCtx c = mkctx(true, 0, 0); step(CS0{}, STt{}, fa, fb, c, I1{}, T{}, IC<LDR ? WI + XI : 0>{}, [&](int i) { if (i < WI) ldW(i, 2, 2); else ldX(i - WI, 2, 2); }); }
    if constexpr (LDR) ldS();
    int slx = 1;
    unsigned snap = 0, t_start = 0;
    unsigned ts7 = 0;
    if constexpr (ROLE == 7) ts7 = t_begin;
    if constexpr (ROLE == 5) {
      t_start = (unsigned)__builtin_amdgcn_s_memrealtime();
      const int t2 = (xcd >> 1) * 4 + (idx >> 3), pg = (xcd >> 2) * 4 + (idx >> 3), tg = (xcd & 3) * 8 + (idx & 7), l8 = lane & 7;
      const int w = l8 == 0 ? SYO(t2) : (l8 == 1 ? pg * 64 : (l8 == 2 ? pg * 64 + 32 : (l8 == 3 ? 1024 + tg * 32 : (l8 == 4 ? (8 + (t2 >> 1)) * 64 : (l8 == 6 ? (t2 >> 1) * 64 : 2048 + bid * 32)))));
      snap = __hip_atomic_load(a.sync + w, __ATOMIC_RELAXED, __HIP_MEMORY_SCOPE_AGENT);
    }
    auto iter = [&](int j, auto LXc, auto LWc, auto WAITc, auto TIc) {
      constexpr bool LX = decltype(LXc)::value && LDR, LW = decltype(LWc)::value && LDR;
      constexpr int WAIT = decltype(WAITc)::value, TI = decltype(TIc)::value;
      constexpr int EX = (LDR && TI < 0) ? WI : 0;
      constexpr int OL = TI == 1 ? 0 : 2, OH = (LDR && TI > 0) ? 2 : OL;
      constexpr int NLT = EX + (LX ? XI : 0) + (LW ? WI : 0) + 2 * (OH - OL), N0 = (NLT + 1) / 2, N1 = NLT - N0;
      if constexpr (LDR) wait_vm<WAIT + (TI > 0 ? 2 * OL : 0)>();
      unsigned osrc = 0;
      if constexpr (OH > OL) { int lx = lane; asm volatile("" : "+v"(lx)); const int row = ROLE * 8 + (lx >> 3); osrc = (unsigned)(row * D_ + ((xcd & 1) * 8 + (idx & 7)) * 64 + ((lx & 7) ^ wswz(row)) * 8) * 2u; }
      if constexpr (ROLE == 6 && TI == 0) { if (j == hj1 || j == hj2) touch(a.W0, htv, (j + HQ) * (64 * LDW * 2)); }
      constexpr int S1 = 2, S3 = 11;
      if constexpr (LDR && TI <= 0) {
        if (j == S1) {
          float sm = 0.f, sq = 0.f;
          const bf16x2_t one2 = __builtin_bit_cast(bf16x2_t, 0x3f803f80u);
#pragma unroll
          for (int i = 0; i < 4; ++i)
#pragma unroll
            for (int e = 0; e < 4; ++e) { const unsigned u = sx[i][e]; const bf16x2_t v = __builtin_bit_cast(bf16x2_t, u); sm = __builtin_amdgcn_fdot2_f32_bf16(v, one2, sm, false); sq = __builtin_amdgcn_fdot2_f32_bf16(v, v, sq, false); }
          sm = sum8l(sm); sq = sum8l(sq);
          sm += __int_as_float(__builtin_amdgcn_update_dpp(0, __float_as_int(sm), 0x128, 0xf, 0xf, false));
          sq += __int_as_float(__builtin_amdgcn_update_dpp(0, __float_as_int(sq), 0x128, 0xf, 0xf, false));
          { auto p = __builtin_amdgcn_permlane16_swap(__float_as_uint(sm), __float_as_uint(sm), false, false); sm = __uint_as_float(p[0]) + __uint_as_float(p[1]); }
          { auto p = __builtin_amdgcn_permlane16_swap(__float_as_uint(sq), __float_as_uint(sq), false, false); sq = __uint_as_float(p[0]) + __uint_as_float(p[1]); }
          const float mu = sm * (1.f / 1024.f);
          float var = sq * (1.f / 1024.f) - mu * mu; var = var > 0.f ? var : 0.f;
          int lx = lane; asm volatile("" : "+v"(lx));
          const unsigned long long now = __builtin_amdgcn_s_memrealtime();
          __builtin_amdgcn_raw_buffer_store_b128((u32x4){__float_as_uint(mu), (unsigned)now, __float_as_uint(__builtin_amdgcn_rsqf(var + 1e-5f)), ~(unsigned)now ^ (unsigned)(now >> 32)}, RSRC(a.cs),
                                                 (lx & 31) == 0 ? (unsigned)(STAB1 + (m0 + tn * 8 + ROLE * 2 + (lx >> 5)) * 16) : 0x80000000u, 0, 16);
          __builtin_amdgcn_sched_barrier(0);
          asm volatile("s_nop 1" ::: "memory");
        }
      }
      if constexpr (ROLE == 7 && TI == 0) {
        if (j == S3) ldT(TNEW);
        if (j == S3 + 1) {
          for (;;) {
            wait_vm<0>();
            int lx = lane; asm volatile("" : "+v"(lx));
            const unsigned ad = lds0 + TNEW + lx * 16;
            u32x4 v[3];
            v[0] = __builtin_bit_cast(u32x4, ds_b128<0>(ad)); v[1] = __builtin_bit_cast(u32x4, ds_b128<1024>(ad)); v[2] = __builtin_bit_cast(u32x4, ds_b128<2048>(ad));
            const unsigned long long now = __builtin_amdgcn_s_memrealtime();
            asm volatile("s_waitcnt lgkmcnt(0)" : "+v"(v[0]), "+v"(v[1]), "+v"(v[2]) :: "memory");
            const unsigned lo = ts7 - 1500u, span = (unsigned)now - lo, hi = (unsigned)(now >> 32);
            auto good = [&](const u32x4& e) { return e[1] - lo <= span && (~e[1] ^ e[3]) == hi; };
            const bool bad = (!good(v[0]) && (tm > 0 || lx >= 16)) || !good(v[1]) || (!good(v[2]) && lx < 16);
            if (__builtin_amdgcn_ballot_w64(bad) == 0 || (unsigned)now - ts7 >= 2500000u) break;
            ldT(TNEW);
          }
        }
      }
      if constexpr (ROLE == 5 && TI == 1) wait_vm<0>();
      if constexpr (ROLE == 7 && TI == 1) code_touch();
      if constexpr (ROLE == 5 && TI == 1) ds_w32(lds0 + SNAP + (lane & 7) * 4, (int)((lane & 7) == 7 ? t_start : snap));
      if constexpr (ROLE == 4 && TI == 0) touch(a.PF1, (bid * 64 + lane) * 128, 0, j == 1 + idx % 12 ? 0x7fffffffu : 0u);
      if constexpr (ROLE >= 4 && TI == 2) {
        const bf16_t* cw = ROLE < 6 ? a.CW0 : a.CW1;
        touch(cw, lane < 56 ? (unsigned)(((ROLE & 1) * 512 + bid) * (DF_ * 2) + lane * 128) : 0x80000000u, 0);
      }
      int xp = slx + 2; if (xp >= 3) xp -= 3;
      auto ld = [&](int i) { if (i < EX) { ldW(i, j + 2, (j + 2) & 3); return; } i -= EX;
                             if (LX && i < XI) ldX(i, j + 2, xp);
                             else if constexpr (LW) ldW(i - (LX ? XI : 0), j + 3, (j + 3) & 3);
                             else { const int q = i - (LX ? XI : 0), t = OL + (q >> 1), h = q & 1;
                               __builtin_amdgcn_raw_ptr_buffer_load_lds(RSRC(a.PF1), LDSP(lds0 + wo_pos(t >> 1) + (t & 1) * 8192 + (ROLE + 4 * h) * 1024), 16,
                                                                        osrc, ((((t & 1) * 8 + (t >> 1)) ^ ((xcd & 3) * 4)) * 2 + h) * (32 * D_ * 2), 0, 0); } };
      { const RdCtx c = mkctx(false, slx * XT, (j & 3) * WU); step1(CS1{}, STt{}, fb, fa, c, I0{}, IC<LDR ? 1 : 0>{}, IC<-4>{}, IC<LDR ? 1 : -100>{}, [&]() { raw_barrier(); }, IC<N0>{}, ld); }
      { const RdCtx c = mkctx(true, slx * XT, (j & 3) * WU); step1(CS0{}, STt{}, fa, fb, c, I1{}, I0{}, IC<LDR ? -4 : -6>{},
          IC<LDR ? -100 : -3>{}, [&]() { if constexpr (TI != 3) { asm volatile("s_waitcnt lgkmcnt(0)" ::: "memory"); raw_barrier(); } }, IC<N1>{}, [&](int i) { ld(i + N0); }); }
      slx = slx + 1; if (slx >= 3) slx = 0;
    };
    using WS_ = IC<2 * WI + XIMIN>;
    if constexpr (!LDR) raw_barrier();
    if constexpr (LDR) iter(1, T{}, T{}, IC<WI + XIMIN + 4>{}, IM1{});
    for (int j = LDR ? 2 : 1; j <= NT - 4; ++j) iter(j, T{}, T{}, WS_{}, I0{});
    iter(NT - 3, T{}, F{}, WS_{}, I1{});
    iter(NT - 2, F{}, F{}, IC<WI + XIMIN>{}, IC<2>{});
    iter(NT - 1, F{}, F{}, I0{}, IC<3>{});
    if constexpr (ROLE == 5) { if (lane == 0) __hip_atomic_fetch_add(a.sync + ((xcd >> 1) * 2 + (idx >> 4)) * 64, 1u, __ATOMIC_RELAXED, __HIP_MEMORY_SCOPE_AGENT); }
    { const RdCtx c = mkctx(false, 0, 0); step(CS1{}, STt{}, fb, fa, c, I0{}, F{}, I0{}, nold); }
  };
  switch (wave) {
    case 0: run(IC<0>{}); break; case 1: run(IC<1>{}); break;
    case 2: run(IC<2>{}); break; case 3: run(IC<3>{}); break;
    case 4: run(IC<4>{}); break; case 5: run(IC<5>{}); break;
    case 6: run(IC<6>{}); break; default: run(IC<7>{}); break;
  }
  STAMP(2);
  {
    float* tcs = (float*)(smem + RING + 64);
    float* tkv = tcs + 128;
    if (fr == 0) { *(f32x4*)(tcs + wm * 64 + wn * 16 + fq * 4) = accs; *(f32x4*)(tkv + wave * 16 + fq * 4) = acch; }
    const int srow = m0 + wave * 16 + fr;
    const bool par = fr & 1;
    float bias[4];
    {
      const unsigned mk = 0x5555u & ((2u << (srow < 15 ? srow : 15)) - 1u), sft = (fr & 14) - fq * 4 + 14;
      const unsigned b0 = ~(mk >> sft), b1 = ~((mk << 16) >> sft);
#pragma unroll
      for (int j = 0; j < 4; ++j) bias[j] = __uint_as_float((unsigned)__builtin_amdgcn_sbfe((int)(j < 2 ? b0 : b1), 2 - 2 * (j & 1), 1) & 0xff61b1e6u);
    }
    unsigned pl = par ? 0x01000c0cu : 0x0c0c0100u, ph = par ? 0x03020c0cu : 0x0c0c0302u;
    asm volatile("" : "+v"(bias[0]), "+v"(bias[1]), "+v"(bias[2]), "+v"(bias[3]), "+v"(pl), "+v"(ph));
    STAMP(9);
    lds_barrier();
    STAMP(6);
    constexpr int RS = 400;
    char* qt = smem;
    f32x4 csn[NF];
    const f32x4 c0a = *(const f32x4*)(tcs + wn * 16 + fq * 4), c0b = *(const f32x4*)(tcs + 64 + wn * 16 + fq * 4);
    csn[1] = *(const f32x4*)(tkv + wn * 16 + fq * 4); csn[2] = *(const f32x4*)(tkv + 64 + wn * 16 + fq * 4);
    f32x2 st[MF + 1];
#pragma unroll
    for (int m = 0; m <= MF; ++m) {
      const int r = m < MF ? 16 + (wm * MF + m) * 16 + fr : fr;
      const f32x4 t4 = *(const f32x4*)(smem + TNEW + r * 16); st[m] = (f32x2){t4[0], t4[2]};
    }
    const f32x4 csh = *(const f32x4*)(tkv + wave * 16 + fq * 4);
    asm volatile("" ::: "memory");
    csn[0] = c0a + c0b;
#pragma unroll
    for (int m = 0; m < MF; ++m) {
      const int r = 16 + (wm * MF + m) * 16 + fr;
      const float mu = st[m][0], rs = st[m][1];
#pragma unroll
      for (int n = 0; n < NF; ++n) {
        const int col = (n * 4 + wn) * 16 + fq * 4;
        const f32x4 cs = csn[n];
        const u32x2 v = {pk_bf16(rs * (acc[n][m][0] - mu * cs[0]), rs * (acc[n][m][1] - mu * cs[1])),
                         pk_bf16(rs * (acc[n][m][2] - mu * cs[2]), rs * (acc[n][m][3] - mu * cs[3]))};
        *(u32x2*)(qt + r * RS + col * 2) = v;
      }
    }
    {
      const float mu = st[MF][0], rs = st[MF][1];
      const int col = (4 + wave) * 16 + fq * 4;
      const u32x2 v = {pk_bf16(rs * (acch[0] - mu * csh[0]), rs * (acch[1] - mu * csh[1])),
                       pk_bf16(rs * (acch[2] - mu * csh[2]), rs * (acch[3] - mu * csh[3]))};
      *(u32x2*)(qt + fr * RS + col * 2) = v;
    }
    STAMP(4);
    lds_barrier();
    STAMP(5);
    const unsigned qa = lds0 + (16 + wave * 16 + fr) * RS + fq * 16;
    const unsigned ka = lds0 + (wave * 16 + fr) * RS + 128 + fq * 16;
    const unsigned va = lds0 + (wave * 16 + fq * 4 + wq) * RS + 256 + wp * 16;
    bf16x8 qf[2], kf[2][2]; s16x4 vf[2][2][2];
    qf[0] = ds_b128<0>(qa); kf[0][0] = ds_b128<0>(ka); kf[0][1] = ds_b128<16 * RS>(ka);
    qf[1] = ds_b128<64>(qa); kf[1][0] = ds_b128<64>(ka); kf[1][1] = ds_b128<16 * RS + 64>(ka);
    vf[0][0][0] = ds_tr<0>(va); vf[0][0][1] = ds_tr<16 * RS>(va); vf[0][1][0] = ds_tr<8>(va); vf[0][1][1] = ds_tr<16 * RS + 8>(va);
    vf[1][0][0] = ds_tr<64>(va); vf[1][0][1] = ds_tr<16 * RS + 64>(va); vf[1][1][0] = ds_tr<72>(va); vf[1][1][1] = ds_tr<16 * RS + 72>(va);
    asm volatile("s_waitcnt lgkmcnt(8)" ::: "memory");
#pragma unroll
    for (int hh = 0; hh < 2; ++hh) asm volatile("" : "+v"(qf[hh]), "+v"(kf[hh][0]), "+v"(kf[hh][1]));
    const float sc2 = 0.17677669529663687f * 1.4426950408889634f;
    u32x4 ov[2];
    const f32x4 z = {0.f, 0.f, 0.f, 0.f};
    f32x4 sc[2][2]; float lg[2][4], mx[2], sum[2];
    auto x16 = [](float v) { auto p = __builtin_amdgcn_permlane16_swap(__float_as_uint(v), __float_as_uint(v), false, false); return (f32x2){__uint_as_float(p[0]), __uint_as_float(p[1])}; };
    auto x32 = [](float v) { auto p = __builtin_amdgcn_permlane32_swap(__float_as_uint(v), __float_as_uint(v), false, false); return (f32x2){__uint_as_float(p[0]), __uint_as_float(p[1])}; };
#pragma unroll
    for (int hh = 0; hh < 2; ++hh) {
      sc[hh][0] = MFMA(kf[hh][0], qf[hh], z);
      sc[hh][1] = MFMA(kf[hh][1], qf[hh], z);
    }
    __builtin_amdgcn_sched_barrier(0);
#pragma unroll
    for (int hh = 0; hh < 2; ++hh) {
#pragma unroll
      for (int j = 0; j < 4; ++j) lg[hh][j] = __builtin_fmaf(par ? sc[hh][j >> 1][(j & 1) * 2 + 1] : sc[hh][j >> 1][(j & 1) * 2], sc2, bias[j]);
      mx[hh] = fmaxf(fmaxf(lg[hh][0], lg[hh][1]), fmaxf(lg[hh][2], lg[hh][3]));
    }
    __builtin_amdgcn_sched_barrier(0);
    { const f32x2 p = x16(mx[0]), q = x16(mx[1]); mx[0] = fmaxf(p[0], p[1]); mx[1] = fmaxf(q[0], q[1]); }
    { const f32x2 p = x32(mx[0]), q = x32(mx[1]); mx[0] = fmaxf(p[0], p[1]); mx[1] = fmaxf(q[0], q[1]); }
    __builtin_amdgcn_sched_barrier(0);
#pragma unroll
    for (int hh = 0; hh < 2; ++hh) {
#pragma unroll
      for (int j = 0; j < 4; ++j) lg[hh][j] = __builtin_amdgcn_exp2f(lg[hh][j] - mx[hh]);
      sum[hh] = (lg[hh][0] + lg[hh][1]) + (lg[hh][2] + lg[hh][3]);
    }
    __builtin_amdgcn_sched_barrier(0);
    asm volatile("s_waitcnt lgkmcnt(0)" ::: "memory");
#pragma unroll
    for (int hh = 0; hh < 2; ++hh) asm volatile("" : "+v"(vf[hh][0][0]), "+v"(vf[hh][0][1]), "+v"(vf[hh][1][0]), "+v"(vf[hh][1][1]));
    { const f32x2 p = x16(sum[0]), q = x16(sum[1]); sum[0] = p[0] + p[1]; sum[1] = q[0] + q[1]; }
    { const f32x2 p = x32(sum[0]), q = x32(sum[1]); sum[0] = __builtin_amdgcn_rcpf(p[0] + p[1]); sum[1] = __builtin_amdgcn_rcpf(q[0] + q[1]); }
    __builtin_amdgcn_sched_barrier(0);
#pragma unroll
    for (int hh = 0; hh < 2; ++hh) {
      const float inv = sum[hh];
      const unsigned c0 = pk_bf16(lg[hh][0] * inv, lg[hh][1] * inv), c1 = pk_bf16(lg[hh][2] * inv, lg[hh][3] * inv);
      const u32x4 pp = {__builtin_amdgcn_perm(0u, c0, pl), __builtin_amdgcn_perm(0u, c0, ph), __builtin_amdgcn_perm(0u, c1, pl), __builtin_amdgcn_perm(0u, c1, ph)};
      const bf16x8 pf = __builtin_bit_cast(bf16x8, pp);
      sc[hh][0] = MFMA(__builtin_shufflevector(vf[hh][0][0], vf[hh][0][1], 0, 1, 2, 3, 4, 5, 6, 7), pf, z);
      sc[hh][1] = MFMA(__builtin_shufflevector(vf[hh][1][0], vf[hh][1][1], 0, 1, 2, 3, 4, 5, 6, 7), pf, z);
    }
    __builtin_amdgcn_sched_barrier(0);
#pragma unroll
    for (int hh = 0; hh < 2; ++hh) ov[hh] = (u32x4){pk_bf16(sc[hh][0][0], sc[hh][0][1]), pk_bf16(sc[hh][0][2], sc[hh][0][3]), pk_bf16(sc[hh][1][0], sc[hh][1][1]), pk_bf16(sc[hh][1][2], sc[hh][1][3])};
    STAMP(10);
#pragma unroll
    for (int hh = 0; hh < 2; ++hh) __builtin_amdgcn_raw_buffer_store_b128(ov[hh], rO, (((m0 >> 4) + wave) * RBO + tn * 8 + hh * 4) * 256 + lane * 16, 0, 16);
    STAMP(11);
  }
  STAMP(7);
  STAMP(8);
  asm volatile("s_waitcnt vmcnt(0)" ::: "memory");
  raw_barrier();
  if (wave != 0) hook();
  if (tid == 0) __hip_atomic_fetch_add(a.sync + SYO(tm), FIN, __ATOMIC_RELAXED, __HIP_MEMORY_SCOPE_AGENT);
  if (wave == 0) hook();
  STAMP(3);
#endif
}

template <class HK>
__device__ __forceinline__ void glu_phase(const GemmArgs& a, HK hook) {
#if defined(__HIP_DEVICE_COMPILE__)
  constexpr int LDW = DF_, WN = 2;
  constexpr int UPOFF = 32 * 224 + 16;
  constexpr int XT = 32768, WU = 14592;
  constexpr int XBASE = 0, WBASE = 3 * XT, RING = WBASE + 4 * WU;
  constexpr int MF = 4, NF = 7;
  constexpr int NT = 16, NXI = 16, NWI = 14, NLW = 4, XI = 4, WI = 4;
  extern __shared__ __attribute__((aligned(16))) char smem[];
  STAMP(0);
  const unsigned lds0 = (unsigned)(size_t)(lds_char_ptr)smem;
  const int tid = threadIdx.x, lane = tid & 63;
  const int wave = __builtin_amdgcn_readfirstlane(tid >> 6);
  const int wm = wave / WN, wn = wave % WN;
  const int bid = blockIdx.x, xcd = bid & 7, idx = bid >> 3;
  const int tm = (xcd / 4) * 4 + idx / 8, tn = (xcd % 4) * 8 + idx % 8;
  const int m0 = tm * 256;
  const int krot = (tm >> 2) * 8;
  const int fr = lane & 15, fq = lane >> 4;
  const int wq = (lane >> 2) & 3, wp = lane & 3;
  const int panel = m0 >> 8;
  constexpr int TCH = RING + 5120;
  constexpr int DONE = TCH + 896;
  auto done_inc = [&]() { if (lane == 0) asm volatile("ds_add_u32 %0, %1" :: "v"(lds0 + DONE), "v"(1) : "memory"); };
  auto touch = [&](const void* base, unsigned voff, unsigned soff, unsigned lim = 0x7fffffffu) {
    __builtin_amdgcn_raw_ptr_buffer_load_lds(RSRCN(base, lim), LDSP(lds0 + TCH), 4, voff, soff, 0, 0);
  };
  const auto rO = RSRC(a.O);

  unsigned xsrc[XI], wsrc[WI];
#pragma unroll
  for (int i = 0; i < XI; ++i) {
    const int j = (wave % NLW) + NLW * i;
    xsrc[i] = (unsigned)((((m0 >> 4) + (j >> 1)) * RBH + (j & 1) * 4) * 256 + lane * 16);
  }
#pragma unroll
  for (int i = 0; i < WI; ++i) {
    int j = (wave % NLW) + NLW * i; if (j >= NWI) j -= NLW;
    const int jj = j >= 7 ? j - 7 : j;
    const int b = jj * 1024 + lane * 16, p = b / 224, pos = (b % 224) >> 4;
    const int row = 8 * ((p & 15) >> 2) + (p & 3) + 4 * (p >> 4);
    wsrc[i] = (unsigned)(row * LDW + tn * 112 + pos * 8) * 2u;
  }
  auto ldX = [&](int i, int tile, int part, int slot) {
    tile = (tile + krot) & 15;
    const int j = wave + NLW * i;
    __builtin_amdgcn_raw_ptr_buffer_load_lds(RSRC(a.X), LDSP(lds0 + XBASE + slot * XT + (part * NXI + j) * 1024), 16, xsrc[i], tile * 2048 + part * (NXI / 2) * RBH * 256, 0, 16);
  };
  auto ldW = [&](int i, int unit, int slot) {
    const auto rW0 = RSRC(a.W0);
    const auto rW1 = RSRC(a.W1);
    int j = wave + NLW * i; if (j >= NWI) j -= NLW;
    unit = (unit + 2 * krot) & 31;
    const bool up = j >= 7;
    __builtin_amdgcn_raw_ptr_buffer_load_lds(up ? rW1 : rW0, LDSP(lds0 + WBASE + slot * WU + (up ? UPOFF + (j - 7) * 1024 : j * 1024)), 16, wsrc[i], unit * 32 * LDW * 2, 0, 0);
  };
  auto issueX = [&](int tile, int part, int slot) {
    if (wave >= NLW) return;
#pragma unroll
    for (int i = 0; i < XI; ++i) ldX(i, tile, part, slot);
  };
  auto issueW = [&](int unit, int slot) {
    if (wave >= NLW) return;
#pragma unroll
    for (int i = 0; i < WI; ++i) ldW(i, unit, slot);
  };

  f32x4 acc[NF][MF];
#pragma unroll
  for (int n = 0; n < NF; ++n)
#pragma unroll
    for (int m = 0; m < MF; ++m) acc[n][m] = (f32x4){0.f, 0.f, 0.f, 0.f};
  f32x4 accs = {0.f, 0.f, 0.f, 0.f};

  const unsigned xa0 = lds0 + XBASE + wm * MF * 16 * 128 + lane * 16, xa1 = xa0 + 1024;
  unsigned wa[2];
#pragma unroll
  for (int c = 0; c < 2; ++c) wa[c] = lds0 + WBASE + (4 * fq + wq) * 224 + wn * 112 + (c == 0 ? wp * 8 : 96 + (wp & 1) * 8 + (wp >> 1) * UPOFF);

  using I0 = IC<0>; using I1 = IC<1>;
  using T = std::true_type; using F = std::false_type;
  typedef Frags<MF, NF> FR;
  struct RdCtx { unsigned xa, wb[2]; };
  auto mkctx = [&](bool ks1, unsigned xoffs, unsigned woffs) {
    RdCtx c; c.xa = (ks1 ? xa1 : xa0) + xoffs; c.wb[0] = wa[0] + woffs; c.wb[1] = wa[1] + woffs;
    return c;
  };
  auto rd_item = [&](FR& f, const RdCtx& c, auto kc) {
    constexpr int k = decltype(kc)::value;
    if constexpr (k < MF) f.x[k] = ds_b128<k * 2048>(c.xa);
    else {
      constexpr int n = k - MF, cc = n == 6, o = n == 6 ? 0 : (n >> 1) * 32 + (n & 1) * UPOFF;
      f.lo[n] = ds_tr<o>(c.wb[cc]); f.hi[n] = ds_tr<o + 16 * 224>(c.wb[cc]);
    }
  };
  auto wait_frags = [&](FR& f) {
    asm volatile("s_waitcnt lgkmcnt(0)" ::: "memory");
#pragma unroll
    for (int m = 0; m < MF; ++m) asm volatile("" : "+v"(f.x[m]));
#pragma unroll
    for (int n = 0; n < NF; ++n) asm volatile("" : "+v"(f.lo[n]), "+v"(f.hi[n]));
  };
  auto step = [&](FR& cur, FR& nxt, const RdCtx& c, auto PFc, auto NLc, auto&& ld) {
    constexpr bool PF = decltype(PFc)::value != 0; constexpr int NL = decltype(NLc)::value;
    constexpr int R = MF + NF, RG = NF - 1;
    static_for<0, NF>([&](auto gc) {
      constexpr int g = decltype(gc)::value;
      if constexpr (PF) static_for<0, R>([&](auto kc) { constexpr int k = decltype(kc)::value; if constexpr (k * RG / R == g) rd_item(nxt, c, kc); });
      static_for<0, NL>([&](auto ic) { constexpr int i = decltype(ic)::value; if constexpr (i * NF / NL == g) ld(i); });
      const bf16x8 w = __builtin_shufflevector(cur.lo[g], cur.hi[g], 0, 1, 2, 3, 4, 5, 6, 7);
#pragma unroll
      for (int m = 0; m < MF; ++m) acc[g][m] = MFMA(w, cur.x[m], acc[g][m]);
      __builtin_amdgcn_sched_barrier(0);
    });
    if constexpr (PF) wait_frags(nxt);
  };
  auto stepg = [&](auto CSc, FR& cur, FR& nxt, const RdCtx& c, auto R0c, auto IBc, auto&& sync, auto NLc, auto&& ld) {
    constexpr bool CS = decltype(CSc)::value; constexpr int R0 = decltype(R0c)::value, IB = decltype(IBc)::value, NL = decltype(NLc)::value;
    constexpr int NM = NF * MF, NR = MF + 2 * NF;
    static_for<0, NM>([&](auto ic) {
      constexpr int I = decltype(ic)::value, g = I / MF, m = I % MF;
      constexpr int RL = IB > 8 ? 20 : 22;
      const bf16x8 w = __builtin_shufflevector(cur.lo[g], cur.hi[g], 0, 1, 2, 3, 4, 5, 6, 7);
      acc[g][m] = MFMA(w, cur.x[m], acc[g][m]);
      __builtin_amdgcn_sched_barrier(0);
      if constexpr (I == IB) sync();
      static_for<0, NR>([&](auto rc) {
        constexpr int r = decltype(rc)::value;
        if constexpr (R0 + r * (RL - R0) / (NR - 1) == I) {
          if constexpr (r < MF) nxt.x[r] = ds_b128<r * 2048>(c.xa);
          else {
            constexpr int n = (r - MF) >> 1, cc = n == 6, o = n == 6 ? 0 : (n >> 1) * 32 + (n & 1) * UPOFF;
            if constexpr ((r - MF) & 1) nxt.hi[n] = ds_tr<o + 16 * 224>(c.wb[cc]); else nxt.lo[n] = ds_tr<o>(c.wb[cc]);
          }
        }
      });
      static_for<0, NL>([&](auto lc) { constexpr int i = decltype(lc)::value; if constexpr (R0 + i * (NM - 1 - R0) / NL == I) ld(i); });
      if constexpr (CS && m == MF - 1) {
        const unsigned sv = fr == g ? 0x3f803f80u : 0u;
        const u32x4 s4 = {sv, sv, sv, sv};
        accs = MFMA(w, __builtin_bit_cast(bf16x8, s4), accs);
      }
      __builtin_amdgcn_sched_barrier(0);
    });
    wait_frags(nxt);
  };
  FR fa, fb;

  f32x4* tb_row = (f32x4*)(smem + RING);
  float* tb_cs = (float*)(tb_row + 256);
  constexpr int RTAB = 524288;
  auto glu_stats_issue = [&]() {
    if (wave == 7) __builtin_amdgcn_raw_ptr_buffer_load_lds(RSRC(a.st), LDSP(lds0 + RING + 3072), 16, ((lane >> 2) * S_ + m0 + tn * 8 + (lane & 3) * 2) * 8, 0, 0, 16);
  };
  auto glu_stats = [&]() {
    int lx = lane; asm volatile("" : "+v"(lx));
    const unsigned sa = lds0 + RING + 3072 + lx * 8;
    f32x2 p0 = ds_b64<0>(sa), p1 = ds_b64<512>(sa);
    asm volatile("s_waitcnt lgkmcnt(0)" : "+v"(p0), "+v"(p1) :: "memory");
    const float mu = sum8(p0[0] + p1[0]) * (1.f / 16.f);
    const float v = sum8((p0[1] + p1[1]) + 64.f * ((p0[0] - mu) * (p0[0] - mu) + (p1[0] - mu) * (p1[0] - mu))) * (1.f / 1024.f) + 1e-5f;
    const float rs = 1.0f / sqrtf(v);
    const f32x4 rv = {mu, -0.10294324f * rs * rs * rs, -2.3022082f * rs, v};
    __builtin_amdgcn_raw_buffer_store_b128(__builtin_bit_cast(u32x4, rv), RSRC(a.cs), lx < 8 ? (unsigned)(RTAB + (m0 + tn * 8 + lx) * 16) : 0x80000000u, 0, 16);
  };
  auto epilogue = [&](auto EAc) __attribute__((always_inline)) {
    STAMP(2);
    constexpr bool EARLY = decltype(EAc)::value;
    static_assert(DONE + 4 == ACNT && TCH + 256 == FPAD, "LDS");
    if constexpr (!EARLY) {
      wait_vm<0>();
      lds_wait8(lds0 + DONE);
      __builtin_amdgcn_s_setprio(2);
      STAMPW(9, 4);
      hook(I0{});
      STAMPW(10, 4);
    } else { __builtin_amdgcn_s_setprio(3); hook(I1{}); }
    asm volatile("" ::: "memory");
    f32x4 cg[4], cu[4], rw[MF], rw6[2];
    int lz = lane; asm volatile("" : "+v"(lz));
    const int fr = lz & 15, fq = lz >> 4;
#pragma unroll
    for (int p = 0; p < 4; ++p) {
      const int o = wn * 56 + p * 16 + (p == 3 ? (fq & 1) : fq) * 4;
      cg[p] = *(const f32x4*)(tb_cs + o); cu[p] = *(const f32x4*)(tb_cs + 112 + o);
    }
#pragma unroll
    for (int m = 0; m < MF; ++m) rw[m] = tb_row[(wm * MF + m) * 16 + fr];
#pragma unroll
    for (int q = 0; q < 2; ++q) rw6[q] = tb_row[(wm * MF + 2 * q + (fq >> 1)) * 16 + fr];
    const int gz = ((m0 >> 4) + wm * 4) * RBA * 256 + fr * 16, g = tn * 2 + wn;
    const int go0 = gz + ((fq & 1) * RBA + (fq >> 1)) * 256, go6 = gz + ((fq & 1) * 2 + (fq >> 1)) * RBA * 256;
    unsigned hv[MF][NF];
    auto put = [&](unsigned a0, unsigned a1, unsigned b0, unsigned b1, int vo, int so) {
      const auto s0 = __builtin_amdgcn_permlane16_swap(a0, b0, false, false), s1 = __builtin_amdgcn_permlane16_swap(a1, b1, false, false);
      __builtin_amdgcn_raw_buffer_store_b128((u32x4){s0[0], s1[0], s0[1], s1[1]}, rO, vo, so, 16);
      __builtin_amdgcn_sched_barrier(0);
      asm volatile("s_nop 1" ::: "memory");
      __builtin_amdgcn_sched_barrier(0);
    };
#define STAGE(body) _Pragma("unroll") for (int i = 0; i < 2 * NN; ++i) { const int m = MB + i / NN, n = N0 + i % NN; const f32x4 k = n == 6 ? rw6[m >> 1] : rw[m]; body; } __builtin_amdgcn_sched_barrier(0)
    auto grp = [&](auto MBc, auto N0c, auto NNc, auto&& mid) {
      constexpr int MB = decltype(MBc)::value, N0 = decltype(N0c)::value, NN = decltype(NNc)::value;
      f32x2 G[2 * NN], U[2 * NN], P[2 * NN];
      __builtin_amdgcn_sched_barrier(0);
      STAGE({ const int e = (n == 6 ? m & 1 : n & 1) * 2; const f32x4 x = n == 6 ? acc[6][m & 2] : acc[n & 6][m]; G[i] = ((f32x2){x[e], x[e + 1]}) - k[0] * ((f32x2){cg[n >> 1][e], cg[n >> 1][e + 1]}); });
      STAGE(P[i] = G[i] * G[i]);
      STAGE(P[i] = P[i] * k[1] + k[2]);
      STAGE(P[i] = G[i] * P[i]);
      STAGE({ const int e = (n == 6 ? m & 1 : n & 1) * 2; const f32x4 x = n == 6 ? acc[6][m | 1] : acc[n | 1][m]; U[i] = ((f32x2){x[e], x[e + 1]}) - k[0] * ((f32x2){cu[n >> 1][e], cu[n >> 1][e + 1]}); });
      STAGE(P[i] = ((f32x2){__builtin_amdgcn_exp2f(P[i][0]), __builtin_amdgcn_exp2f(P[i][1])}));
      mid();
      __builtin_amdgcn_sched_barrier(0);
      STAGE(P[i] = P[i] * k[3] + k[3]);
      STAGE(P[i] = ((f32x2){__builtin_amdgcn_rcpf(P[i][0]), __builtin_amdgcn_rcpf(P[i][1])}));
      STAGE(U[i] = G[i] * U[i]);
      STAGE(U[i] = U[i] * P[i]);
      STAGE({ hv[m][n] = pk_bf16(U[i][0], U[i][1]); asm volatile("" : "+v"(hv[m][n])); });
    };
#undef STAGE
    using I2 = IC<2>; using I3 = IC<3>; using I4 = IC<4>;
    grp(I0{}, I0{}, I4{}, [&]() {});
    grp(I2{}, I0{}, I4{}, [&]() {
#pragma unroll
      for (int p = 0; p < 2; ++p) put(hv[0][2 * p], hv[0][2 * p + 1], hv[1][2 * p], hv[1][2 * p + 1], go0 + p * 512, g * 1024); });
#pragma unroll
    for (int p = 0; p < 2; ++p) put(hv[2][2 * p], hv[2][2 * p + 1], hv[3][2 * p], hv[3][2 * p + 1], go0 + p * 512, g * 1024 + 2 * RBA * 256);
    asm volatile("" ::: "memory");
    __builtin_amdgcn_sched_barrier(0);
    STAMP(4);
    STAMPW(11, 4);
    __builtin_amdgcn_s_setprio(EARLY ? 1 : 0);
#pragma unroll
    for (int q = 0; q < 2; ++q)
#pragma unroll
      for (int e = 0; e < 4; ++e) {
        const float x0 = acc[6][2 * q][e], x1 = acc[6][2 * q + 1][e];
        auto sw = __builtin_amdgcn_permlane32_swap(__float_as_uint(x0), __float_as_uint(x1), false, false);
        acc[6][2 * q][e] = __uint_as_float(sw[0]); acc[6][2 * q + 1][e] = __uint_as_float(sw[1]);
      }
    const int sb = (256 + g * 3) * 256;
    grp(I0{}, I4{}, I3{}, [&]() {});
    if (!EARLY) { wait_vm<0>(); lds_inc(lds0 + ACNT); STAMPW(6, 4); }
    grp(I2{}, I4{}, I3{}, [&]() { put(hv[0][4], hv[0][5], hv[1][4], hv[1][5], go0, sb); });
    put(hv[2][4], hv[2][5], hv[3][4], hv[3][5], go0, sb + 2 * RBA * 256);
    put(hv[0][6], hv[1][6], hv[2][6], hv[3][6], go6, sb + 512);
    asm volatile("" ::: "memory");
    if constexpr (!EARLY) lds_inc(lds0 + ACNT + 4);
  };

  constexpr int WR = 2;
  if (wave == 5) { ds_w32(lds0 + DONE, 0); ds_w32(lds0 + DONE + 4, 0); ds_w32(lds0 + DONE + 8, 0); }
  STAMP(9); STAMP(6);
  auto check_tile = [&](int t) {
    int lx = lane; asm volatile("" : "+v"(lx));
    const unsigned ad = lds0 + XBASE + t * XT + wave * 1024 + lx * 16;
    if (__builtin_expect(marked<8>(ad), 0)) {
      for (int tries = 0;; ++tries) {
        if (tries >= KN_TRY) wait_count(lds0, a.sync + panel * 64, snap_get(lds0, 1), 32u, 2u);
#pragma unroll 1
        for (int u = t; u < t + 1 + (t == 0); ++u) { issueX(u, 0, u); issueX(u, 1, u); }
        if (t == 0) wait_vm<2 * XI>(); else wait_vm<0>();
        if (tries >= KN_TRY || !marked<8>(ad)) break;
      }
    }
  };
  auto run = [&](auto LDRc) __attribute__((always_inline)) {
    constexpr bool LDR = decltype(LDRc)::value;
    if constexpr (LDR) {
      issueX(0, 0, 0); issueX(0, 1, 0); issueX(1, 0, 1); issueX(1, 1, 1);
      issueW(2, 2 ^ WR); issueW(3, 3 ^ WR); wait_vm<2 * XI + 2 * WI>();
      check_tile(0);
    }
    lds_barrier();
    if constexpr (!LDR) { if (wave == 4) __builtin_amdgcn_raw_ptr_buffer_load_lds(RSRCN(a.sync + panel * 64, 4), LDSP(lds0 + TCH + 256), 4, lane * 4, 0, 0, 16); }
    { const RdCtx c = mkctx(false, 0, WR * WU); static_for<0, MF + NF>([&](auto kc) { rd_item(fa, c, kc); }); }
    wait_frags(fa);
    STAMP(1);
    { const RdCtx c = mkctx(true, 0, (WR + 1) * WU); step(fa, fb, c, T{}, IC<LDR ? 2 * XI : 0>{}, [&](int i) { ldX(i % XI, 2, i / XI, 2); }); }
    int xs = 1;
    unsigned long long tb = (unsigned long long)a.PF0; unsigned tv = 0, tl = 0x7fffffffu, ts = 1835008u, tmask = 0; int tj = 1, tsh = 1;
    if constexpr (!LDR) {
      unsigned long long c0, c1; CODE_ADDR(c0, "k_all"); CODE_ADDR(c1, "k_zend");
      constexpr int CTJ = 11, WJ = 1, WE = 8, DJ = 6, KT = 4;
      tj = DJ;
      if (wave == 4) { tb = c0; tl = c1 - c0 < 262144ull ? (unsigned)(c1 - c0) : 0u; ts = 0; tmask = 1u << CTJ; }
      if (wave == 5) tmask = 0x55u << (DJ + (idx & 1));
      if (wave == 6) { tb = (unsigned long long)((idx & 16) ? a.W1 : a.W0); ts = 64 * LDW * 2; tmask = ((1u << (WE - KT)) - 1u) << WJ; tj = WJ - krot - KT; tsh = 0; }
      const int l4 = lane / 14;
      tv = wave == 4 ? (unsigned)((lane * 32 + idx) * 128)
         : (lane >= 56 ? 0x80000000u : (wave == 5 ? (unsigned)(bid * 7168 + lane * 128) : (unsigned)(((idx & 15) * 4 + l4) * (LDW * 2) + (xcd & 3) * 1792 + (lane - l4 * 14) * 128)));
    }
    const int js = 2 * (tm & 3) + 1;
    const auto rC = RSRC(a.cs);
    unsigned* ccnt = a.sync + 1024 + tn * 32;
    constexpr int JR = 10, JS = 2;
    const int jf = wave == 4 ? NT - 4 : (wave == 7 ? JR : 64);
    const unsigned* fcnt = wave == 4 ? ccnt : a.sync + panel * 64 + 32;
    const int jm = (idx >> 4) == ((xcd >> 1) & 1) ? 3 : 64;
    int mo = (((m0 >> 4) + wave * 4) * RBA + tn * 8) * 256;
    const unsigned evm = tmask | (8u << js) | (wave == 4 ? 3u << (NT - 4) : 0u) | (wave == 7 ? ((5u << JS) | 2u | (11u << JR)) : 0u);
    constexpr int CFL = TCH + 256, CPB = XBASE + ((NT - 3) % 3) * XT;
    auto iter = [&](int j, auto LWc, auto LXc, auto WAITc, auto CAc, auto CBc) {
      constexpr bool LW = decltype(LWc)::value && LDR, LX = decltype(LXc)::value && LDR, LP = LW && !LX;
      if constexpr (LDR) {
        wait_vm<decltype(WAITc)::value>();
        if constexpr (LX) { if (__builtin_expect(j <= 2, 0)) check_tile(j); }
        if constexpr (LX) {
          if ((unsigned)(j - jm) < 8u) {
            int lx = lane; asm volatile("" : "+v"(lx));
            unsigned on = 0xffffffffu; asm volatile("" : "+v"(on));
            __builtin_amdgcn_raw_buffer_store_b128((u32x4){on, on, on, on}, rO, lx * 16, mo, 16);
            mo += (j & 1) ? 1024 : RBA * 256 - 1024;
          }
          if (j == jm + 9) { if (lane == 0) __hip_atomic_fetch_add(a.sync + (8 + panel) * 64, 1u, __ATOMIC_RELAXED, __HIP_MEMORY_SCOPE_AGENT); }
        }
      }
      if constexpr (!LDR) {
        if (__builtin_expect((evm >> j) & 1, 0)) {
          if (j == js + 3) {
            if ((tmask >> (j - 1)) & 1) wait_vm<1>(); else wait_vm<0>();
            if (lane == 0) __hip_atomic_fetch_add(ccnt, 1u, __ATOMIC_RELAXED, __HIP_MEMORY_SCOPE_AGENT);
          }
          if (j == 1) glu_stats_issue();
          if (j == JS && wave == 7) { wait_vm<0>(); glu_stats(); }
          if (j == JS + 2 && wave == 7) {
            if (j == js + 2) wait_vm<1>(); else wait_vm<0>();
            if (lane == 0) __hip_atomic_fetch_add((unsigned*)fcnt, 1u, __ATOMIC_RELAXED, __HIP_MEMORY_SCOPE_AGENT);
          }
          {
            int lx = lane; asm volatile("" : "+v"(lx));
            if (j == jf) __builtin_amdgcn_raw_ptr_buffer_load_lds(RSRCN(fcnt, 4), LDSP(lds0 + CFL + (wave & 1) * 256), 4, lx * 4, 0, 0, 16);
            if (j == jf + 1) {
              wait_vm<0>();
              int fv = ds_r32(lds0 + CFL + (wave & 1) * 256);
              const unsigned f0 = snap_get(lds0, 3 - (wave & 1));
              asm volatile("" : "+v"(fv));
              while ((unsigned)__builtin_amdgcn_readfirstlane(fv) - f0 < 32u && in_time(lds0, 3u))
                asm volatile("global_load_dword %0, %1, %2 sc1\n\ts_waitcnt vmcnt(0)" : "=v"(fv) : "v"(0), "s"(fcnt) : "memory");
              if (wave == 7) {
#pragma unroll
                for (int i = 0; i < 4; ++i) __builtin_amdgcn_raw_ptr_buffer_load_lds(rC, LDSP(lds0 + RING + i * 1024), 16, lx * 16, RTAB + m0 * 16 + i * 1024, 0, 16);
              }
            }
            if (j == jf + 3) wait_vm<0>();
          }
          if ((tmask >> j) & 1) touch((const void*)tb, tv, (((j - tj) >> tsh) & 15) * ts, tl);
        }
        __builtin_amdgcn_sched_barrier(0);
      }
      int xp = xs + 2; if (xp >= 3) xp -= 3;
      const int ws = ((j & 1) * 2) ^ WR;
      auto lda = [&](int i) { ldW(i % WI, 2 * j + 2 + i / WI, (ws ^ 2) + i / WI); };
      auto ldb = [&](int i) {
          if constexpr (LX) ldX(i % XI, j + 2, i / XI, xp);
          else __builtin_amdgcn_raw_ptr_buffer_load_lds(rC, LDSP(lds0 + CPB + wave * 8192 + i * 1024), 16, lane * 16, (tn * 2 + wn) * 7168 + i * 1024, 0, 16); };
      using NLA = IC<LW ? 2 * WI : 0>; using NLB = IC<LX ? 2 * XI : (LP ? 7 : 0)>;
      { const RdCtx c = mkctx(false, xs * XT, ws * WU); stepg(CAc, fb, fa, c, IC<LDR ? 2 : 0>{}, IC<LDR ? 1 : -1>{}, [&]() { raw_barrier(); }, NLA{}, lda); }
      { const RdCtx c = mkctx(true, xs * XT, (ws + 1) * WU); stepg(CBc, fa, fb, c, I0{}, IC<LDR ? -1 : 25>{},
          [&]() { asm volatile("s_waitcnt lgkmcnt(0)" ::: "memory"); if (decltype(WAITc)::value == 0 || j + 1 < NT) raw_barrier(); }, NLB{}, ldb); }
      xs = xs + 1; if (xs >= 3) xs = 0;
    };
    using WX_ = IC<2 * XI>;
    bf16x8 pv[8];
    if constexpr (LDR) {
      for (int j = 1; j < NT - 2; ++j) iter(j, T{}, T{}, WX_{}, F{}, F{});
      iter(NT - 2, T{}, F{}, WX_{}, F{}, F{});
      __builtin_amdgcn_s_setprio(3);
      iter(NT - 1, F{}, F{}, IC<7>{}, F{}, F{});
      wait_vm<0>();
      const unsigned ca = lds0 + CPB + wave * 8192 + fq * 1792 + (fr < 14 ? fr : 0) * 16;
      static_for<0, 8>([&](auto ic) { constexpr int i = decltype(ic)::value; pv[i] = ds_b128<i * 224>(ca); });
    } else {
      int j = 1;
      if (wave == 4) {
        wait_vm<0>();
        int fv = ds_r32(lds0 + TCH + 256);
        const unsigned f0 = snap_get(lds0, 1);
        asm volatile("" : "+v"(fv));
        if (((unsigned)__builtin_amdgcn_readfirstlane(fv) - f0) >> 16 < 32u) wait_count(lds0, a.sync + panel * 64, f0, 32u, 2u);
      }
      raw_barrier();
#pragma unroll 1
      for (; j < js; ++j) iter(j, F{}, F{}, I0{}, F{}, F{});
      const int ja = wm == 2 ? js + 2 : js;
#pragma unroll 1
      for (; j < ja; ++j) iter(j, F{}, F{}, I0{}, T{}, F{});
#pragma unroll 1
      for (; j < js + 2; ++j) iter(j, F{}, F{}, I0{}, F{}, T{});
      { int lx = lane; asm volatile("" : "+v"(lx));
        __builtin_amdgcn_raw_buffer_store_b128(__builtin_bit_cast(u32x4, accs), rC, (lx & 15) < NF ? (unsigned)((tn * 4 + wn * 2) * 3584 + (tm * 2 + (wm & 1)) * 224
                + ((lx & 15) < 6 ? (lx & 1) * 3584 + ((lx & 15) >> 1) * 64 + (lx >> 4) * 16 : (lx >> 5) * 3584 + 192 + (lx & 16))) : 0x80000000u, 0, 16); }
#pragma unroll 1
      for (; j < NT; ++j) iter(j, F{}, F{}, I1{}, F{}, F{});
      done_inc();
    }
    { const RdCtx c = mkctx(false, 0, 0); step(fb, fa, c, F{}, IC<LDR ? 2 : 0>{}, [&](int i) {
        if (i == 1) {
          asm volatile("s_waitcnt lgkmcnt(0)" ::: "memory");
#pragma unroll
          for (int k = 0; k < 8; ++k) asm volatile("" : "+v"(pv[k]));
          f32x4 cv = ((__builtin_bit_cast(f32x4, pv[0]) + __builtin_bit_cast(f32x4, pv[1])) + (__builtin_bit_cast(f32x4, pv[2]) + __builtin_bit_cast(f32x4, pv[3])))
                   + ((__builtin_bit_cast(f32x4, pv[4]) + __builtin_bit_cast(f32x4, pv[5])) + (__builtin_bit_cast(f32x4, pv[6]) + __builtin_bit_cast(f32x4, pv[7])));
#pragma unroll
          for (int k = 0; k < 4; ++k) { const float t = cv[k]; auto sw = __builtin_amdgcn_permlane16_swap(__float_as_uint(t), __float_as_uint(t), false, false); cv[k] = __uint_as_float(sw[0]) + __uint_as_float(sw[1]); }
          if ((lane & 31) < 14) *(f32x4*)(tb_cs + (lane >> 5) * 112 + wn * 56 + fr * 4) = cv;
          done_inc();
        } }); }
    if constexpr (LDR) __builtin_amdgcn_s_setprio(0);
    epilogue(LDRc);
  };
  if (wave < NLW) run(T{}); else run(F{});
  STAMP(7);
  STAMP(8);
  STAMPW(12, 4);
  STAMP(3);
#endif
}

#define KN_CT 7
#define KN_E 6
#define KN_B3 0
#define KN_WB 2
#define KN_GW 1
#define XAUXW 16
#define XAUXD 16
#define KN_WS 2
#define KN_HE 0
#define KN_FL 22
#define KN_LD 0
#define KN_DM 0
#define KN_LX 0
#define KN_TP 5
#define STAMPV(i) STAMP(i)
struct FragW { s16x4 lo[4], hi[4]; };
template <int MODE, int LO, int HI>
__device__ __forceinline__ void wo_phase(const GemmArgs& a, const GemmArgs& nx) {
#if defined(__HIP_DEVICE_COMPILE__)
  extern __shared__ __attribute__((aligned(16))) char smem[];
  if constexpr (MODE != 1) STAMP(0);
  const unsigned lds0 = (unsigned)(size_t)(lds_char_ptr)smem;
  const int tid = threadIdx.x, lane = tid & 63;
  const int wave = __builtin_amdgcn_readfirstlane(tid >> 6);
  const int bid = blockIdx.x, xcd = bid & 7, idx = bid >> 3;
  const int tm = (xcd >> 1) * 4 + (idx >> 3), tn = (xcd & 1) * 8 + (idx & 7);
  const int m0 = tm * 128, panel = m0 >> 8, rot = (xcd & 3) * 4;
  if constexpr (MODE == 1) {
    const auto rW = __builtin_amdgcn_make_buffer_rsrc((void*)a.W0, 0, 0x7fffffff, 0x00020000);
    const int row = wave * 8 + (lane >> 3);
    const unsigned wsrc = (unsigned)(row * D_ + tn * 64 + ((lane & 7) ^ wswz(row)) * 8) * 2u;
    static_for<LO, HI>([&](auto ic) { constexpr int i = decltype(ic)::value;
      __builtin_amdgcn_raw_ptr_buffer_load_lds(rW, (lds_void_ptr)(size_t)(lds0 + wo_pos(i >> 1) + (i & 1) * 8192 + wave * 1024), 16, wsrc, (((i & 1) * 8 + (i >> 1)) ^ rot) * (64 * D_ * 2), 0, 0); });
  } else {
    constexpr int PAD = 57344;
    const int g = wave >> 2, rw = wave & 3;
    const int fr = lane & 15, fq = lane >> 4, wq = (lane >> 2) & 3, wp = lane & 3;
    unsigned wa[4];
#pragma unroll
    for (int c = 0; c < 4; ++c) { const int row = 8 * fq + wq; wa[c] = lds0 + g * 8192 + row * 128 + (((c << 1) ^ wswz(row)) << 4) + wp * 8; }
    FragW f[3];
    auto rd = [&](auto ic, auto rc) {
      constexpr int i = decltype(ic)::value, r = decltype(rc)::value, P = wo_pos(i >> 1) + (i & 1) * 4096, B = P & ~32767, o = P & 32767;
      if constexpr (r & 1) f[i % 3].hi[r >> 1] = ds_tr<o + 512>(wa[r >> 1] + B); else f[i % 3].lo[r >> 1] = ds_tr<o>(wa[r >> 1] + B);
    };
    auto pin = [&](FragW& q) {
#pragma unroll
      for (int n = 0; n < 4; ++n) asm volatile("" : "+v"(q.lo[n]), "+v"(q.hi[n]));
    };
    static_for<0, 16>([&](auto rc) { constexpr int r = decltype(rc)::value; rd(std::integral_constant<int, r / 8>{}, std::integral_constant<int, r % 8>{}); });
    if constexpr (KN_CT & 4) { if (wave == 2) {
      unsigned long long c0, c1; CODE_ADDR(c0, "k_all"); CODE_ADDR(c1, "k_zend");
      wait_vm<0>();
      __builtin_amdgcn_raw_ptr_buffer_load_lds(__builtin_amdgcn_make_buffer_rsrc((void*)c0, 0, c1 - c0 < 262144ull ? (unsigned)(c1 - c0) : 0u, 0x00020000), (lds_void_ptr)(size_t)(lds0 + PAD), 4, (unsigned)((lane * 32 + idx) * 128), 0, 0, 0); } }
    if (wave == 1) wait_count(lds0, a.sync + SYO(tm), snap_get(lds0, 0), 16u, 1u);
    if constexpr (KN_E & 2) {
      if (wave == 3) {
        const unsigned* c2 = a.sync + ((lane & 1) ? (xcd >> 2) * 4 + (idx >> 3) : panel) * 64;
        const unsigned sa = snap_get(lds0, 6), sb = snap_get(lds0, 1), s0 = (lane & 1) ? sb : sa;
        while (__builtin_amdgcn_ballot_w64(((__hip_atomic_load(c2, __ATOMIC_RELAXED, __HIP_MEMORY_SCOPE_AGENT) - s0) & 0xffffu) < 32u) != 0 && in_time(lds0, 1u)) {}
      }
    }
    const auto rX = __builtin_amdgcn_make_buffer_rsrc((void*)a.X, 0, 0x7fffffff, 0x00020000);
    const int rb = (m0 >> 4) + rw * 2 + g;
    unsigned xs[2] = {(unsigned)(rb * RBO * 256 + lane * 16), (unsigned)((rb ^ 1) * RBO * 256 + lane * 16)};
    const int ck = (fq & 1) * 2 + (fq >> 1);
    unsigned orow = (unsigned)((rb * 16 + fr) * (D_ * 2) + tn * 128 + ck * 16), ofrg = (unsigned)((rb * RBH + tn * 8 + ck) * 256 + fr * 16);
    asm volatile("" : "+v"(xs[0]), "+v"(xs[1]), "+v"(orow), "+v"(ofrg));
    STAMP(9);
    if constexpr (!KN_B3) { if (!(KN_CT & 4) || wave != 2) wait_vm<0>(); }
    lds_barrier();
    STAMP(6);
    pin(f[0]); pin(f[1]);
    u32x4 xr[16][2], res[2];
#define KN_XD 4
    auto ldX = [&](int i, int m) {
      if (i < 16) xr[i][m] = __builtin_amdgcn_raw_buffer_load_b128(rX, xs[m] + (i & 1) * 1024, ((g * 8 + (i >> 1)) ^ rot) * 2048, XAUXW);
      else if (i == 16) res[m] = __builtin_amdgcn_raw_buffer_load_b128(__builtin_amdgcn_make_buffer_rsrc((void*)a.R, 0, 0x7fffffff, 0x00020000), orow + m * 64, 0, 0);
    };
#pragma unroll
    for (int i = 0; i < KN_XD; ++i) { ldX(i, 0); ldX(i, 1); __builtin_amdgcn_sched_barrier(0); }
    unsigned gsrc[2];
#pragma unroll
    for (int i = 0; i < 2; ++i) {
      const int j = wave + 8 * i, jj = j >= 7 ? j - 7 : j;
      const int b = jj * 1024 + lane * 16, q = b / 224, pos = (b % 224) >> 4;
      gsrc[i] = (unsigned)((8 * ((q & 15) >> 2) + (q & 3) + 4 * (q >> 4)) * DF_ + ((xcd & 3) * 8 + (idx & 7)) * 112 + pos * 8) * 2u;
      asm volatile("" : "+v"(gsrc[i]));
    }
    f32x4 acc[4][2];
#pragma unroll
    for (int n = 0; n < 4; ++n) { acc[n][0] = (f32x4){0.f, 0.f, 0.f, 0.f}; acc[n][1] = acc[n][0]; }
    __builtin_amdgcn_sched_barrier(0);
    static_for<0, 16>([&](auto ic) {
      constexpr int i = decltype(ic)::value;
      if constexpr (i >= 2) { asm volatile("s_waitcnt lgkmcnt(%0)" :: "n"(i < 15 ? 8 : 0) : "memory"); pin(f[i % 3]); }
      if constexpr (KN_B3 && i == (KN_WB & ~1) - 2) raw_barrier();
      static_for<0, 8>([&](auto jc) {
        constexpr int j = decltype(jc)::value, n = j >> 1, m = j & 1;
        const bf16x8 w = __builtin_shufflevector(f[i % 3].lo[n], f[i % 3].hi[n], 0, 1, 2, 3, 4, 5, 6, 7);
        acc[n][m] = __builtin_amdgcn_mfma_f32_16x16x32_bf16(w, __builtin_bit_cast(bf16x8, xr[i][m]), acc[n][m], 0, 0, 0);
        if constexpr (i + 2 < 16) rd(std::integral_constant<int, i + 2>{}, jc);
        if constexpr (j == 1 || j == 5) ldX(i + KN_XD, j >> 2);
        __builtin_amdgcn_sched_barrier(0);
      });
      if constexpr (i == 0) STAMP(1);
    });
    STAMP(2);
    auto slot = [&](int w) { return (w < 6 ? 32768 : WOB - 6 * 4096) + w * 4096 + lane * 16; };
#pragma unroll
    for (int n = 0; n < 4; ++n) *(f32x4*)(smem + slot(wave) + n * 1024) = acc[n][1];
    asm volatile("" : "+v"(res[0]), "+v"(res[1]));
    lds_barrier();
    STAMP(4);
    f32x4 pa[4];
    static_for<0, 4>([&](auto nc) { constexpr int n = decltype(nc)::value; pa[n] = __builtin_bit_cast(f32x4, ds_b128<n * 1024>(lds0 + slot(wave ^ 4))); });
    if constexpr (KN_GW) {
      auto ldG = [&](int i, int u) { const int j = wave + 8 * i;
        __builtin_amdgcn_raw_ptr_buffer_load_lds(__builtin_amdgcn_make_buffer_rsrc((void*)(j >= 7 ? nx.W1 : nx.W0), 0, 0x7fffffff, 0x00020000),
                          (lds_void_ptr)(size_t)(lds0 + 98304 + (u ^ 2) * 14592 + (j >= 7 ? 7184 + (j - 7) * 1024 : j * 1024)), 16, gsrc[i], (u + (xcd >> 2) * 16) * 32 * DF_ * 2, 0, 0); };
      ldG(0, 0); ldG(0, 1);
      if (wave < 6) { ldG(1, 0); ldG(1, 1); }
    }
    asm volatile("s_waitcnt lgkmcnt(0)" : "+v"(pa[0]), "+v"(pa[1]), "+v"(pa[2]), "+v"(pa[3]) :: "memory");
    const auto rO = __builtin_amdgcn_make_buffer_rsrc((void*)a.O, 0, 0x7fffffff, 0x00020000);
    u32x4 o[2];
#pragma unroll
    for (int i = 0; i < 2; ++i) {
      const f32x4 u = acc[2 * i][0] + pa[2 * i], v = acc[2 * i + 1][0] + pa[2 * i + 1];
      const auto s0 = __builtin_amdgcn_permlane16_swap(pk_bf16(u[0], u[1]), pk_bf16(v[0], v[1]), false, false);
      const auto s1 = __builtin_amdgcn_permlane16_swap(pk_bf16(u[2], u[3]), pk_bf16(v[2], v[3]), false, false);
      const u32x4 q4 = {s0[0], s1[0], s0[1], s1[1]};
#pragma unroll
      for (int j = 0; j < 4; ++j) { const unsigned r = res[i][j], q = q4[j]; o[i][j] = pk_bf16(bflo(r) + bflo(q), bfhi(r) + bfhi(q)); }
      __builtin_amdgcn_raw_buffer_store_b128(o[i], rO, ofrg + i * 1024, 0, 16);
    }
    auto sum4 = [](float v) {
      auto p = __builtin_amdgcn_permlane16_swap(__float_as_uint(v), __float_as_uint(v), false, false); v = __uint_as_float(p[0]) + __uint_as_float(p[1]);
      auto q = __builtin_amdgcn_permlane32_swap(__float_as_uint(v), __float_as_uint(v), false, false); return __uint_as_float(q[0]) + __uint_as_float(q[1]); };
    f32x2 e[8], sm = {0.f, 0.f};
#pragma unroll
    for (int j = 0; j < 8; ++j) { const unsigned q = o[j >> 2][j & 3]; e[j] = (f32x2){bflo(q), bfhi(q)}; sm += e[j]; }
    const float mu = sum4(sm[0] + sm[1]) * (1.f / 64.f);
    f32x2 t2 = {0.f, 0.f};
#pragma unroll
    for (int j = 0; j < 8; ++j) { const f32x2 d = e[j] - mu; t2 += d * d; }
    const float q2 = sum4(t2[0] + t2[1]);
    __builtin_amdgcn_raw_buffer_store_b64((u32x2){__float_as_uint(mu), __float_as_uint(q2)}, __builtin_amdgcn_make_buffer_rsrc((void*)a.st, 0, 0x7fffffff, 0x00020000),
                                          lane < 16 ? (unsigned)((tn * S_ + rb * 16 + lane) * 8) : 0x80000000u, 0, 16);
    STAMP(7);
    STAMP(8);
    asm volatile("s_waitcnt vmcnt(0) lgkmcnt(0)" ::: "memory");
    raw_barrier();
    if (tid == 0) __hip_atomic_fetch_add(a.sync + panel * 64, FIN, __ATOMIC_RELAXED, __HIP_MEMORY_SCOPE_AGENT);
    STAMP(3);
  }
#endif
}

struct FragE { bf16x8 x[4]; s16x4 lo[4], hi[4]; };
template <int A, int NT, int DX, int DW, int PW> struct Sched2 {
  static constexpr int nw(int t) { return (t + DW >= PW && t + DW < NT) ? (t + DW < A ? 4 : 2) : 0; }
  static constexpr int nx(int t) { return t + DX < NT ? 4 : 0; }
  static constexpr int cnt(int t) {
    if (t + 1 >= NT) return 63;
    int tot = 0, pos = 0;
    for (int i = 0; i < DX - 1; ++i) { tot += 4; if (i == t + 1) pos = tot; }
    for (int i = -1; i < t; ++i) {
      tot += nw(i); if (nw(i) && i + DW == t + 1) pos = tot;
      tot += nx(i); if (nx(i) && i + DX == t + 1) pos = tot;
    }
    return tot - pos;
  }
  static constexpr bool same(int t, int r) { return nw(t) == nw(r) && nx(t) == nx(r) && cnt(t) == cnt(r); }
  static constexpr int lo(int r) { int t = r; while (t > 0 && same(t - 1, r)) --t; return t; }
  static constexpr int hi(int r) { int t = r; while (same(t + 1, r)) ++t; return t + 1; }
};
template <int MODE, int A, int DX, int DW, int PW, int HTD, int TA, int TB, int TL = 0>
__device__ __forceinline__ void dn_phase(const GemmArgs& a) {
#if defined(__HIP_DEVICE_COMPILE__)
  constexpr int NT = DF_ / 64 - A, SX = DX + 1, SW = 4, WB = SX * 16384, PAD = WB + SW * 16384;
  static_assert(A % 2 == 0 && NT % 2 == 0 && PW <= SW && PW <= A && TA % 2 == 0 && TB % 2 == 0 && TA >= 2 && TB >= TA && A + TB + 6 <= NT, "schedule");
  using SC = Sched2<A, NT, DX, DW, PW>;
  extern __shared__ __attribute__((aligned(16))) char smem[];
  if constexpr (MODE != 1) STAMP(0);
  const unsigned lds0 = (unsigned)(size_t)(lds_char_ptr)smem;
  const int tid = threadIdx.x;
  int lane = tid & 63; asm volatile("" : "+v"(lane));
  const int wave = __builtin_amdgcn_readfirstlane(tid >> 6);
  const int bid = blockIdx.x, xcd = bid & 7, idx = bid >> 3;
  const int tm = (xcd >> 1) * 4 + (idx >> 3), tn = (xcd & 1) * 8 + (idx & 7);
  const int m0 = tm * 128, panel = m0 >> 8;
  unsigned* pcnt = a.sync + (8 + (xcd >> 2) * 4 + (idx >> 3)) * 64;
  const unsigned orow = (unsigned)((m0 + wave * 16 + (lane >> 3)) * (D_ * 2) + tn * 128 + (lane & 7) * 16);
  const unsigned ofrg = (unsigned)((((m0 >> 4) + wave) * RBH + tn * 8 + (lane & 7)) * 256 + (lane >> 3) * 16);
  u32x4 res[2];
  auto res_loads = [&]() {
    const auto rR = __builtin_amdgcn_make_buffer_rsrc((void*)a.R, 0, 0x7fffffff, 0x00020000);
#pragma unroll
    for (int i = 0; i < 2; ++i) res[i] = __builtin_amdgcn_raw_buffer_load_b128(rR, ofrg, i * 128, 16);
  };
  constexpr int STG = ((NT - 2) % SX) * 16384;
  if (MODE == 1 || wave < 4) {
    const int lw = wave & 3;
    static_assert((KN_E & 4) && A == 24, "walk");
    constexpr int V = 4;
    const int ra = (xcd & 3) * 8, rb = (xcd & 3) * 6;
    auto ktile2 = [&](int t, int e) {
      const int v1 = (ra + t + 4 + 12 * e - (((t - 4) >> 31) & (4 + 8 * e))) & 31, q = rb + t - 8 - (((t - 24) >> 31) & (8 - 8 * e)), v2 = q + 8 + (((q - 24) >> 31) & 24), m = (t - 16) >> 31;
      return (v1 & m) | (v2 & ~m); };
    auto ktile = [&](int t) { return ktile2(t, idx & 1); };
    auto wrow = [&](int kt, int j) { const int p = kt * 8 + j, b = p - 256, v1 = p + 3 * (p >> 2), v2 = b + 4 * ((b * 21846) >> 16) + 4, m = b >> 31; return ((v1 & m) | (v2 & ~m)) * 8; };
    const auto rX = __builtin_amdgcn_make_buffer_rsrc((void*)a.X, 0, 0x7fffffff, 0x00020000);
    const auto rW = __builtin_amdgcn_make_buffer_rsrc((void*)a.W0, 0, 0x7fffffff, 0x00020000);
    const unsigned xsrc = (unsigned)((((m0 >> 4) + (lw >> 1)) * RBA + (lw & 1) * 4) * 256 + lane * 16);
    unsigned wsrc;
    { const int row = lw * 8 + (lane >> 3); wsrc = (unsigned)((lane >> 3) * D_ + tn * 64 + ((lane & 7) ^ wswz(row)) * 8) * 2u; }
    const unsigned wsrp = wsrc ^ 128u;
    auto ldXo = [&](int so, int slot) {
#pragma unroll
      for (int i = 0; i < 4; ++i)
        __builtin_amdgcn_raw_ptr_buffer_load_lds(rX, (lds_void_ptr)(size_t)(lds0 + slot * 16384 + (lw + 4 * i) * 1024), 16, xsrc, so + i * (2 * RBA * 256), 0, XAUXD);
    };
    auto ldX = [&](int t, int slot) { ldXo(ktile(t) * 2048, slot); };
    int o0, o1, ox;
    auto offs = [&](int t) { const int kt = ktile(t + DW); o0 = wrow(kt, lw) * (D_ * 2); o1 = wrow(kt, lw + 4) * (D_ * 2); ox = ktile(t + DX) * 2048; asm volatile("" : "+s"(o0), "+s"(o1), "+s"(ox)); };
    auto ldW = [&](int slot, auto P1c) {
      constexpr bool P1 = decltype(P1c)::value;
#pragma unroll
      for (int i = 0; i < 2; ++i) {
        __builtin_amdgcn_raw_ptr_buffer_load_lds(rW, (lds_void_ptr)(size_t)(lds0 + WB + slot * 16384 + (lw + 4 * i) * 1024), 16, wsrc, i ? o1 : o0, 0, 0);
        if constexpr (P1) __builtin_amdgcn_raw_ptr_buffer_load_lds(rW, (lds_void_ptr)(size_t)(lds0 + WB + slot * 16384 + 8192 + (lw + 4 * i) * 1024), 16, wsrp, i ? o1 : o0, 0, 0);
      }
    };
    const unsigned* fcnt = a.sync + (8 + panel) * 64;
    auto ldF = [&](int o) { __builtin_amdgcn_raw_ptr_buffer_load_lds(__builtin_amdgcn_make_buffer_rsrc((void*)fcnt, 0, 4, 0x00020000), (lds_void_ptr)(size_t)(lds0 + PAD + o), 4, lane * 4, 0, 0, 16); };
    auto rdF = [&](int o) { int fv = ds_r32(lds0 + PAD + o); asm volatile("s_waitcnt lgkmcnt(0)" : "+v"(fv) :: "memory"); return (unsigned)__builtin_amdgcn_readfirstlane(fv); };
    const int kf = ra + (idx & 1) * 4;
    int ht; unsigned htv;
    auto hset = [&]() {
      int idh = idx; asm volatile("" : "+s"(idh));
      const int slc = (3 - ((unsigned)(size_t)a.W0 >> 7)) & 7, pr = (idh & 7) >> 1;
      const bool hlp = pr != (slc >> 1) && lw < 3;
      const int hk = ((idh >> 3) * 3 + pr - (pr > (slc >> 1) ? 1 : 0)) * 2 + (idh & 1) + lw * 24;
      const int htau = hk < 48 ? hk >> 1 : hk - 24, hkt = ktile2(htau < 32 ? htau : 0, hk & 1);
      ht = (hlp && hk < DF_ / 64 && htau >= KN_WS) ? htau - HTD : 1000;
      htv = (unsigned)((wrow(hkt, lane >> 3) + (lane & 7)) * (D_ * 2) + ((xcd & 1) * 8 + slc) * 128);
    };
    auto help = [&]() { __builtin_amdgcn_raw_ptr_buffer_load_lds(rW, (lds_void_ptr)(size_t)(lds0 + FPAD - 256), 4, htv, 0, 0, 0); };
    auto ldW0 = [&](int i) {
      const int so = (kf * 112 + lw * 8) * (D_ * 2);
      __builtin_amdgcn_raw_ptr_buffer_load_lds(rW, (lds_void_ptr)(size_t)(lds0 + WB + (i >> 1) * 16384 + (lw + 4 * (i & 1)) * 1024), 16, wsrc, so + i * (56 * D_ * 2), 0, 0);
      __builtin_amdgcn_raw_ptr_buffer_load_lds(rW, (lds_void_ptr)(size_t)(lds0 + WB + (i >> 1) * 16384 + 8192 + (lw + 4 * (i & 1)) * 1024), 16, wsrp, so + i * (56 * D_ * 2), 0, 0);
    };
    if constexpr (MODE == 1) {
      static_assert(PW == 4, "first tiles");
      if constexpr (TL == 1) ldF(FPAD - PAD);
      if constexpr (TL == 2) { if (lw == 0) { hset(); if (ht < -1) help(); } }
      if constexpr (TL == 0 || TL == 3) {
#pragma unroll
        for (int i = 0; i < 4; ++i) ldW0(i + (TL ? 4 : 0));
      }
      return;
    }
    int f0v = ds_r32(lds0 + SNAP + 16);
    wait_vm<3>();
    lds_inc(lds0 + ACNT);
    STAMPW(4, 0);
    const unsigned f1 = rdF(FPAD - PAD);
    asm volatile("" : "+v"(f0v));
    const unsigned f0 = __builtin_amdgcn_readfirstlane(f0v);
    const bool early = ((f1 - f0) & 0xffffu) >= 64u;
    lds_wait8(lds0 + ACNT);
    STAMPW(9, 0);
#define KN_X0 0
    auto ldX0 = [&](int i) { __builtin_amdgcn_raw_ptr_buffer_load_lds(rX, (lds_void_ptr)(size_t)(lds0 + (i >> 2) * 16384 + (lw + 4 * (i & 3)) * 1024), 16, xsrc, (kf + (i >> 2)) * 2048 + (i & 3) * (2 * RBA * 256), 0, XAUXD); };
#pragma unroll
    for (int i = 0; i < 4 * KN_X0; ++i) ldX0(i);
    asm volatile("" ::: "memory");
    if (tid == 192) __hip_atomic_fetch_add(pcnt, 1u << 16, __ATOMIC_RELAXED, __HIP_MEMORY_SCOPE_AGENT);
    asm volatile("" : "+v"(lane) :: "memory");
    hset();
    if constexpr (KN_X0 < 3) {
      asm volatile("" :: "s"(ht), "v"(htv));
      lds_wait8(lds0 + ACNT + 4, 4);
      STAMPW(5, 0);
#pragma unroll
      for (int i = 4 * KN_X0; i < 12; ++i) ldX0(i);
      asm volatile("" ::: "memory");
    }
#pragma unroll
    for (int i = 2 * KN_WS; i < 8; ++i) ldW0(i);
    asm volatile("" ::: "memory");
    static_assert(HTD <= 11, "only the first loader wave helps that early");
    if (!KN_HE && ht < -1) help();
    {
      static_assert(DX == 4 && DW == 4 && PW == 4 && KN_WS == 2 && SC::cnt(3) == 16 && SC::cnt(4) == 16 && V == DX, "schedule");
      int x4 = 0;
#pragma unroll 1
      for (int t = -1; t < V - 1; ++t) {
        static_assert(KN_WS == 4 || KN_WS == 2, "W tiles");
        offs(t);
        if (t == 1) wait_vm<8>(); else wait_vm<16>();
        if (!KN_FL && t == -1 && lw == 0) ldF(256);
        if (__builtin_expect(marked<4>(lds0 + (t + 1) * 16384 + lw * 1024 + lane * 16) || !early, 0)) {
          for (int tries = early ? 0 : KN_TRY;; ++tries) {
            if (tries >= KN_TRY) wait_field(lds0, fcnt, f0, 16, 4u);
#pragma unroll 1
            for (int u = t + 1; u < V && u < t + DX; ++u) ldX(u, u);
            if (KN_WS == 4 && t < 1) wait_vm<8>(); else wait_vm<0>();
            if (tries >= KN_TRY || !marked<4>(lds0 + (t + 1) * 16384 + lw * 1024 + lane * 16)) break;
          }
        }
        if (t == -1) STAMPW(6, 0);
        if (t == 1 && lw == 0) {
          if ((((rdF(256) - f0) >> 16) & 255u) < 32u) wait_field(lds0, fcnt, f0, 16, 4u);
        }
        raw_barrier();
        if (KN_FL && t == -1 && lw == 0) { ldF(256); asm volatile("" ::: "memory"); }
        if (t == 1) ldXo(x4, 4);
        x4 = ox;
        if (t >= 0) ldW((t + DW) & 3, std::true_type{});
        if (t != 0) ldXo(ox, t >= 1 ? t - 1 : t + DX);
        if (t == ht) help();
      }
    }
    auto fixed = [&](auto tc) {
      constexpr int t = decltype(tc)::value;
#define KN_TR 4
      if constexpr (SC::nw(t) > 0) offs(t);
      wait_vm<(t > NT - KN_TR && t < NT - 1) ? SC::cnt(t) + 2 : SC::cnt(t)>();
      raw_barrier();
      static_assert(SC::nx(t) == 0 || SC::nw(t) > 0, "offsets");
      if constexpr (SC::nw(t) > 0) ldW((t + DW) % SW, std::integral_constant<bool, (t + DW < A)>{});
      if constexpr (SC::nx(t) > 0) ldXo(ox, (t + DX) % SX);
      if constexpr (t + HTD < NT) { if (t == ht) help(); }
      if constexpr (t == NT - KN_TR) { static_assert(SC::nw(t + 1) == 0 && SC::nx(t + 1) == 0, "last loads"); asm volatile("" ::: "memory"); res_loads(); }
    };
    const int tf = lw == 0 ? 13 - DX : 1000;
    auto steady = [&](auto Lc, auto Hc) {
      constexpr int L = decltype(Lc)::value, H = decltype(Hc)::value;
      int sx = (L + DX) % SX * 16384 + lw * 1024 + lds0, sw = (L + DW) % SW * 16384;
      constexpr bool P1 = L + DW < A;
      static_assert(DX == DW && SW == 4, "offsets");
      offs(L);
      auto lw2 = [&](int i, int o) {
        __builtin_amdgcn_raw_ptr_buffer_load_lds(rW, (lds_void_ptr)(size_t)(lds0 + WB + sw + (lw + 4 * i) * 1024), 16, wsrc, o, 0, 0);
        if constexpr (P1) __builtin_amdgcn_raw_ptr_buffer_load_lds(rW, (lds_void_ptr)(size_t)(lds0 + WB + sw + 8192 + (lw + 4 * i) * 1024), 16, wsrp, o, 0, 0);
        __builtin_amdgcn_sched_barrier(0); };
      auto lx1 = [&](int i) { __builtin_amdgcn_raw_ptr_buffer_load_lds(rX, (lds_void_ptr)(size_t)(sx + i * 4096), 16, xsrc, ox + i * (2 * RBA * 256), 0, XAUXD); __builtin_amdgcn_sched_barrier(0); };
#pragma unroll 1
      for (int t = L; t < H; ++t) {
        wait_vm<SC::cnt(L)>();
        if constexpr (L <= 8) { static_assert(H > 16 - DX, "check"); if (t == tf + 3) {
          if ((rdF(256) - f0) >> 24 < 32u) wait_field(lds0, fcnt, f0, 24, 5u);
        } }
        raw_barrier();
#define KN_LG 0
        if (KN_LD && P1) __builtin_amdgcn_s_sleep(KN_LD);
        __builtin_amdgcn_sched_barrier(0);
        lw2(0, o0); if (KN_LG && P1) __builtin_amdgcn_s_sleep(KN_LG); __builtin_amdgcn_sched_barrier(0); lw2(1, o1);
        int nx = ktile(t + 1 + DX) * 2048; sw = (sw + 16384) & 65535; asm volatile("" : "+s"(nx), "+s"(sw)); __builtin_amdgcn_sched_barrier(0);
        lx1(0);
        o0 = wrow(nx >> 11, lw) * (D_ * 2); asm volatile("" : "+s"(o0)); __builtin_amdgcn_sched_barrier(0);
        lx1(1);
        o1 = wrow(nx >> 11, lw + 4) * (D_ * 2); asm volatile("" : "+s"(o1)); __builtin_amdgcn_sched_barrier(0);
        lx1(2);
        int ns = sx + 16384; ns = ns >= (int)lds0 + SX * 16384 ? ns - SX * 16384 : ns; asm volatile("" : "+s"(ns)); __builtin_amdgcn_sched_barrier(0);
        lx1(3);
        ox = nx; sx = ns;
        if (t == ht) help();
        if constexpr (L <= 8) { if (t == tf) ldF(256); }
      }
    };
    constexpr int L1 = V ? V - 1 : SC::lo(A / 2), H1 = SC::hi(A / 2), R2 = NT - (DX > DW ? DX : DW) - 1, L2 = SC::lo(R2), H2 = SC::hi(R2);
    static_assert(SC::nw(L1) == 4 && SC::nx(L1) == 4 && SC::nw(L2) == 2 && SC::nx(L2) == 4 && H1 <= L2, "steady states");
    if constexpr (!V) static_for<0, L1 + 1>([&](auto ic) { fixed(std::integral_constant<int, decltype(ic)::value - 1>{}); });
    steady(std::integral_constant<int, L1>{}, std::integral_constant<int, H1>{});
    static_for<H1, L2>(fixed);
    steady(std::integral_constant<int, L2>{}, std::integral_constant<int, H2>{});
    static_for<H2, NT>(fixed);
    asm volatile("" : "+v"(res[0]), "+v"(res[1]));
  } else {
    if constexpr (MODE == 1) return;
    const int fr = lane & 15, fq = lane >> 4, wq = (lane >> 2) & 3, wp = lane & 3, cw = wave & 3, wm = cw >> 1, wn = cw & 1;
    const unsigned f0 = snap_get(lds0, 5);
#define KN_DP 0
    if (KN_DP) __builtin_amdgcn_s_setprio(KN_DP);
    f32x4 acc[4][4];
#pragma unroll
    for (int n = 0; n < 4; ++n)
#pragma unroll
      for (int m = 0; m < 4; ++m) acc[n][m] = (f32x4){0.f, 0.f, 0.f, 0.f};
    const unsigned xa = lds0 + wm * 8192 + lane * 16;
    unsigned wa[2];
#pragma unroll
    for (int c = 0; c < 2; ++c) { const int row = 8 * fq + wq; wa[c] = lds0 + WB + row * 128 + ((((wn * 2 + c) << 1) ^ wswz(row)) << 4) + wp * 8; }
    struct Ctx { unsigned x, w[2]; };
    auto mk = [&](unsigned xo, unsigned wo) { Ctx c; c.x = xa + xo; c.w[0] = wa[0] + wo; c.w[1] = wa[1] + wo; return c; };
    auto rd = [&](FragE& f, const Ctx& c, auto KSc, auto kc) {
      constexpr int KS = decltype(KSc)::value, k = decltype(kc)::value;
      if constexpr (k < 4) f.x[k] = ds_b128<KS * 1024 + k * 2048>(c.x);
      else { constexpr int n = k - 4, o = KS * 4096 + (n >> 1) * 8192; f.lo[n] = ds_tr<o>(c.w[n & 1]); f.hi[n] = ds_tr<o + 512>(c.w[n & 1]); }
    };
    auto pin = [&](FragE& f, auto Nc) {
#pragma unroll
      for (int m = 0; m < 4; ++m) asm volatile("" : "+v"(f.x[m]));
#pragma unroll
      for (int n = 0; n < decltype(Nc)::value; ++n) asm volatile("" : "+v"(f.lo[n]), "+v"(f.hi[n]));
    };
    using I0 = std::integral_constant<int, 0>; using I1 = std::integral_constant<int, 1>; using I2 = std::integral_constant<int, 2>; using I4 = std::integral_constant<int, 4>;
    FragE fa, fb, fc, fd;
    raw_barrier();
    { const Ctx c = mk(0, 0); static_for<0, 8>([&](auto kc) { rd(fa, c, I0{}, kc); }); asm volatile("s_waitcnt lgkmcnt(4)" ::: "memory"); static_for<0, 8>([&](auto kc) { rd(fb, c, I1{}, kc); }); }
    asm volatile("s_waitcnt lgkmcnt(0)" ::: "memory");
    pin(fa, I4{}); pin(fb, I4{});
    STAMP(1);
    static_assert((SW & (SW - 1)) == 0, "W ring");
    int sx = 16384, sw = 16384;
    Ctx cn = mk(sx, sw);
#define KN_SP4 26
#define KN_IL 1
#define KN_IB 1
    auto rd1 = [&](FragE& f, const Ctx& c, auto KSc, auto rc) {
      constexpr int KS = decltype(KSc)::value, r = decltype(rc)::value;
      if constexpr (r < 4) f.x[r] = ds_b128<KS * 1024 + r * 2048>(c.x);
      else { constexpr int n = (r - 4) >> 1, o = KS * 4096 + (n >> 1) * 8192; if constexpr (r & 1) f.hi[n] = ds_tr<o + 512>(c.w[n & 1]); else f.lo[n] = ds_tr<o>(c.w[n & 1]); }
    };
    auto tile = [&](FragE& c0, FragE& c1, FragE& n0, FragE& n1, auto NCc, auto NNc, auto&& post) {
      constexpr int NC = decltype(NCc)::value, NN = decltype(NNc)::value, RI = 4 + NN, RG = NC == 4 ? 3 : 2;
      constexpr int R1 = 4 + 2 * NN, NM = 8 * NC;
      const Ctx c = cn;
      static_for<0, NM>([&](auto ic) {
        constexpr int I = decltype(ic)::value, G = I / 4, m = I % 4, ks = G / NC, n = G % NC;
        FragE& cur = ks ? c1 : c0;
        constexpr int SP = NC == 4 ? (NN == 4 ? KN_SP4 : 20) : 12;
        constexpr int before = I <= KN_IB ? 0 : ((I - KN_IB) * 2 * R1 / SP < 2 * R1 ? (I - KN_IB) * 2 * R1 / SP : 2 * R1);
        constexpr int after = I + 1 <= KN_IB ? 0 : ((I + 1 - KN_IB) * 2 * R1 / SP < 2 * R1 ? (I + 1 - KN_IB) * 2 * R1 / SP : 2 * R1);
        if constexpr (I == NM / 2) { asm volatile("s_waitcnt lgkmcnt(%0)" :: "n"(before < 15 ? before : 15) : "memory"); pin(c1, NCc); }
        const bf16x8 w = __builtin_shufflevector(cur.lo[n], cur.hi[n], 0, 1, 2, 3, 4, 5, 6, 7);
        acc[n][m] = __builtin_amdgcn_mfma_f32_16x16x32_bf16(w, cur.x[m], acc[n][m], 0, 0, 0);
        __builtin_amdgcn_sched_barrier(0);
        if constexpr (I + 1 == KN_IB) { asm volatile("s_waitcnt lgkmcnt(0)" ::: "memory"); raw_barrier(); post(); }
        static_for<before, after>([&](auto rc) { constexpr int r = decltype(rc)::value;
          if constexpr (r < R1) rd1(n0, c, I0{}, rc); else rd1(n1, c, I1{}, std::integral_constant<int, r - R1>{}); });
        if constexpr (I == NM - 4) { sw = (sw + 16384) & (SW * 16384 - 1); asm volatile("" : "+s"(sw)); }
        if constexpr (I == NM - 3) { cn.w[0] = wa[0] + sw; cn.w[1] = wa[1] + sw; asm volatile("" : "+v"(cn.w[0]), "+v"(cn.w[1])); }
        if constexpr (I == NM - 5) { sx = sx + 16384; asm volatile("" : "+s"(sx)); }
        if constexpr (I == NM - 2) { sx = sx == SX * 16384 ? 0 : sx; asm volatile("" : "+s"(sx)); }
        if constexpr (I == NM - 1) { cn.x = xa + sx; asm volatile("" : "+v"(cn.x)); }
        __builtin_amdgcn_sched_barrier(0);
      });
      asm volatile("s_waitcnt lgkmcnt(%0)" :: "n"(R1) : "memory");
      pin(n0, NNc);
    };
    auto nop = [&]() {};
    for (int t = 0; t < A - 2; t += 2) {
      tile(fa, fb, fc, fd, I4{}, I4{}, [&]() { if (t == 2) { if (tid == 256) __hip_atomic_fetch_add(pcnt, 1u << 24, __ATOMIC_RELAXED, __HIP_MEMORY_SCOPE_AGENT); } });
      tile(fc, fd, fa, fb, I4{}, I4{}, nop);
      if (t == 0) wait_vm<0>();
    }
    tile(fa, fb, fc, fd, I4{}, I4{}, nop); tile(fc, fd, fa, fb, I4{}, I2{}, nop);
    const auto rS = __builtin_amdgcn_make_buffer_rsrc((void*)a.slab, 0, 0x7fffffff, 0x00020000);
    STAMP(10);
#pragma unroll
    for (int m = 0; m < 4; ++m)
      __builtin_amdgcn_raw_buffer_store_b128((u32x4){pk_bf16(acc[2][m][0], acc[2][m][1]), pk_bf16(acc[2][m][2], acc[2][m][3]), pk_bf16(acc[3][m][0], acc[3][m][1]), pk_bf16(acc[3][m][2], acc[3][m][3])},
                                             rS, (((tm * 16 + (tn ^ 1)) * 4 + cw) * 4 + m) * 1024 + lane * 16, 0, (KN_LX & 1) ? 0 : 16);
    auto pairs = [&](int n) {
#pragma unroll 1
      for (int t = 0; t < n; t += 2) { tile(fa, fb, fc, fd, I2{}, I2{}, nop); tile(fc, fd, fa, fb, I2{}, I2{}, nop); } };
    const unsigned* flag = a.sync + 2048 + bid * 32;
    u32x4 pp[4];
    auto ldP = [&]() {
#pragma unroll
      for (int i = 0; i < 4; ++i) pp[i] = __builtin_amdgcn_raw_buffer_load_b128(rS, (((tm * 16 + tn) * 4 + cw) * 4 + i) * 1024 + lane * 16, 0, 16);
    };
    if constexpr (KN_DM) {
      static_assert(KN_E & 4, "snapshot");
      pairs(KN_TP & ~1);
      STAMPV(4);
      static_for<(KN_TP & ~1), NT - A>([&](auto tc) {
        constexpr int t = decltype(tc)::value;
        auto post = [&]() { if constexpr (t == KN_TP) { ldP(); res_loads(); } };
        if constexpr (t & 1) tile(fc, fd, fa, fb, I2{}, I2{}, post); else tile(fa, fb, fc, fd, I2{}, I2{}, post);
      });
    } else {
    pairs(TA);
    STAMPV(11);
    wait_vm<0>();
    STAMPV(12);
    tile(fa, fb, fc, fd, I2{}, I2{}, [&]() { if (tid == 256) __hip_atomic_fetch_add(a.sync + 2048 + (bid ^ 8) * 32, 1u, __ATOMIC_RELAXED, (KN_LX & 2) ? __HIP_MEMORY_SCOPE_WORKGROUP : __HIP_MEMORY_SCOPE_AGENT); });
    tile(fc, fd, fa, fb, I2{}, I2{}, nop);
    if constexpr (TB > TA) pairs(TB - TA);
    unsigned fv = 1u;
    if (wave == 4) fv = __hip_atomic_load(flag, __ATOMIC_RELAXED, __HIP_MEMORY_SCOPE_AGENT);
    tile(fa, fb, fc, fd, I2{}, I2{}, nop);
    tile(fc, fd, fa, fb, I2{}, I2{}, nop);
    if (wave == 4) {
      while (__builtin_amdgcn_readfirstlane(fv) == f0 && in_time(lds0, 6u)) fv = __hip_atomic_load(flag, __ATOMIC_RELAXED, __HIP_MEMORY_SCOPE_AGENT);
    }
    STAMPV(4);
    tile(fa, fb, fc, fd, I2{}, I2{}, [&]() {
#pragma unroll
      for (int i = 0; i < 4; ++i) pp[i] = __builtin_amdgcn_raw_buffer_load_b128(rS, (((tm * 16 + tn) * 4 + cw) * 4 + i) * 1024 + lane * 16, 0, 16);
      res_loads();
    });
    tile(fc, fd, fa, fb, I2{}, I2{}, nop);
    if constexpr (NT - A - TB - 6 > 0) pairs(NT - A - TB - 6);
    }
    asm volatile("s_waitcnt lgkmcnt(0)" ::: "memory");
    pin(fa, I2{}); pin(fb, I2{});
    STAMP(2);
    if constexpr (KN_DM) {
      auto anym = [&]() {
        unsigned m = 0;
#pragma unroll
        for (int i = 0; i < 4; ++i) { m = max(max(m, pp[i][0]), pp[i][1]); m = max(max(m, pp[i][2]), pp[i][3]); }
        return __builtin_amdgcn_ballot_w64(m == 0xffffffffu) != 0;
      };
      asm volatile("s_waitcnt vmcnt(0)" : "+v"(res[0]), "+v"(res[1]) :: "memory");
      if (lane == 0) __hip_atomic_fetch_add(a.sync + 2048 + (bid ^ 8) * 32, FIN, __ATOMIC_RELAXED, __HIP_MEMORY_SCOPE_AGENT);
      if (__builtin_expect(anym(), 0)) {
        for (int tries = 0;; ++tries) {
          if (tries >= KN_TRY) wait_count(lds0, flag, f0, 4u, 6u);
          ldP();
          asm volatile("" : "+v"(pp[0]), "+v"(pp[1]), "+v"(pp[2]), "+v"(pp[3]));
          if (tries >= KN_TRY || !anym()) break;
        }
      }
      STAMPV(12);
    }
#pragma unroll
    for (int m = 0; m < 4; ++m)
#pragma unroll
      for (int n = 0; n < 2; ++n) {
        const unsigned q0 = pp[m][n * 2], q1 = pp[m][n * 2 + 1];
        const f32x4 v = acc[n][m] + (f32x4){bflo(q0), bfhi(q0), bflo(q1), bfhi(q1)};
        *(u32x2*)(smem + STG + (wm * 64 + m * 16 + fr) * 128 + ((((wn * 2 + n) * 2 + (fq >> 1)) ^ (fr & 7)) << 4) + (fq & 1) * 8) = (u32x2){pk_bf16(v[0], v[1]), pk_bf16(v[2], v[3])};
      }
    STAMPV(5);
    asm volatile("" : "+v"(res[0]), "+v"(res[1]));
  }
  lds_barrier();
  {
    const auto rO = __builtin_amdgcn_make_buffer_rsrc((void*)a.O, 0, 0x7fffffff, 0x00020000);
    u32x4 o[2];
    int lz = lane; asm volatile("" : "+v"(lz));
#pragma unroll
    for (int i = 0; i < 2; ++i) {
      const u32x4 v = *(const u32x4*)(smem + STG + (wave * 16 + i * 8 + (lz >> 3)) * 128 + (((lz & 7) ^ (lz >> 3)) << 4));
#pragma unroll
      for (int j = 0; j < 4; ++j) { const unsigned r = res[i][j], q = v[j]; o[i][j] = pk_bf16(bflo(r) + bflo(q), bfhi(r) + bfhi(q)); }
    }
    asm volatile("" : "+v"(o[0]), "+v"(o[1]));
#pragma unroll
#define KN_OAUX 0
    for (int i = 0; i < 2; ++i) __builtin_amdgcn_raw_buffer_store_b128(o[i], rO, orow, i * 8 * D_ * 2, KN_OAUX);
  }
  STAMP(7);
  STAMP(8);
  STAMP(3);
#endif
}

struct Ptrs { const bf16_t *x, *wqkv, *wo, *wg, *wu, *wd; bf16_t* out; char* ws; };
#define DBG(i)
#define WS_O ((bf16_t*)(p.ws + (8u << 20)))
#define WS_SL ((float*)WS_O)
#define WS_H ((bf16_t*)(p.ws + (16u << 20)))
#define WS_ST ((float*)(p.ws + (24u << 20)))
#define WS_CS ((float*)(p.ws + (25u << 20)))
#define WS_SY ((unsigned*)(p.ws + (26u << 20)))
#define WS_A ((bf16_t*)(p.ws + (28u << 20)))
#if defined(__HIP_DEVICE_COMPILE__)
#define ARG_WARM(v) do { asm volatile("s_load_dword %0, %1, 0x0" : "=s"(v) : "s"(__builtin_amdgcn_kernarg_segment_ptr())); __builtin_amdgcn_sched_barrier(0); } while (0)
#else
#define ARG_WARM(v)
#endif
struct Args4 { GemmArgs q, b, c, d; };
extern "C" __global__ __launch_bounds__(512, 1) void k_all(Ptrs p) {
  unsigned warm = 0;
  ARG_WARM(warm);
#define PAD(k)
  const Args4 a{GemmArgs{p.x, p.wqkv, nullptr, nullptr, WS_O, (float*)WS_H, p.wg, p.wu, WS_CS, p.wd, p.wo, WS_SY, (float*)WS_A, warm DBG(0)},
    GemmArgs{WS_O, p.wo, nullptr, p.x, WS_H, WS_ST, nullptr, nullptr, nullptr, p.wd, nullptr, WS_SY, nullptr, 0u DBG(1)},
    GemmArgs{WS_H, p.wg, p.wu, p.out, WS_A, WS_ST, nullptr, nullptr, WS_CS, p.wd, nullptr, WS_SY, WS_SL, 0u DBG(2)},
    GemmArgs{WS_A, p.wd, nullptr, WS_H, p.out, nullptr, nullptr, nullptr, nullptr, nullptr, nullptr, WS_SY, WS_SL, 0u DBG(3)}};
  k1_phase(a.q, [&]() { wo_phase<1, 2, 16>(a.b, a.c); });
  PAD(1);
  wo_phase<2, 0, 0>(a.b, a.c);
  PAD(2);
  glu_phase(a.c, [&](auto tc) { dn_phase<1, 24, 4, 4, 4, 10, 2, 2, decltype(tc)::value>(a.d); });
  PAD(3);
  dn_phase<2, 24, 4, 4, 4, 10, 2, 2>(a.d);
  PAD(4);
}

extern "C" __global__ void k_zend() {}

extern "C" void kernel_launch(void* const* d_in, const int* in_sizes, int n_in, void* d_out, int out_size, void* d_ws,
                              size_t ws_size, hipStream_t stream) {
  const Ptrs p{(const bf16_t*)d_in[0], (const bf16_t*)d_in[1], (const bf16_t*)d_in[2], (const bf16_t*)d_in[4], (const bf16_t*)d_in[5], (const bf16_t*)d_in[6], (bf16_t*)d_out, (char*)d_ws};
  constexpr int L_all = SNAP + 64;
  static_assert(SNAP >= 3 * 256 * 128 + 4 * 14592 + 4096 + 1024 + 256 + 512 + 128 + 64 && L_all <= 163840, "LDS");
  (void)hipFuncSetAttribute((const void*)k_all, hipFuncAttributeMaxDynamicSharedMemorySize, L_all);
  k_all<<<256, 512, L_all, stream>>>(p);
}
```

```cpp
#define PADAT 4
#define PADUS 80
#define KN_OAUX 16
#include <hip/hip_runtime.h>
#include <hip/hip_bf16.h>
#include <stdint.h>
#include <type_traits>

typedef unsigned short bf16_t;
typedef short bf16x8 __attribute__((ext_vector_type(8)));
typedef short s16x4 __attribute__((ext_vector_type(4)));
typedef float f32x4 __attribute__((ext_vector_type(4)));
typedef float f32x2 __attribute__((ext_vector_type(2)));
typedef unsigned u32x2 __attribute__((ext_vector_type(2)));
typedef unsigned u32x4 __attribute__((ext_vector_type(4)));
typedef __bf16 bf16x2_t __attribute__((ext_vector_type(2)));
typedef __attribute__((address_space(3))) void* lds_void_ptr;
typedef __attribute__((address_space(3))) char* lds_char_ptr;

constexpr int S_ = 2048, D_ = 1024, DF_ = 3584;
constexpr int RBO = D_ / 8, RBH = D_ / 8, RBA = DF_ / 8;

__device__ __forceinline__ float bflo(unsigned v) { return __uint_as_float(v << 16); }
__device__ __forceinline__ float bfhi(unsigned v) { return __uint_as_float(v & 0xffff0000u); }
__device__ __forceinline__ unsigned pk_bf16(float lo, float hi) {
  f32x2 v = {lo, hi};
  bf16x2_t b = __builtin_convertvector(v, bf16x2_t);
  return __builtin_bit_cast(unsigned, b);
}
__device__ __forceinline__ int wswz(int row) { return (((row >> 1) & 1) | (((row >> 3) & 1) << 1)) << 1; }

template <int OFF> __device__ __forceinline__ s16x4 ds_tr(unsigned addr) {
  s16x4 r; asm volatile("ds_read_b64_tr_b16 %0, %1 offset:%2" : "=v"(r) : "v"(addr), "n"(OFF)); return r;
}
template <int OFF> __device__ __forceinline__ bf16x8 ds_b128(unsigned addr) {
  bf16x8 r; asm volatile("ds_read_b128 %0, %1 offset:%2" : "=v"(r) : "v"(addr), "n"(OFF)); return r;
}
template <int OFF> __device__ __forceinline__ f32x2 ds_b64(unsigned addr) {
  f32x2 r; asm volatile("ds_read_b64 %0, %1 offset:%2" : "=v"(r) : "v"(addr), "n"(OFF)); return r;
}
__device__ __forceinline__ float sum8(float v) {
  v += __int_as_float(__builtin_amdgcn_update_dpp(0, __float_as_int(v), 0x128, 0xf, 0xf, false));
  auto a = __builtin_amdgcn_permlane16_swap(__float_as_uint(v), __float_as_uint(v), false, false);
  v = __uint_as_float(a[0]) + __uint_as_float(a[1]);
  auto b = __builtin_amdgcn_permlane32_swap(__float_as_uint(v), __float_as_uint(v), false, false);
  return __uint_as_float(b[0]) + __uint_as_float(b[1]);
}
__device__ __forceinline__ float sum8l(float v) {
  v += __int_as_float(__builtin_amdgcn_update_dpp(0, __float_as_int(v), 0xb1, 0xf, 0xf, false));
  v += __int_as_float(__builtin_amdgcn_update_dpp(0, __float_as_int(v), 0x4e, 0xf, 0xf, false));
  return v + __int_as_float(__builtin_amdgcn_update_dpp(0, __float_as_int(v), 0x141, 0xf, 0xf, false));
}
__device__ __forceinline__ int ds_r32(unsigned addr) { int r; asm volatile("ds_read_b32 %0, %1" : "=v"(r) : "v"(addr)); return r; }
__device__ __forceinline__ void ds_w32(unsigned addr, int v) { asm volatile("ds_write_b32 %0, %1" :: "v"(addr), "v"(v) : "memory"); }
template <int N> __device__ __forceinline__ void wait_vm() { asm volatile("s_waitcnt vmcnt(%0)" ::"n"(N) : "memory"); }
__device__ __forceinline__ void raw_barrier() { asm volatile("" ::: "memory"); __builtin_amdgcn_s_barrier(); asm volatile("" ::: "memory"); }
__device__ __forceinline__ void lds_barrier() { asm volatile("s_waitcnt lgkmcnt(0)" ::: "memory"); __builtin_amdgcn_s_barrier(); asm volatile("" ::: "memory"); }

template <int I, int N, class F> __device__ __forceinline__ void static_for(F&& f) {
  if constexpr (I < N) { f(std::integral_constant<int, I>{}); static_for<I + 1, N>(f); }
}
template <int V> using IC = std::integral_constant<int, V>;
#define RSRC(p) __builtin_amdgcn_make_buffer_rsrc((void*)(p), 0, 0x7fffffff, 0x00020000)
#define RSRCN(p, n) __builtin_amdgcn_make_buffer_rsrc((void*)(p), 0, (n), 0x00020000)
#define LDSP(a) ((lds_void_ptr)(size_t)(a))
#define MFMA(a, b, c) __builtin_amdgcn_mfma_f32_16x16x32_bf16(a, b, c, 0, 0, 0)
#define CODE_ADDR(v, sym) asm volatile("s_getpc_b64 s[40:41]\n\ts_add_u32 s40, s40, " sym "@rel32@lo+4\n\ts_addc_u32 s41, s41, " sym "@rel32@hi+12\n\ts_mov_b64 %0, s[40:41]" : "=s"(v) :: "s40", "s41", "scc")
#define MERGED 1
constexpr int SNAP = 162752;
constexpr int STAB1 = 786432;
constexpr int SY_O = 12288;
#define SYO(t) (SY_O + (t) * 32 + (((((t) >> 3) ^ 1) & 1) << 11))
constexpr unsigned FIN = 65536u;
constexpr int KN_TRY = 6;
__device__ __forceinline__ unsigned snap_get(unsigned lds0, int i) {
  int v = ds_r32(lds0 + SNAP + i * 4);
  asm volatile("s_waitcnt lgkmcnt(0)" : "+v"(v) :: "memory");
  return (unsigned)__builtin_amdgcn_readfirstlane(v);
}
__device__ __forceinline__ bool in_time(unsigned lds0, unsigned level) { return (unsigned)__builtin_amdgcn_s_memrealtime() - snap_get(lds0, 7) < level * 5000000u; }
__device__ __forceinline__ void wait_count(unsigned lds0, const unsigned* cnt, unsigned v0, unsigned n, unsigned level) {
  while (((unsigned)__builtin_amdgcn_readfirstlane(__hip_atomic_load(cnt, __ATOMIC_RELAXED, __HIP_MEMORY_SCOPE_AGENT)) - v0) >> 16 < n && in_time(lds0, level)) {}
}
__device__ __forceinline__ void wait_field(unsigned lds0, const unsigned* cnt, unsigned v0, int sh, unsigned level) {
  while (((((unsigned)__builtin_amdgcn_readfirstlane(__hip_atomic_load(cnt, __ATOMIC_RELAXED, __HIP_MEMORY_SCOPE_AGENT)) - v0) >> sh) & 255u) < 32u && in_time(lds0, level)) {}
}
constexpr int ACNT = SNAP - 60;
__device__ __forceinline__ void lds_inc(unsigned addr) { if (__builtin_amdgcn_mbcnt_hi(~0u, __builtin_amdgcn_mbcnt_lo(~0u, 0u)) == 0) asm volatile("ds_add_u32 %0, %1" :: "v"(addr), "v"(1) : "memory"); }
constexpr int FPAD = SNAP - 704;
__device__ __forceinline__ void lds_wait8(unsigned addr, int n = 8) {
  int dv; unsigned polls = 0;
  do { dv = ds_r32(addr); asm volatile("s_waitcnt lgkmcnt(0)" : "+v"(dv) :: "memory"); } while (__builtin_amdgcn_readfirstlane(dv) < n && ++polls < 40000000u);
}
template <int N> __device__ __forceinline__ bool marked(unsigned addr) {
  bf16x8 v[N];
  static_for<0, N>([&](auto ic) { constexpr int i = decltype(ic)::value; v[i] = ds_b128<i * 4096>(addr); });
  asm volatile("s_waitcnt lgkmcnt(0)" ::: "memory");
  unsigned m = 0;
#pragma unroll
  for (int i = 0; i < N; ++i) {
    asm volatile("" : "+v"(v[i]));
    const u32x4 u = __builtin_bit_cast(u32x4, v[i]);
    m = max(max(m, u[0]), u[1]); m = max(max(m, u[2]), u[3]);
  }
  return __builtin_amdgcn_ballot_w64(m == 0xffffffffu) != 0;
}

struct GemmArgs {
  const bf16_t* X;
  const bf16_t* W0;
  const bf16_t* W1;
  const bf16_t* R;
  bf16_t* O;
  float* st;
  const bf16_t* CW0;
  const bf16_t* CW1;
  float* cs;
  const bf16_t* PF0;
  const bf16_t* PF1;
  unsigned* sync;
  float* slab;
  unsigned warm;
};
#define STAMP(i)
#define STAMPW(i, w)
template <int MF, int NF> struct Frags { bf16x8 x[MF]; s16x4 lo[NF], hi[NF]; };

constexpr int WOB = 57600;
constexpr int wo_pos(int s) { return s < 6 ? WOB + s * 16384 : (s - 6) * 16384; }
template <class HK>
__device__ __forceinline__ void k1_phase(const GemmArgs& a, HK hook) {
#if defined(__HIP_DEVICE_COMPILE__)
  constexpr int K = D_, LDW = 3 * D_, WN = 4;
  constexpr int HR = 16, STRIDE = 128;
  constexpr int XT = 144 * 128, WU = 24576;
  constexpr int XBASE = 0, WBASE = 3 * XT, RING = WBASE + 4 * WU;
  constexpr int MF = 4, NF = 3, MFX = 5;
  constexpr int NT = 16, NXI = 18, NLW = 4, XI = 5, XIMIN = 4, WI = 6;
  extern __shared__ __attribute__((aligned(16))) char smem[];
  STAMP(0);
  const unsigned lds0 = (unsigned)(size_t)(lds_char_ptr)smem;
  const int tid = threadIdx.x, lane = tid & 63;
  const int wave = __builtin_amdgcn_readfirstlane(tid >> 6);
  const int wm = wave / WN, wn = wave % WN;
  const int bid = blockIdx.x, xcd = bid & 7, idx = bid >> 3;
  const int tm = (xcd / 4) * 8 + idx / 4, tn = (xcd % 4) * 4 + idx % 4;
  const int m0 = tm * 128;
  const int fr = lane & 15, fq = lane >> 4;
  const int wq = (lane >> 2) & 3, wp = lane & 3;
  constexpr int TCH = RING + 2304;
  auto touch = [&](const void* base, unsigned voff, unsigned soff, unsigned lim = 0x7fffffffu) {
    __builtin_amdgcn_raw_ptr_buffer_load_lds(RSRCN(base, lim), LDSP(lds0 + TCH), 4, voff, soff, 0, 0);
  };
  const auto rO = RSRC(a.O);

  unsigned xsrc[XI], wsrc[WI];
  {
    const int w4 = wave & 3, lr = lane >> 3;
    const unsigned xv = (unsigned)(lr * (K * 2) + (((lane & 7) ^ ((w4 * 4 + (lr >> 1)) & 7)) << 4));
    const int r0 = m0 - HR + w4 * 8;
#pragma unroll
    for (int i = 0; i < XI; ++i) xsrc[i] = xv + (unsigned)((i == 0 ? (r0 < 0 ? 0 : r0) : r0 + 32 * (i < 4 ? i : 4)) * (K * 2));
    const unsigned wv = (unsigned)((w4 * 8 + lr) * (LDW * 2) + tn * 128 + (((lane & 7) ^ wswz(w4 * 8 + lr)) << 4));
#pragma unroll
    for (int i = 0; i < WI; ++i) wsrc[i] = wv + (unsigned)((i & 1) * 32 * (LDW * 2) + (i >> 1) * 2048);
  }
  auto ldX = [&](int i, int tile, int slot) {
    const int j = wave + NLW * i;
    if (j >= NXI) return;
    __builtin_amdgcn_raw_ptr_buffer_load_lds(RSRC(a.X), LDSP(lds0 + XBASE + slot * XT + j * 1024), 16, xsrc[i], tile * 128, 0, 0);
  };
  auto ldW = [&](int i, int unit, int slot) {
    const int j = wave + NLW * i;
    __builtin_amdgcn_raw_ptr_buffer_load_lds(RSRC(a.W0), LDSP(lds0 + WBASE + slot * WU + j * 1024), 16, wsrc[i], unit * 64 * LDW * 2, 0, 0);
  };
  auto issueX = [&](int tile, int slot) {
    if (wave >= NLW) return;
#pragma unroll
    for (int i = 0; i < XI; ++i) ldX(i, tile, slot);
  };
  auto issueW = [&](int unit, int slot) {
    if (wave >= NLW) return;
#pragma unroll
    for (int i = 0; i < WI; ++i) ldW(i, unit, slot);
  };

  f32x4 acc[NF][MF];
#pragma unroll
  for (int n = 0; n < NF; ++n)
#pragma unroll
    for (int m = 0; m < MF; ++m) acc[n][m] = (f32x4){0.f, 0.f, 0.f, 0.f};
  f32x4 acch = {0.f, 0.f, 0.f, 0.f};
  f32x4 accs = {0.f, 0.f, 0.f, 0.f};
  constexpr int ONES = RING;

  unsigned xa0, xa1, xh0, xh1;
  { const unsigned o = fr * 128 + ((fq ^ ((fr >> 1) & 7)) << 4), mo = (HR + wm * MF * 16) * 128;
    xa0 = lds0 + XBASE + mo + o; xa1 = lds0 + XBASE + mo + (o ^ 64); xh0 = lds0 + XBASE + o; xh1 = lds0 + XBASE + (o ^ 64); }
  unsigned wa;
  { const int row = 8 * fq + wq; wa = lds0 + WBASE + row * STRIDE + (((wn << 1) ^ wswz(row)) << 4) + wp * 8; }

  using I0 = IC<0>; using I1 = IC<1>;
  using T = std::true_type; using F = std::false_type;
  typedef Frags<MFX, NF> FR;
  struct RdCtx { unsigned xa, xh, wb; };
  auto mkctx = [&](bool ks1, unsigned xoffs, unsigned woffs) {
    RdCtx c; c.xa = (ks1 ? xa1 : xa0) + xoffs; c.xh = fr == 0 ? lds0 + ONES : (ks1 ? xh1 : xh0) + xoffs; c.wb = wa + woffs;
    return c;
  };
  auto rd_item = [&](FR& f, const RdCtx& c, auto KSc, auto kc) {
    constexpr int KS = decltype(KSc)::value, k = decltype(kc)::value;
    if constexpr (k < MF) f.x[k] = ds_b128<k * 2048>(c.xa);
    else if constexpr (k < MFX) f.x[k] = ds_b128<0>(c.xh);
    else {
      constexpr int n = k - MFX, o = n * 8192 + KS * 32 * STRIDE;
      f.lo[n] = ds_tr<o>(c.wb); f.hi[n] = ds_tr<o + 4 * STRIDE>(c.wb);
    }
  };
  auto wait_frags = [&](FR& f) {
    asm volatile("s_waitcnt lgkmcnt(0)" ::: "memory");
#pragma unroll
    for (int m = 0; m < MFX; ++m) asm volatile("" : "+v"(f.x[m]));
#pragma unroll
    for (int n = 0; n < NF; ++n) asm volatile("" : "+v"(f.lo[n]), "+v"(f.hi[n]));
  };
  auto step = [&](auto CSc, auto STc, FR& cur, FR& nxt, const RdCtx& c, auto KSc, auto PFc, auto NLc, auto&& ld) {
    constexpr bool PF = decltype(PFc)::value != 0; constexpr int NL = decltype(NLc)::value;
    constexpr int R = MFX + NF, RG = NF - 1;
    constexpr bool CS = decltype(CSc)::value; constexpr int ST = decltype(STc)::value;
    static_for<0, NF>([&](auto gc) {
      constexpr int g = decltype(gc)::value;
      if constexpr (PF) static_for<0, R>([&](auto kc) { constexpr int k = decltype(kc)::value; if constexpr (k * RG / R == g) rd_item(nxt, c, KSc, kc); });
      static_for<0, NL>([&](auto ic) { constexpr int i = decltype(ic)::value; if constexpr (i * NF / NL == g) ld(i); });
      const bf16x8 w = __builtin_shufflevector(cur.lo[g], cur.hi[g], 0, 1, 2, 3, 4, 5, 6, 7);
#pragma unroll
      for (int m = 0; m < MF; ++m) acc[g][m] = MFMA(w, cur.x[m], acc[g][m]);
      if constexpr (g == 1 + ST / WN) acch = MFMA(w, cur.x[MF], acch);
      if constexpr (CS && g == 0) accs = MFMA(w, cur.x[MF], accs);
      __builtin_amdgcn_sched_barrier(0);
    });
    if constexpr (PF) wait_frags(nxt);
  };
  auto step1 = [&](auto CSc, auto STc, FR& cur, FR& nxt, const RdCtx& c, auto KSc, auto R0c, auto RLc, auto IBc, auto&& sync, auto NLc, auto&& ld) {
    constexpr bool CS = decltype(CSc)::value; constexpr int ST = decltype(STc)::value, NL = decltype(NLc)::value, R0 = decltype(R0c)::value;
    constexpr int E0 = CS ? 1 : 0, E1 = 1 == 1 + ST / WN ? 1 : 0, E2 = 2 == 1 + ST / WN ? 1 : 0;
    constexpr int NM = 12 + E0 + E1 + E2, NR = MFX + 2 * NF;
    constexpr int IB = decltype(IBc)::value < 0 && decltype(IBc)::value > -100 ? NM + decltype(IBc)::value : decltype(IBc)::value;
    constexpr int RL = decltype(RLc)::value < 0 ? NM + decltype(RLc)::value : decltype(RLc)::value;
    static_for<0, NM>([&](auto ic) {
      constexpr int I = decltype(ic)::value;
      constexpr int g = I < 4 + E0 ? 0 : (I < 8 + E0 + E1 ? 1 : 2), q = I - (g == 0 ? 0 : (g == 1 ? 4 + E0 : 8 + E0 + E1));
      const bf16x8 w = __builtin_shufflevector(cur.lo[g], cur.hi[g], 0, 1, 2, 3, 4, 5, 6, 7);
      if constexpr (q < 4) acc[g][q] = MFMA(w, cur.x[q], acc[g][q]);
      else if constexpr (g == 0) accs = MFMA(w, cur.x[MF], accs);
      else acch = MFMA(w, cur.x[MF], acch);
      __builtin_amdgcn_sched_barrier(0);
      if constexpr (I == IB) sync();
      static_for<0, NR>([&](auto rc) {
        constexpr int r = decltype(rc)::value;
        if constexpr (R0 + r * (RL - R0) / (NR - 1) == I) {
          if constexpr (r < MFX) rd_item(nxt, c, KSc, rc);
          else {
            constexpr int n = (r - MFX) >> 1, o = n * 8192 + decltype(KSc)::value * 32 * STRIDE;
            if constexpr ((r - MFX) & 1) nxt.hi[n] = ds_tr<o + 4 * STRIDE>(c.wb); else nxt.lo[n] = ds_tr<o>(c.wb);
          }
        }
      });
      static_for<0, NL>([&](auto lc) { constexpr int i = decltype(lc)::value; if constexpr (R0 + i * (NM - R0) / NL == I) ld(i); });
      __builtin_amdgcn_sched_barrier(0);
    });
    wait_frags(nxt);
  };
  FR fa, fb;
  using IM1 = IC<-1>;

  asm volatile("s_waitcnt lgkmcnt(0)" :: "s"(a.warm), "v"(wsrc[WI - 1]), "v"(xsrc[XI - 1]));
  issueW(0, 0); issueX(0, 0); issueW(1, 1); issueX(1, 1);
  u32x4 sx[4];
  const unsigned t_begin = (unsigned)__builtin_amdgcn_s_memrealtime();
  constexpr int TNEW = RING + 3072;
  auto ldT = [&](int dst) {
    int lx = lane; asm volatile("" : "+v"(lx));
    const auto rT = RSRCN((const char*)a.cs + STAB1, 2048 * 16);
    const unsigned to = (unsigned)((m0 - 16) * 16 + lx * 16);
    __builtin_amdgcn_raw_ptr_buffer_load_lds(rT, LDSP(lds0 + dst), 16, to, 0, 0, 16);
    __builtin_amdgcn_raw_ptr_buffer_load_lds(rT, LDSP(lds0 + dst + 1024), 16, to + 1024u, 0, 0, 16);
    if (lx < 16) __builtin_amdgcn_raw_ptr_buffer_load_lds(rT, LDSP(lds0 + dst + 2048), 16, to + 2048u, 0, 0, 16);
  };
  auto ldS = [&]() {
    int lx = lane; asm volatile("" : "+v"(lx));
    const unsigned so = (unsigned)((m0 + tn * 8 + wave * 2 + (lx >> 5)) * (K * 2) + (lx & 31) * 16);
#pragma unroll
    for (int i = 0; i < 4; ++i) sx[i] = __builtin_amdgcn_raw_buffer_load_b128(RSRC(a.X), so, i * 512, 0);
  };
  __builtin_amdgcn_sched_barrier(0);
  if (wave == 4) {
    if (lane < 4) *(unsigned*)(smem + ONES + lane * 4) = 0x3f803f80u;
  }
  if (wave == 5) {
    const int p2 = (xcd >> 1) * 2 + (idx >> 4), mb = (xcd & 1) * 16 + (idx & 15);
#pragma unroll
    for (int i = 0; i < 3; ++i)
      __builtin_amdgcn_raw_buffer_store_b128((u32x4){~0u, ~0u, ~0u, ~0u}, RSRC(a.st), lane * 16 + i * 1024, ((p2 * 16 + (mb >> 1)) * RBH + (xcd >> 2) * 64 + (mb & 1) * 12) * 256, 16);
  }
  constexpr int HQ = 5;
  const int slc = (3 - ((unsigned)(size_t)a.W0 >> 7) - (xcd & 3) * 4) & 7;
  const int hid = (idx >> 2) * 3 + (idx & 3) - ((idx & 3) > slc ? 1 : 0);
  const bool hlp = slc < 4 && (idx & 3) != slc;
  const int hj1 = hlp ? hid / 3 - HQ : -100, hj2 = hlp ? hid / 3 + 8 - HQ : -100;
  const unsigned htv = (unsigned)(lane * (LDW * 2) + (hid % 3) * 2048 + ((xcd & 3) * 4 + slc) * 128);
  if (wave == 6 && hlp && hj1 <= 0) touch(a.W0, htv, (hid / 3) * (64 * LDW * 2));
  auto code_touch = [&]() {
    unsigned long long c0, c1; CODE_ADDR(c0, "k_all"); CODE_ADDR(c1, "k_zend");
    const unsigned len = c1 - c0 < 262144ull ? (unsigned)(c1 - c0) : 0u;
    touch((const void*)c0, (unsigned)((lane * 32 + idx) * 128), 0, len);
  };
  if (wave == 7) code_touch();
  if (wave < NLW) wait_vm<WI + XIMIN>();
  lds_barrier();
  { const RdCtx c = mkctx(false, 0, 0); static_for<0, MFX + NF>([&](auto kc) { rd_item(fa, c, I0{}, kc); }); }
  wait_frags(fa);
  STAMP(1);
  auto run = [&](auto ROLEc) __attribute__((always_inline)) {
    constexpr int ROLE = decltype(ROLEc)::value;
    constexpr bool LDR = ROLE < NLW;
    using CS0 = std::integral_constant<bool, ROLE / WN == 0>; using CS1 = std::integral_constant<bool, ROLE / WN == 1>;
    using STt = IC<ROLE>;
    auto nold = [&](int) {};
    { const RdCtx c = mkctx(true, 0, 0); step(CS0{}, STt{}, fa, fb, c, I1{}, T{}, IC<LDR ? WI + XI : 0>{}, [&](int i) { if (i < WI) ldW(i, 2, 2); else ldX(i - WI, 2, 2); }); }
    if constexpr (LDR) ldS();
    int slx = 1;
    unsigned snap = 0, t_start = 0;
    unsigned ts7 = 0;
    if constexpr (ROLE == 7) ts7 = t_begin;
    if constexpr (ROLE == 5) {
      t_start = (unsigned)__builtin_amdgcn_s_memrealtime();
      const int t2 = (xcd >> 1) * 4 + (idx >> 3), pg = (xcd >> 2) * 4 + (idx >> 3), tg = (xcd & 3) * 8 + (idx & 7), l8 = lane & 7;
      const int w = l8 == 0 ? SYO(t2) : (l8 == 1 ? pg * 64 : (l8 == 2 ? pg * 64 + 32 : (l8 == 3 ? 1024 + tg * 32 : (l8 == 4 ? (8 + (t2 >> 1)) * 64 : (l8 == 6 ? (t2 >> 1) * 64 : 2048 + bid * 32)))));
      snap = __hip_atomic_load(a.sync + w, __ATOMIC_RELAXED, __HIP_MEMORY_SCOPE_AGENT);
    }
    auto iter = [&](int j, auto LXc, auto LWc, auto WAITc, auto TIc) {
      constexpr bool LX = decltype(LXc)::value && LDR, LW = decltype(LWc)::value && LDR;
      constexpr int WAIT = decltype(WAITc)::value, TI = decltype(TIc)::value;
      constexpr int EX = (LDR && TI < 0) ? WI : 0;
      constexpr int OL = TI == 1 ? 0 : 2, OH = (LDR && TI > 0) ? 2 : OL;
      constexpr int NLT = EX + (LX ? XI : 0) + (LW ? WI : 0) + 2 * (OH - OL), N0 = (NLT + 1) / 2, N1 = NLT - N0;
      if constexpr (LDR) wait_vm<WAIT + (TI > 0 ? 2 * OL : 0)>();
      unsigned osrc = 0;
      if constexpr (OH > OL) { int lx = lane; asm volatile("" : "+v"(lx)); const int row = ROLE * 8 + (lx >> 3); osrc = (unsigned)(row * D_ + ((xcd & 1) * 8 + (idx & 7)) * 64 + ((lx & 7) ^ wswz(row)) * 8) * 2u; }
      if constexpr (ROLE == 6 && TI == 0) { if (j == hj1 || j == hj2) touch(a.W0, htv, (j + HQ) * (64 * LDW * 2)); }
      constexpr int S1 = 2, S3 = 11;
      if constexpr (LDR && TI <= 0) {
        if (j == S1) {
          float sm = 0.f, sq = 0.f;
          const bf16x2_t one2 = __builtin_bit_cast(bf16x2_t, 0x3f803f80u);
#pragma unroll
          for (int i = 0; i < 4; ++i)
#pragma unroll
            for (int e = 0; e < 4; ++e) { const unsigned u = sx[i][e]; const bf16x2_t v = __builtin_bit_cast(bf16x2_t, u); sm = __builtin_amdgcn_fdot2_f32_bf16(v, one2, sm, false); sq = __builtin_amdgcn_fdot2_f32_bf16(v, v, sq, false); }
          sm = sum8l(sm); sq = sum8l(sq);
          sm += __int_as_float(__builtin_amdgcn_update_dpp(0, __float_as_int(sm), 0x128, 0xf, 0xf, false));
          sq += __int_as_float(__builtin_amdgcn_update_dpp(0, __float_as_int(sq), 0x128, 0xf, 0xf, false));
          { auto p = __builtin_amdgcn_permlane16_swap(__float_as_uint(sm), __float_as_uint(sm), false, false); sm = __uint_as_float(p[0]) + __uint_as_float(p[1]); }
          { auto p = __builtin_amdgcn_permlane16_swap(__float_as_uint(sq), __float_as_uint(sq), false, false); sq = __uint_as_float(p[0]) + __uint_as_float(p[1]); }
          const float mu = sm * (1.f / 1024.f);
          float var = sq * (1.f / 1024.f) - mu * mu; var = var > 0.f ? var : 0.f;
          int lx = lane; asm volatile("" : "+v"(lx));
          const unsigned long long now = __builtin_amdgcn_s_memrealtime();
          __builtin_amdgcn_raw_buffer_store_b128((u32x4){__float_as_uint(mu), (unsigned)now, __float_as_uint(__builtin_amdgcn_rsqf(var + 1e-5f)), ~(unsigned)now ^ (unsigned)(now >> 32)}, RSRC(a.cs),
                                                 (lx & 31) == 0 ? (unsigned)(STAB1 + (m0 + tn * 8 + ROLE * 2 + (lx >> 5)) * 16) : 0x80000000u, 0, 16);
          __builtin_amdgcn_sched_barrier(0);
          asm volatile("s_nop 1" ::: "memory");
        }
      }
      if constexpr (ROLE == 7 && TI == 0) {
        if (j == S3) ldT(TNEW);
        if (j == S3 + 1) {
          for (;;) {
            wait_vm<0>();
            int lx = lane; asm volatile("" : "+v"(lx));
            const unsigned ad = lds0 + TNEW + lx * 16;
            u32x4 v[3];
            v[0] = __builtin_bit_cast(u32x4, ds_b128<0>(ad)); v[1] = __builtin_bit_cast(u32x4, ds_b128<1024>(ad)); v[2] = __builtin_bit_cast(u32x4, ds_b128<2048>(ad));
            const unsigned long long now = __builtin_amdgcn_s_memrealtime();
            asm volatile("s_waitcnt lgkmcnt(0)" : "+v"(v[0]), "+v"(v[1]), "+v"(v[2]) :: "memory");
            const unsigned lo = ts7 - 1500u, span = (unsigned)now - lo, hi = (unsigned)(now >> 32);
            auto good = [&](const u32x4& e) { return e[1] - lo <= span && (~e[1] ^ e[3]) == hi; };
            const bool bad = (!good(v[0]) && (tm > 0 || lx >= 16)) || !good(v[1]) || (!good(v[2]) && lx < 16);
            if (__builtin_amdgcn_ballot_w64(bad) == 0 || (unsigned)now - ts7 >= 2500000u) break;
            ldT(TNEW);
          }
        }
      }
      if constexpr (ROLE == 5 && TI == 1) wait_vm<0>();
      if constexpr (ROLE == 7 && TI == 1) code_touch();
      if constexpr (ROLE == 5 && TI == 1) ds_w32(lds0 + SNAP + (lane & 7) * 4, (int)((lane & 7) == 7 ? t_start : snap));
      if constexpr (ROLE == 4 && TI == 0) touch(a.PF1, (bid * 64 + lane) * 128, 0, j == 1 + idx % 12 ? 0x7fffffffu : 0u);
      if constexpr (ROLE >= 4 && TI == 2) {
        const bf16_t* cw = ROLE < 6 ? a.CW0 : a.CW1;
        touch(cw, lane < 56 ? (unsigned)(((ROLE & 1) * 512 + bid) * (DF_ * 2) + lane * 128) : 0x80000000u, 0);
      }
      int xp = slx + 2; if (xp >= 3) xp -= 3;
      auto ld = [&](int i) { if (i < EX) { ldW(i, j + 2, (j + 2) & 3); return; } i -= EX;
                             if (LX && i < XI) ldX(i, j + 2, xp);
                             else if constexpr (LW) ldW(i - (LX ? XI : 0), j + 3, (j + 3) & 3);
                             else { const int q = i - (LX ? XI : 0), t = OL + (q >> 1), h = q & 1;
                               __builtin_amdgcn_raw_ptr_buffer_load_lds(RSRC(a.PF1), LDSP(lds0 + wo_pos(t >> 1) + (t & 1) * 8192 + (ROLE + 4 * h) * 1024), 16,
                                                                        osrc, ((((t & 1) * 8 + (t >> 1)) ^ ((xcd & 3) * 4)) * 2 + h) * (32 * D_ * 2), 0, 0); } };
      { const RdCtx c = mkctx(false, slx * XT, (j & 3) * WU); step1(CS1{}, STt{}, fb, fa, c, I0{}, IC<LDR ? 1 : 0>{}, IC<-4>{}, IC<LDR ? 1 : -100>{}, [&]() { raw_barrier(); }, IC<N0>{}, ld); }
      { const RdCtx c = mkctx(true, slx * XT, (j & 3) * WU); step1(CS0{}, STt{}, fa, fb, c, I1{}, I0{}, IC<LDR ? -4 : -6>{},
          IC<LDR ? -100 : -3>{}, [&]() { if constexpr (TI != 3) { asm volatile("s_waitcnt lgkmcnt(0)" ::: "memory"); raw_barrier(); } }, IC<N1>{}, [&](int i) { ld(i + N0); }); }
      slx = slx + 1; if (slx >= 3) slx = 0;
    };
    using WS_ = IC<2 * WI + XIMIN>;
    if constexpr (!LDR) raw_barrier();
    if constexpr (LDR) iter(1, T{}, T{}, IC<WI + XIMIN + 4>{}, IM1{});
    for (int j = LDR ? 2 : 1; j <= NT - 4; ++j) iter(j, T{}, T{}, WS_{}, I0{});
    iter(NT - 3, T{}, F{}, WS_{}, I1{});
    iter(NT - 2, F{}, F{}, IC<WI + XIMIN>{}, IC<2>{});
    iter(NT - 1, F{}, F{}, I0{}, IC<3>{});
    if constexpr (ROLE == 5) { if (lane == 0) __hip_atomic_fetch_add(a.sync + ((xcd >> 1) * 2 + (idx >> 4)) * 64, 1u, __ATOMIC_RELAXED, __HIP_MEMORY_SCOPE_AGENT); }
    { const RdCtx c = mkctx(false, 0, 0); step(CS1{}, STt{}, fb, fa, c, I0{}, F{}, I0{}, nold); }
  };
  switch (wave) {
    case 0: run(IC<0>{}); break; case 1: run(IC<1>{}); break;
    case 2: run(IC<2>{}); break; case 3: run(IC<3>{}); break;
    case 4: run(IC<4>{}); break; case 5: run(IC<5>{}); break;
    case 6: run(IC<6>{}); break; default: run(IC<7>{}); break;
  }
  STAMP(2);
  {
    float* tcs = (float*)(smem + RING + 64);
    float* tkv = tcs + 128;
    if (fr == 0) { *(f32x4*)(tcs + wm * 64 + wn * 16 + fq * 4) = accs; *(f32x4*)(tkv + wave * 16 + fq * 4) = acch; }
    const int srow = m0 + wave * 16 + fr;
    const bool par = fr & 1;
    float bias[4];
    {
      const unsigned mk = 0x5555u & ((2u << (srow < 15 ? srow : 15)) - 1u), sft = (fr & 14) - fq * 4 + 14;
      const unsigned b0 = ~(mk >> sft), b1 = ~((mk << 16) >> sft);
#pragma unroll
      for (int j = 0; j < 4; ++j) bias[j] = __uint_as_float((unsigned)__builtin_amdgcn_sbfe((int)(j < 2 ? b0 : b1), 2 - 2 * (j & 1), 1) & 0xff61b1e6u);
    }
    unsigned pl = par ? 0x01000c0cu : 0x0c0c0100u, ph = par ? 0x03020c0cu : 0x0c0c0302u;
    asm volatile("" : "+v"(bias[0]), "+v"(bias[1]), "+v"(bias[2]), "+v"(bias[3]), "+v"(pl), "+v"(ph));
    STAMP(9);
    lds_barrier();
    STAMP(6);
    constexpr int RS = 400;
    char* qt = smem;
    f32x4 csn[NF];
    const f32x4 c0a = *(const f32x4*)(tcs + wn * 16 + fq * 4), c0b = *(const f32x4*)(tcs + 64 + wn * 16 + fq * 4);
    csn[1] = *(const f32x4*)(tkv + wn * 16 + fq * 4); csn[2] = *(const f32x4*)(tkv + 64 + wn * 16 + fq * 4);
    f32x2 st[MF + 1];
#pragma unroll
    for (int m = 0; m <= MF; ++m) {
      const int r = m < MF ? 16 + (wm * MF + m) * 16 + fr : fr;
      const f32x4 t4 = *(const f32x4*)(smem + TNEW + r * 16); st[m] = (f32x2){t4[0], t4[2]};
    }
    const f32x4 csh = *(const f32x4*)(tkv + wave * 16 + fq * 4);
    asm volatile("" ::: "memory");
    csn[0] = c0a + c0b;
#pragma unroll
    for (int m = 0; m < MF; ++m) {
      const int r = 16 + (wm * MF + m) * 16 + fr;
      const float mu = st[m][0], rs = st[m][1];
#pragma unroll
      for (int n = 0; n < NF; ++n) {
        const int col = (n * 4 + wn) * 16 + fq * 4;
        const f32x4 cs = csn[n];
        const u32x2 v = {pk_bf16(rs * (acc[n][m][0] - mu * cs[0]), rs * (acc[n][m][1] - mu * cs[1])),
                         pk_bf16(rs * (acc[n][m][2] - mu * cs[2]), rs * (acc[n][m][3] - mu * cs[3]))};
        *(u32x2*)(qt + r * RS + col * 2) = v;
      }
    }
    {
      const float mu = st[MF][0], rs = st[MF][1];
      const int col = (4 + wave) * 16 + fq * 4;
      const u32x2 v = {pk_bf16(rs * (acch[0] - mu * csh[0]), rs * (acch[1] - mu * csh[1])),
                       pk_bf16(rs * (acch[2] - mu * csh[2]), rs * (acch[3] - mu * csh[3]))};
      *(u32x2*)(qt + fr * RS + col * 2) = v;
    }
    STAMP(4);
    lds_barrier();
    STAMP(5);
    const unsigned qa = lds0 + (16 + wave * 16 + fr) * RS + fq * 16;
    const unsigned ka = lds0 + (wave * 16 + fr) * RS + 128 + fq * 16;
    const unsigned va = lds0 + (wave * 16 + fq * 4 + wq) * RS + 256 + wp * 16;
    bf16x8 qf[2], kf[2][2]; s16x4 vf[2][2][2];
    qf[0] = ds_b128<0>(qa); kf[0][0] = ds_b128<0>(ka); kf[0][1] = ds_b128<16 * RS>(ka);
    qf[1] = ds_b128<64>(qa); kf[1][0] = ds_b128<64>(ka); kf[1][1] = ds_b128<16 * RS + 64>(ka);
    vf[0][0][0] = ds_tr<0>(va); vf[0][0][1] = ds_tr<16 * RS>(va); vf[0][1][0] = ds_tr<8>(va); vf[0][1][1] = ds_tr<16 * RS + 8>(va);
    vf[1][0][0] = ds_tr<64>(va); vf[1][0][1] = ds_tr<16 * RS + 64>(va); vf[1][1][0] = ds_tr<72>(va); vf[1][1][1] = ds_tr<16 * RS + 72>(va);
    asm volatile("s_waitcnt lgkmcnt(8)" ::: "memory");
#pragma unroll
    for (int hh = 0; hh < 2; ++hh) asm volatile("" : "+v"(qf[hh]), "+v"(kf[hh][0]), "+v"(kf[hh][1]));
    const float sc2 = 0.17677669529663687f * 1.4426950408889634f;
    u32x4 ov[2];
    const f32x4 z = {0.f, 0.f, 0.f, 0.f};
    f32x4 sc[2][2]; float lg[2][4], mx[2], sum[2];
    auto x16 = [](float v) { auto p = __builtin_amdgcn_permlane16_swap(__float_as_uint(v), __float_as_uint(v), false, false); return (f32x2){__uint_as_float(p[0]), __uint_as_float(p[1])}; };
    auto x32 = [](float v) { auto p = __builtin_amdgcn_permlane32_swap(__float_as_uint(v), __float_as_uint(v), false, false); return (f32x2){__uint_as_float(p[0]), __uint_as_float(p[1])}; };
#pragma unroll
    for (int hh = 0; hh < 2; ++hh) {
      sc[hh][0] = MFMA(kf[hh][0], qf[hh], z);
      sc[hh][1] = MFMA(kf[hh][1], qf[hh], z);
    }
    __builtin_amdgcn_sched_barrier(0);
#pragma unroll
    for (int hh = 0; hh < 2; ++hh) {
#pragma unroll
      for (int j = 0; j < 4; ++j) lg[hh][j] = __builtin_fmaf(par ? sc[hh][j >> 1][(j & 1) * 2 + 1] : sc[hh][j >> 1][(j & 1) * 2], sc2, bias[j]);
      mx[hh] = fmaxf(fmaxf(lg[hh][0], lg[hh][1]), fmaxf(lg[hh][2], lg[hh][3]));
    }
    __builtin_amdgcn_sched_barrier(0);
    { const f32x2 p = x16(mx[0]), q = x16(mx[1]); mx[0] = fmaxf(p[0], p[1]); mx[1] = fmaxf(q[0], q[1]); }
    { const f32x2 p = x32(mx[0]), q = x32(mx[1]); mx[0] = fmaxf(p[0], p[1]); mx[1] = fmaxf(q[0], q[1]); }
    __builtin_amdgcn_sched_barrier(0);
#pragma unroll
    for (int hh = 0; hh < 2; ++hh) {
#pragma unroll
      for (int j = 0; j < 4; ++j) lg[hh][j] = __builtin_amdgcn_exp2f(lg[hh][j] - mx[hh]);
      sum[hh] = (lg[hh][0] + lg[hh][1]) + (lg[hh][2] + lg[hh][3]);
    }
    __builtin_amdgcn_sched_barrier(0);
    asm volatile("s_waitcnt lgkmcnt(0)" ::: "memory");
#pragma unroll
    for (int hh = 0; hh < 2; ++hh) asm volatile("" : "+v"(vf[hh][0][0]), "+v"(vf[hh][0][1]), "+v"(vf[hh][1][0]), "+v"(vf[hh][1][1]));
    { const f32x2 p = x16(sum[0]), q = x16(sum[1]); sum[0] = p[0] + p[1]; sum[1] = q[0] + q[1]; }
    { const f32x2 p = x32(sum[0]), q = x32(sum[1]); sum[0] = __builtin_amdgcn_rcpf(p[0] + p[1]); sum[1] = __builtin_amdgcn_rcpf(q[0] + q[1]); }
    __builtin_amdgcn_sched_barrier(0);
#pragma unroll
    for (int hh = 0; hh < 2; ++hh) {
      const float inv = sum[hh];
      const unsigned c0 = pk_bf16(lg[hh][0] * inv, lg[hh][1] * inv), c1 = pk_bf16(lg[hh][2] * inv, lg[hh][3] * inv);
      const u32x4 pp = {__builtin_amdgcn_perm(0u, c0, pl), __builtin_amdgcn_perm(0u, c0, ph), __builtin_amdgcn_perm(0u, c1, pl), __builtin_amdgcn_perm(0u, c1, ph)};
      const bf16x8 pf = __builtin_bit_cast(bf16x8, pp);
      sc[hh][0] = MFMA(__builtin_shufflevector(vf[hh][0][0], vf[hh][0][1], 0, 1, 2, 3, 4, 5, 6, 7), pf, z);
      sc[hh][1] = MFMA(__builtin_shufflevector(vf[hh][1][0], vf[hh][1][1], 0, 1, 2, 3, 4, 5, 6, 7), pf, z);
    }
    __builtin_amdgcn_sched_barrier(0);
#pragma unroll
    for (int hh = 0; hh < 2; ++hh) ov[hh] = (u32x4){pk_bf16(sc[hh][0][0], sc[hh][0][1]), pk_bf16(sc[hh][0][2], sc[hh][0][3]), pk_bf16(sc[hh][1][0], sc[hh][1][1]), pk_bf16(sc[hh][1][2], sc[hh][1][3])};
    STAMP(10);
#pragma unroll
    for (int hh = 0; hh < 2; ++hh) __builtin_amdgcn_raw_buffer_store_b128(ov[hh], rO, (((m0 >> 4) + wave) * RBO + tn * 8 + hh * 4) * 256 + lane * 16, 0, 16);
    STAMP(11);
  }
  STAMP(7);
  STAMP(8);
  asm volatile("s_waitcnt vmcnt(0)" ::: "memory");
  raw_barrier();
  if (wave != 0) hook();
  if (tid == 0) __hip_atomic_fetch_add(a.sync + SYO(tm), FIN, __ATOMIC_RELAXED, __HIP_MEMORY_SCOPE_AGENT);
  if (wave == 0) hook();
  STAMP(3);
#endif
}

template <class HK>
__device__ __forceinline__ void glu_phase(const GemmArgs& a, HK hook) {
#if defined(__HIP_DEVICE_COMPILE__)
  constexpr int LDW = DF_, WN = 2;
  constexpr int UPOFF = 32 * 224 + 16;
  constexpr int XT = 32768, WU = 14592;
  constexpr int XBASE = 0, WBASE = 3 * XT, RING = WBASE + 4 * WU;
  constexpr int MF = 4, NF = 7;
  constexpr int NT = 16, NXI = 16, NWI = 14, NLW = 4, XI = 4, WI = 4;
  extern __shared__ __attribute__((aligned(16))) char smem[];
  STAMP(0);
  const unsigned lds0 = (unsigned)(size_t)(lds_char_ptr)smem;
  const int tid = threadIdx.x, lane = tid & 63;
  const int wave = __builtin_amdgcn_readfirstlane(tid >> 6);
  const int wm = wave / WN, wn = wave % WN;
  const int bid = blockIdx.x, xcd = bid & 7, idx = bid >> 3;
  const int tm = (xcd / 4) * 4 + idx / 8, tn = (xcd % 4) * 8 + idx % 8;
  const int m0 = tm * 256;
  const int krot = (tm >> 2) * 8;
  const int fr = lane & 15, fq = lane >> 4;
  const int wq = (lane >> 2) & 3, wp = lane & 3;
  const int panel = m0 >> 8;
  constexpr int TCH = RING + 5120;
  constexpr int DONE = TCH + 896;
  auto done_inc = [&]() { if (lane == 0) asm volatile("ds_add_u32 %0, %1" :: "v"(lds0 + DONE), "v"(1) : "memory"); };
  auto touch = [&](const void* base, unsigned voff, unsigned soff, unsigned lim = 0x7fffffffu) {
    __builtin_amdgcn_raw_ptr_buffer_load_lds(RSRCN(base, lim), LDSP(lds0 + TCH), 4, voff, soff, 0, 0);
  };
  const auto rO = RSRC(a.O);

  unsigned xsrc[XI], wsrc[WI];
#pragma unroll
  for (int i = 0; i < XI; ++i) {
    const int j = (wave % NLW) + NLW * i;
    xsrc[i] = (unsigned)((((m0 >> 4) + (j >> 1)) * RBH + (j & 1) * 4) * 256 + lane * 16);
  }
#pragma unroll
  for (int i = 0; i < WI; ++i) {
    int j = (wave % NLW) + NLW * i; if (j >= NWI) j -= NLW;
    const int jj = j >= 7 ? j - 7 : j;
    const int b = jj * 1024 + lane * 16, p = b / 224, pos = (b % 224) >> 4;
    const int row = 8 * ((p & 15) >> 2) + (p & 3) + 4 * (p >> 4);
    wsrc[i] = (unsigned)(row * LDW + tn * 112 + pos * 8) * 2u;
  }
  auto ldX = [&](int i, int tile, int part, int slot) {
    tile = (tile + krot) & 15;
    const int j = wave + NLW * i;
    __builtin_amdgcn_raw_ptr_buffer_load_lds(RSRC(a.X), LDSP(lds0 + XBASE + slot * XT + (part * NXI + j) * 1024), 16, xsrc[i], tile * 2048 + part * (NXI / 2) * RBH * 256, 0, 16);
  };
  auto ldW = [&](int i, int unit, int slot) {
    const auto rW0 = RSRC(a.W0);
    const auto rW1 = RSRC(a.W1);
    int j = wave + NLW * i; if (j >= NWI) j -= NLW;
    unit = (unit + 2 * krot) & 31;
    const bool up = j >= 7;
    __builtin_amdgcn_raw_ptr_buffer_load_lds(up ? rW1 : rW0, LDSP(lds0 + WBASE + slot * WU + (up ? UPOFF + (j - 7) * 1024 : j * 1024)), 16, wsrc[i], unit * 32 * LDW * 2, 0, 0);
  };
  auto issueX = [&](int tile, int part, int slot) {
    if (wave >= NLW) return;
#pragma unroll
    for (int i = 0; i < XI; ++i) ldX(i, tile, part, slot);
  };
  auto issueW = [&](int unit, int slot) {
    if (wave >= NLW) return;
#pragma unroll
    for (int i = 0; i < WI; ++i) ldW(i, unit, slot);
  };

  f32x4 acc[NF][MF];
#pragma unroll
  for (int n = 0; n < NF; ++n)
#pragma unroll
    for (int m = 0; m < MF; ++m) acc[n][m] = (f32x4){0.f, 0.f, 0.f, 0.f};
  f32x4 accs = {0.f, 0.f, 0.f, 0.f};

  const unsigned xa0 = lds0 + XBASE + wm * MF * 16 * 128 + lane * 16, xa1 = xa0 + 1024;
  unsigned wa[2];
#pragma unroll
  for (int c = 0; c < 2; ++c) wa[c] = lds0 + WBASE + (4 * fq + wq) * 224 + wn * 112 + (c == 0 ? wp * 8 : 96 + (wp & 1) * 8 + (wp >> 1) * UPOFF);

  using I0 = IC<0>; using I1 = IC<1>;
  using T = std::true_type; using F = std::false_type;
  typedef Frags<MF, NF> FR;
  struct RdCtx { unsigned xa, wb[2]; };
  auto mkctx = [&](bool ks1, unsigned xoffs, unsigned woffs) {
    RdCtx c; c.xa = (ks1 ? xa1 : xa0) + xoffs; c.wb[0] = wa[0] + woffs; c.wb[1] = wa[1] + woffs;
    return c;
  };
  auto rd_item = [&](FR& f, const RdCtx& c, auto kc) {
    constexpr int k = decltype(kc)::value;
    if constexpr (k < MF) f.x[k] = ds_b128<k * 2048>(c.xa);
    else {
      constexpr int n = k - MF, cc = n == 6, o = n == 6 ? 0 : (n >> 1) * 32 + (n & 1) * UPOFF;
      f.lo[n] = ds_tr<o>(c.wb[cc]); f.hi[n] = ds_tr<o + 16 * 224>(c.wb[cc]);
    }
  };
  auto wait_frags = [&](FR& f) {
    asm volatile("s_waitcnt lgkmcnt(0)" ::: "memory");
#pragma unroll
    for (int m = 0; m < MF; ++m) asm volatile("" : "+v"(f.x[m]));
#pragma unroll
    for (int n = 0; n < NF; ++n) asm volatile("" : "+v"(f.lo[n]), "+v"(f.hi[n]));
  };
  auto step = [&](FR& cur, FR& nxt, const RdCtx& c, auto PFc, auto NLc, auto&& ld) {
    constexpr bool PF = decltype(PFc)::value != 0; constexpr int NL = decltype(NLc)::value;
    constexpr int R = MF + NF, RG = NF - 1;
    static_for<0, NF>([&](auto gc) {
      constexpr int g = decltype(gc)::value;
      if constexpr (PF) static_for<0, R>([&](auto kc) { constexpr int k = decltype(kc)::value; if constexpr (k * RG / R == g) rd_item(nxt, c, kc); });
      static_for<0, NL>([&](auto ic) { constexpr int i = decltype(ic)::value; if constexpr (i * NF / NL == g) ld(i); });
      const bf16x8 w = __builtin_shufflevector(cur.lo[g], cur.hi[g], 0, 1, 2, 3, 4, 5, 6, 7);
#pragma unroll
      for (int m = 0; m < MF; ++m) acc[g][m] = MFMA(w, cur.x[m], acc[g][m]);
      __builtin_amdgcn_sched_barrier(0);
    });
    if constexpr (PF) wait_frags(nxt);
  };
  auto stepg = [&](auto CSc, FR& cur, FR& nxt, const RdCtx& c, auto R0c, auto IBc, auto&& sync, auto NLc, auto&& ld) {
    constexpr bool CS = decltype(CSc)::value; constexpr int R0 = decltype(R0c)::value, IB = decltype(IBc)::value, NL = decltype(NLc)::value;
    constexpr int NM = NF * MF, NR = MF + 2 * NF;
    static_for<0, NM>([&](auto ic) {
      constexpr int I = decltype(ic)::value, g = I / MF, m = I % MF;
      constexpr int RL = IB > 8 ? 20 : 22;
      const bf16x8 w = __builtin_shufflevector(cur.lo[g], cur.hi[g], 0, 1, 2, 3, 4, 5, 6, 7);
      acc[g][m] = MFMA(w, cur.x[m], acc[g][m]);
      __builtin_amdgcn_sched_barrier(0);
      if constexpr (I == IB) sync();
      static_for<0, NR>([&](auto rc) {
        constexpr int r = decltype(rc)::value;
        if constexpr (R0 + r * (RL - R0) / (NR - 1) == I) {
          if constexpr (r < MF) nxt.x[r] = ds_b128<r * 2048>(c.xa);
          else {
            constexpr int n = (r - MF) >> 1, cc = n == 6, o = n == 6 ? 0 : (n >> 1) * 32 + (n & 1) * UPOFF;
            if constexpr ((r - MF) & 1) nxt.hi[n] = ds_tr<o + 16 * 224>(c.wb[cc]); else nxt.lo[n] = ds_tr<o>(c.wb[cc]);
          }
        }
      });
      static_for<0, NL>([&](auto lc) { constexpr int i = decltype(lc)::value; if constexpr (R0 + i * (NM - 1 - R0) / NL == I) ld(i); });
      if constexpr (CS && m == MF - 1) {
        const unsigned sv = fr == g ? 0x3f803f80u : 0u;
        const u32x4 s4 = {sv, sv, sv, sv};
        accs = MFMA(w, __builtin_bit_cast(bf16x8, s4), accs);
      }
      __builtin_amdgcn_sched_barrier(0);
    });
    wait_frags(nxt);
  };
  FR fa, fb;

  f32x4* tb_row = (f32x4*)(smem + RING);
  float* tb_cs = (float*)(tb_row + 256);
  constexpr int RTAB = 524288;
  auto glu_stats_issue = [&]() {
    if (wave == 7) __builtin_amdgcn_raw_ptr_buffer_load_lds(RSRC(a.st), LDSP(lds0 + RING + 3072), 16, ((lane >> 2) * S_ + m0 + tn * 8 + (lane & 3) * 2) * 8, 0, 0, 16);
  };
  auto glu_stats = [&]() {
    int lx = lane; asm volatile("" : "+v"(lx));
    const unsigned sa = lds0 + RING + 3072 + lx * 8;
    f32x2 p0 = ds_b64<0>(sa), p1 = ds_b64<512>(sa);
    asm volatile("s_waitcnt lgkmcnt(0)" : "+v"(p0), "+v"(p1) :: "memory");
    const float mu = sum8(p0[0] + p1[0]) * (1.f / 16.f);
    const float v = sum8((p0[1] + p1[1]) + 64.f * ((p0[0] - mu) * (p0[0] - mu) + (p1[0] - mu) * (p1[0] - mu))) * (1.f / 1024.f) + 1e-5f;
    const float rs = 1.0f / sqrtf(v);
    const f32x4 rv = {mu, -0.10294324f * rs * rs * rs, -2.3022082f * rs, v};
    __builtin_amdgcn_raw_buffer_store_b128(__builtin_bit_cast(u32x4, rv), RSRC(a.cs), lx < 8 ? (unsigned)(RTAB + (m0 + tn * 8 + lx) * 16) : 0x80000000u, 0, 16);
  };
  auto epilogue = [&](auto EAc) __attribute__((always_inline)) {
    STAMP(2);
    constexpr bool EARLY = decltype(EAc)::value;
    static_assert(DONE + 4 == ACNT && TCH + 256 == FPAD, "LDS");
    if constexpr (!EARLY) {
      wait_vm<0>();
      lds_wait8(lds0 + DONE);
      __builtin_amdgcn_s_setprio(2);
      STAMPW(9, 4);
      hook(I0{});
      STAMPW(10, 4);
    } else { __builtin_amdgcn_s_setprio(3); hook(I1{}); }
    asm volatile("" ::: "memory");
    f32x4 cg[4], cu[4], rw[MF], rw6[2];
    int lz = lane; asm volatile("" : "+v"(lz));
    const int fr = lz & 15, fq = lz >> 4;
#pragma unroll
    for (int p = 0; p < 4; ++p) {
      const int o = wn * 56 + p * 16 + (p == 3 ? (fq & 1) : fq) * 4;
      cg[p] = *(const f32x4*)(tb_cs + o); cu[p] = *(const f32x4*)(tb_cs + 112 + o);
    }
#pragma unroll
    for (int m = 0; m < MF; ++m) rw[m] = tb_row[(wm * MF + m) * 16 + fr];
#pragma unroll
    for (int q = 0; q < 2; ++q) rw6[q] = tb_row[(wm * MF + 2 * q + (fq >> 1)) * 16 + fr];
    const int gz = ((m0 >> 4) + wm * 4) * RBA * 256 + fr * 16, g = tn * 2 + wn;
    const int go0 = gz + ((fq & 1) * RBA + (fq >> 1)) * 256, go6 = gz + ((fq & 1) * 2 + (fq >> 1)) * RBA * 256;
    unsigned hv[MF][NF];
    auto put = [&](unsigned a0, unsigned a1, unsigned b0, unsigned b1, int vo, int so) {
      const auto s0 = __builtin_amdgcn_permlane16_swap(a0, b0, false, false), s1 = __builtin_amdgcn_permlane16_swap(a1, b1, false, false);
      __builtin_amdgcn_raw_buffer_store_b128((u32x4){s0[0], s1[0], s0[1], s1[1]}, rO, vo, so, 16);
      __builtin_amdgcn_sched_barrier(0);
      asm volatile("s_nop 1" ::: "memory");
      __builtin_amdgcn_sched_barrier(0);
    };
#define STAGE(body) _Pragma("unroll") for (int i = 0; i < 2 * NN; ++i) { const int m = MB + i / NN, n = N0 + i % NN; const f32x4 k = n == 6 ? rw6[m >> 1] : rw[m]; body; } __builtin_amdgcn_sched_barrier(0)
    auto grp = [&](auto MBc, auto N0c, auto NNc, auto&& mid) {
      constexpr int MB = decltype(MBc)::value, N0 = decltype(N0c)::value, NN = decltype(NNc)::value;
      f32x2 G[2 * NN], U[2 * NN], P[2 * NN];
      __builtin_amdgcn_sched_barrier(0);
      STAGE({ const int e = (n == 6 ? m & 1 : n & 1) * 2; const f32x4 x = n == 6 ? acc[6][m & 2] : acc[n & 6][m]; G[i] = ((f32x2){x[e], x[e + 1]}) - k[0] * ((f32x2){cg[n >> 1][e], cg[n >> 1][e + 1]}); });
      STAGE(P[i] = G[i] * G[i]);
      STAGE(P[i] = P[i] * k[1] + k[2]);
      STAGE(P[i] = G[i] * P[i]);
      STAGE({ const int e = (n == 6 ? m & 1 : n & 1) * 2; const f32x4 x = n == 6 ? acc[6][m | 1] : acc[n | 1][m]; U[i] = ((f32x2){x[e], x[e + 1]}) - k[0] * ((f32x2){cu[n >> 1][e], cu[n >> 1][e + 1]}); });
      STAGE(P[i] = ((f32x2){__builtin_amdgcn_exp2f(P[i][0]), __builtin_amdgcn_exp2f(P[i][1])}));
      mid();
      __builtin_amdgcn_sched_barrier(0);
      STAGE(P[i] = P[i] * k[3] + k[3]);
      STAGE(P[i] = ((f32x2){__builtin_amdgcn_rcpf(P[i][0]), __builtin_amdgcn_rcpf(P[i][1])}));
      STAGE(U[i] = G[i] * U[i]);
      STAGE(U[i] = U[i] * P[i]);
      STAGE({ hv[m][n] = pk_bf16(U[i][0], U[i][1]); asm volatile("" : "+v"(hv[m][n])); });
    };
#undef STAGE
    using I2 = IC<2>; using I3 = IC<3>; using I4 = IC<4>;
    grp(I0{}, I0{}, I4{}, [&]() {});
    grp(I2{}, I0{}, I4{}, [&]() {
#pragma unroll
      for (int p = 0; p < 2; ++p) put(hv[0][2 * p], hv[0][2 * p + 1], hv[1][2 * p], hv[1][2 * p + 1], go0 + p * 512, g * 1024); });
#pragma unroll
    for (int p = 0; p < 2; ++p) put(hv[2][2 * p], hv[2][2 * p + 1], hv[3][2 * p], hv[3][2 * p + 1], go0 + p * 512, g * 1024 + 2 * RBA * 256);
    asm volatile("" ::: "memory");
    __builtin_amdgcn_sched_barrier(0);
    STAMP(4);
    STAMPW(11, 4);
    __builtin_amdgcn_s_setprio(EARLY ? 1 : 0);
#pragma unroll
    for (int q = 0; q < 2; ++q)
#pragma unroll
      for (int e = 0; e < 4; ++e) {
        const float x0 = acc[6][2 * q][e], x1 = acc[6][2 * q + 1][e];
        auto sw = __builtin_amdgcn_permlane32_swap(__float_as_uint(x0), __float_as_uint(x1), false, false);
        acc[6][2 * q][e] = __uint_as_float(sw[0]); acc[6][2 * q + 1][e] = __uint_as_float(sw[1]);
      }
    const int sb = (256 + g * 3) * 256;
    grp(I0{}, I4{}, I3{}, [&]() {});
    if (!EARLY) { wait_vm<0>(); lds_inc(lds0 + ACNT); STAMPW(6, 4); }
    grp(I2{}, I4{}, I3{}, [&]() { put(hv[0][4], hv[0][5], hv[1][4], hv[1][5], go0, sb); });
    put(hv[2][4], hv[2][5], hv[3][4], hv[3][5], go0, sb + 2 * RBA * 256);
    put(hv[0][6], hv[1][6], hv[2][6], hv[3][6], go6, sb + 512);
    asm volatile("" ::: "memory");
    if constexpr (!EARLY) lds_inc(lds0 + ACNT + 4);
  };

  constexpr int WR = 2;
  if (wave == 5) { ds_w32(lds0 + DONE, 0); ds_w32(lds0 + DONE + 4, 0); ds_w32(lds0 + DONE + 8, 0); }
  STAMP(9); STAMP(6);
  auto check_tile = [&](int t) {
    int lx = lane; asm volatile("" : "+v"(lx));
    const unsigned ad = lds0 + XBASE + t * XT + wave * 1024 + lx * 16;
    if (__builtin_expect(marked<8>(ad), 0)) {
      for (int tries = 0;; ++tries) {
        if (tries >= KN_TRY) wait_count(lds0, a.sync + panel * 64, snap_get(lds0, 1), 32u, 2u);
#pragma unroll 1
        for (int u = t; u < t + 1 + (t == 0); ++u) { issueX(u, 0, u); issueX(u, 1, u); }
        if (t == 0) wait_vm<2 * XI>(); else wait_vm<0>();
        if (tries >= KN_TRY || !marked<8>(ad)) break;
      }
    }
  };
  auto run = [&](auto LDRc) __attribute__((always_inline)) {
    constexpr bool LDR = decltype(LDRc)::value;
    if constexpr (LDR) {
      issueX(0, 0, 0); issueX(0, 1, 0); issueX(1, 0, 1); issueX(1, 1, 1);
      issueW(2, 2 ^ WR); issueW(3, 3 ^ WR); wait_vm<2 * XI + 2 * WI>();
      check_tile(0);
    }
    lds_barrier();
    if constexpr (!LDR) { if (wave == 4) __builtin_amdgcn_raw_ptr_buffer_load_lds(RSRCN(a.sync + panel * 64, 4), LDSP(lds0 + TCH + 256), 4, lane * 4, 0, 0, 16); }
    { const RdCtx c = mkctx(false, 0, WR * WU); static_for<0, MF + NF>([&](auto kc) { rd_item(fa, c, kc); }); }
    wait_frags(fa);
    STAMP(1);
    { const RdCtx c = mkctx(true, 0, (WR + 1) * WU); step(fa, fb, c, T{}, IC<LDR ? 2 * XI : 0>{}, [&](int i) { ldX(i % XI, 2, i / XI, 2); }); }
    int xs = 1;
    unsigned long long tb = (unsigned long long)a.PF0; unsigned tv = 0, tl = 0x7fffffffu, ts = 1835008u, tmask = 0; int tj = 1, tsh = 1;
    if constexpr (!LDR) {
      unsigned long long c0, c1; CODE_ADDR(c0, "k_all"); CODE_ADDR(c1, "k_zend");
      constexpr int CTJ = 11, WJ = 1, WE = 8, DJ = 6, KT = 4;
      tj = DJ;
      if (wave == 4) { tb = c0; tl = c1 - c0 < 262144ull ? (unsigned)(c1 - c0) : 0u; ts = 0; tmask = 1u << CTJ; }
      if (wave == 5) tmask = 0x55u << (DJ + (idx & 1));
      if (wave == 6) { tb = (unsigned long long)((idx & 16) ? a.W1 : a.W0); ts = 64 * LDW * 2; tmask = ((1u << (WE - KT)) - 1u) << WJ; tj = WJ - krot - KT; tsh = 0; }
      const int l4 = lane / 14;
      tv = wave == 4 ? (unsigned)((lane * 32 + idx) * 128)
         : (lane >= 56 ? 0x80000000u : (wave == 5 ? (unsigned)(bid * 7168 + lane * 128) : (unsigned)(((idx & 15) * 4 + l4) * (LDW * 2) + (xcd & 3) * 1792 + (lane - l4 * 14) * 128)));
    }
    const int js = 2 * (tm & 3) + 1;
    const auto rC = RSRC(a.cs);
    unsigned* ccnt = a.sync + 1024 + tn * 32;
    constexpr int JR = 10, JS = 2;
    const int jf = wave == 4 ? NT - 4 : (wave == 7 ? JR : 64);
    const unsigned* fcnt = wave == 4 ? ccnt : a.sync + panel * 64 + 32;
    const int jm = (idx >> 4) == ((xcd >> 1) & 1) ? 3 : 64;
    int mo = (((m0 >> 4) + wave * 4) * RBA + tn * 8) * 256;
    const unsigned evm = tmask | (8u << js) | (wave == 4 ? 3u << (NT - 4) : 0u) | (wave == 7 ? ((5u << JS) | 2u | (11u << JR)) : 0u);
    constexpr int CFL = TCH + 256, CPB = XBASE + ((NT - 3) % 3) * XT;
    auto iter = [&](int j, auto LWc, auto LXc, auto WAITc, auto CAc, auto CBc) {
      constexpr bool LW = decltype(LWc)::value && LDR, LX = decltype(LXc)::value && LDR, LP = LW && !LX;
      if constexpr (LDR) {
        wait_vm<decltype(WAITc)::value>();
        if constexpr (LX) { if (__builtin_expect(j <= 2, 0)) check_tile(j); }
        if constexpr (LX) {
          if ((unsigned)(j - jm) < 8u) {
            int lx = lane; asm volatile("" : "+v"(lx));
            unsigned on = 0xffffffffu; asm volatile("" : "+v"(on));
            __builtin_amdgcn_raw_buffer_store_b128((u32x4){on, on, on, on}, rO, lx * 16, mo, 16);
            mo += (j & 1) ? 1024 : RBA * 256 - 1024;
          }
          if (j == jm + 9) { if (lane == 0) __hip_atomic_fetch_add(a.sync + (8 + panel) * 64, 1u, __ATOMIC_RELAXED, __HIP_MEMORY_SCOPE_AGENT); }
        }
      }
      if constexpr (!LDR) {
        if (__builtin_expect((evm >> j) & 1, 0)) {
          if (j == js + 3) {
            if ((tmask >> (j - 1)) & 1) wait_vm<1>(); else wait_vm<0>();
            if (lane == 0) __hip_atomic_fetch_add(ccnt, 1u, __ATOMIC_RELAXED, __HIP_MEMORY_SCOPE_AGENT);
          }
          if (j == 1) glu_stats_issue();
          if (j == JS && wave == 7) { wait_vm<0>(); glu_stats(); }
          if (j == JS + 2 && wave == 7) {
            if (j == js + 2) wait_vm<1>(); else wait_vm<0>();
            if (lane == 0) __hip_atomic_fetch_add((unsigned*)fcnt, 1u, __ATOMIC_RELAXED, __HIP_MEMORY_SCOPE_AGENT);
          }
          {
            int lx = lane; asm volatile("" : "+v"(lx));
            if (j == jf) __builtin_amdgcn_raw_ptr_buffer_load_lds(RSRCN(fcnt, 4), LDSP(lds0 + CFL + (wave & 1) * 256), 4, lx * 4, 0, 0, 16);
            if (j == jf + 1) {
              wait_vm<0>();
              int fv = ds_r32(lds0 + CFL + (wave & 1) * 256);
              const unsigned f0 = snap_get(lds0, 3 - (wave & 1));
              asm volatile("" : "+v"(fv));
              while ((unsigned)__builtin_amdgcn_readfirstlane(fv) - f0 < 32u && in_time(lds0, 3u))
                asm volatile("global_load_dword %0, %1, %2 sc1\n\ts_waitcnt vmcnt(0)" : "=v"(fv) : "v"(0), "s"(fcnt) : "memory");
              if (wave == 7) {
#pragma unroll
                for (int i = 0; i < 4; ++i) __builtin_amdgcn_raw_ptr_buffer_load_lds(rC, LDSP(lds0 + RING + i * 1024), 16, lx * 16, RTAB + m0 * 16 + i * 1024, 0, 16);
              }
            }
            if (j == jf + 3) wait_vm<0>();
          }
          if ((tmask >> j) & 1) touch((const void*)tb, tv, (((j - tj) >> tsh) & 15) * ts, tl);
        }
        __builtin_amdgcn_sched_barrier(0);
      }
      int xp = xs + 2; if (xp >= 3) xp -= 3;
      const int ws = ((j & 1) * 2) ^ WR;
      auto lda = [&](int i) { ldW(i % WI, 2 * j + 2 + i / WI, (ws ^ 2) + i / WI); };
      auto ldb = [&](int i) {
          if constexpr (LX) ldX(i % XI, j + 2, i / XI, xp);
          else __builtin_amdgcn_raw_ptr_buffer_load_lds(rC, LDSP(lds0 + CPB + wave * 8192 + i * 1024), 16, lane * 16, (tn * 2 + wn) * 7168 + i * 1024, 0, 16); };
      using NLA = IC<LW ? 2 * WI : 0>; using NLB = IC<LX ? 2 * XI : (LP ? 7 : 0)>;
      { const RdCtx c = mkctx(false, xs * XT, ws * WU); stepg(CAc, fb, fa, c, IC<LDR ? 2 : 0>{}, IC<LDR ? 1 : -1>{}, [&]() { raw_barrier(); }, NLA{}, lda); }
      { const RdCtx c = mkctx(true, xs * XT, (ws + 1) * WU); stepg(CBc, fa, fb, c, I0{}, IC<LDR ? -1 : 25>{},
          [&]() { asm volatile("s_waitcnt lgkmcnt(0)" ::: "memory"); if (decltype(WAITc)::value == 0 || j + 1 < NT) raw_barrier(); }, NLB{}, ldb); }
      xs = xs + 1; if (xs >= 3) xs = 0;
    };
    using WX_ = IC<2 * XI>;
    bf16x8 pv[8];
    if constexpr (LDR) {
      for (int j = 1; j < NT - 2; ++j) iter(j, T{}, T{}, WX_{}, F{}, F{});
      iter(NT - 2, T{}, F{}, WX_{}, F{}, F{});
      __builtin_amdgcn_s_setprio(3);
      iter(NT - 1, F{}, F{}, IC<7>{}, F{}, F{});
      wait_vm<0>();
      const unsigned ca = lds0 + CPB + wave * 8192 + fq * 1792 + (fr < 14 ? fr : 0) * 16;
      static_for<0, 8>([&](auto ic) { constexpr int i = decltype(ic)::value; pv[i] = ds_b128<i * 224>(ca); });
    } else {
      int j = 1;
      if (wave == 4) {
        wait_vm<0>();
        int fv = ds_r32(lds0 + TCH + 256);
        const unsigned f0 = snap_get(lds0, 1);
        asm volatile("" : "+v"(fv));
        if (((unsigned)__builtin_amdgcn_readfirstlane(fv) - f0) >> 16 < 32u) wait_count(lds0, a.sync + panel * 64, f0, 32u, 2u);
      }
      raw_barrier();
#pragma unroll 1
      for (; j < js; ++j) iter(j, F{}, F{}, I0{}, F{}, F{});
      const int ja = wm == 2 ? js + 2 : js;
#pragma unroll 1
      for (; j < ja; ++j) iter(j, F{}, F{}, I0{}, T{}, F{});
#pragma unroll 1
      for (; j < js + 2; ++j) iter(j, F{}, F{}, I0{}, F{}, T{});
      { int lx = lane; asm volatile("" : "+v"(lx));
        __builtin_amdgcn_raw_buffer_store_b128(__builtin_bit_cast(u32x4, accs), rC, (lx & 15) < NF ? (unsigned)((tn * 4 + wn * 2) * 3584 + (tm * 2 + (wm & 1)) * 224
                + ((lx & 15) < 6 ? (lx & 1) * 3584 + ((lx & 15) >> 1) * 64 + (lx >> 4) * 16 : (lx >> 5) * 3584 + 192 + (lx & 16))) : 0x80000000u, 0, 16); }
#pragma unroll 1
      for (; j < NT; ++j) iter(j, F{}, F{}, I1{}, F{}, F{});
      done_inc();
    }
    { const RdCtx c = mkctx(false, 0, 0); step(fb, fa, c, F{}, IC<LDR ? 2 : 0>{}, [&](int i) {
        if (i == 1) {
          asm volatile("s_waitcnt lgkmcnt(0)" ::: "memory");
#pragma unroll
          for (int k = 0; k < 8; ++k) asm volatile("" : "+v"(pv[k]));
          f32x4 cv = ((__builtin_bit_cast(f32x4, pv[0]) + __builtin_bit_cast(f32x4, pv[1])) + (__builtin_bit_cast(f32x4, pv[2]) + __builtin_bit_cast(f32x4, pv[3])))
                   + ((__builtin_bit_cast(f32x4, pv[4]) + __builtin_bit_cast(f32x4, pv[5])) + (__builtin_bit_cast(f32x4, pv[6]) + __builtin_bit_cast(f32x4, pv[7])));
#pragma unroll
          for (int k = 0; k < 4; ++k) { const float t = cv[k]; auto sw = __builtin_amdgcn_permlane16_swap(__float_as_uint(t), __float_as_uint(t), false, false); cv[k] = __uint_as_float(sw[0]) + __uint_as_float(sw[1]); }
          if ((lane & 31) < 14) *(f32x4*)(tb_cs + (lane >> 5) * 112 + wn * 56 + fr * 4) = cv;
          done_inc();
        } }); }
    if constexpr (LDR) __builtin_amdgcn_s_setprio(0);
    epilogue(LDRc);
  };
  if (wave < NLW) run(T{}); else run(F{});
  STAMP(7);
  STAMP(8);
  STAMPW(12, 4);
  STAMP(3);
#endif
}

#define KN_CT 7
#define KN_E 6
#define KN_B3 0
#define KN_WB 2
#define KN_GW 1
#define XAUXW 16
#define XAUXD 16
#define KN_WS 2
#define KN_HE 0
#define KN_FL 22
#define KN_LD 0
#define KN_DM 0
#define KN_LX 0
#define KN_TP 5
#define STAMPV(i) STAMP(i)
struct FragW { s16x4 lo[4], hi[4]; };
template <int MODE, int LO, int HI>
__device__ __forceinline__ void wo_phase(const GemmArgs& a, const GemmArgs& nx) {
#if defined(__HIP_DEVICE_COMPILE__)
  extern __shared__ __attribute__((aligned(16))) char smem[];
  if constexpr (MODE != 1) STAMP(0);
  const unsigned lds0 = (unsigned)(size_t)(lds_char_ptr)smem;
  const int tid = threadIdx.x, lane = tid & 63;
  const int wave = __builtin_amdgcn_readfirstlane(tid >> 6);
  const int bid = blockIdx.x, xcd = bid & 7, idx = bid >> 3;
  const int tm = (xcd >> 1) * 4 + (idx >> 3), tn = (xcd & 1) * 8 + (idx & 7);
  const int m0 = tm * 128, panel = m0 >> 8, rot = (xcd & 3) * 4;
  if constexpr (MODE == 1) {
    const auto rW = __builtin_amdgcn_make_buffer_rsrc((void*)a.W0, 0, 0x7fffffff, 0x00020000);
    const int row = wave * 8 + (lane >> 3);
    const unsigned wsrc = (unsigned)(row * D_ + tn * 64 + ((lane & 7) ^ wswz(row)) * 8) * 2u;
    static_for<LO, HI>([&](auto ic) { constexpr int i = decltype(ic)::value;
      __builtin_amdgcn_raw_ptr_buffer_load_lds(rW, (lds_void_ptr)(size_t)(lds0 + wo_pos(i >> 1) + (i & 1) * 8192 + wave * 1024), 16, wsrc, (((i & 1) * 8 + (i >> 1)) ^ rot) * (64 * D_ * 2), 0, 0); });
  } else {
    constexpr int PAD = 57344;
    const int g = wave >> 2, rw = wave & 3;
    const int fr = lane & 15, fq = lane >> 4, wq = (lane >> 2) & 3, wp = lane & 3;
    unsigned wa[4];
#pragma unroll
    for (int c = 0; c < 4; ++c) { const int row = 8 * fq + wq; wa[c] = lds0 + g * 8192 + row * 128 + (((c << 1) ^ wswz(row)) << 4) + wp * 8; }
    FragW f[3];
    auto rd = [&](auto ic, auto rc) {
      constexpr int i = decltype(ic)::value, r = decltype(rc)::value, P = wo_pos(i >> 1) + (i & 1) * 4096, B = P & ~32767, o = P & 32767;
      if constexpr (r & 1) f[i % 3].hi[r >> 1] = ds_tr<o + 512>(wa[r >> 1] + B); else f[i % 3].lo[r >> 1] = ds_tr<o>(wa[r >> 1] + B);
    };
    auto pin = [&](FragW& q) {
#pragma unroll
      for (int n = 0; n < 4; ++n) asm volatile("" : "+v"(q.lo[n]), "+v"(q.hi[n]));
    };
    static_for<0, 16>([&](auto rc) { constexpr int r = decltype(rc)::value; rd(std::integral_constant<int, r / 8>{}, std::integral_constant<int, r % 8>{}); });
    if constexpr (KN_CT & 4) { if (wave == 2) {
      unsigned long long c0, c1; CODE_ADDR(c0, "k_all"); CODE_ADDR(c1, "k_zend");
      wait_vm<0>();
      __builtin_amdgcn_raw_ptr_buffer_load_lds(__builtin_amdgcn_make_buffer_rsrc((void*)c0, 0, c1 - c0 < 262144ull ? (unsigned)(c1 - c0) : 0u, 0x00020000), (lds_void_ptr)(size_t)(lds0 + PAD), 4, (unsigned)((lane * 32 + idx) * 128), 0, 0, 0); } }
    if (wave == 1) wait_count(lds0, a.sync + SYO(tm), snap_get(lds0, 0), 16u, 1u);
    if constexpr (KN_E & 2) {
      if (wave == 3) {
        const unsigned* c2 = a.sync + ((lane & 1) ? (xcd >> 2) * 4 + (idx >> 3) : panel) * 64;
        const unsigned sa = snap_get(lds0, 6), sb = snap_get(lds0, 1), s0 = (lane & 1) ? sb : sa;
        while (__builtin_amdgcn_ballot_w64(((__hip_atomic_load(c2, __ATOMIC_RELAXED, __HIP_MEMORY_SCOPE_AGENT) - s0) & 0xffffu) < 32u) != 0 && in_time(lds0, 1u)) {}
      }
    }
    const auto rX = __builtin_amdgcn_make_buffer_rsrc((void*)a.X, 0, 0x7fffffff, 0x00020000);
    const int rb = (m0 >> 4) + rw * 2 + g;
    unsigned xs[2] = {(unsigned)(rb * RBO * 256 + lane * 16), (unsigned)((rb ^ 1) * RBO * 256 + lane * 16)};
    const int ck = (fq & 1) * 2 + (fq >> 1);
    unsigned orow = (unsigned)((rb * 16 + fr) * (D_ * 2) + tn * 128 + ck * 16), ofrg = (unsigned)((rb * RBH + tn * 8 + ck) * 256 + fr * 16);
    asm volatile("" : "+v"(xs[0]), "+v"(xs[1]), "+v"(orow), "+v"(ofrg));
    STAMP(9);
    if constexpr (!KN_B3) { if (!(KN_CT & 4) || wave != 2) wait_vm<0>(); }
    lds_barrier();
    STAMP(6);
    pin(f[0]); pin(f[1]);
    u32x4 xr[16][2], res[2];
#define KN_XD 4
    auto ldX = [&](int i, int m) {
      if (i < 16) xr[i][m] = __builtin_amdgcn_raw_buffer_load_b128(rX, xs[m] + (i & 1) * 1024, ((g * 8 + (i >> 1)) ^ rot) * 2048, XAUXW);
      else if (i == 16) res[m] = __builtin_amdgcn_raw_buffer_load_b128(__builtin_amdgcn_make_buffer_rsrc((void*)a.R, 0, 0x7fffffff, 0x00020000), orow + m * 64, 0, 0);
    };
#pragma unroll
    for (int i = 0; i < KN_XD; ++i) { ldX(i, 0); ldX(i, 1); __builtin_amdgcn_sched_barrier(0); }
    unsigned gsrc[2];
#pragma unroll
    for (int i = 0; i < 2; ++i) {
      const int j = wave + 8 * i, jj = j >= 7 ? j - 7 : j;
      const int b = jj * 1024 + lane * 16, q = b / 224, pos = (b % 224) >> 4;
      gsrc[i] = (unsigned)((8 * ((q & 15) >> 2) + (q & 3) + 4 * (q >> 4)) * DF_ + ((xcd & 3) * 8 + (idx & 7)) * 112 + pos * 8) * 2u;
      asm volatile("" : "+v"(gsrc[i]));
    }
    f32x4 acc[4][2];
#pragma unroll
    for (int n = 0; n < 4; ++n) { acc[n][0] = (f32x4){0.f, 0.f, 0.f, 0.f}; acc[n][1] = acc[n][0]; }
    __builtin_amdgcn_sched_barrier(0);
    static_for<0, 16>([&](auto ic) {
      constexpr int i = decltype(ic)::value;
      if constexpr (i >= 2) { asm volatile("s_waitcnt lgkmcnt(%0)" :: "n"(i < 15 ? 8 : 0) : "memory"); pin(f[i % 3]); }
      if constexpr (KN_B3 && i == (KN_WB & ~1) - 2) raw_barrier();
      static_for<0, 8>([&](auto jc) {
        constexpr int j = decltype(jc)::value, n = j >> 1, m = j & 1;
        const bf16x8 w = __builtin_shufflevector(f[i % 3].lo[n], f[i % 3].hi[n], 0, 1, 2, 3, 4, 5, 6, 7);
        acc[n][m] = __builtin_amdgcn_mfma_f32_16x16x32_bf16(w, __builtin_bit_cast(bf16x8, xr[i][m]), acc[n][m], 0, 0, 0);
        if constexpr (i + 2 < 16) rd(std::integral_constant<int, i + 2>{}, jc);
        if constexpr (j == 1 || j == 5) ldX(i + KN_XD, j >> 2);
        __builtin_amdgcn_sched_barrier(0);
      });
      if constexpr (i == 0) STAMP(1);
    });
    STAMP(2);
    auto slot = [&](int w) { return (w < 6 ? 32768 : WOB - 6 * 4096) + w * 4096 + lane * 16; };
#pragma unroll
    for (int n = 0; n < 4; ++n) *(f32x4*)(smem + slot(wave) + n * 1024) = acc[n][1];
    asm volatile("" : "+v"(res[0]), "+v"(res[1]));
    lds_barrier();
    STAMP(4);
    f32x4 pa[4];
    static_for<0, 4>([&](auto nc) { constexpr int n = decltype(nc)::value; pa[n] = __builtin_bit_cast(f32x4, ds_b128<n * 1024>(lds0 + slot(wave ^ 4))); });
    if constexpr (KN_GW) {
      auto ldG = [&](int i, int u) { const int j = wave + 8 * i;
        __builtin_amdgcn_raw_ptr_buffer_load_lds(__builtin_amdgcn_make_buffer_rsrc((void*)(j >= 7 ? nx.W1 : nx.W0), 0, 0x7fffffff, 0x00020000),
                          (lds_void_ptr)(size_t)(lds0 + 98304 + (u ^ 2) * 14592 + (j >= 7 ? 7184 + (j - 7) * 1024 : j * 1024)), 16, gsrc[i], (u + (xcd >> 2) * 16) * 32 * DF_ * 2, 0, 0); };
      ldG(0, 0); ldG(0, 1);
      if (wave < 6) { ldG(1, 0); ldG(1, 1); }
    }
    asm volatile("s_waitcnt lgkmcnt(0)" : "+v"(pa[0]), "+v"(pa[1]), "+v"(pa[2]), "+v"(pa[3]) :: "memory");
    const auto rO = __builtin_amdgcn_make_buffer_rsrc((void*)a.O, 0, 0x7fffffff, 0x00020000);
    u32x4 o[2];
#pragma unroll
    for (int i = 0; i < 2; ++i) {
      const f32x4 u = acc[2 * i][0] + pa[2 * i], v = acc[2 * i + 1][0] + pa[2 * i + 1];
      const auto s0 = __builtin_amdgcn_permlane16_swap(pk_bf16(u[0], u[1]), pk_bf16(v[0], v[1]), false, false);
      const auto s1 = __builtin_amdgcn_permlane16_swap(pk_bf16(u[2], u[3]), pk_bf16(v[2], v[3]), false, false);
      const u32x4 q4 = {s0[0], s1[0], s0[1], s1[1]};
#pragma unroll
      for (int j = 0; j < 4; ++j) { const unsigned r = res[i][j], q = q4[j]; o[i][j] = pk_bf16(bflo(r) + bflo(q), bfhi(r) + bfhi(q)); }
      __builtin_amdgcn_raw_buffer_store_b128(o[i], rO, ofrg + i * 1024, 0, 16);
    }
    auto sum4 = [](float v) {
      auto p = __builtin_amdgcn_permlane16_swap(__float_as_uint(v), __float_as_uint(v), false, false); v = __uint_as_float(p[0]) + __uint_as_float(p[1]);
      auto q = __builtin_amdgcn_permlane32_swap(__float_as_uint(v), __float_as_uint(v), false, false); return __uint_as_float(q[0]) + __uint_as_float(q[1]); };
    f32x2 e[8], sm = {0.f, 0.f};
#pragma unroll
    for (int j = 0; j < 8; ++j) { const unsigned q = o[j >> 2][j & 3]; e[j] = (f32x2){bflo(q), bfhi(q)}; sm += e[j]; }
    const float mu = sum4(sm[0] + sm[1]) * (1.f / 64.f);
    f32x2 t2 = {0.f, 0.f};
#pragma unroll
    for (int j = 0; j < 8; ++j) { const f32x2 d = e[j] - mu; t2 += d * d; }
    const float q2 = sum4(t2[0] + t2[1]);
    __builtin_amdgcn_raw_buffer_store_b64((u32x2){__float_as_uint(mu), __float_as_uint(q2)}, __builtin_amdgcn_make_buffer_rsrc((void*)a.st, 0, 0x7fffffff, 0x00020000),
                                          lane < 16 ? (unsigned)((tn * S_ + rb * 16 + lane) * 8) : 0x80000000u, 0, 16);
    STAMP(7);
    STAMP(8);
    asm volatile("s_waitcnt vmcnt(0) lgkmcnt(0)" ::: "memory");
    raw_barrier();
    if (tid == 0) __hip_atomic_fetch_add(a.sync + panel * 64, FIN, __ATOMIC_RELAXED, __HIP_MEMORY_SCOPE_AGENT);
    STAMP(3);
  }
#endif
}

struct FragE { bf16x8 x[4]; s16x4 lo[4], hi[4]; };
template <int A, int NT, int DX, int DW, int PW> struct Sched2 {
  static constexpr int nw(int t) { return (t + DW >= PW && t + DW < NT) ? (t + DW < A ? 4 : 2) : 0; }
  static constexpr int nx(int t) { return t + DX < NT ? 4 : 0; }
  static constexpr int cnt(int t) {
    if (t + 1 >= NT) return 63;
    int tot = 0, pos = 0;
    for (int i = 0; i < DX - 1; ++i) { tot += 4; if (i == t + 1) pos = tot; }
    for (int i = -1; i < t; ++i) {
      tot += nw(i); if (nw(i) && i + DW == t + 1) pos = tot;
      tot += nx(i); if (nx(i) && i + DX == t + 1) pos = tot;
    }
    return tot - pos;
  }
  static constexpr bool same(int t, int r) { return nw(t) == nw(r) && nx(t) == nx(r) && cnt(t) == cnt(r); }
  static constexpr int lo(int r) { int t = r; while (t > 0 && same(t - 1, r)) --t; return t; }
  static constexpr int hi(int r) { int t = r; while (same(t + 1, r)) ++t; return t + 1; }
};
template <int MODE, int A, int DX, int DW, int PW, int HTD, int TA, int TB, int TL = 0>
__device__ __forceinline__ void dn_phase(const GemmArgs& a) {
#if defined(__HIP_DEVICE_COMPILE__)
  constexpr int NT = DF_ / 64 - A, SX = DX + 1, SW = 4, WB = SX * 16384, PAD = WB + SW * 16384;
  static_assert(A % 2 == 0 && NT % 2 == 0 && PW <= SW && PW <= A && TA % 2 == 0 && TB % 2 == 0 && TA >= 2 && TB >= TA && A + TB + 6 <= NT, "schedule");
  using SC = Sched2<A, NT, DX, DW, PW>;
  extern __shared__ __attribute__((aligned(16))) char smem[];
  if constexpr (MODE != 1) STAMP(0);
  const unsigned lds0 = (unsigned)(size_t)(lds_char_ptr)smem;
  const int tid = threadIdx.x;
  int lane = tid & 63; asm volatile("" : "+v"(lane));
  const int wave = __builtin_amdgcn_readfirstlane(tid >> 6);
  const int bid = blockIdx.x, xcd = bid & 7, idx = bid >> 3;
  const int tm = (xcd >> 1) * 4 + (idx >> 3), tn = (xcd & 1) * 8 + (idx & 7);
  const int m0 = tm * 128, panel = m0 >> 8;
  unsigned* pcnt = a.sync + (8 + (xcd >> 2) * 4 + (idx >> 3)) * 64;
  const unsigned orow = (unsigned)((m0 + wave * 16 + (lane >> 3)) * (D_ * 2) + tn * 128 + (lane & 7) * 16);
  const unsigned ofrg = (unsigned)((((m0 >> 4) + wave) * RBH + tn * 8 + (lane & 7)) * 256 + (lane >> 3) * 16);
  u32x4 res[2];
  auto res_loads = [&]() {
    const auto rR = __builtin_amdgcn_make_buffer_rsrc((void*)a.R, 0, 0x7fffffff, 0x00020000);
#pragma unroll
    for (int i = 0; i < 2; ++i) res[i] = __builtin_amdgcn_raw_buffer_load_b128(rR, ofrg, i * 128, 16);
  };
  constexpr int STG = ((NT - 2) % SX) * 16384;
  if (MODE == 1 || wave < 4) {
    const int lw = wave & 3;
    static_assert((KN_E & 4) && A == 24, "walk");
    constexpr int V = 4;
    const int ra = (xcd & 3) * 8, rb = (xcd & 3) * 6;
    auto ktile2 = [&](int t, int e) {
      const int v1 = (ra + t + 4 + 12 * e - (((t - 4) >> 31) & (4 + 8 * e))) & 31, q = rb + t - 8 - (((t - 24) >> 31) & (8 - 8 * e)), v2 = q + 8 + (((q - 24) >> 31) & 24), m = (t - 16) >> 31;
      return (v1 & m) | (v2 & ~m); };
    auto ktile = [&](int t) { return ktile2(t, idx & 1); };
    auto wrow = [&](int kt, int j) { const int p = kt * 8 + j, b = p - 256, v1 = p + 3 * (p >> 2), v2 = b + 4 * ((b * 21846) >> 16) + 4, m = b >> 31; return ((v1 & m) | (v2 & ~m)) * 8; };
    const auto rX = __builtin_amdgcn_make_buffer_rsrc((void*)a.X, 0, 0x7fffffff, 0x00020000);
    const auto rW = __builtin_amdgcn_make_buffer_rsrc((void*)a.W0, 0, 0x7fffffff, 0x00020000);
    const unsigned xsrc = (unsigned)((((m0 >> 4) + (lw >> 1)) * RBA + (lw & 1) * 4) * 256 + lane * 16);
    unsigned wsrc;
    { const int row = lw * 8 + (lane >> 3); wsrc = (unsigned)((lane >> 3) * D_ + tn * 64 + ((lane & 7) ^ wswz(row)) * 8) * 2u; }
    const unsigned wsrp = wsrc ^ 128u;
    auto ldXo = [&](int so, int slot) {
#pragma unroll
      for (int i = 0; i < 4; ++i)
        __builtin_amdgcn_raw_ptr_buffer_load_lds(rX, (lds_void_ptr)(size_t)(lds0 + slot * 16384 + (lw + 4 * i) * 1024), 16, xsrc, so + i * (2 * RBA * 256), 0, XAUXD);
    };
    auto ldX = [&](int t, int slot) { ldXo(ktile(t) * 2048, slot); };
    int o0, o1, ox;
    auto offs = [&](int t) { const int kt = ktile(t + DW); o0 = wrow(kt, lw) * (D_ * 2); o1 = wrow(kt, lw + 4) * (D_ * 2); ox = ktile(t + DX) * 2048; asm volatile("" : "+s"(o0), "+s"(o1), "+s"(ox)); };
    auto ldW = [&](int slot, auto P1c) {
      constexpr bool P1 = decltype(P1c)::value;
#pragma unroll
      for (int i = 0; i < 2; ++i) {
        __builtin_amdgcn_raw_ptr_buffer_load_lds(rW, (lds_void_ptr)(size_t)(lds0 + WB + slot * 16384 + (lw + 4 * i) * 1024), 16, wsrc, i ? o1 : o0, 0, 0);
        if constexpr (P1) __builtin_amdgcn_raw_ptr_buffer_load_lds(rW, (lds_void_ptr)(size_t)(lds0 + WB + slot * 16384 + 8192 + (lw + 4 * i) * 1024), 16, wsrp, i ? o1 : o0, 0, 0);
      }
    };
    const unsigned* fcnt = a.sync + (8 + panel) * 64;
    auto ldF = [&](int o) { __builtin_amdgcn_raw_ptr_buffer_load_lds(__builtin_amdgcn_make_buffer_rsrc((void*)fcnt, 0, 4, 0x00020000), (lds_void_ptr)(size_t)(lds0 + PAD + o), 4, lane * 4, 0, 0, 16); };
    auto rdF = [&](int o) { int fv = ds_r32(lds0 + PAD + o); asm volatile("s_waitcnt lgkmcnt(0)" : "+v"(fv) :: "memory"); return (unsigned)__builtin_amdgcn_readfirstlane(fv); };
    const int kf = ra + (idx & 1) * 4;
    int ht; unsigned htv;
    auto hset = [&]() {
      int idh = idx; asm volatile("" : "+s"(idh));
      const int slc = (3 - ((unsigned)(size_t)a.W0 >> 7)) & 7, pr = (idh & 7) >> 1;
      const bool hlp = pr != (slc >> 1) && lw < 3;
      const int hk = ((idh >> 3) * 3 + pr - (pr > (slc >> 1) ? 1 : 0)) * 2 + (idh & 1) + lw * 24;
      const int htau = hk < 48 ? hk >> 1 : hk - 24, hkt = ktile2(htau < 32 ? htau : 0, hk & 1);
      ht = (hlp && hk < DF_ / 64 && htau >= KN_WS) ? htau - HTD : 1000;
      htv = (unsigned)((wrow(hkt, lane >> 3) + (lane & 7)) * (D_ * 2) + ((xcd & 1) * 8 + slc) * 128);
    };
    auto help = [&]() { __builtin_amdgcn_raw_ptr_buffer_load_lds(rW, (lds_void_ptr)(size_t)(lds0 + FPAD - 256), 4, htv, 0, 0, 0); };
    auto ldW0 = [&](int i) {
      const int so = (kf * 112 + lw * 8) * (D_ * 2);
      __builtin_amdgcn_raw_ptr_buffer_load_lds(rW, (lds_void_ptr)(size_t)(lds0 + WB + (i >> 1) * 16384 + (lw + 4 * (i & 1)) * 1024), 16, wsrc, so + i * (56 * D_ * 2), 0, 0);
      __builtin_amdgcn_raw_ptr_buffer_load_lds(rW, (lds_void_ptr)(size_t)(lds0 + WB + (i >> 1) * 16384 + 8192 + (lw + 4 * (i & 1)) * 1024), 16, wsrp, so + i * (56 * D_ * 2), 0, 0);
    };
    if constexpr (MODE == 1) {
      static_assert(PW == 4, "first tiles");
      if constexpr (TL == 1) ldF(FPAD - PAD);
      if constexpr (TL == 2) { if (lw == 0) { hset(); if (ht < -1) help(); } }
      if constexpr (TL == 0 || TL == 3) {
#pragma unroll
        for (int i = 0; i < 4; ++i) ldW0(i + (TL ? 4 : 0));
      }
      return;
    }
    int f0v = ds_r32(lds0 + SNAP + 16);
    wait_vm<3>();
    lds_inc(lds0 + ACNT);
    STAMPW(4, 0);
    const unsigned f1 = rdF(FPAD - PAD);
    asm volatile("" : "+v"(f0v));
    const unsigned f0 = __builtin_amdgcn_readfirstlane(f0v);
    const bool early = ((f1 - f0) & 0xffffu) >= 64u;
    lds_wait8(lds0 + ACNT);
    STAMPW(9, 0);
#define KN_X0 0
    auto ldX0 = [&](int i) { __builtin_amdgcn_raw_ptr_buffer_load_lds(rX, (lds_void_ptr)(size_t)(lds0 + (i >> 2) * 16384 + (lw + 4 * (i & 3)) * 1024), 16, xsrc, (kf + (i >> 2)) * 2048 + (i & 3) * (2 * RBA * 256), 0, XAUXD); };
#pragma unroll
    for (int i = 0; i < 4 * KN_X0; ++i) ldX0(i);
    asm volatile("" ::: "memory");
    if (tid == 192) __hip_atomic_fetch_add(pcnt, 1u << 16, __ATOMIC_RELAXED, __HIP_MEMORY_SCOPE_AGENT);
    asm volatile("" : "+v"(lane) :: "memory");
    hset();
    if constexpr (KN_X0 < 3) {
      asm volatile("" :: "s"(ht), "v"(htv));
      lds_wait8(lds0 + ACNT + 4, 4);
      STAMPW(5, 0);
#pragma unroll
      for (int i = 4 * KN_X0; i < 12; ++i) ldX0(i);
      asm volatile("" ::: "memory");
    }
#pragma unroll
    for (int i = 2 * KN_WS; i < 8; ++i) ldW0(i);
    asm volatile("" ::: "memory");
    static_assert(HTD <= 11, "only the first loader wave helps that early");
    if (!KN_HE && ht < -1) help();
    {
      static_assert(DX == 4 && DW == 4 && PW == 4 && KN_WS == 2 && SC::cnt(3) == 16 && SC::cnt(4) == 16 && V == DX, "schedule");
      int x4 = 0;
#pragma unroll 1
      for (int t = -1; t < V - 1; ++t) {
        static_assert(KN_WS == 4 || KN_WS == 2, "W tiles");
        offs(t);
        if (t == 1) wait_vm<8>(); else wait_vm<16>();
        if (!KN_FL && t == -1 && lw == 0) ldF(256);
        if (__builtin_expect(marked<4>(lds0 + (t + 1) * 16384 + lw * 1024 + lane * 16) || !early, 0)) {
          for (int tries = early ? 0 : KN_TRY;; ++tries) {
            if (tries >= KN_TRY) wait_field(lds0, fcnt, f0, 16, 4u);
#pragma unroll 1
            for (int u = t + 1; u < V && u < t + DX; ++u) ldX(u, u);
            if (KN_WS == 4 && t < 1) wait_vm<8>(); else wait_vm<0>();
            if (tries >= KN_TRY || !marked<4>(lds0 + (t + 1) * 16384 + lw * 1024 + lane * 16)) break;
          }
        }
        if (t == -1) STAMPW(6, 0);
        if (t == 1 && lw == 0) {
          if ((((rdF(256) - f0) >> 16) & 255u) < 32u) wait_field(lds0, fcnt, f0, 16, 4u);
        }
        raw_barrier();
        if (KN_FL && t == -1 && lw == 0) { ldF(256); asm volatile("" ::: "memory"); }
        if (t == 1) ldXo(x4, 4);
        x4 = ox;
        if (t >= 0) ldW((t + DW) & 3, std::true_type{});
        if (t != 0) ldXo(ox, t >= 1 ? t - 1 : t + DX);
        if (t == ht) help();
      }
    }
    auto fixed = [&](auto tc) {
      constexpr int t = decltype(tc)::value;
#define KN_TR 4
      if constexpr (SC::nw(t) > 0) offs(t);
      wait_vm<(t > NT - KN_TR && t < NT - 1) ? SC::cnt(t) + 2 : SC::cnt(t)>();
      raw_barrier();
      static_assert(SC::nx(t) == 0 || SC::nw(t) > 0, "offsets");
      if constexpr (SC::nw(t) > 0) ldW((t + DW) % SW, std::integral_constant<bool, (t + DW < A)>{});
      if constexpr (SC::nx(t) > 0) ldXo(ox, (t + DX) % SX);
      if constexpr (t + HTD < NT) { if (t == ht) help(); }
      if constexpr (t == NT - KN_TR) { static_assert(SC::nw(t + 1) == 0 && SC::nx(t + 1) == 0, "last loads"); asm volatile("" ::: "memory"); res_loads(); }
    };
    const int tf = lw == 0 ? 13 - DX : 1000;
    auto steady = [&](auto Lc, auto Hc) {
      constexpr int L = decltype(Lc)::value, H = decltype(Hc)::value;
      int sx = (L + DX) % SX * 16384 + lw * 1024 + lds0, sw = (L + DW) % SW * 16384;
      constexpr bool P1 = L + DW < A;
      static_assert(DX == DW && SW == 4, "offsets");
      offs(L);
      auto lw2 = [&](int i, int o) {
        __builtin_amdgcn_raw_ptr_buffer_load_lds(rW, (lds_void_ptr)(size_t)(lds0 + WB + sw + (lw + 4 * i) * 1024), 16, wsrc, o, 0, 0);
        if constexpr (P1) __builtin_amdgcn_raw_ptr_buffer_load_lds(rW, (lds_void_ptr)(size_t)(lds0 + WB + sw + 8192 + (lw + 4 * i) * 1024), 16, wsrp, o, 0, 0);
        __builtin_amdgcn_sched_barrier(0); };
      auto lx1 = [&](int i) { __builtin_amdgcn_raw_ptr_buffer_load_lds(rX, (lds_void_ptr)(size_t)(sx + i * 4096), 16, xsrc, ox + i * (2 * RBA * 256), 0, XAUXD); __builtin_amdgcn_sched_barrier(0); };
#pragma unroll 1
      for (int t = L; t < H; ++t) {
        wait_vm<SC::cnt(L)>();
        if constexpr (L <= 8) { static_assert(H > 16 - DX, "check"); if (t == tf + 3) {
          if ((rdF(256) - f0) >> 24 < 32u) wait_field(lds0, fcnt, f0, 24, 5u);
        } }
        raw_barrier();
#define KN_LG 0
        if (KN_LD && P1) __builtin_amdgcn_s_sleep(KN_LD);
        __builtin_amdgcn_sched_barrier(0);
        lw2(0, o0); if (KN_LG && P1) __builtin_amdgcn_s_sleep(KN_LG); __builtin_amdgcn_sched_barrier(0); lw2(1, o1);
        int nx = ktile(t + 1 + DX) * 2048; sw = (sw + 16384) & 65535; asm volatile("" : "+s"(nx), "+s"(sw)); __builtin_amdgcn_sched_barrier(0);
        lx1(0);
        o0 = wrow(nx >> 11, lw) * (D_ * 2); asm volatile("" : "+s"(o0)); __builtin_amdgcn_sched_barrier(0);
        lx1(1);
        o1 = wrow(nx >> 11, lw + 4) * (D_ * 2); asm volatile("" : "+s"(o1)); __builtin_amdgcn_sched_barrier(0);
        lx1(2);
        int ns = sx + 16384; ns = ns >= (int)lds0 + SX * 16384 ? ns - SX * 16384 : ns; asm volatile("" : "+s"(ns)); __builtin_amdgcn_sched_barrier(0);
        lx1(3);
        ox = nx; sx = ns;
        if (t == ht) help();
        if constexpr (L <= 8) { if (t == tf) ldF(256); }
      }
    };
    constexpr int L1 = V ? V - 1 : SC::lo(A / 2), H1 = SC::hi(A / 2), R2 = NT - (DX > DW ? DX : DW) - 1, L2 = SC::lo(R2), H2 = SC::hi(R2);
    static_assert(SC::nw(L1) == 4 && SC::nx(L1) == 4 && SC::nw(L2) == 2 && SC::nx(L2) == 4 && H1 <= L2, "steady states");
    if constexpr (!V) static_for<0, L1 + 1>([&](auto ic) { fixed(std::integral_constant<int, decltype(ic)::value - 1>{}); });
    steady(std::integral_constant<int, L1>{}, std::integral_constant<int, H1>{});
    static_for<H1, L2>(fixed);
    steady(std::integral_constant<int, L2>{}, std::integral_constant<int, H2>{});
    static_for<H2, NT>(fixed);
    asm volatile("" : "+v"(res[0]), "+v"(res[1]));
  } else {
    if constexpr (MODE == 1) return;
    const int fr = lane & 15, fq = lane >> 4, wq = (lane >> 2) & 3, wp = lane & 3, cw = wave & 3, wm = cw >> 1, wn = cw & 1;
    const unsigned f0 = snap_get(lds0, 5);
#define KN_DP 0
    if (KN_DP) __builtin_amdgcn_s_setprio(KN_DP);
    f32x4 acc[4][4];
#pragma unroll
    for (int n = 0; n < 4; ++n)
#pragma unroll
      for (int m = 0; m < 4; ++m) acc[n][m] = (f32x4){0.f, 0.f, 0.f, 0.f};
    const unsigned xa = lds0 + wm * 8192 + lane * 16;
    unsigned wa[2];
#pragma unroll
    for (int c = 0; c < 2; ++c) { const int row = 8 * fq + wq; wa[c] = lds0 + WB + row * 128 + ((((wn * 2 + c) << 1) ^ wswz(row)) << 4) + wp * 8; }
    struct Ctx { unsigned x, w[2]; };
    auto mk = [&](unsigned xo, unsigned wo) { Ctx c; c.x = xa + xo; c.w[0] = wa[0] + wo; c.w[1] = wa[1] + wo; return c; };
    auto rd = [&](FragE& f, const Ctx& c, auto KSc, auto kc) {
      constexpr int KS = decltype(KSc)::value, k = decltype(kc)::value;
      if constexpr (k < 4) f.x[k] = ds_b128<KS * 1024 + k * 2048>(c.x);
      else { constexpr int n = k - 4, o = KS * 4096 + (n >> 1) * 8192; f.lo[n] = ds_tr<o>(c.w[n & 1]); f.hi[n] = ds_tr<o + 512>(c.w[n & 1]); }
    };
    auto pin = [&](FragE& f, auto Nc) {
#pragma unroll
      for (int m = 0; m < 4; ++m) asm volatile("" : "+v"(f.x[m]));
#pragma unroll
      for (int n = 0; n < decltype(Nc)::value; ++n) asm volatile("" : "+v"(f.lo[n]), "+v"(f.hi[n]));
    };
    using I0 = std::integral_constant<int, 0>; using I1 = std::integral_constant<int, 1>; using I2 = std::integral_constant<int, 2>; using I4 = std::integral_constant<int, 4>;
    FragE fa, fb, fc, fd;
    raw_barrier();
    { const Ctx c = mk(0, 0); static_for<0, 8>([&](auto kc) { rd(fa, c, I0{}, kc); }); asm volatile("s_waitcnt lgkmcnt(4)" ::: "memory"); static_for<0, 8>([&](auto kc) { rd(fb, c, I1{}, kc); }); }
    asm volatile("s_waitcnt lgkmcnt(0)" ::: "memory");
    pin(fa, I4{}); pin(fb, I4{});
    STAMP(1);
    static_assert((SW & (SW - 1)) == 0, "W ring");
    int sx = 16384, sw = 16384;
    Ctx cn = mk(sx, sw);
#define KN_SP4 26
#define KN_IL 1
#define KN_IB 1
    auto rd1 = [&](FragE& f, const Ctx& c, auto KSc, auto rc) {
      constexpr int KS = decltype(KSc)::value, r = decltype(rc)::value;
      if constexpr (r < 4) f.x[r] = ds_b128<KS * 1024 + r * 2048>(c.x);
      else { constexpr int n = (r - 4) >> 1, o = KS * 4096 + (n >> 1) * 8192; if constexpr (r & 1) f.hi[n] = ds_tr<o + 512>(c.w[n & 1]); else f.lo[n] = ds_tr<o>(c.w[n & 1]); }
    };
    auto tile = [&](FragE& c0, FragE& c1, FragE& n0, FragE& n1, auto NCc, auto NNc, auto&& post) {
      constexpr int NC = decltype(NCc)::value, NN = decltype(NNc)::value, RI = 4 + NN, RG = NC == 4 ? 3 : 2;
      constexpr int R1 = 4 + 2 * NN, NM = 8 * NC;
      const Ctx c = cn;
      static_for<0, NM>([&](auto ic) {
        constexpr int I = decltype(ic)::value, G = I / 4, m = I % 4, ks = G / NC, n = G % NC;
        FragE& cur = ks ? c1 : c0;
        constexpr int SP = NC == 4 ? (NN == 4 ? KN_SP4 : 20) : 12;
        constexpr int before = I <= KN_IB ? 0 : ((I - KN_IB) * 2 * R1 / SP < 2 * R1 ? (I - KN_IB) * 2 * R1 / SP : 2 * R1);
        constexpr int after = I + 1 <= KN_IB ? 0 : ((I + 1 - KN_IB) * 2 * R1 / SP < 2 * R1 ? (I + 1 - KN_IB) * 2 * R1 / SP : 2 * R1);
        if constexpr (I == NM / 2) { asm volatile("s_waitcnt lgkmcnt(%0)" :: "n"(before < 15 ? before : 15) : "memory"); pin(c1, NCc); }
        const bf16x8 w = __builtin_shufflevector(cur.lo[n], cur.hi[n], 0, 1, 2, 3, 4, 5, 6, 7);
        acc[n][m] = __builtin_amdgcn_mfma_f32_16x16x32_bf16(w, cur.x[m], acc[n][m], 0, 0, 0);
        __builtin_amdgcn_sched_barrier(0);
        if constexpr (I + 1 == KN_IB) { asm volatile("s_waitcnt lgkmcnt(0)" ::: "memory"); raw_barrier(); post(); }
        static_for<before, after>([&](auto rc) { constexpr int r = decltype(rc)::value;
          if constexpr (r < R1) rd1(n0, c, I0{}, rc); else rd1(n1, c, I1{}, std::integral_constant<int, r - R1>{}); });
        if constexpr (I == NM - 4) { sw = (sw + 16384) & (SW * 16384 - 1); asm volatile("" : "+s"(sw)); }
        if constexpr (I == NM - 3) { cn.w[0] = wa[0] + sw; cn.w[1] = wa[1] + sw; asm volatile("" : "+v"(cn.w[0]), "+v"(cn.w[1])); }
        if constexpr (I == NM - 5) { sx = sx + 16384; asm volatile("" : "+s"(sx)); }
        if constexpr (I == NM - 2) { sx = sx == SX * 16384 ? 0 : sx; asm volatile("" : "+s"(sx)); }
        if constexpr (I == NM - 1) { cn.x = xa + sx; asm volatile("" : "+v"(cn.x)); }
        __builtin_amdgcn_sched_barrier(0);
      });
      asm volatile("s_waitcnt lgkmcnt(%0)" :: "n"(R1) : "memory");
      pin(n0, NNc);
    };
    auto nop = [&]() {};
    for (int t = 0; t < A - 2; t += 2) {
      tile(fa, fb, fc, fd, I4{}, I4{}, [&]() { if (t == 2) { if (tid == 256) __hip_atomic_fetch_add(pcnt, 1u << 24, __ATOMIC_RELAXED, __HIP_MEMORY_SCOPE_AGENT); } });
      tile(fc, fd, fa, fb, I4{}, I4{}, nop);
      if (t == 0) wait_vm<0>();
    }
    tile(fa, fb, fc, fd, I4{}, I4{}, nop); tile(fc, fd, fa, fb, I4{}, I2{}, nop);
    const auto rS = __builtin_amdgcn_make_buffer_rsrc((void*)a.slab, 0, 0x7fffffff, 0x00020000);
    STAMP(10);
#pragma unroll
    for (int m = 0; m < 4; ++m)
      __builtin_amdgcn_raw_buffer_store_b128((u32x4){pk_bf16(acc[2][m][0], acc[2][m][1]), pk_bf16(acc[2][m][2], acc[2][m][3]), pk_bf16(acc[3][m][0], acc[3][m][1]), pk_bf16(acc[3][m][2], acc[3][m][3])},
                                             rS, (((tm * 16 + (tn ^ 1)) * 4 + cw) * 4 + m) * 1024 + lane * 16, 0, (KN_LX & 1) ? 0 : 16);
    auto pairs = [&](int n) {
#pragma unroll 1
      for (int t = 0; t < n; t += 2) { tile(fa, fb, fc, fd, I2{}, I2{}, nop); tile(fc, fd, fa, fb, I2{}, I2{}, nop); } };
    const unsigned* flag = a.sync + 2048 + bid * 32;
    u32x4 pp[4];
    auto ldP = [&]() {
#pragma unroll
      for (int i = 0; i < 4; ++i) pp[i] = __builtin_amdgcn_raw_buffer_load_b128(rS, (((tm * 16 + tn) * 4 + cw) * 4 + i) * 1024 + lane * 16, 0, 16);
    };
    if constexpr (KN_DM) {
      static_assert(KN_E & 4, "snapshot");
      pairs(KN_TP & ~1);
      STAMPV(4);
      static_for<(KN_TP & ~1), NT - A>([&](auto tc) {
        constexpr int t = decltype(tc)::value;
        auto post = [&]() { if constexpr (t == KN_TP) { ldP(); res_loads(); } };
        if constexpr (t & 1) tile(fc, fd, fa, fb, I2{}, I2{}, post); else tile(fa, fb, fc, fd, I2{}, I2{}, post);
      });
    } else {
    pairs(TA);
    STAMPV(11);
    wait_vm<0>();
    STAMPV(12);
    tile(fa, fb, fc, fd, I2{}, I2{}, [&]() { if (tid == 256) __hip_atomic_fetch_add(a.sync + 2048 + (bid ^ 8) * 32, 1u, __ATOMIC_RELAXED, (KN_LX & 2) ? __HIP_MEMORY_SCOPE_WORKGROUP : __HIP_MEMORY_SCOPE_AGENT); });
    tile(fc, fd, fa, fb, I2{}, I2{}, nop);
    if constexpr (TB > TA) pairs(TB - TA);
    unsigned fv = 1u;
    if (wave == 4) fv = __hip_atomic_load(flag, __ATOMIC_RELAXED, __HIP_MEMORY_SCOPE_AGENT);
    tile(fa, fb, fc, fd, I2{}, I2{}, nop);
    tile(fc, fd, fa, fb, I2{}, I2{}, nop);
    if (wave == 4) {
      while (__builtin_amdgcn_readfirstlane(fv) == f0 && in_time(lds0, 6u)) fv = __hip_atomic_load(flag, __ATOMIC_RELAXED, __HIP_MEMORY_SCOPE_AGENT);
    }
    STAMPV(4);
    tile(fa, fb, fc, fd, I2{}, I2{}, [&]() {
#pragma unroll
      for (int i = 0; i < 4; ++i) pp[i] = __builtin_amdgcn_raw_buffer_load_b128(rS, (((tm * 16 + tn) * 4 + cw) * 4 + i) * 1024 + lane * 16, 0, 16);
      res_loads();
    });
    tile(fc, fd, fa, fb, I2{}, I2{}, nop);
    if constexpr (NT - A - TB - 6 > 0) pairs(NT - A - TB - 6);
    }
    asm volatile("s_waitcnt lgkmcnt(0)" ::: "memory");
    pin(fa, I2{}); pin(fb, I2{});
    STAMP(2);
    if constexpr (KN_DM) {
      auto anym = [&]() {
        unsigned m = 0;
#pragma unroll
        for (int i = 0; i < 4; ++i) { m = max(max(m, pp[i][0]), pp[i][1]); m = max(max(m, pp[i][2]), pp[i][3]); }
        return __builtin_amdgcn_ballot_w64(m == 0xffffffffu) != 0;
      };
      asm volatile("s_waitcnt vmcnt(0)" : "+v"(res[0]), "+v"(res[1]) :: "memory");
      if (lane == 0) __hip_atomic_fetch_add(a.sync + 2048 + (bid ^ 8) * 32, FIN, __ATOMIC_RELAXED, __HIP_MEMORY_SCOPE_AGENT);
      if (__builtin_expect(anym(), 0)) {
        for (int tries = 0;; ++tries) {
          if (tries >= KN_TRY) wait_count(lds0, flag, f0, 4u, 6u);
          ldP();
          asm volatile("" : "+v"(pp[0]), "+v"(pp[1]), "+v"(pp[2]), "+v"(pp[3]));
          if (tries >= KN_TRY || !anym()) break;
        }
      }
      STAMPV(12);
    }
#pragma unroll
    for (int m = 0; m < 4; ++m)
#pragma unroll
      for (int n = 0; n < 2; ++n) {
        const unsigned q0 = pp[m][n * 2], q1 = pp[m][n * 2 + 1];
        const f32x4 v = acc[n][m] + (f32x4){bflo(q0), bfhi(q0), bflo(q1), bfhi(q1)};
        *(u32x2*)(smem + STG + (wm * 64 + m * 16 + fr) * 128 + ((((wn * 2 + n) * 2 + (fq >> 1)) ^ (fr & 7)) << 4) + (fq & 1) * 8) = (u32x2){pk_bf16(v[0], v[1]), pk_bf16(v[2], v[3])};
      }
    STAMPV(5);
    asm volatile("" : "+v"(res[0]), "+v"(res[1]));
  }
  lds_barrier();
  {
    const auto rO = __builtin_amdgcn_make_buffer_rsrc((void*)a.O, 0, 0x7fffffff, 0x00020000);
    u32x4 o[2];
    int lz = lane; asm volatile("" : "+v"(lz));
#pragma unroll
    for (int i = 0; i < 2; ++i) {
      const u32x4 v = *(const u32x4*)(smem + STG + (wave * 16 + i * 8 + (lz >> 3)) * 128 + (((lz & 7) ^ (lz >> 3)) << 4));
#pragma unroll
      for (int j = 0; j < 4; ++j) { const unsigned r = res[i][j], q = v[j]; o[i][j] = pk_bf16(bflo(r) + bflo(q), bfhi(r) + bfhi(q)); }
    }
    asm volatile("" : "+v"(o[0]), "+v"(o[1]));
#pragma unroll
    for (int i = 0; i < 2; ++i) __builtin_amdgcn_raw_buffer_store_b128(o[i], rO, orow, i * 8 * D_ * 2, KN_OAUX);
  }
  STAMP(7);
  STAMP(8);
  STAMP(3);
#endif
}

struct Ptrs { const bf16_t *x, *wqkv, *wo, *wg, *wu, *wd; bf16_t* out; char* ws; };
#define DBG(i)
#define WS_O ((bf16_t*)(p.ws + (8u << 20)))
#define WS_SL ((float*)WS_O)
#define WS_H ((bf16_t*)(p.ws + (16u << 20)))
#define WS_ST ((float*)(p.ws + (24u << 20)))
#define WS_CS ((float*)(p.ws + (25u << 20)))
#define WS_SY ((unsigned*)(p.ws + (26u << 20)))
#define WS_A ((bf16_t*)(p.ws + (28u << 20)))
#if defined(__HIP_DEVICE_COMPILE__)
#define ARG_WARM(v) do { asm volatile("s_load_dword %0, %1, 0x0" : "=s"(v) : "s"(__builtin_amdgcn_kernarg_segment_ptr())); __builtin_amdgcn_sched_barrier(0); } while (0)
#else
#define ARG_WARM(v)
#endif
struct Args4 { GemmArgs q, b, c, d; };
extern "C" __global__ __launch_bounds__(512, 1) void k_all(Ptrs p) {
  unsigned warm = 0;
  ARG_WARM(warm);
  const unsigned pad0 = (unsigned)__builtin_amdgcn_s_memrealtime();
#define PAD(k) do { if (PADAT == k) { while ((unsigned)__builtin_amdgcn_s_memrealtime() - pad0 < PADUS * 100u) __builtin_amdgcn_s_sleep(2); } } while (0)
  const Args4 a{GemmArgs{p.x, p.wqkv, nullptr, nullptr, WS_O, (float*)WS_H, p.wg, p.wu, WS_CS, p.wd, p.wo, WS_SY, (float*)WS_A, warm DBG(0)},
    GemmArgs{WS_O, p.wo, nullptr, p.x, WS_H, WS_ST, nullptr, nullptr, nullptr, p.wd, nullptr, WS_SY, nullptr, 0u DBG(1)},
    GemmArgs{WS_H, p.wg, p.wu, p.out, WS_A, WS_ST, nullptr, nullptr, WS_CS, p.wd, nullptr, WS_SY, WS_SL, 0u DBG(2)},
    GemmArgs{WS_A, p.wd, nullptr, WS_H, p.out, nullptr, nullptr, nullptr, nullptr, nullptr, nullptr, WS_SY, WS_SL, 0u DBG(3)}};
  k1_phase(a.q, [&]() { wo_phase<1, 2, 16>(a.b, a.c); });
  PAD(1);
  wo_phase<2, 0, 0>(a.b, a.c);
  PAD(2);
  glu_phase(a.c, [&](auto tc) { dn_phase<1, 24, 4, 4, 4, 10, 2, 2, decltype(tc)::value>(a.d); });
  PAD(3);
  dn_phase<2, 24, 4, 4, 4, 10, 2, 2>(a.d);
  PAD(4);
}

extern "C" __global__ void k_zend() {}

extern "C" void kernel_launch(void* const* d_in, const int* in_sizes, int n_in, void* d_out, int out_size, void* d_ws,
                              size_t ws_size, hipStream_t stream) {
  const Ptrs p{(const bf16_t*)d_in[0], (const bf16_t*)d_in[1], (const bf16_t*)d_in[2], (const bf16_t*)d_in[4], (const bf16_t*)d_in[5], (const bf16_t*)d_in[6], (bf16_t*)d_out, (char*)d_ws};
  constexpr int L_all = SNAP + 64;
  static_assert(SNAP >= 3 * 256 * 128 + 4 * 14592 + 4096 + 1024 + 256 + 512 + 128 + 64 && L_all <= 163840, "LDS");
  (void)hipFuncSetAttribute((const void*)k_all, hipFuncAttributeMaxDynamicSharedMemorySize, L_all);
  k_all<<<256, 512, L_all, stream>>>(p);
}
```

```cpp
#include <hip/hip_runtime.h>
#include <hip/hip_bf16.h>
#include <stdint.h>
#include <type_traits>

typedef unsigned short bf16_t;
typedef short bf16x8 __attribute__((ext_vector_type(8)));
typedef short s16x4 __attribute__((ext_vector_type(4)));
typedef float f32x4 __attribute__((ext_vector_type(4)));
typedef float f32x2 __attribute__((ext_vector_type(2)));
typedef unsigned u32x2 __attribute__((ext_vector_type(2)));
typedef unsigned u32x4 __attribute__((ext_vector_type(4)));
typedef __bf16 bf16x2_t __attribute__((ext_vector_type(2)));
typedef __attribute__((address_space(3))) void* lds_void_ptr;
typedef __attribute__((address_space(3))) char* lds_char_ptr;

constexpr int S_ = 2048, D_ = 1024, DF_ = 3584;
constexpr int RBO = D_ / 8, RBH = D_ / 8, RBA = DF_ / 8;

__device__ __forceinline__ float bflo(unsigned v) { return __uint_as_float(v << 16); }
__device__ __forceinline__ float bfhi(unsigned v) { return __uint_as_float(v & 0xffff0000u); }
__device__ __forceinline__ unsigned pk_bf16(float lo, float hi) {
  f32x2 v = {lo, hi};
  bf16x2_t b = __builtin_convertvector(v, bf16x2_t);
  return __builtin_bit_cast(unsigned, b);
}
__device__ __forceinline__ int wswz(int row) { return (((row >> 1) & 1) | (((row >> 3) & 1) << 1)) << 1; }

template <int OFF> __device__ __forceinline__ s16x4 ds_tr(unsigned addr) {
  s16x4 r; asm volatile("ds_read_b64_tr_b16 %0, %1 offset:%2" : "=v"(r) : "v"(addr), "n"(OFF)); return r;
}
template <int OFF> __device__ __forceinline__ bf16x8 ds_b128(unsigned addr) {
  bf16x8 r; asm volatile("ds_read_b128 %0, %1 offset:%2" : "=v"(r) : "v"(addr), "n"(OFF)); return r;
}
template <int OFF> __device__ __forceinline__ f32x2 ds_b64(unsigned addr) {
  f32x2 r; asm volatile("ds_read_b64 %0, %1 offset:%2" : "=v"(r) : "v"(addr), "n"(OFF)); return r;
}
__device__ __forceinline__ float sum8(float v) {
  v += __int_as_float(__builtin_amdgcn_update_dpp(0, __float_as_int(v), 0x128, 0xf, 0xf, false));
  auto a = __builtin_amdgcn_permlane16_swap(__float_as_uint(v), __float_as_uint(v), false, false);
  v = __uint_as_float(a[0]) + __uint_as_float(a[1]);
  auto b = __builtin_amdgcn_permlane32_swap(__float_as_uint(v), __float_as_uint(v), false, false);
  return __uint_as_float(b[0]) + __uint_as_float(b[1]);
}
__device__ __forceinline__ float sum8l(float v) {
  v += __int_as_float(__builtin_amdgcn_update_dpp(0, __float_as_int(v), 0xb1, 0xf, 0xf, false));
  v += __int_as_float(__builtin_amdgcn_update_dpp(0, __float_as_int(v), 0x4e, 0xf, 0xf, false));
  return v + __int_as_float(__builtin_amdgcn_update_dpp(0, __float_as_int(v), 0x141, 0xf, 0xf, false));
}
template <int OFF> __device__ __forceinline__ void ds_w128(unsigned addr, u32x4 v) { asm volatile("ds_write_b128 %0, %1 offset:%2" :: "v"(addr), "v"(v), "n"(OFF) : "memory"); }
__device__ __forceinline__ int ds_r32(unsigned addr) { int r; asm volatile("ds_read_b32 %0, %1" : "=v"(r) : "v"(addr)); return r; }
__device__ __forceinline__ void ds_w32(unsigned addr, int v) { asm volatile("ds_write_b32 %0, %1" :: "v"(addr), "v"(v) : "memory"); }
template <int N> __device__ __forceinline__ void wait_vm() { asm volatile("s_waitcnt vmcnt(%0)" ::"n"(N) : "memory"); }
__device__ __forceinline__ void raw_barrier() { asm volatile("" ::: "memory"); __builtin_amdgcn_s_barrier(); asm volatile("" ::: "memory"); }
__device__ __forceinline__ void lds_barrier() { asm volatile("s_waitcnt lgkmcnt(0)" ::: "memory"); __builtin_amdgcn_s_barrier(); asm volatile("" ::: "memory"); }

template <int I, int N, class F> __device__ __forceinline__ void static_for(F&& f) {
  if constexpr (I < N) { f(std::integral_constant<int, I>{}); static_for<I + 1, N>(f); }
}
template <int V> using IC = std::integral_constant<int, V>;
#define RSRC(p) __builtin_amdgcn_make_buffer_rsrc((void*)(p), 0, 0x7fffffff, 0x00020000)
#define RSRCN(p, n) __builtin_amdgcn_make_buffer_rsrc((void*)(p), 0, (n), 0x00020000)
#define LDSP(a) ((lds_void_ptr)(size_t)(a))
#define MFMA(a, b, c) __builtin_amdgcn_mfma_f32_16x16x32_bf16(a, b, c, 0, 0, 0)
#define CODE_ADDR(v, sym) asm volatile("s_getpc_b64 s[40:41]\n\ts_add_u32 s40, s40, " sym "@rel32@lo+4\n\ts_addc_u32 s41, s41, " sym "@rel32@hi+12\n\ts_mov_b64 %0, s[40:41]" : "=s"(v) :: "s40", "s41", "scc")
#define MERGED 1
constexpr int SNAP = 162752;
constexpr int STAB1 = 786432;
constexpr int EPOCH = 0;
constexpr int F_MARK = 4096, F_H = 4096 + 2048, F_ROW = 8192, F_COL = 16384;
#define F_O(t) (32768 + ((t) & 7) * 1024 + (((((t) >> 3) ^ 1) & 1) << 13))
#define KN_WE 2
#define KN_SPF 0
constexpr int F_ACT = 65536;
constexpr int F_PART = 131072;
constexpr int KN_TRY = 6;
struct Tag { unsigned lo, hi; };
__device__ __forceinline__ Tag tag_get(unsigned lds0) {
  f32x2 v = ds_b64<0>(lds0 + SNAP);
  asm volatile("s_waitcnt lgkmcnt(0)" : "+v"(v) :: "memory");
  return Tag{(unsigned)__builtin_amdgcn_readfirstlane(__float_as_int(v[0])) + 8u, (unsigned)__builtin_amdgcn_readfirstlane(__float_as_int(v[1]))};
}
__device__ __forceinline__ bool in_time(unsigned lds0) {
  int v = ds_r32(lds0 + SNAP + 28);
  asm volatile("s_waitcnt lgkmcnt(0)" : "+v"(v) :: "memory");
  return (unsigned)__builtin_amdgcn_s_memrealtime() - (unsigned)__builtin_amdgcn_readfirstlane(v) < 20000000u;
}
__device__ __forceinline__ int lane_id() { return __builtin_amdgcn_mbcnt_hi(~0u, __builtin_amdgcn_mbcnt_lo(~0u, 0u)); }
#if defined(__HIP_DEVICE_COMPILE__)
__device__ __forceinline__ void flag_set(const unsigned* sync, int off, Tag t, unsigned state) {
  __builtin_amdgcn_raw_buffer_store_b64((u32x2){t.lo + state, t.hi}, RSRC(sync), lane_id() == 0 ? 0u : 0x80000000u, off, 16);
}
__device__ __forceinline__ u32x2 flag_get(const unsigned* sync, int off, int n) { return __builtin_amdgcn_raw_buffer_load_b64(RSRC(sync), lane_id() < n ? lane_id() * 8u : 0x80000000u, off, 16); }
__device__ __forceinline__ bool flags_up(u32x2 v, int n, Tag t, unsigned state) { return __builtin_amdgcn_ballot_w64(lane_id() < n && (v[0] - t.lo - state > 7u - state || v[1] != t.hi)) == 0; }
__device__ __forceinline__ bool words_up(unsigned v, Tag t) { return __builtin_amdgcn_ballot_w64(v != ((lane_id() & 1) ? t.hi : t.lo)) == 0; }
__device__ __forceinline__ void wait_flags(unsigned lds0, const unsigned* sync, int off, int n, unsigned state) {
  const Tag t = tag_get(lds0);
  while (!flags_up(flag_get(sync, off, n), n, t, state) && in_time(lds0)) {}
}
#endif
__device__ __forceinline__ void lds_wait8(unsigned addr, int n = 8) {
  int dv; unsigned polls = 0;
  do { dv = ds_r32(addr); asm volatile("s_waitcnt lgkmcnt(0)" : "+v"(dv) :: "memory"); } while (__builtin_amdgcn_readfirstlane(dv) < n && ++polls < 40000000u);
}
template <int N> __device__ __forceinline__ bool marked(unsigned addr) {
  bf16x8 v[N];
  static_for<0, N>([&](auto ic) { constexpr int i = decltype(ic)::value; v[i] = ds_b128<i * 4096>(addr); });
  asm volatile("s_waitcnt lgkmcnt(0)" ::: "memory");
  unsigned m = 0;
#pragma unroll
  for (int i = 0; i < N; ++i) {
    asm volatile("" : "+v"(v[i]));
    const u32x4 u = __builtin_bit_cast(u32x4, v[i]);
    m = max(max(m, u[0]), u[1]); m = max(max(m, u[2]), u[3]);
  }
  return __builtin_amdgcn_ballot_w64(m == 0xffffffffu) != 0;
}

struct GemmArgs {
  const bf16_t* X;
  const bf16_t* W0;
  const bf16_t* W1;
  const bf16_t* R;
  bf16_t* O;
  float* st;
  const bf16_t* CW0;
  const bf16_t* CW1;
  float* cs;
  const bf16_t* PF0;
  const bf16_t* PF1;
  unsigned* sync;
  float* slab;
  unsigned warm;
};
#define STAMP(i)
#define STAMPW(i, w)
template <int MF, int NF> struct Frags { bf16x8 x[MF]; s16x4 lo[NF], hi[NF]; };

__device__ __forceinline__ unsigned dn_wsrc(int lane, int wave) { return (unsigned)((lane >> 3) * (D_ * 2) + (((lane & 7) ^ wswz((wave & 3) * 8 + (lane >> 3))) << 4)); }
constexpr int OWNE = 0, OWNM = 32768;
#define KN_P0 2
#define KN_P1 3
#define KN_P2 0
#define KN_P3 1
constexpr int WOB = 57600;
constexpr int wo_pos(int s) { return s < 6 ? WOB + s * 16384 : (s - 6) * 16384; }
template <class HK>
__device__ __forceinline__ void k1_phase(const GemmArgs& a, HK hook) {
#if defined(__HIP_DEVICE_COMPILE__)
  constexpr int K = D_, LDW = 3 * D_, WN = 4;
  constexpr int HR = 16, STRIDE = 128;
  constexpr int XT = 144 * 128, WU = 24576;
  constexpr int XBASE = 0, WBASE = 3 * XT, RING = WBASE + 4 * WU;
  constexpr int MF = 4, NF = 3, MFX = 5;
  constexpr int NT = 16, NXI = 18, NLW = 4, XI = 5, XIMIN = 4, WI = 6;
  extern __shared__ __attribute__((aligned(16))) char smem[];
  STAMP(0);
  const unsigned lds0 = (unsigned)(size_t)(lds_char_ptr)smem;
  const int tid = threadIdx.x, lane = tid & 63;
  const int wave = __builtin_amdgcn_readfirstlane(tid >> 6);
  const int wm = wave / WN, wn = wave % WN;
  const int bid = blockIdx.x, xcd = bid & 7, idx = bid >> 3;
  const int tm = (xcd / 4) * 8 + idx / 4, tn = (xcd % 4) * 4 + idx % 4;
  const int m0 = tm * 128;
  const int fr = lane & 15, fq = lane >> 4;
  const int wq = (lane >> 2) & 3, wp = lane & 3;
  constexpr int TCH = RING + 2304;
  auto touch = [&](const void* base, unsigned voff, unsigned soff, unsigned lim = 0x7fffffffu) {
    __builtin_amdgcn_raw_ptr_buffer_load_lds(RSRCN(base, lim), LDSP(lds0 + TCH), 4, voff, soff, 0, 0);
  };
  const auto rO = RSRC(a.O);

  unsigned xsrc[XI], wsrc[WI];
  {
    const int w4 = wave & 3, lr = lane >> 3;
    const unsigned xv = (unsigned)(lr * (K * 2) + (((lane & 7) ^ ((w4 * 4 + (lr >> 1)) & 7)) << 4));
    const int r0 = m0 - HR + w4 * 8;
#pragma unroll
    for (int i = 0; i < XI; ++i) xsrc[i] = xv + (unsigned)((i == 0 ? (r0 < 0 ? 0 : r0) : r0 + 32 * (i < 4 ? i : 4)) * (K * 2));
    const unsigned wv = (unsigned)((w4 * 8 + lr) * (LDW * 2) + tn * 128 + (((lane & 7) ^ wswz(w4 * 8 + lr)) << 4));
#pragma unroll
    for (int i = 0; i < WI; ++i) wsrc[i] = wv + (unsigned)((i & 1) * 32 * (LDW * 2) + (i >> 1) * 2048);
  }
  auto ldX = [&](int i, int tile, int slot) {
    const int j = wave + NLW * i;
    if (j >= NXI) return;
    __builtin_amdgcn_raw_ptr_buffer_load_lds(RSRC(a.X), LDSP(lds0 + XBASE + slot * XT + j * 1024), 16, xsrc[i], tile * 128, 0, 0);
  };
  auto ldW = [&](int i, int unit, int slot) {
    const int j = wave + NLW * i;
    __builtin_amdgcn_raw_ptr_buffer_load_lds(RSRC(a.W0), LDSP(lds0 + WBASE + slot * WU + j * 1024), 16, wsrc[i], unit * 64 * LDW * 2, 0, 0);
  };
  auto issueX = [&](int tile, int slot) {
    if (wave >= NLW) return;
#pragma unroll
    for (int i = 0; i < XI; ++i) ldX(i, tile, slot);
  };
  auto issueW = [&](int unit, int slot) {
    if (wave >= NLW) return;
#pragma unroll
    for (int i = 0; i < WI; ++i) ldW(i, unit, slot);
  };

  f32x4 acc[NF][MF];
#pragma unroll
  for (int n = 0; n < NF; ++n)
#pragma unroll
    for (int m = 0; m < MF; ++m) acc[n][m] = (f32x4){0.f, 0.f, 0.f, 0.f};
  f32x4 acch = {0.f, 0.f, 0.f, 0.f};
  f32x4 accs = {0.f, 0.f, 0.f, 0.f};
  constexpr int ONES = RING;

  unsigned xa0, xa1, xh0, xh1;
  { const unsigned o = fr * 128 + ((fq ^ ((fr >> 1) & 7)) << 4), mo = (HR + wm * MF * 16) * 128;
    xa0 = lds0 + XBASE + mo + o; xa1 = lds0 + XBASE + mo + (o ^ 64); xh0 = lds0 + XBASE + o; xh1 = lds0 + XBASE + (o ^ 64); }
  unsigned wa;
  { const int row = 8 * fq + wq; wa = lds0 + WBASE + row * STRIDE + (((wn << 1) ^ wswz(row)) << 4) + wp * 8; }

  using I0 = IC<0>; using I1 = IC<1>;
  using T = std::true_type; using F = std::false_type;
  typedef Frags<MFX, NF> FR;
  struct RdCtx { unsigned xa, xh, wb; };
  auto mkctx = [&](bool ks1, unsigned xoffs, unsigned woffs) {
    RdCtx c; c.xa = (ks1 ? xa1 : xa0) + xoffs; c.xh = fr == 0 ? lds0 + ONES : (ks1 ? xh1 : xh0) + xoffs; c.wb = wa + woffs;
    return c;
  };
  auto rd_item = [&](FR& f, const RdCtx& c, auto KSc, auto kc) {
    constexpr int KS = decltype(KSc)::value, k = decltype(kc)::value;
    if constexpr (k < MF) f.x[k] = ds_b128<k * 2048>(c.xa);
    else if constexpr (k < MFX) f.x[k] = ds_b128<0>(c.xh);
    else {
      constexpr int n = k - MFX, o = n * 8192 + KS * 32 * STRIDE;
      f.lo[n] = ds_tr<o>(c.wb); f.hi[n] = ds_tr<o + 4 * STRIDE>(c.wb);
    }
  };
  auto wait_frags = [&](FR& f) {
    asm volatile("s_waitcnt lgkmcnt(0)" ::: "memory");
#pragma unroll
    for (int m = 0; m < MFX; ++m) asm volatile("" : "+v"(f.x[m]));
#pragma unroll
    for (int n = 0; n < NF; ++n) asm volatile("" : "+v"(f.lo[n]), "+v"(f.hi[n]));
  };
  auto step = [&](auto CSc, auto STc, FR& cur, FR& nxt, const RdCtx& c, auto KSc, auto PFc, auto NLc, auto&& ld) {
    constexpr bool PF = decltype(PFc)::value != 0; constexpr int NL = decltype(NLc)::value;
    constexpr int R = MFX + NF, RG = NF - 1;
    constexpr bool CS = decltype(CSc)::value; constexpr int ST = decltype(STc)::value;
    static_for<0, NF>([&](auto gc) {
      constexpr int g = decltype(gc)::value;
      if constexpr (PF) static_for<0, R>([&](auto kc) { constexpr int k = decltype(kc)::value; if constexpr (k * RG / R == g) rd_item(nxt, c, KSc, kc); });
      static_for<0, NL>([&](auto ic) { constexpr int i = decltype(ic)::value; if constexpr (i * NF / NL == g) ld(i); });
      const bf16x8 w = __builtin_shufflevector(cur.lo[g], cur.hi[g], 0, 1, 2, 3, 4, 5, 6, 7);
#pragma unroll
      for (int m = 0; m < MF; ++m) acc[g][m] = MFMA(w, cur.x[m], acc[g][m]);
      if constexpr (g == 1 + ST / WN) acch = MFMA(w, cur.x[MF], acch);
      if constexpr (CS && g == 0) accs = MFMA(w, cur.x[MF], accs);
      __builtin_amdgcn_sched_barrier(0);
    });
    if constexpr (PF) wait_frags(nxt);
  };
  auto step1 = [&](auto CSc, auto STc, FR& cur, FR& nxt, const RdCtx& c, auto KSc, auto R0c, auto RLc, auto IBc, auto&& sync, auto NLc, auto&& ld) {
    constexpr bool CS = decltype(CSc)::value; constexpr int ST = decltype(STc)::value, NL = decltype(NLc)::value, R0 = decltype(R0c)::value;
    constexpr int E0 = CS ? 1 : 0, E1 = 1 == 1 + ST / WN ? 1 : 0, E2 = 2 == 1 + ST / WN ? 1 : 0;
    constexpr int NM = 12 + E0 + E1 + E2, NR = MFX + 2 * NF;
    constexpr int IB = decltype(IBc)::value < 0 && decltype(IBc)::value > -100 ? NM + decltype(IBc)::value : decltype(IBc)::value;
    constexpr int RL = decltype(RLc)::value < 0 ? NM + decltype(RLc)::value : decltype(RLc)::value;
    static_for<0, NM>([&](auto ic) {
      constexpr int I = decltype(ic)::value;
      constexpr int g = I < 4 + E0 ? 0 : (I < 8 + E0 + E1 ? 1 : 2), q = I - (g == 0 ? 0 : (g == 1 ? 4 + E0 : 8 + E0 + E1));
      const bf16x8 w = __builtin_shufflevector(cur.lo[g], cur.hi[g], 0, 1, 2, 3, 4, 5, 6, 7);
      if constexpr (q < 4) acc[g][q] = MFMA(w, cur.x[q], acc[g][q]);
      else if constexpr (g == 0) accs = MFMA(w, cur.x[MF], accs);
      else acch = MFMA(w, cur.x[MF], acch);
      __builtin_amdgcn_sched_barrier(0);
      if constexpr (I == IB) sync();
      static_for<0, NR>([&](auto rc) {
        constexpr int r = decltype(rc)::value;
        if constexpr (R0 + r * (RL - R0) / (NR - 1) == I) {
          if constexpr (r < MFX) rd_item(nxt, c, KSc, rc);
          else {
            constexpr int n = (r - MFX) >> 1, o = n * 8192 + decltype(KSc)::value * 32 * STRIDE;
            if constexpr ((r - MFX) & 1) nxt.hi[n] = ds_tr<o + 4 * STRIDE>(c.wb); else nxt.lo[n] = ds_tr<o>(c.wb);
          }
        }
      });
      static_for<0, NL>([&](auto lc) { constexpr int i = decltype(lc)::value; if constexpr (R0 + i * (NM - R0) / NL == I) ld(i); });
      __builtin_amdgcn_sched_barrier(0);
    });
    wait_frags(nxt);
  };
  FR fa, fb;
  using IM1 = IC<-1>;

  asm volatile("s_waitcnt lgkmcnt(0)" :: "s"(a.warm), "v"(wsrc[WI - 1]), "v"(xsrc[XI - 1]));
  issueW(0, 0); issueX(0, 0); issueW(1, 1); issueX(1, 1);
  u32x4 sx[4];
  constexpr int TNEW = RING + 3072;
  auto ldT = [&](int dst) {
    int lx = lane; asm volatile("" : "+v"(lx));
    const auto rT = RSRCN((const char*)a.cs + STAB1, 2048 * 16);
    const unsigned to = (unsigned)((m0 - 16) * 16 + lx * 16);
    __builtin_amdgcn_raw_ptr_buffer_load_lds(rT, LDSP(lds0 + dst), 16, to, 0, 0, 16);
    __builtin_amdgcn_raw_ptr_buffer_load_lds(rT, LDSP(lds0 + dst + 1024), 16, to + 1024u, 0, 0, 16);
    if (lx < 16) __builtin_amdgcn_raw_ptr_buffer_load_lds(rT, LDSP(lds0 + dst + 2048), 16, to + 2048u, 0, 0, 16);
  };
  auto ldS = [&]() {
    int lx = lane; asm volatile("" : "+v"(lx));
    const unsigned so = (unsigned)((m0 + tn * 8 + wave * 2 + (lx >> 5)) * (K * 2) + (lx & 31) * 16);
#pragma unroll
    for (int i = 0; i < 4; ++i) sx[i] = __builtin_amdgcn_raw_buffer_load_b128(RSRC(a.X), so, i * 512, 0);
  };
  __builtin_amdgcn_sched_barrier(0);
  if (wave == 4) {
    if (lane < 4) *(unsigned*)(smem + ONES + lane * 4) = 0x3f803f80u;
  }
  if (wave == 5) {
    if (lane < 2) __builtin_amdgcn_raw_ptr_buffer_load_lds(RSRC(a.sync), LDSP(lds0 + SNAP), 4, lane * 4, EPOCH, 0, 16);
    ds_w32(lds0 + SNAP + 28, (int)__builtin_amdgcn_s_memrealtime());
#define KN_PT 1
    if constexpr (KN_PT) touch(a.cs, lane < 8 ? (unsigned)((const char*)a.sync - (const char*)a.cs + bid * 1024 + lane * 128) : (lane == 8 ? (unsigned)(STAB1 + (m0 + tn * 8) * 16) : (lane < 23 ? (unsigned)(bid * 1792 + (lane - 9) * 128) : 0x80000000u)), 0);
    const int p2 = (xcd >> 1) * 2 + (idx >> 4), mb = (xcd & 1) * 16 + (idx & 15);
#pragma unroll
    for (int i = 0; i < 3; ++i)
      __builtin_amdgcn_raw_buffer_store_b128((u32x4){~0u, ~0u, ~0u, ~0u}, RSRC(a.st), lane * 16 + i * 1024, ((p2 * 16 + (mb >> 1)) * RBH + (xcd >> 2) * 64 + (mb & 1) * 12) * 256, 16);
  }
  constexpr int HQ = 5;
  const int slc = (3 - ((unsigned)(size_t)a.W0 >> 7) - (xcd & 3) * 4) & 7;
  const int hid = (idx >> 2) * 3 + (idx & 3) - ((idx & 3) > slc ? 1 : 0);
  const bool hlp = slc < 4 && (idx & 3) != slc;
  const int hj1 = hlp ? hid / 3 - HQ : -100, hj2 = hlp ? hid / 3 + 8 - HQ : -100;
  const unsigned htv = (unsigned)(lane * (LDW * 2) + (hid % 3) * 2048 + ((xcd & 3) * 4 + slc) * 128);
  if (wave == 6 && hlp && hj1 <= 0) touch(a.W0, htv, (hid / 3) * (64 * LDW * 2));
  auto code_touch = [&]() {
    unsigned long long c0, c1; CODE_ADDR(c0, "k_all"); CODE_ADDR(c1, "k_zend");
    const unsigned len = c1 - c0 < 262144ull ? (unsigned)(c1 - c0) : 0u;
    touch((const void*)c0, (unsigned)((lane * 32 + idx) * 128), 0, len);
  };
  if (wave == 7) code_touch();
  if (wave < NLW) wait_vm<WI + XIMIN>();
  lds_barrier();
  { const RdCtx c = mkctx(false, 0, 0); static_for<0, MFX + NF>([&](auto kc) { rd_item(fa, c, I0{}, kc); }); }
  wait_frags(fa);
  STAMP(1);
  auto run = [&](auto ROLEc) __attribute__((always_inline)) {
    constexpr int ROLE = decltype(ROLEc)::value;
    constexpr bool LDR = ROLE < NLW;
    using CS0 = std::integral_constant<bool, ROLE / WN == 0>; using CS1 = std::integral_constant<bool, ROLE / WN == 1>;
    using STt = IC<ROLE>;
    auto nold = [&](int) {};
    { const RdCtx c = mkctx(true, 0, 0); step(CS0{}, STt{}, fa, fb, c, I1{}, T{}, IC<LDR ? WI + XI : 0>{}, [&](int i) { if (i < WI) ldW(i, 2, 2); else ldX(i - WI, 2, 2); }); }
    if constexpr (LDR) ldS();
    int slx = 1;
    if constexpr (ROLE == 5) wait_vm<3 + KN_PT>();
    auto iter = [&](int j, auto LXc, auto LWc, auto WAITc, auto TIc) {
      constexpr bool LX = decltype(LXc)::value && LDR, LW = decltype(LWc)::value && LDR;
      constexpr int WAIT = decltype(WAITc)::value, TI = decltype(TIc)::value;
      constexpr int EX = (LDR && TI < 0) ? WI : 0;
      constexpr int OL = TI == 1 ? 0 : 2, OH = (LDR && TI > 0) ? 2 : OL;
      constexpr int NLT = EX + (LX ? XI : 0) + (LW ? WI : 0) + 2 * (OH - OL), N0 = (NLT + 1) / 2, N1 = NLT - N0;
      if constexpr (LDR) wait_vm<WAIT + (TI > 0 ? 2 * OL : 0)>();
      unsigned osrc = 0;
      if constexpr (OH > OL) { int lx = lane; asm volatile("" : "+v"(lx)); const int row = ROLE * 8 + (lx >> 3); osrc = (unsigned)(row * D_ + ((xcd & 1) * 8 + (idx & 7)) * 64 + ((lx & 7) ^ wswz(row)) * 8) * 2u; }
      if constexpr (ROLE == 6 && TI == 0) { if (j == hj1 || j == hj2) touch(a.W0, htv, (j + HQ) * (64 * LDW * 2)); }
      constexpr int S1 = 2, S3 = 11;
      if constexpr (LDR && TI <= 0) {
        if (j == S1) {
          float sm = 0.f, sq = 0.f;
          const bf16x2_t one2 = __builtin_bit_cast(bf16x2_t, 0x3f803f80u);
#pragma unroll
          for (int i = 0; i < 4; ++i)
#pragma unroll
            for (int e = 0; e < 4; ++e) { const unsigned u = sx[i][e]; const bf16x2_t v = __builtin_bit_cast(bf16x2_t, u); sm = __builtin_amdgcn_fdot2_f32_bf16(v, one2, sm, false); sq = __builtin_amdgcn_fdot2_f32_bf16(v, v, sq, false); }
          sm = sum8l(sm); sq = sum8l(sq);
          sm += __int_as_float(__builtin_amdgcn_update_dpp(0, __float_as_int(sm), 0x128, 0xf, 0xf, false));
          sq += __int_as_float(__builtin_amdgcn_update_dpp(0, __float_as_int(sq), 0x128, 0xf, 0xf, false));
          { auto p = __builtin_amdgcn_permlane16_swap(__float_as_uint(sm), __float_as_uint(sm), false, false); sm = __uint_as_float(p[0]) + __uint_as_float(p[1]); }
          { auto p = __builtin_amdgcn_permlane16_swap(__float_as_uint(sq), __float_as_uint(sq), false, false); sq = __uint_as_float(p[0]) + __uint_as_float(p[1]); }
          const float mu = sm * (1.f / 1024.f);
          float var = sq * (1.f / 1024.f) - mu * mu; var = var > 0.f ? var : 0.f;
          int lx = lane; asm volatile("" : "+v"(lx));
          const Tag tg = tag_get(lds0);
          __builtin_amdgcn_raw_buffer_store_b128((u32x4){__float_as_uint(mu), tg.lo, __float_as_uint(__builtin_amdgcn_rsqf(var + 1e-5f)), tg.lo + tg.hi}, RSRC(a.cs),
                                                 (lx & 31) == 0 ? (unsigned)(STAB1 + (m0 + tn * 8 + ROLE * 2 + (lx >> 5)) * 16) : 0x80000000u, 0, 16);
          __builtin_amdgcn_sched_barrier(0);
          asm volatile("s_nop 1" ::: "memory");
        }
      }
      if constexpr (ROLE == 7 && TI == 0) {
        if (j == S3) ldT(TNEW);
        if (j == S3 + 1) {
          for (;;) {
            wait_vm<0>();
            int lx = lane; asm volatile("" : "+v"(lx));
            const unsigned ad = lds0 + TNEW + lx * 16;
            u32x4 v[3];
            v[0] = __builtin_bit_cast(u32x4, ds_b128<0>(ad)); v[1] = __builtin_bit_cast(u32x4, ds_b128<1024>(ad)); v[2] = __builtin_bit_cast(u32x4, ds_b128<2048>(ad));
            const Tag tg = tag_get(lds0);
            asm volatile("" : "+v"(v[0]), "+v"(v[1]), "+v"(v[2]) :: "memory");
            auto good = [&](const u32x4& e) { return e[1] == tg.lo && e[3] == tg.lo + tg.hi; };
            const bool bad = (!good(v[0]) && (tm > 0 || lx >= 16)) || !good(v[1]) || (!good(v[2]) && lx < 16);
            if (__builtin_amdgcn_ballot_w64(bad) == 0 || !in_time(lds0)) break;
            ldT(TNEW);
          }
        }
      }
      if constexpr (ROLE == 5 && TI == 1) wait_vm<0>();
      if constexpr (ROLE == 7 && TI == 1) code_touch();
      if constexpr (ROLE == 4 && TI == 0) touch(a.PF1, (bid * 64 + lane) * 128, 0, j == 1 + idx % 12 ? 0x7fffffffu : 0u);
      if constexpr (ROLE >= 4 && TI == 2 && !KN_SPF) {
        const bf16_t* cw = ROLE < 6 ? a.CW0 : a.CW1;
        touch(cw, lane < 56 ? (unsigned)(((ROLE & 1) * 512 + bid) * (DF_ * 2) + lane * 128) : 0x80000000u, 0);
      }
      int xp = slx + 2; if (xp >= 3) xp -= 3;
      auto ld = [&](int i) { if (i < EX) { ldW(i, j + 2, (j + 2) & 3); return; } i -= EX;
                             if (LX && i < XI) ldX(i, j + 2, xp);
                             else if constexpr (LW) ldW(i - (LX ? XI : 0), j + 3, (j + 3) & 3);
                             else { const int q = i - (LX ? XI : 0), t = OL + (q >> 1), h = q & 1;
                               __builtin_amdgcn_raw_ptr_buffer_load_lds(RSRC(a.PF1), LDSP(lds0 + wo_pos(t >> 1) + (t & 1) * 8192 + (ROLE + 4 * h) * 1024), 16,
                                                                        osrc, ((((t & 1) * 8 + (t >> 1)) ^ ((xcd & 3) * 4)) * 2 + h) * (32 * D_ * 2), 0, 0); } };
      { const RdCtx c = mkctx(false, slx * XT, (j & 3) * WU); step1(CS1{}, STt{}, fb, fa, c, I0{}, IC<LDR ? 1 : 0>{}, IC<-4>{}, IC<LDR ? 1 : -100>{}, [&]() { raw_barrier(); }, IC<N0>{}, ld); }
      { const RdCtx c = mkctx(true, slx * XT, (j & 3) * WU); step1(CS0{}, STt{}, fa, fb, c, I1{}, I0{}, IC<LDR ? -4 : -6>{},
          IC<LDR ? -100 : -3>{}, [&]() { if constexpr (TI != 3) { asm volatile("s_waitcnt lgkmcnt(0)" ::: "memory"); raw_barrier(); } }, IC<N1>{}, [&](int i) { ld(i + N0); }); }
      slx = slx + 1; if (slx >= 3) slx = 0;
    };
    using WS_ = IC<2 * WI + XIMIN>;
    if constexpr (!LDR) raw_barrier();
    if constexpr (LDR) iter(1, T{}, T{}, IC<WI + XIMIN + 4>{}, IM1{});
    for (int j = LDR ? 2 : 1; j <= NT - 4; ++j) iter(j, T{}, T{}, WS_{}, I0{});
    iter(NT - 3, T{}, F{}, WS_{}, I1{});
    iter(NT - 2, F{}, F{}, IC<WI + XIMIN>{}, IC<2>{});
    iter(NT - 1, F{}, F{}, I0{}, IC<3>{});
    if constexpr (ROLE == 5) flag_set(a.sync, F_MARK + ((xcd >> 1) * 2 + (idx >> 4)) * 256 + ((xcd & 1) * 16 + (idx & 15)) * 8, tag_get(lds0), 0);
    { const RdCtx c = mkctx(false, 0, 0); step(CS1{}, STt{}, fb, fa, c, I0{}, F{}, I0{}, nold); }
  };
  switch (wave) {
    case 0: run(IC<0>{}); break; case 1: run(IC<1>{}); break;
    case 2: run(IC<2>{}); break; case 3: run(IC<3>{}); break;
    case 4: run(IC<4>{}); break; case 5: run(IC<5>{}); break;
    case 6: run(IC<6>{}); break; default: run(IC<7>{}); break;
  }
  STAMP(2);
  {
    float* tcs = (float*)(smem + RING + 64);
    float* tkv = tcs + 128;
    if (fr == 0) { *(f32x4*)(tcs + wm * 64 + wn * 16 + fq * 4) = accs; *(f32x4*)(tkv + wave * 16 + fq * 4) = acch; }
    const int srow = m0 + wave * 16 + fr;
    const bool par = fr & 1;
    float bias[4];
    {
      const unsigned mk = 0x5555u & ((2u << (srow < 15 ? srow : 15)) - 1u), sft = (fr & 14) - fq * 4 + 14;
      const unsigned b0 = ~(mk >> sft), b1 = ~((mk << 16) >> sft);
#pragma unroll
      for (int j = 0; j < 4; ++j) bias[j] = __uint_as_float((unsigned)__builtin_amdgcn_sbfe((int)(j < 2 ? b0 : b1), 2 - 2 * (j & 1), 1) & 0xff61b1e6u);
    }
    unsigned pl = par ? 0x01000c0cu : 0x0c0c0100u, ph = par ? 0x03020c0cu : 0x0c0c0302u;
    asm volatile("" : "+v"(bias[0]), "+v"(bias[1]), "+v"(bias[2]), "+v"(bias[3]), "+v"(pl), "+v"(ph));
    STAMP(9);
    lds_barrier();
    STAMP(6);
    constexpr int RS = 400;
    char* qt = smem;
    f32x4 csn[NF];
    const f32x4 c0a = *(const f32x4*)(tcs + wn * 16 + fq * 4), c0b = *(const f32x4*)(tcs + 64 + wn * 16 + fq * 4);
    csn[1] = *(const f32x4*)(tkv + wn * 16 + fq * 4); csn[2] = *(const f32x4*)(tkv + 64 + wn * 16 + fq * 4);
    f32x2 st[MF + 1];
#pragma unroll
    for (int m = 0; m <= MF; ++m) {
      const int r = m < MF ? 16 + (wm * MF + m) * 16 + fr : fr;
      const f32x4 t4 = *(const f32x4*)(smem + TNEW + r * 16); st[m] = (f32x2){t4[0], t4[2]};
    }
    const f32x4 csh = *(const f32x4*)(tkv + wave * 16 + fq * 4);
    asm volatile("" ::: "memory");
    csn[0] = c0a + c0b;
    static_for<0, MF>([&](auto mc) {
      constexpr int m = decltype(mc)::value;
      __builtin_amdgcn_sched_barrier(0);
      hook(IC<2 + 2 * m>{}, IC<(KN_WE < 4 + 2 * m ? KN_WE : 4 + 2 * m)>{});
      __builtin_amdgcn_sched_barrier(0);
      const int r = 16 + (wm * MF + m) * 16 + fr;
      const float mu = st[m][0], rs = st[m][1];
#pragma unroll
      for (int n = 0; n < NF; ++n) {
        const int col = (n * 4 + wn) * 16 + fq * 4;
        const f32x4 cs = csn[n];
        const u32x2 v = {pk_bf16(rs * (acc[n][m][0] - mu * cs[0]), rs * (acc[n][m][1] - mu * cs[1])),
                         pk_bf16(rs * (acc[n][m][2] - mu * cs[2]), rs * (acc[n][m][3] - mu * cs[3]))};
        *(u32x2*)(qt + r * RS + col * 2) = v;
      }
    });
    {
      const float mu = st[MF][0], rs = st[MF][1];
      const int col = (4 + wave) * 16 + fq * 4;
      const u32x2 v = {pk_bf16(rs * (acch[0] - mu * csh[0]), rs * (acch[1] - mu * csh[1])),
                       pk_bf16(rs * (acch[2] - mu * csh[2]), rs * (acch[3] - mu * csh[3]))};
      *(u32x2*)(qt + fr * RS + col * 2) = v;
    }
    STAMP(4);
    lds_barrier();
    STAMP(5);
    const unsigned qa = lds0 + (16 + wave * 16 + fr) * RS + fq * 16;
    const unsigned ka = lds0 + (wave * 16 + fr) * RS + 128 + fq * 16;
    const unsigned va = lds0 + (wave * 16 + fq * 4 + wq) * RS + 256 + wp * 16;
    bf16x8 qf[2], kf[2][2]; s16x4 vf[2][2][2];
    qf[0] = ds_b128<0>(qa); kf[0][0] = ds_b128<0>(ka); kf[0][1] = ds_b128<16 * RS>(ka);
    qf[1] = ds_b128<64>(qa); kf[1][0] = ds_b128<64>(ka); kf[1][1] = ds_b128<16 * RS + 64>(ka);
    vf[0][0][0] = ds_tr<0>(va); vf[0][0][1] = ds_tr<16 * RS>(va); vf[0][1][0] = ds_tr<8>(va); vf[0][1][1] = ds_tr<16 * RS + 8>(va);
    vf[1][0][0] = ds_tr<64>(va); vf[1][0][1] = ds_tr<16 * RS + 64>(va); vf[1][1][0] = ds_tr<72>(va); vf[1][1][1] = ds_tr<16 * RS + 72>(va);
    hook(IC<10>{}, IC<KN_WE>{});
    asm volatile("s_waitcnt lgkmcnt(8)" ::: "memory");
#pragma unroll
    for (int hh = 0; hh < 2; ++hh) asm volatile("" : "+v"(qf[hh]), "+v"(kf[hh][0]), "+v"(kf[hh][1]));
    const float sc2 = 0.17677669529663687f * 1.4426950408889634f;
    u32x4 ov[2];
    const f32x4 z = {0.f, 0.f, 0.f, 0.f};
    f32x4 sc[2][2]; float lg[2][4], mx[2], sum[2];
    auto x16 = [](float v) { auto p = __builtin_amdgcn_permlane16_swap(__float_as_uint(v), __float_as_uint(v), false, false); return (f32x2){__uint_as_float(p[0]), __uint_as_float(p[1])}; };
    auto x32 = [](float v) { auto p = __builtin_amdgcn_permlane32_swap(__float_as_uint(v), __float_as_uint(v), false, false); return (f32x2){__uint_as_float(p[0]), __uint_as_float(p[1])}; };
#pragma unroll
    for (int hh = 0; hh < 2; ++hh) {
      sc[hh][0] = MFMA(kf[hh][0], qf[hh], z);
      sc[hh][1] = MFMA(kf[hh][1], qf[hh], z);
    }
    __builtin_amdgcn_sched_barrier(0);
#pragma unroll
    for (int hh = 0; hh < 2; ++hh) {
#pragma unroll
      for (int j = 0; j < 4; ++j) lg[hh][j] = __builtin_fmaf(par ? sc[hh][j >> 1][(j & 1) * 2 + 1] : sc[hh][j >> 1][(j & 1) * 2], sc2, bias[j]);
      mx[hh] = fmaxf(fmaxf(lg[hh][0], lg[hh][1]), fmaxf(lg[hh][2], lg[hh][3]));
    }
    __builtin_amdgcn_sched_barrier(0);
    { const f32x2 p = x16(mx[0]), q = x16(mx[1]); mx[0] = fmaxf(p[0], p[1]); mx[1] = fmaxf(q[0], q[1]); }
    { const f32x2 p = x32(mx[0]), q = x32(mx[1]); mx[0] = fmaxf(p[0], p[1]); mx[1] = fmaxf(q[0], q[1]); }
    __builtin_amdgcn_sched_barrier(0);
#pragma unroll
    for (int hh = 0; hh < 2; ++hh) {
#pragma unroll
      for (int j = 0; j < 4; ++j) lg[hh][j] = __builtin_amdgcn_exp2f(lg[hh][j] - mx[hh]);
      sum[hh] = (lg[hh][0] + lg[hh][1]) + (lg[hh][2] + lg[hh][3]);
    }
    __builtin_amdgcn_sched_barrier(0);
    asm volatile("s_waitcnt lgkmcnt(0)" ::: "memory");
#pragma unroll
    for (int hh = 0; hh < 2; ++hh) asm volatile("" : "+v"(vf[hh][0][0]), "+v"(vf[hh][0][1]), "+v"(vf[hh][1][0]), "+v"(vf[hh][1][1]));
    { const f32x2 p = x16(sum[0]), q = x16(sum[1]); sum[0] = p[0] + p[1]; sum[1] = q[0] + q[1]; }
    { const f32x2 p = x32(sum[0]), q = x32(sum[1]); sum[0] = __builtin_amdgcn_rcpf(p[0] + p[1]); sum[1] = __builtin_amdgcn_rcpf(q[0] + q[1]); }
    __builtin_amdgcn_sched_barrier(0);
#pragma unroll
    for (int hh = 0; hh < 2; ++hh) {
      const float inv = sum[hh];
      const unsigned c0 = pk_bf16(lg[hh][0] * inv, lg[hh][1] * inv), c1 = pk_bf16(lg[hh][2] * inv, lg[hh][3] * inv);
      const u32x4 pp = {__builtin_amdgcn_perm(0u, c0, pl), __builtin_amdgcn_perm(0u, c0, ph), __builtin_amdgcn_perm(0u, c1, pl), __builtin_amdgcn_perm(0u, c1, ph)};
      const bf16x8 pf = __builtin_bit_cast(bf16x8, pp);
      sc[hh][0] = MFMA(__builtin_shufflevector(vf[hh][0][0], vf[hh][0][1], 0, 1, 2, 3, 4, 5, 6, 7), pf, z);
      sc[hh][1] = MFMA(__builtin_shufflevector(vf[hh][1][0], vf[hh][1][1], 0, 1, 2, 3, 4, 5, 6, 7), pf, z);
    }
    __builtin_amdgcn_sched_barrier(0);
#pragma unroll
    for (int hh = 0; hh < 2; ++hh) ov[hh] = (u32x4){pk_bf16(sc[hh][0][0], sc[hh][0][1]), pk_bf16(sc[hh][0][2], sc[hh][0][3]), pk_bf16(sc[hh][1][0], sc[hh][1][1]), pk_bf16(sc[hh][1][2], sc[hh][1][3])};
    STAMP(10);
#pragma unroll
    for (int hh = 0; hh < 2; ++hh) __builtin_amdgcn_raw_buffer_store_b128(ov[hh], rO, (((m0 >> 4) + wave) * RBO + tn * 8 + hh * 4) * 256 + lane * 16, 0, 16);
    STAMP(11);
  }
  STAMP(7);
  STAMP(8);
  const Tag tg = tag_get(lds0);
  asm volatile("s_waitcnt vmcnt(0)" ::: "memory");
  flag_set(a.sync, F_O(tm) + (wave >> 1) * 256 + tn * 16 + (wave & 1) * 8, tg, 0);
  raw_barrier();
  hook(IC<KN_WE>{}, IC<16>{});
  STAMP(3);
#endif
}

struct Late { u32x4 v[3]; };
template <class HK>
__device__ __forceinline__ void glu_phase(const GemmArgs& a, HK hook, Late& late) {
#if defined(__HIP_DEVICE_COMPILE__)
  constexpr int LDW = DF_, WN = 2;
  constexpr int UPOFF = 32 * 224 + 16;
  constexpr int XT = 32768, WU = 14592;
  constexpr int XBASE = 0, WBASE = 3 * XT, RING = WBASE + 4 * WU;
  constexpr int MF = 4, NF = 7;
  constexpr int NT = 16, NXI = 16, NWI = 14, NLW = 4, XI = 4, WI = 4;
  extern __shared__ __attribute__((aligned(16))) char smem[];
  STAMP(0);
  const unsigned lds0 = (unsigned)(size_t)(lds_char_ptr)smem;
  const int tid = threadIdx.x, lane = tid & 63;
  const int wave = __builtin_amdgcn_readfirstlane(tid >> 6);
  const int wm = wave / WN, wn = wave % WN;
  const int bid = blockIdx.x, xcd = bid & 7, idx = bid >> 3;
  const int tm = (xcd / 4) * 4 + idx / 8, tn = (xcd % 4) * 8 + idx % 8;
  const int m0 = tm * 256;
  const int krot = (tm >> 2) * 8;
  const int fr = lane & 15, fq = lane >> 4;
  const int wq = (lane >> 2) & 3, wp = lane & 3;
  const int panel = m0 >> 8;
  constexpr int TCH = RING + 5120;
  constexpr int DONE = TCH + 896;
  auto done_inc = [&]() { if (lane == 0) asm volatile("ds_add_u32 %0, %1" :: "v"(lds0 + DONE), "v"(1) : "memory"); };
  auto touch = [&](const void* base, unsigned voff, unsigned soff, unsigned lim = 0x7fffffffu) {
    __builtin_amdgcn_raw_ptr_buffer_load_lds(RSRCN(base, lim), LDSP(lds0 + TCH), 4, voff, soff, 0, 0);
  };
  const auto rO = RSRC(a.O);

  unsigned xsrc[XI], wsrc[WI];
#pragma unroll
  for (int i = 0; i < XI; ++i) {
    const int j = (wave % NLW) + NLW * i;
    xsrc[i] = (unsigned)((((m0 >> 4) + (j >> 1)) * RBH + (j & 1) * 4) * 256 + lane * 16);
  }
#pragma unroll
  for (int i = 0; i < WI; ++i) {
    int j = (wave % NLW) + NLW * i; if (j >= NWI) j -= NLW;
    const int jj = j >= 7 ? j - 7 : j;
    const int b = jj * 1024 + lane * 16, p = b / 224, pos = (b % 224) >> 4;
    const int row = 8 * ((p & 15) >> 2) + (p & 3) + 4 * (p >> 4);
    wsrc[i] = (unsigned)(row * LDW + tn * 112 + pos * 8) * 2u;
  }
  auto ldX = [&](int i, int tile, int part, int slot) {
    tile = (tile + krot) & 15;
    const int j = wave + NLW * i;
    __builtin_amdgcn_raw_ptr_buffer_load_lds(RSRC(a.X), LDSP(lds0 + XBASE + slot * XT + (part * NXI + j) * 1024), 16, xsrc[i], tile * 2048 + part * (NXI / 2) * RBH * 256, 0, 16);
  };
  auto ldW = [&](int i, int unit, int slot) {
    const auto rW0 = RSRC(a.W0);
    const auto rW1 = RSRC(a.W1);
    int j = wave + NLW * i; if (j >= NWI) j -= NLW;
    unit = (unit + 2 * krot) & 31;
    const bool up = j >= 7;
    __builtin_amdgcn_raw_ptr_buffer_load_lds(up ? rW1 : rW0, LDSP(lds0 + WBASE + slot * WU + (up ? UPOFF + (j - 7) * 1024 : j * 1024)), 16, wsrc[i], unit * 32 * LDW * 2, 0, 0);
  };
  auto issueX = [&](int tile, int part, int slot) {
    if (wave >= NLW) return;
#pragma unroll
    for (int i = 0; i < XI; ++i) ldX(i, tile, part, slot);
  };
  auto issueW = [&](int unit, int slot) {
    if (wave >= NLW) return;
#pragma unroll
    for (int i = 0; i < WI; ++i) ldW(i, unit, slot);
  };

  f32x4 acc[NF][MF];
#pragma unroll
  for (int n = 0; n < NF; ++n)
#pragma unroll
    for (int m = 0; m < MF; ++m) acc[n][m] = (f32x4){0.f, 0.f, 0.f, 0.f};
  f32x4 accs = {0.f, 0.f, 0.f, 0.f};

  const unsigned xa0 = lds0 + XBASE + wm * MF * 16 * 128 + lane * 16, xa1 = xa0 + 1024;
  unsigned wa[2];
#pragma unroll
  for (int c = 0; c < 2; ++c) wa[c] = lds0 + WBASE + (4 * fq + wq) * 224 + wn * 112 + (c == 0 ? wp * 8 : 96 + (wp & 1) * 8 + (wp >> 1) * UPOFF);

  using I0 = IC<0>; using I1 = IC<1>;
  using T = std::true_type; using F = std::false_type;
  typedef Frags<MF, NF> FR;
  struct RdCtx { unsigned xa, wb[2]; };
  auto mkctx = [&](bool ks1, unsigned xoffs, unsigned woffs) {
    RdCtx c; c.xa = (ks1 ? xa1 : xa0) + xoffs; c.wb[0] = wa[0] + woffs; c.wb[1] = wa[1] + woffs;
    return c;
  };
  auto rd_item = [&](FR& f, const RdCtx& c, auto kc) {
    constexpr int k = decltype(kc)::value;
    if constexpr (k < MF) f.x[k] = ds_b128<k * 2048>(c.xa);
    else {
      constexpr int n = k - MF, cc = n == 6, o = n == 6 ? 0 : (n >> 1) * 32 + (n & 1) * UPOFF;
      f.lo[n] = ds_tr<o>(c.wb[cc]); f.hi[n] = ds_tr<o + 16 * 224>(c.wb[cc]);
    }
  };
  auto wait_frags = [&](FR& f) {
    asm volatile("s_waitcnt lgkmcnt(0)" ::: "memory");
#pragma unroll
    for (int m = 0; m < MF; ++m) asm volatile("" : "+v"(f.x[m]));
#pragma unroll
    for (int n = 0; n < NF; ++n) asm volatile("" : "+v"(f.lo[n]), "+v"(f.hi[n]));
  };
  auto step = [&](FR& cur, FR& nxt, const RdCtx& c, auto PFc, auto NLc, auto&& ld) {
    constexpr bool PF = decltype(PFc)::value != 0; constexpr int NL = decltype(NLc)::value;
    constexpr int R = MF + NF, RG = NF - 1;
    static_for<0, NF>([&](auto gc) {
      constexpr int g = decltype(gc)::value;
      if constexpr (PF) static_for<0, R>([&](auto kc) { constexpr int k = decltype(kc)::value; if constexpr (k * RG / R == g) rd_item(nxt, c, kc); });
      static_for<0, NL>([&](auto ic) { constexpr int i = decltype(ic)::value; if constexpr (i * NF / NL == g) ld(i); });
      const bf16x8 w = __builtin_shufflevector(cur.lo[g], cur.hi[g], 0, 1, 2, 3, 4, 5, 6, 7);
#pragma unroll
      for (int m = 0; m < MF; ++m) acc[g][m] = MFMA(w, cur.x[m], acc[g][m]);
      __builtin_amdgcn_sched_barrier(0);
    });
    if constexpr (PF) wait_frags(nxt);
  };
  auto stepg = [&](auto CSc, FR& cur, FR& nxt, const RdCtx& c, auto R0c, auto IBc, auto&& sync, auto NLc, auto&& ld) {
    constexpr bool CS = decltype(CSc)::value; constexpr int R0 = decltype(R0c)::value, IB = decltype(IBc)::value, NL = decltype(NLc)::value;
    constexpr int NM = NF * MF, NR = MF + 2 * NF;
    static_for<0, NM>([&](auto ic) {
      constexpr int I = decltype(ic)::value, g = I / MF, m = I % MF;
      constexpr int RL = IB > 8 ? 20 : 22;
      const bf16x8 w = __builtin_shufflevector(cur.lo[g], cur.hi[g], 0, 1, 2, 3, 4, 5, 6, 7);
      acc[g][m] = MFMA(w, cur.x[m], acc[g][m]);
      __builtin_amdgcn_sched_barrier(0);
      if constexpr (I == IB) sync();
      static_for<0, NR>([&](auto rc) {
        constexpr int r = decltype(rc)::value;
        if constexpr (R0 + r * (RL - R0) / (NR - 1) == I) {
          if constexpr (r < MF) nxt.x[r] = ds_b128<r * 2048>(c.xa);
          else {
            constexpr int n = (r - MF) >> 1, cc = n == 6, o = n == 6 ? 0 : (n >> 1) * 32 + (n & 1) * UPOFF;
            if constexpr ((r - MF) & 1) nxt.hi[n] = ds_tr<o + 16 * 224>(c.wb[cc]); else nxt.lo[n] = ds_tr<o>(c.wb[cc]);
          }
        }
      });
      static_for<0, NL>([&](auto lc) { constexpr int i = decltype(lc)::value; if constexpr (R0 + i * (NM - 1 - R0) / NL == I) ld(i); });
      if constexpr (CS && m == MF - 1) {
        const unsigned sv = fr == g ? 0x3f803f80u : 0u;
        const u32x4 s4 = {sv, sv, sv, sv};
        accs = MFMA(w, __builtin_bit_cast(bf16x8, s4), accs);
      }
      __builtin_amdgcn_sched_barrier(0);
    });
    wait_frags(nxt);
  };
  FR fa, fb;

  f32x4* tb_row = (f32x4*)(smem + RING);
  float* tb_cs = (float*)(tb_row + 256);
  constexpr int RTAB = 524288;
  auto glu_stats_issue = [&]() {
    if (wave == 7) __builtin_amdgcn_raw_ptr_buffer_load_lds(RSRC(a.st), LDSP(lds0 + RING + 3072), 16, ((lane >> 2) * S_ + m0 + tn * 8 + (lane & 3) * 2) * 8, 0, 0, 16);
  };
  auto glu_stats = [&]() {
    int lx = lane; asm volatile("" : "+v"(lx));
    const unsigned sa = lds0 + RING + 3072 + lx * 8;
    f32x2 p0 = ds_b64<0>(sa), p1 = ds_b64<512>(sa);
    asm volatile("s_waitcnt lgkmcnt(0)" : "+v"(p0), "+v"(p1) :: "memory");
    const float mu = sum8(p0[0] + p1[0]) * (1.f / 16.f);
    const float v = sum8((p0[1] + p1[1]) + 64.f * ((p0[0] - mu) * (p0[0] - mu) + (p1[0] - mu) * (p1[0] - mu))) * (1.f / 1024.f) + 1e-5f;
    const float rs = 1.0f / sqrtf(v);
    const f32x4 rv = {mu, -0.10294324f * rs * rs * rs, -2.3022082f * rs, v};
    __builtin_amdgcn_raw_buffer_store_b128(__builtin_bit_cast(u32x4, rv), RSRC(a.cs), lx < 8 ? (unsigned)(RTAB + (m0 + tn * 8 + lx) * 16) : 0x80000000u, 0, 16);
  };
  auto epilogue = [&](auto EAc) __attribute__((always_inline)) {
    STAMP(2);
    constexpr bool EARLY = decltype(EAc)::value;
    if constexpr (!EARLY) {
      lds_wait8(lds0 + DONE);
      __builtin_amdgcn_s_setprio(KN_P0);
      STAMPW(9, 4);
    } else __builtin_amdgcn_s_setprio(KN_P1);
    asm volatile("" ::: "memory");
    f32x4 cg[4], cu[4], rw[MF], rw6[2];
    int lz = lane; asm volatile("" : "+v"(lz));
    const int fr = lz & 15, fq = lz >> 4;
#pragma unroll
    for (int p = 0; p < 4; ++p) {
      const int o = wn * 56 + p * 16 + (p == 3 ? (fq & 1) : fq) * 4;
      cg[p] = *(const f32x4*)(tb_cs + o); cu[p] = *(const f32x4*)(tb_cs + 112 + o);
    }
#pragma unroll
    for (int m = 0; m < MF; ++m) rw[m] = tb_row[(wm * MF + m) * 16 + fr];
#pragma unroll
    for (int q = 0; q < 2; ++q) rw6[q] = tb_row[(wm * MF + 2 * q + (fq >> 1)) * 16 + fr];
    const int gz = ((m0 >> 4) + wm * 4) * RBA * 256 + fr * 16;
    const int go0 = gz + ((fq & 1) * RBA + (fq >> 1)) * 256;
    const unsigned le = lds0 + OWNE + (wm * 4 + (fq & 1)) * 2048 + (wn * 4 + (fq >> 1)) * 256 + fr * 16, lm = lds0 + OWNM + (wm * 4 + (fq & 1)) * 1024 + (wn * 2 + (fq >> 1)) * 256 + fr * 16;
    unsigned hv[MF][NF];
    unsigned dw = dn_wsrc(lz, wave); asm volatile("" : "+v"(dw));
    const Tag tg = tag_get(lds0);
    auto put = [&](unsigned a0, unsigned a1, unsigned b0, unsigned b1, int vo, int so, unsigned la, auto LOc) {
      const auto s0 = __builtin_amdgcn_permlane16_swap(a0, b0, false, false), s1 = __builtin_amdgcn_permlane16_swap(a1, b1, false, false);
      const u32x4 v = {s0[0], s1[0], s0[1], s1[1]};
      __builtin_amdgcn_raw_buffer_store_b128(v, rO, vo, so, 16);
      if constexpr (decltype(LOc)::value >= 0) ds_w128<decltype(LOc)::value>(la, v);
      __builtin_amdgcn_sched_barrier(0);
      asm volatile("s_nop 1" ::: "memory");
      __builtin_amdgcn_sched_barrier(0);
    };
#define STAGE(body) _Pragma("unroll") for (int i = 0; i < 2 * NN; ++i) { const int m = MB + i / NN, n = N0 + i % NN; const f32x4 k = n == 6 ? rw6[m >> 1] : rw[m]; body; } __builtin_amdgcn_sched_barrier(0)
    auto grp = [&](auto MBc, auto N0c, auto NNc, auto&& mid, auto&& at) {
      constexpr int MB = decltype(MBc)::value, N0 = decltype(N0c)::value, NN = decltype(NNc)::value;
      f32x2 G[2 * NN], U[2 * NN], P[2 * NN];
      __builtin_amdgcn_sched_barrier(0);
      STAGE({ const int e = (n == 6 ? m & 1 : n & 1) * 2; const f32x4 x = n == 6 ? acc[6][m & 2] : acc[n & 6][m]; G[i] = ((f32x2){x[e], x[e + 1]}) - k[0] * ((f32x2){cg[n >> 1][e], cg[n >> 1][e + 1]}); });
      STAGE(P[i] = G[i] * G[i]);
      at(I0{});
      STAGE(P[i] = P[i] * k[1] + k[2]);
      STAGE(P[i] = G[i] * P[i]);
      at(I1{});
      STAGE({ const int e = (n == 6 ? m & 1 : n & 1) * 2; const f32x4 x = n == 6 ? acc[6][m | 1] : acc[n | 1][m]; U[i] = ((f32x2){x[e], x[e + 1]}) - k[0] * ((f32x2){cu[n >> 1][e], cu[n >> 1][e + 1]}); });
      STAGE(P[i] = ((f32x2){__builtin_amdgcn_exp2f(P[i][0]), __builtin_amdgcn_exp2f(P[i][1])}));
      mid();
      at(IC<2>{});
      __builtin_amdgcn_sched_barrier(0);
      STAGE(P[i] = P[i] * k[3] + k[3]);
      STAGE(P[i] = ((f32x2){__builtin_amdgcn_rcpf(P[i][0]), __builtin_amdgcn_rcpf(P[i][1])}));
      at(IC<3>{});
      STAGE(U[i] = G[i] * U[i]);
      STAGE(U[i] = U[i] * P[i]);
      STAGE({ hv[m][n] = pk_bf16(U[i][0], U[i][1]); asm volatile("" : "+v"(hv[m][n])); });
    };
#undef STAGE
    using I2 = IC<2>; using I3 = IC<3>; using I4 = IC<4>;
    const int se = (tn * 12 + wn * 4) * 256;
#define KN_PE 2
    auto none = [&](auto) {};
    auto put01 = [&]() { put(hv[0][0], hv[0][1], hv[1][0], hv[1][1], go0, se, le, I0{}); put(hv[0][2], hv[0][3], hv[1][2], hv[1][3], go0 + 512, se, le, IC<512>{}); };
    grp(I0{}, I0{}, I4{}, [&]() { if constexpr (EARLY) lds_wait8(lds0 + DONE); }, [&](auto tc) { constexpr int t = decltype(tc)::value;
      if constexpr (EARLY) { if constexpr (t >= 2) { hook(IC<3 + 2 * t>{}, dw); hook(IC<4 + 2 * t>{}, dw); } } else hook(IC<7 + t>{}, dw); });
    hook(IC<11>{}, dw);
    grp(I2{}, I0{}, I4{}, [&]() {}, [&](auto tc) { constexpr int t = decltype(tc)::value; if constexpr (t == 0) hook(IC<12>{}, dw); if constexpr (t == KN_PE) put01(); });
    put(hv[2][0], hv[2][1], hv[3][0], hv[3][1], go0, se + 2 * RBA * 256, le, IC<4096>{}); put(hv[2][2], hv[2][3], hv[3][2], hv[3][3], go0 + 512, se + 2 * RBA * 256, le, IC<4096 + 512>{});
    asm volatile("" ::: "memory");
    __builtin_amdgcn_sched_barrier(0);
    STAMP(4);
    STAMPW(11, 4);
    __builtin_amdgcn_s_setprio(EARLY ? KN_P3 : KN_P2);
#pragma unroll
    for (int q = 0; q < 2; ++q)
#pragma unroll
      for (int e = 0; e < 4; ++e) {
        const float x0 = acc[6][2 * q][e], x1 = acc[6][2 * q + 1][e];
        auto sw = __builtin_amdgcn_permlane32_swap(__float_as_uint(x0), __float_as_uint(x1), false, false);
        acc[6][2 * q][e] = __uint_as_float(sw[0]); acc[6][2 * q + 1][e] = __uint_as_float(sw[1]);
      }
    grp(I0{}, I4{}, I3{}, [&]() {}, none);
    { wait_vm<0>(); flag_set(a.sync, F_ACT + ((panel * 4 + wm) * 64 + tn * 2 + wn) * 8, tg, 1); STAMPW(6, 4); STAMP(10); }
    auto keep = [&](unsigned a0, unsigned a1, unsigned b0, unsigned b1) {
      const auto s0 = __builtin_amdgcn_permlane16_swap(a0, b0, false, false), s1 = __builtin_amdgcn_permlane16_swap(a1, b1, false, false);
      return (u32x4){s0[0], s1[0], s0[1], s1[1]}; };
    grp(I2{}, I4{}, I3{}, [&]() { late.v[0] = keep(hv[0][4], hv[0][5], hv[1][4], hv[1][5]); ds_w128<0>(lm, late.v[0]); }, none);
    late.v[1] = keep(hv[2][4], hv[2][5], hv[3][4], hv[3][5]); ds_w128<2048>(lm, late.v[1]);
    late.v[2] = keep(hv[0][6], hv[1][6], hv[2][6], hv[3][6]);
    asm volatile("" ::: "memory");
    __builtin_amdgcn_s_setprio(0);
  };

  constexpr int WR = 2;
  if (wave == 5) ds_w32(lds0 + DONE, 0);
  STAMP(9); STAMP(6);
  auto check_tile = [&](int t) {
    int lx = lane; asm volatile("" : "+v"(lx));
    const unsigned ad = lds0 + XBASE + t * XT + wave * 1024 + lx * 16;
    if (__builtin_expect(marked<8>(ad), 0)) {
      for (int tries = 0;; ++tries) {
        if (tries >= KN_TRY) wait_flags(lds0, a.sync, F_H + panel * 256, 32, 0);
#pragma unroll 1
        for (int u = t; u < t + 1 + (t == 0); ++u) { issueX(u, 0, u); issueX(u, 1, u); }
        if (t == 0) wait_vm<2 * XI>(); else wait_vm<0>();
        if (tries >= KN_TRY || !marked<8>(ad)) break;
      }
    }
  };
  auto run = [&](auto LDRc) __attribute__((always_inline)) {
    constexpr bool LDR = decltype(LDRc)::value;
    if constexpr (LDR) {
      issueX(0, 0, 0); issueX(0, 1, 0); issueX(1, 0, 1); issueX(1, 1, 1);
      issueW(2, 2 ^ WR); issueW(3, 3 ^ WR); wait_vm<2 * XI + 2 * WI>();
      check_tile(0);
    }
    lds_barrier();
    if constexpr (!LDR) { if (wave == 4) __builtin_amdgcn_raw_ptr_buffer_load_lds(RSRC(a.sync), LDSP(lds0 + TCH + 256), 4, lane * 4, F_H + panel * 256, 0, 16); }
    { const RdCtx c = mkctx(false, 0, WR * WU); static_for<0, MF + NF>([&](auto kc) { rd_item(fa, c, kc); }); }
    wait_frags(fa);
    STAMP(1);
    { const RdCtx c = mkctx(true, 0, (WR + 1) * WU); step(fa, fb, c, T{}, IC<LDR ? 2 * XI : 0>{}, [&](int i) { ldX(i % XI, 2, i / XI, 2); }); }
    int xs = 1;
    unsigned long long tb = (unsigned long long)a.PF0; unsigned tv = 0, tl = 0x7fffffffu, ts = 14336u, tmask = 0; int tj = 1;
    if constexpr (!LDR) {
      unsigned long long c0, c1; CODE_ADDR(c0, "k_all"); CODE_ADDR(c1, "k_zend");
#define KN_DJ 10
      constexpr int CTJ = 11, WJ = 1, WE = 8, DJ = KN_DJ, KT = 4;
      tj = DJ;
      if (wave == 4) { tb = c0; tl = c1 - c0 < 262144ull ? (unsigned)(c1 - c0) : 0u; ts = 0; tmask = 1u << CTJ; }
      if (wave == 5) tmask = 0xfu << DJ;
      if (wave == 6) { tb = (unsigned long long)((idx & 16) ? a.W1 : a.W0); ts = 64 * LDW * 2; tmask = ((1u << (WE - KT)) - 1u) << WJ; tj = WJ - krot - KT; }
      const int l4 = lane / 14;
      tv = wave == 4 ? (unsigned)((lane * 32 + idx) * 128)
         : (lane >= 56 ? 0x80000000u : (wave == 5 ? (unsigned)(((xcd & 3) * 32 + idx) * 57344 + lane * 128) : (unsigned)(((idx & 15) * 4 + l4) * (LDW * 2) + (xcd & 3) * 1792 + (lane - l4 * 14) * 128)));
    }
    const int js = 2 * (tm & 3) + 1;
    const auto rC = RSRC(a.cs);
    constexpr int JR = 10, JS = 2;
    const int jf = wave == 4 ? NT - 4 : (wave == 7 ? JR : 64);
    const int fof = wave == 4 ? F_COL + tn * 256 : F_ROW + panel * 256;
    const unsigned evm = tmask | (8u << js) | (wave == 4 ? 3u << (NT - 4) : 0u) | (wave == 7 ? ((5u << JS) | 2u | (11u << JR)) : 0u);
    constexpr int CFL = TCH + 256, CPB = XBASE + ((NT - 3) % 3) * XT;
    auto iter = [&](int j, auto LWc, auto LXc, auto WAITc, auto CAc, auto CBc) {
      constexpr bool LW = decltype(LWc)::value && LDR, LX = decltype(LXc)::value && LDR, LP = LW && !LX;
      if constexpr (LDR) {
        wait_vm<decltype(WAITc)::value>();
        if constexpr (LX) { if (__builtin_expect(j <= 2, 0)) check_tile(j); }
      }
      if constexpr (!LDR) {
        if (__builtin_expect((evm >> j) & 1, 0)) {
          if (j == js + 3) {
            if ((tmask >> (j - 1)) & 1) wait_vm<1>(); else wait_vm<0>();
            flag_set(a.sync, F_COL + tn * 256 + (tm * 4 + wave - 4) * 8, tag_get(lds0), 0);
          }
          if (j == 1) glu_stats_issue();
          if (j == JS && wave == 7) { wait_vm<0>(); glu_stats(); }
          if (j == JS + 2 && wave == 7) {
            if (j == js + 2) wait_vm<1>(); else wait_vm<0>();
            flag_set(a.sync, fof + tn * 8, tag_get(lds0), 0);
          }
          {
            int lx = lane; asm volatile("" : "+v"(lx));
            if (j == jf) __builtin_amdgcn_raw_ptr_buffer_load_lds(RSRC(a.sync), LDSP(lds0 + CFL + (wave & 1) * 256), 4, lx * 4, fof, 0, 16);
            if (j == jf + 1) {
              wait_vm<0>();
              int fv = ds_r32(lds0 + CFL + (wave & 1) * 256 + lx * 4);
              const Tag tg = tag_get(lds0);
              asm volatile("" : "+v"(fv));
              while (!words_up((unsigned)fv, tg) && in_time(lds0))
                asm volatile("global_load_dword %0, %1, %2 sc1\n\ts_waitcnt vmcnt(0)" : "=v"(fv) : "v"(lx * 4), "s"((const char*)a.sync + fof) : "memory");
              if (wave == 7) {
#pragma unroll
                for (int i = 0; i < 4; ++i) __builtin_amdgcn_raw_ptr_buffer_load_lds(rC, LDSP(lds0 + RING + i * 1024), 16, lx * 16, RTAB + m0 * 16 + i * 1024, 0, 16);
              }
            }
            if (j == jf + 3) wait_vm<0>();
          }
          if ((tmask >> j) & 1) { touch((const void*)tb, tv, ((j - tj) & 15) * ts, tl); if (wave == 5) touch((const void*)tb, tv, ((j - tj) & 15) * ts + 7168, tl); }
        }
        __builtin_amdgcn_sched_barrier(0);
      }
      int xp = xs + 2; if (xp >= 3) xp -= 3;
      const int ws = ((j & 1) * 2) ^ WR;
      constexpr bool LD = LDR && !LW;
      unsigned dw = 0;
      if constexpr (LD) { int lx = lane; asm volatile("" : "+v"(lx)); dw = dn_wsrc(lx, wave); }
      auto ldd = [&](int i) { static_for<0, 14>([&](auto kc) { if (i == decltype(kc)::value) hook(IC<64 + decltype(kc)::value>{}, dw); }); };
      auto lda = [&](int i) { if constexpr (LD) ldd(i); else ldW(i % WI, 2 * j + 2 + i / WI, (ws ^ 2) + i / WI); };
      auto ldb = [&](int i) {
          if constexpr (LX) ldX(i % XI, j + 2, i / XI, xp);
          else if constexpr (LD) ldd(7 + i);
          else __builtin_amdgcn_raw_ptr_buffer_load_lds(rC, LDSP(lds0 + CPB + wave * 8192 + i * 1024), 16, lane * 16, (tn * 2 + wn) * 7168 + i * 1024, 0, 16); };
      using NLA = IC<LW ? 2 * WI : (LD ? 7 : 0)>; using NLB = IC<LX ? 2 * XI : (LP || LD ? 7 : 0)>;
      { const RdCtx c = mkctx(false, xs * XT, ws * WU); stepg(CAc, fb, fa, c, IC<LDR ? 2 : 0>{}, IC<LDR ? 1 : -1>{}, [&]() { raw_barrier(); }, NLA{}, lda); }
      { const RdCtx c = mkctx(true, xs * XT, (ws + 1) * WU); stepg(CBc, fa, fb, c, I0{}, IC<LDR ? -1 : 25>{},
          [&]() { asm volatile("s_waitcnt lgkmcnt(0)" ::: "memory"); if (decltype(WAITc)::value == 0 || j + 1 < NT) raw_barrier(); }, NLB{}, ldb); }
      xs = xs + 1; if (xs >= 3) xs = 0;
    };
    using WX_ = IC<2 * XI>;
    bf16x8 pv[8];
    if constexpr (LDR) {
      for (int j = 1; j < NT - 2; ++j) iter(j, T{}, T{}, WX_{}, F{}, F{});
      iter(NT - 2, T{}, F{}, WX_{}, F{}, F{});
      __builtin_amdgcn_s_setprio(3);
      iter(NT - 1, F{}, F{}, IC<7>{}, F{}, F{});
      wait_vm<14>();
      const unsigned ca = lds0 + CPB + wave * 8192 + fq * 1792 + (fr < 14 ? fr : 0) * 16;
      static_for<0, 8>([&](auto ic) { constexpr int i = decltype(ic)::value; pv[i] = ds_b128<i * 224>(ca); });
    } else {
      int j = 1;
      if (wave == 4) {
        wait_vm<0>();
        int fv = ds_r32(lds0 + TCH + 256 + lane * 4);
        const Tag tg = tag_get(lds0);
        asm volatile("" : "+v"(fv));
        if (!words_up((unsigned)fv, tg)) wait_flags(lds0, a.sync, F_H + panel * 256, 32, 0);
      }
      raw_barrier();
#pragma unroll 1
      for (; j < js; ++j) iter(j, F{}, F{}, I0{}, F{}, F{});
      const int ja = wm == 2 ? js + 2 : js;
#pragma unroll 1
      for (; j < ja; ++j) iter(j, F{}, F{}, I0{}, T{}, F{});
#pragma unroll 1
      for (; j < js + 2; ++j) iter(j, F{}, F{}, I0{}, F{}, T{});
      { int lx = lane; asm volatile("" : "+v"(lx));
        __builtin_amdgcn_raw_buffer_store_b128(__builtin_bit_cast(u32x4, accs), rC, (lx & 15) < NF ? (unsigned)((tn * 4 + wn * 2) * 3584 + (tm * 2 + (wm & 1)) * 224
                + ((lx & 15) < 6 ? (lx & 1) * 3584 + ((lx & 15) >> 1) * 64 + (lx >> 4) * 16 : (lx >> 5) * 3584 + 192 + (lx & 16))) : 0x80000000u, 0, 16); }
#pragma unroll 1
      for (; j < NT; ++j) iter(j, F{}, F{}, I1{}, F{}, F{});
      wait_vm<0>();
      done_inc();
    }
    { const RdCtx c = mkctx(false, 0, 0); step(fb, fa, c, F{}, IC<LDR ? 2 : 0>{}, [&](int i) {
        if (i == 1) {
          asm volatile("s_waitcnt lgkmcnt(0)" ::: "memory");
#pragma unroll
          for (int k = 0; k < 8; ++k) asm volatile("" : "+v"(pv[k]));
          f32x4 cv = ((__builtin_bit_cast(f32x4, pv[0]) + __builtin_bit_cast(f32x4, pv[1])) + (__builtin_bit_cast(f32x4, pv[2]) + __builtin_bit_cast(f32x4, pv[3])))
                   + ((__builtin_bit_cast(f32x4, pv[4]) + __builtin_bit_cast(f32x4, pv[5])) + (__builtin_bit_cast(f32x4, pv[6]) + __builtin_bit_cast(f32x4, pv[7])));
#pragma unroll
          for (int k = 0; k < 4; ++k) { const float t = cv[k]; auto sw = __builtin_amdgcn_permlane16_swap(__float_as_uint(t), __float_as_uint(t), false, false); cv[k] = __uint_as_float(sw[0]) + __uint_as_float(sw[1]); }
          if ((lane & 31) < 14) *(f32x4*)(tb_cs + (lane >> 5) * 112 + wn * 56 + fr * 4) = cv;
          done_inc();
        } }); }
    if constexpr (LDR) __builtin_amdgcn_s_setprio(0);
    epilogue(LDRc);
  };
  if (wave < NLW) run(T{}); else run(F{});
  STAMP(7);
  STAMP(8);
  STAMPW(12, 4);
  STAMP(3);
#endif
}

#define KN_CT 7
#define KN_E 6
#define KN_B3 0
#define KN_WB 2
#define KN_GW 1
#define XAUXW 16
#define XAUXD 16
#define KN_WS 2
#define KN_HE 0
#define KN_FL 22
#define KN_LD 0
#define KN_DM 0
#define KN_LX 0
#define STAMPV(i) STAMP(i)
struct FragW { s16x4 lo[4], hi[4]; };
template <int MODE, int LO, int HI>
__device__ __forceinline__ void wo_phase(const GemmArgs& a, const GemmArgs& nx) {
#if defined(__HIP_DEVICE_COMPILE__)
  extern __shared__ __attribute__((aligned(16))) char smem[];
  if constexpr (MODE != 1) STAMP(0);
  const unsigned lds0 = (unsigned)(size_t)(lds_char_ptr)smem;
  const int tid = threadIdx.x, lane = tid & 63;
  const int wave = __builtin_amdgcn_readfirstlane(tid >> 6);
  const int bid = blockIdx.x, xcd = bid & 7, idx = bid >> 3;
  const int tm = (xcd >> 1) * 4 + (idx >> 3), tn = (xcd & 1) * 8 + (idx & 7);
  const int m0 = tm * 128, panel = m0 >> 8, rot = (xcd & 3) * 4;
  if constexpr (MODE == 1) {
    const auto rW = __builtin_amdgcn_make_buffer_rsrc((void*)a.W0, 0, 0x7fffffff, 0x00020000);
    const int row = wave * 8 + (lane >> 3);
    const unsigned wsrc = (unsigned)(row * D_ + tn * 64 + ((lane & 7) ^ wswz(row)) * 8) * 2u;
    static_for<LO, HI>([&](auto ic) { constexpr int i = decltype(ic)::value;
      __builtin_amdgcn_raw_ptr_buffer_load_lds(rW, (lds_void_ptr)(size_t)(lds0 + wo_pos(i >> 1) + (i & 1) * 8192 + wave * 1024), 16, wsrc, (((i & 1) * 8 + (i >> 1)) ^ rot) * (64 * D_ * 2), 0, 0); });
  } else {
    constexpr int PAD = 57344;
    const int g = wave >> 2, rw = wave & 3;
    const int fr = lane & 15, fq = lane >> 4, wq = (lane >> 2) & 3, wp = lane & 3;
    unsigned wa[4];
#pragma unroll
    for (int c = 0; c < 4; ++c) { const int row = 8 * fq + wq; wa[c] = lds0 + g * 8192 + row * 128 + (((c << 1) ^ wswz(row)) << 4) + wp * 8; }
    FragW f[3];
    auto rd = [&](auto ic, auto rc) {
      constexpr int i = decltype(ic)::value, r = decltype(rc)::value, P = wo_pos(i >> 1) + (i & 1) * 4096, B = P & ~32767, o = P & 32767;
      if constexpr (r & 1) f[i % 3].hi[r >> 1] = ds_tr<o + 512>(wa[r >> 1] + B); else f[i % 3].lo[r >> 1] = ds_tr<o>(wa[r >> 1] + B);
    };
    auto pin = [&](FragW& q) {
#pragma unroll
      for (int n = 0; n < 4; ++n) asm volatile("" : "+v"(q.lo[n]), "+v"(q.hi[n]));
    };
    static_for<0, 16>([&](auto rc) { constexpr int r = decltype(rc)::value; rd(std::integral_constant<int, r / 8>{}, std::integral_constant<int, r % 8>{}); });
    const Tag tg = tag_get(lds0);
    pin(f[0]); pin(f[1]);
    if (wave == 2) {
      unsigned long long c0, c1; CODE_ADDR(c0, "k_all"); CODE_ADDR(c1, "k_zend");
      __builtin_amdgcn_raw_ptr_buffer_load_lds(RSRCN(c0, c1 - c0 < 262144ull ? (unsigned)(c1 - c0) : 0u), LDSP(lds0 + PAD), 4, (unsigned)((lane * 32 + idx) * 128), 0, 0, 0); }
    unsigned sdum = 0;
    if constexpr (KN_SPF) {
      const char* sp = (const char*)(wave < 4 ? nx.W0 : nx.W1) + (size_t)((((wave >> 1) & 1) * 512 + bid) * (DF_ * 2) + (wave & 1) * DF_);
#define SL4(o) "s_load_dword %0, %1, " #o "\n\ts_load_dword %0, %1, " #o "+128\n\ts_load_dword %0, %1, " #o "+256\n\ts_load_dword %0, %1, " #o "+384\n\t"
      asm volatile(SL4(0) SL4(512) SL4(1024) SL4(1536) SL4(2048) SL4(2560) SL4(3072) : "=&s"(sdum) : "s"(sp));
    }
    if (wave == 3) {
      const unsigned fo = (unsigned)(((lane & 32) ? (xcd >> 2) * 4 + (idx >> 3) : panel) * 256 + (lane & 31) * 8);
      while (!flags_up(__builtin_amdgcn_raw_buffer_load_b64(RSRC(a.sync), fo, F_MARK, 16), 64, tg, 0) && in_time(lds0)) {}
    }
    const auto rX = __builtin_amdgcn_make_buffer_rsrc((void*)a.X, 0, 0x7fffffff, 0x00020000);
    const int rb = (m0 >> 4) + rw * 2 + g;
    unsigned xs[2] = {(unsigned)(rb * RBO * 256 + lane * 16), (unsigned)((rb ^ 1) * RBO * 256 + lane * 16)};
    const int ck = (fq & 1) * 2 + (fq >> 1);
    unsigned orow = (unsigned)((rb * 16 + fr) * (D_ * 2) + tn * 128 + ck * 16), ofrg = (unsigned)((rb * RBH + tn * 8 + ck) * 256 + fr * 16);
    asm volatile("" : "+v"(xs[0]), "+v"(xs[1]), "+v"(orow), "+v"(ofrg));
    STAMP(9);
    const int fo_off = F_O(tm) + rw * 256 + ((g ^ (rot >> 3)) & 1) * 128;
    const unsigned first = (rot & 4) ? 0xff00u : 0x00ffu;
    auto down = [&](u32x2 v) { return (unsigned)__builtin_amdgcn_ballot_w64(v[0] != tg.lo || v[1] != tg.hi) & 0xffffu; };
    unsigned dn, polls = 0;
    do { dn = down(flag_get(a.sync, fo_off, 16)); asm volatile("" ::: "memory"); } while ((dn & first) && ((++polls & 1023u) || in_time(lds0)));
    STAMP(6);
    u32x2 fl = {0u, 0u};
    u32x4 xr[16][2], res[2];
#define KN_XD 4
    auto ldX = [&](int i, int m) {
      if (i < 16) xr[i][m] = __builtin_amdgcn_raw_buffer_load_b128(rX, xs[m] + (i & 1) * 1024, ((g * 8 + (i >> 1)) ^ rot) * 2048, XAUXW);
      else if (i == 16) res[m] = __builtin_amdgcn_raw_buffer_load_b128(__builtin_amdgcn_make_buffer_rsrc((void*)a.R, 0, 0x7fffffff, 0x00020000), orow + m * 64, 0, 0);
    };
#pragma unroll
    for (int i = 0; i < KN_XD; ++i) { ldX(i, 0); ldX(i, 1); __builtin_amdgcn_sched_barrier(0); }
    unsigned gsrc[2];
#pragma unroll
    for (int i = 0; i < 2; ++i) {
      const int j = wave + 8 * i, jj = j >= 7 ? j - 7 : j;
      const int b = jj * 1024 + lane * 16, q = b / 224, pos = (b % 224) >> 4;
      gsrc[i] = (unsigned)((8 * ((q & 15) >> 2) + (q & 3) + 4 * (q >> 4)) * DF_ + ((xcd & 3) * 8 + (idx & 7)) * 112 + pos * 8) * 2u;
      asm volatile("" : "+v"(gsrc[i]));
    }
    f32x4 acc[4][2];
#pragma unroll
    for (int n = 0; n < 4; ++n) { acc[n][0] = (f32x4){0.f, 0.f, 0.f, 0.f}; acc[n][1] = acc[n][0]; }
    __builtin_amdgcn_sched_barrier(0);
    static_for<0, 16>([&](auto ic) {
      constexpr int i = decltype(ic)::value;
      if constexpr (i >= 2) { asm volatile("s_waitcnt lgkmcnt(%0)" :: "n"(i < 15 ? 8 : 0) : "memory"); pin(f[i % 3]); }
      if constexpr (i == (KN_WE > 4 ? (KN_WE & ~1) - 2 : 0)) { if constexpr (i == 0) wait_vm<2 * KN_XD>(); raw_barrier(); }
      if constexpr (i == 8 - KN_XD - 2) fl = flag_get(a.sync, fo_off, 16);
      if constexpr (i == 8 - KN_XD) { if (__builtin_expect(dn && down(fl), 0)) wait_flags(lds0, a.sync, fo_off, 16, 0); }
      static_for<0, 8>([&](auto jc) {
        constexpr int j = decltype(jc)::value, n = j >> 1, m = j & 1;
        const bf16x8 w = __builtin_shufflevector(f[i % 3].lo[n], f[i % 3].hi[n], 0, 1, 2, 3, 4, 5, 6, 7);
        acc[n][m] = __builtin_amdgcn_mfma_f32_16x16x32_bf16(w, __builtin_bit_cast(bf16x8, xr[i][m]), acc[n][m], 0, 0, 0);
        if constexpr (i + 2 < 16) rd(std::integral_constant<int, i + 2>{}, jc);
        if constexpr (j == 1 || j == 5) ldX(i + KN_XD, j >> 2);
        __builtin_amdgcn_sched_barrier(0);
      });
      if constexpr (i == 0) STAMP(1);
    });
    STAMP(2);
    auto slot = [&](int w) { return (w < 6 ? 32768 : WOB - 6 * 4096) + w * 4096 + lane * 16; };
#pragma unroll
    for (int n = 0; n < 4; ++n) *(f32x4*)(smem + slot(wave) + n * 1024) = acc[n][1];
    asm volatile("" : "+v"(res[0]), "+v"(res[1]));
    lds_barrier();
    asm volatile("" :: "s"(sdum));
    STAMP(4);
    f32x4 pa[4];
    static_for<0, 4>([&](auto nc) { constexpr int n = decltype(nc)::value; pa[n] = __builtin_bit_cast(f32x4, ds_b128<n * 1024>(lds0 + slot(wave ^ 4))); });
    if constexpr (KN_GW) {
      auto ldG = [&](int i, int u) { const int j = wave + 8 * i;
        __builtin_amdgcn_raw_ptr_buffer_load_lds(__builtin_amdgcn_make_buffer_rsrc((void*)(j >= 7 ? nx.W1 : nx.W0), 0, 0x7fffffff, 0x00020000),
                          (lds_void_ptr)(size_t)(lds0 + 98304 + (u ^ 2) * 14592 + (j >= 7 ? 7184 + (j - 7) * 1024 : j * 1024)), 16, gsrc[i], (u + (xcd >> 2) * 16) * 32 * DF_ * 2, 0, 0); };
      ldG(0, 0); ldG(0, 1);
      if (wave < 6) { ldG(1, 0); ldG(1, 1); }
    }
    asm volatile("s_waitcnt lgkmcnt(0)" : "+v"(pa[0]), "+v"(pa[1]), "+v"(pa[2]), "+v"(pa[3]) :: "memory");
    const auto rO = __builtin_amdgcn_make_buffer_rsrc((void*)a.O, 0, 0x7fffffff, 0x00020000);
    u32x4 o[2];
#pragma unroll
    for (int i = 0; i < 2; ++i) {
      const f32x4 u = acc[2 * i][0] + pa[2 * i], v = acc[2 * i + 1][0] + pa[2 * i + 1];
      const auto s0 = __builtin_amdgcn_permlane16_swap(pk_bf16(u[0], u[1]), pk_bf16(v[0], v[1]), false, false);
      const auto s1 = __builtin_amdgcn_permlane16_swap(pk_bf16(u[2], u[3]), pk_bf16(v[2], v[3]), false, false);
      const u32x4 q4 = {s0[0], s1[0], s0[1], s1[1]};
#pragma unroll
      for (int j = 0; j < 4; ++j) { const unsigned r = res[i][j], q = q4[j]; o[i][j] = pk_bf16(bflo(r) + bflo(q), bfhi(r) + bfhi(q)); }
      __builtin_amdgcn_raw_buffer_store_b128(o[i], rO, ofrg + i * 1024, 0, 16);
    }
    auto sum4 = [](float v) {
      auto p = __builtin_amdgcn_permlane16_swap(__float_as_uint(v), __float_as_uint(v), false, false); v = __uint_as_float(p[0]) + __uint_as_float(p[1]);
      auto q = __builtin_amdgcn_permlane32_swap(__float_as_uint(v), __float_as_uint(v), false, false); return __uint_as_float(q[0]) + __uint_as_float(q[1]); };
    f32x2 e[8], sm = {0.f, 0.f};
#pragma unroll
    for (int j = 0; j < 8; ++j) { const unsigned q = o[j >> 2][j & 3]; e[j] = (f32x2){bflo(q), bfhi(q)}; sm += e[j]; }
    const float mu = sum4(sm[0] + sm[1]) * (1.f / 64.f);
    f32x2 t2 = {0.f, 0.f};
#pragma unroll
    for (int j = 0; j < 8; ++j) { const f32x2 d = e[j] - mu; t2 += d * d; }
    const float q2 = sum4(t2[0] + t2[1]);
    __builtin_amdgcn_raw_buffer_store_b64((u32x2){__float_as_uint(mu), __float_as_uint(q2)}, __builtin_amdgcn_make_buffer_rsrc((void*)a.st, 0, 0x7fffffff, 0x00020000),
                                          lane < 16 ? (unsigned)((tn * S_ + rb * 16 + lane) * 8) : 0x80000000u, 0, 16);
    STAMP(7);
    STAMP(8);
    asm volatile("s_waitcnt vmcnt(0) lgkmcnt(0)" ::: "memory");
    raw_barrier();
    if (wave == 0) flag_set(a.sync, F_H + panel * 256 + ((tm & 1) * 16 + tn) * 8, tg, 0);
    STAMP(3);
  }
#endif
}

struct FragD { s16x4 lo[8], hi[8]; };
enum { O_X, O_W, O_SL, O_SP, O_FA, O_FP, O_QC, O_QD, O_QP, O_RES, O_LP, O_CE, O_CC, O_CD, O_CP, O_BAR };
constexpr int DN_A = 16, DN_PRE = 13;
constexpr unsigned char DN_OPS[][4] = {
 {1,0,O_CE,2}, {1,0,O_QC,0}, {1,2,O_X,6}, {1,5,O_X,7}, {1,9,O_X,8}, {1,13,O_X,9}, {2,2,O_X,10}, {2,5,O_X,11}, {2,9,O_X,12}, {2,13,O_SL,0},
 {3,0,O_CE,7}, {3,2,O_X,13}, {3,5,O_X,14}, {3,9,O_X,15}, {3,13,O_SL,1}, {4,2,O_BAR,12}, {4,3,O_X,16}, {4,6,O_X,17}, {4,10,O_X,18}, {4,13,O_SL,2},
 {5,2,O_X,19}, {5,5,O_X,20}, {5,9,O_W,14}, {5,13,O_W,13}, {6,2,O_X,21}, {6,5,O_X,22}, {6,9,O_W,16}, {6,13,O_W,15}, {7,0,O_FA,0}, {7,2,O_X,23}, {7,5,O_X,24}, {7,9,O_X,25}, {7,13,O_W,17},
 {8,2,O_X,26}, {8,5,O_X,27}, {8,9,O_X,28}, {8,13,O_W,18}, {9,2,O_X,29}, {9,5,O_X,30}, {9,9,O_X,31}, {9,13,O_W,19}, {9,16,O_QC,0}, {10,2,O_X,32}, {10,7,O_X,33}, {10,12,O_W,20},
 {11,0,O_CC,0}, {11,2,O_X,34}, {11,5,O_X,35}, {11,9,O_X,36}, {11,13,O_W,21}, {12,2,O_BAR,17}, {12,3,O_X,37}, {12,6,O_X,38}, {12,10,O_X,39}, {12,13,O_W,22},
 {13,2,O_X,40}, {13,5,O_X,41}, {13,9,O_X,42}, {13,13,O_W,23}, {14,2,O_X,43}, {14,5,O_X,44}, {14,9,O_X,45}, {14,13,O_W,24}, {14,16,O_QD,0},
 {15,2,O_X,46}, {15,7,O_X,47}, {15,12,O_W,25}, {16,0,O_CD,0}, {16,2,O_BAR,23}, {16,3,O_X,48}, {16,6,O_X,49}, {16,10,O_X,50}, {16,13,O_W,26},
 {17,2,O_X,51}, {17,5,O_X,52}, {17,9,O_X,53}, {17,13,O_W,27}, {18,2,O_X,54}, {18,5,O_X,55}, {18,9,O_W,28}, {18,13,O_RES,0}, {19,2,O_W,30}, {19,9,O_W,29},
 {20,2,O_W,31}, {20,5,O_SP,0}, {20,9,O_SP,1}, {20,13,O_RES,1}, {21,2,O_X,56}, {21,5,O_X,57}, {21,9,O_SP,2}, {21,13,O_SP,3}, {22,2,O_X,58}, {22,5,O_X,59}, {22,9,O_X,60}, {22,13,O_X,61},
 {23,2,O_BAR,31}, {23,3,O_X,62}, {23,10,O_X,63}, {24,0,O_FP,0}, {24,0,O_FP,1}, {24,2,O_SP,4}, {24,5,O_SP,5}, {26,0,O_FP,2}, {27,8,O_QP,2},
 {30,0,O_CP,0}, {30,0,O_CP,1}, {30,2,O_LP,0}, {30,3,O_LP,1}, {30,5,O_LP,2}, {30,7,O_LP,3}, {31,0,O_CP,2}, {31,2,O_LP,4}, {31,5,O_LP,5},
};
constexpr int DN_N = sizeof(DN_OPS) / 4, DN_H = (24 - DN_A) / 2, DN_B = DN_A + DN_H, NS = 32;
constexpr int dn_nm(int v) { return v < 24 ? 16 : 8; }
constexpr int dn_unit(int v, int n) { return (v < DN_A || v >= 24) ? v : DN_A + 2 * ((v - DN_A) % DN_H) + (n >> 2); }
constexpr int dn_wpos(int u, int h) { constexpr int P[19] = {8, 9, 10, 11, 16, 17, 18, 6, 7, 12, 13, 14, 15, 0, 1, 2, 3, 4, 5}; return P[u % 19] * 8192 + h * 4096; }
constexpr int dn_first(int v, int g) { int i = 0; while (i < DN_N && (DN_OPS[i][0] < v || (DN_OPS[i][0] == v && DN_OPS[i][1] < g))) ++i; return i; }
constexpr int dn_find(int kind, int arg) { for (int i = 0; i < DN_N; ++i) if (DN_OPS[i][2] == kind && DN_OPS[i][3] == arg) return i; return -1; }
constexpr int dn_vm(int lo, int hi) { int c = 0; for (int i = lo; i < hi; ++i) c += DN_OPS[i][2] <= O_LP; return c; }
constexpr bool dn_bar(int v) { for (int i = 0; i < DN_N; ++i) if (DN_OPS[i][0] == v && DN_OPS[i][2] == O_BAR) return true; return false; }
constexpr int dn_cnt(int w, int i) { int p = -1; for (int u = DN_PRE; u <= w; ++u) if (dn_find(O_W, u) > p) p = dn_find(O_W, u); return p >= 0 ? dn_vm(p + 1, i) : 63; }
constexpr bool dn_ok() {
  int have = DN_PRE - 1, bstep = 0;
  for (int v = 0; v + 1 < NS; ++v) {
    for (int i = dn_first(v, 0); i < dn_first(v + 1, 0); ++i) {
      if (DN_OPS[i][2] == O_BAR) { if (DN_OPS[i][3] > have) have = DN_OPS[i][3]; bstep = v; for (int u = DN_PRE; u <= have; ++u) if (dn_find(O_W, u) < 0 || dn_find(O_W, u) > i) return false; }
      if (DN_OPS[i][2] == O_W) {
        if (bstep < 1) return false;
        for (int t = bstep + 1; t < NS; ++t) for (int n = 0; n < 8; n += 4) if (dn_unit(t, n) < DN_OPS[i][3] && dn_wpos(dn_unit(t, n), 0) == dn_wpos(DN_OPS[i][3], 0)) return false;
      }
    }
    if (dn_unit(v + 1, 7) > have) return false;
  }
  for (int x = 6; x < 64; ++x) {
    int n = 0; for (int i = 0; i < dn_find(O_X, x); ++i) if (DN_OPS[i][2] == O_CE) n = DN_OPS[i][3];
    if (x < 34 ? n < (x - 2) / 4 : dn_find(O_X, x) < dn_find(x < 48 ? O_CC : O_CD, 0)) return false;
  }
  return dn_find(O_SP, 0) >= dn_first(DN_B, 0) && dn_find(O_SP, 4) >= dn_first(24, 0);
}
static_assert(dn_ok(), "schedule");
constexpr int dn_rg(int v, int r) { const int r0 = dn_bar(v) ? 2 : 1; return r0 + r * (dn_nm(v) - r0 + 1) / 16; }
constexpr int dn_rd(int v, int I) { int c = 0; if (v + 1 < NS) for (int r = 0; r < 16; ++r) c += dn_rg(v, r) <= I; return c; }
#define KN_SLP 5
#define KN_PRI 1
template <int MODE, int U>
__device__ __forceinline__ void dn_phase(const GemmArgs& a, const Late& late, unsigned wsrc = 0) {
#if defined(__HIP_DEVICE_COMPILE__)
  extern __shared__ __attribute__((aligned(16))) char smem[];
  if constexpr (MODE != 1) STAMP(0);
  const unsigned lds0 = (unsigned)(size_t)(lds_char_ptr)smem;
  const int tid = threadIdx.x;
  int lane = tid & 63; asm volatile("" : "+v"(lane));
  const int wave = __builtin_amdgcn_readfirstlane(tid >> 6);
  const int bid = blockIdx.x, xcd = bid & 7, idx = bid >> 3;
  const int panel = (xcd >> 2) * 4 + (idx >> 3), c = idx & 7, q = xcd & 3;
  const int g4 = wave & 3, hh = U >= 64 ? U & 1 : wave >> 2;
#define KN_ABL 0
  const auto rW = RSRCN(a.W0, (KN_ABL & 1) ? 0 : 0x7fffffff);
  if constexpr (MODE != 1) wsrc = dn_wsrc(lane, wave);
  auto uj = [&](int v) { return q * 8 + ((c + (v < 3 ? 0 : (v < 17 ? (v - 1) >> 1 : v - 16))) & 7); };
  auto us = [](int v) { return v < 3 ? v : (v < 17 ? (v - 1) & 1 : 2); };
  auto ldW = [&](int v) {
    const int row = v < 24 ? uj(v) * 112 + (us(v) < 2 ? us(v) * 56 + g4 * 8 : 32 + (g4 & 1) * 8 + (g4 >> 1) * 56) : ((((v + c) & 7) * 2 + hh) * 4 + g4) * 56 + 48;
    __builtin_amdgcn_raw_ptr_buffer_load_lds(rW, LDSP(lds0 + dn_wpos(v, 0) + hh * 4096 + g4 * 1024), 16, wsrc, row * (D_ * 2) + c * 256 + (((v < 24 ? hh : 0) ^ (q >> 1)) << 7), 0, 0);
  };
  const int fa_off = F_ACT + (panel * 4 + (wave >> 1)) * 512;
  if constexpr (MODE == 1) {
    if constexpr (U < 32) { if constexpr (U < DN_PRE) ldW(U); }
    else if constexpr (U >= 64) ldW((U - 64) >> 1);
    return;
  }
  const Tag tg = tag_get(lds0);
  const int fr = lane & 15, fq = lane >> 4, wq = (lane >> 2) & 3, wp = lane & 3;
  const int rb = wave * 2 + (q & 1);
  f32x4 acc[8][2];
#pragma unroll
  for (int n = 0; n < 8; ++n) { acc[n][0] = (f32x4){0.f, 0.f, 0.f, 0.f}; acc[n][1] = acc[n][0]; }
  unsigned wa[3][4];
#pragma unroll
  for (int i = 0; i < 4; ++i) { const int row = 8 * fq + wq; wa[0][i] = lds0 + row * 128 + (((i << 1) ^ wswz(row)) << 4) + wp * 8; wa[1][i] = wa[0][i] + 65536; wa[2][i] = wa[0][i] + 131072; }
  const auto rX = RSRCN(a.X, (KN_ABL & 2) ? 0 : 0x7fffffff);
  const unsigned xs[2] = {(unsigned)((panel * 16 + rb) * RBA * 256 + lane * 16), (unsigned)((panel * 16 + (rb ^ 1)) * RBA * 256 + lane * 16)};
  FragD fa, fb;
  u32x4 xr[32][2];
  auto rdw = [&](FragD& f, auto vc, auto rc) {
    constexpr int v = decltype(vc)::value, r = decltype(rc)::value, n = r >> 1, u = dn_unit(v, n);
    constexpr int p = dn_wpos(u, (v < DN_A || v >= 24) ? n >> 2 : (v < DN_B ? 1 : 0)), o = (p & 65535) + (r & 1) * 512, b = p >> 16;
    if constexpr (r & 1) f.hi[n] = ds_tr<o>(wa[b][n & 3]); else f.lo[n] = ds_tr<o>(wa[b][n & 3]);
  };
  auto ldX = [&](int v, int m) {
    if (v < 24) xr[v][m] = __builtin_amdgcn_raw_buffer_load_b128(rX, xs[m], uj(v) * 3072 + us(v) * 1024, 16);
    else xr[v][m] = __builtin_amdgcn_raw_buffer_load_b128(rX, xs[0] + m * 1024, 384 * 256 + ((v + c) & 7) * 2048, 16);
  };
  const auto rS = RSRC(a.slab);
  const int ck = (fq & 1) * 2 + (fq >> 1);
  u32x4 pp[3][2] = {}, res[2];
  auto ldP = [&](int j, bool ok) { pp[j >> 1][j & 1] = __builtin_amdgcn_raw_buffer_load_b128(RSRCN(a.slab, ok ? 0x7fffffff : 0), lane * 16 + (j & 1) * 1024, ((bid * 8 + wave) * 3 + (j >> 1)) * 2048, 16); };
  auto st128 = [&](u32x4 v, auto rs, unsigned vo, int so) {
    __builtin_amdgcn_raw_buffer_store_b128(v, rs, vo, so, 16);
    __builtin_amdgcn_sched_barrier(0);
    asm volatile("s_nop 1" ::: "memory");
    __builtin_amdgcn_sched_barrier(0); };
  auto stP = [&](int j) {
    const int k = 3 - (j >> 1), i = j & 1;
    const f32x4 s0 = acc[(k >> 1) * 4 + 2 * i][k & 1], s1 = acc[(k >> 1) * 4 + 2 * i + 1][k & 1];
    st128((u32x4){pk_bf16(s0[0], s0[1]), pk_bf16(s0[2], s0[3]), pk_bf16(s1[0], s1[1]), pk_bf16(s1[2], s1[3])}, rS, lane * 16 + i * 1024, (((bid ^ k) * 8 + wave) * 3 + (j >> 1)) * 2048); };
  auto stL = [&](int i) {
    const int so = (panel * 16 + (wave >> 1) * 4 + (i == 1 ? 2 : 0)) * RBA * 256 + (i < 2 ? (q * 8 + c) * 12 + 8 + (wave & 1) * 2 : 384 + (q * 8 + c) * 2 + (wave & 1)) * 256;
    st128(late.v[i], RSRC(a.X), (unsigned)((i < 2 ? (fq & 1) * RBA + (fq >> 1) : ck * RBA) * 256 + fr * 16), so); };
  const int fp_off = F_PART + (bid * 8 + wave) * 64;
  unsigned got = 0;
  u32x2 fl = {0u, 0u};
  auto rot16 = [&](int n) { const unsigned m = ((1u << (2 * n)) - 1u) << (2 * c + 2); return (unsigned long long)((m | (m >> 16)) & 0xffffu); };
#define TS5(i)
#define KN_Q0 0
  if constexpr (KN_Q0) fl = flag_get(a.sync, fa_off + q * 128, 16);
  STAMP(9);
  lds_barrier();
  STAMP(6);
  if constexpr (!KN_Q0) fl = flag_get(a.sync, fa_off + q * 128, 16);
  {
    const unsigned e0 = lds0 + OWNE + rb * 2048 + lane * 16, e1 = lds0 + OWNE + (rb ^ 1) * 2048 + lane * 16, m0 = lds0 + OWNM + rb * 1024 + lane * 16, m1 = lds0 + OWNM + (rb ^ 1) * 1024 + lane * 16;
    xr[0][0] = __builtin_bit_cast(u32x4, ds_b128<0>(e0)); xr[0][1] = __builtin_bit_cast(u32x4, ds_b128<0>(e1));
    xr[1][0] = __builtin_bit_cast(u32x4, ds_b128<1024>(e0)); xr[1][1] = __builtin_bit_cast(u32x4, ds_b128<1024>(e1));
    xr[2][0] = __builtin_bit_cast(u32x4, ds_b128<0>(m0)); xr[2][1] = __builtin_bit_cast(u32x4, ds_b128<0>(m1));
    static_for<0, 16>([&](auto rc) { rdw(fa, IC<0>{}, rc); });
  }
  auto op = [&](auto ic) {
    constexpr int i = decltype(ic)::value, kind = DN_OPS[i][2], arg = DN_OPS[i][3];
    if constexpr (kind == O_X) ldX(arg >> 1, arg & 1);
    if constexpr (kind == O_W) ldW(arg);
    if constexpr (kind == O_SL) stL(arg);
    if constexpr (kind == O_SP) stP(arg);
    if constexpr (kind == O_FA) { wait_vm<dn_vm(dn_find(O_SL, 2) + 1, i)>(); flag_set(a.sync, F_ACT + ((panel * 4 + (wave >> 1)) * 64 + (q * 8 + c) * 2 + (wave & 1)) * 8, tg, 2); }
    if constexpr (kind == O_FP) { wait_vm<dn_vm(dn_find(O_SP, 2 * arg + 1) + 1, i)>(); if constexpr (arg) STAMP(10 + arg); flag_set(a.sync, F_PART + ((bid ^ (3 - arg)) * 8 + wave) * 64 + arg * 8, tg, 0); }
    if constexpr (kind == O_QC) fl = flag_get(a.sync, fa_off + q * 128, 16);
    if constexpr (kind == O_QD) fl = flag_get(a.sync, fa_off, 64);
    if constexpr (kind == O_QP) fl = flag_get(a.sync, fp_off, 3);
    if constexpr (kind == O_RES) res[arg] = __builtin_amdgcn_raw_buffer_load_b128(RSRC(a.R), (unsigned)(((panel * 16 + rb) * RBH + c * 16 + (q >> 1) * 8 + ck) * 256 + fr * 16), arg * 1024, 16);
    if constexpr (kind == O_LP) ldP(arg, (got >> (arg >> 1)) & 1);
    if constexpr (kind == O_CE) {
      const unsigned long long need = rot16(arg);
      auto up = [&]() { return (__builtin_amdgcn_ballot_w64(fl[0] - tg.lo - 1u > 6u || fl[1] != tg.hi) & need) == 0; };
      if (__builtin_expect(!up(), 0)) { do fl = flag_get(a.sync, fa_off + q * 128, 16); while (!up() && in_time(lds0)); }
      STAMP(4);
    }
    if constexpr (kind == O_CC) { if (__builtin_expect(!flags_up(fl, 16, tg, 2), 0)) wait_flags(lds0, a.sync, fa_off + q * 128, 16, 2); }
    if constexpr (kind == O_CD) { if (__builtin_expect(!flags_up(fl, 64, tg, 2), 0)) wait_flags(lds0, a.sync, fa_off, 64, 2); }
    if constexpr (kind == O_CP) got |= (unsigned)__builtin_amdgcn_ballot_w64(fl[0] == tg.lo && fl[1] == tg.hi) & (1u << arg);
    if constexpr (kind == O_BAR) { if constexpr (!(KN_ABL & 4)) { wait_vm<dn_cnt(arg, i)>(); lds_barrier(); } }
    __builtin_amdgcn_sched_barrier(0);
  };
  static_for<0, NS>([&](auto vc) {
    constexpr int v = decltype(vc)::value, NM = dn_nm(v), NRD = v + 1 < NS ? 16 : 0;
    FragD& cur = (v & 1) ? fb : fa; FragD& nxt = (v & 1) ? fa : fb;
    TS5(v);
    if constexpr (KN_PRI == 1) __builtin_amdgcn_s_setprio(3 - (v & 3));
    if constexpr (KN_PRI == 2) { if (wave >= 4) __builtin_amdgcn_s_setprio(v & 1); else __builtin_amdgcn_s_setprio(1 - (v & 1)); }
    if constexpr (KN_PRI == 3) { if constexpr (!(v & 1)) __builtin_amdgcn_s_setprio(3 - ((v >> 1) & 3)); }
    static_for<dn_first(v, 0), dn_first(v, 1)>(op);
    __builtin_amdgcn_sched_barrier(0);
    static_for<0, NM>([&](auto ic) {
      constexpr int I = decltype(ic)::value, n = NM == 16 ? I >> 1 : I, m = NM == 16 ? I & 1 : I >> 2;
      if constexpr (I % (NM / 4) == 0) {
        constexpr int left = 12 - 2 * n + dn_rd(v, I);
        asm volatile("s_waitcnt lgkmcnt(%0)" :: "n"(left < 15 ? left : 15) : "memory");
        asm volatile("" : "+v"(cur.lo[n]), "+v"(cur.hi[n]), "+v"(cur.lo[n + 1]), "+v"(cur.hi[n + 1]));
        if constexpr (v == 0 && I == 0) {
#pragma unroll
          for (int i = 0; i < 3; ++i) asm volatile("" : "+v"(xr[i][0]), "+v"(xr[i][1]));
          STAMP(1);
        }
        __builtin_amdgcn_sched_barrier(0);
      }
      const bf16x8 w = __builtin_shufflevector(cur.lo[n], cur.hi[n], 0, 1, 2, 3, 4, 5, 6, 7);
      constexpr int an = v < DN_A ? n : (v < DN_B ? 4 : 0) + (n & 3), am = v < 24 ? m : 0;
      acc[an][am] = MFMA(w, __builtin_bit_cast(bf16x8, xr[dn_unit(v, n)][m]), acc[an][am]);
      __builtin_amdgcn_sched_barrier(0);
      static_for<dn_first(v, I + 1), dn_first(v, I + 2)>(op);
      static_for<0, NRD>([&](auto rc) { constexpr int r = decltype(rc)::value; if constexpr (dn_rg(v, r) == I + 1) rdw(nxt, IC<v + 1>{}, rc); });
      __builtin_amdgcn_sched_barrier(0);
    });
    if constexpr (v == 23) STAMP(10);
  });
  STAMP(2);
  TS5(NS);
  static_for<dn_first(NS, 0), DN_N>(op);
  if (got != 7u) {
    for (;;) {
      const u32x2 f0 = flag_get(a.sync, fp_off, 3);
      __builtin_amdgcn_s_sleep(KN_SLP);
      const u32x2 f1 = flag_get(a.sync, fp_off, 3);
      if (flags_up(f0, 3, tg, 0)) break;
      if (flags_up(f1, 3, tg, 0) || !in_time(lds0)) break;
    }
    u32x4 pe[6];
#pragma unroll
    for (int j = 5; j >= 0; --j) { pe[j] = pp[j >> 1][j & 1]; ldP(j, !((got >> (j >> 1)) & 1)); }
#pragma unroll
    for (int j = 5; j >= 0; --j) pp[j >> 1][j & 1] |= pe[j];
  }
  TS5(NS + 1);
  STAMP(5);
  const auto rO = RSRC(a.O);
  const unsigned orow = (unsigned)(((panel * 16 + rb) * 16 + fr) * (D_ * 2) + c * 256 + (q >> 1) * 128 + ck * 16);
#define KN_OAUX 0
#pragma unroll
  for (int i = 0; i < 2; ++i) {
    f32x4 u = acc[2 * i][0], w = acc[2 * i + 1][0];
#pragma unroll
    for (int k = 0; k < 3; ++k) {
      const unsigned q0 = pp[k][i][0], q1 = pp[k][i][1], q2 = pp[k][i][2], q3 = pp[k][i][3];
      u += (f32x4){bflo(q0), bfhi(q0), bflo(q1), bfhi(q1)}; w += (f32x4){bflo(q2), bfhi(q2), bflo(q3), bfhi(q3)};
    }
    const auto s0 = __builtin_amdgcn_permlane16_swap(pk_bf16(u[0], u[1]), pk_bf16(w[0], w[1]), false, false);
    const auto s1 = __builtin_amdgcn_permlane16_swap(pk_bf16(u[2], u[3]), pk_bf16(w[2], w[3]), false, false);
    const u32x4 q4 = {s0[0], s1[0], s0[1], s1[1]};
    u32x4 o;
#pragma unroll
    for (int j = 0; j < 4; ++j) { const unsigned r = res[i][j], x = q4[j]; o[j] = pk_bf16(bflo(r) + bflo(x), bfhi(r) + bfhi(x)); }
    __builtin_amdgcn_raw_buffer_store_b128(o, rO, orow + i * 64, 0, KN_OAUX);
  }
  if (bid == 0 && wave == 0) flag_set(a.sync, EPOCH, tg, 0);
  STAMP(7);
  STAMP(8);
  STAMP(3);
#endif
}

struct Ptrs { const bf16_t *x, *wqkv, *wo, *wg, *wu, *wd; bf16_t* out; char* ws; };
#define DBG(i)
#define WS_O ((bf16_t*)(p.ws + (8u << 20)))
#define WS_SL ((float*)(p.ws + (42u << 20)))
#define WS_H ((bf16_t*)(p.ws + (16u << 20)))
#define WS_ST ((float*)(p.ws + (24u << 20)))
#define WS_CS ((float*)(p.ws + (25u << 20)))
#define WS_SY ((unsigned*)(p.ws + (26u << 20)))
#define WS_A ((bf16_t*)(p.ws + (28u << 20)))
#if defined(__HIP_DEVICE_COMPILE__)
#define ARG_WARM(v) do { asm volatile("s_load_dword %0, %1, 0x0" : "=s"(v) : "s"(__builtin_amdgcn_kernarg_segment_ptr())); __builtin_amdgcn_sched_barrier(0); } while (0)
#else
#define ARG_WARM(v)
#endif
struct Args4 { GemmArgs q, b, c, d; };
extern "C" __global__ __launch_bounds__(512, 1) void k_all(Ptrs p) {
  unsigned warm = 0;
  ARG_WARM(warm);
#define PAD(k)
  const Args4 a{GemmArgs{p.x, p.wqkv, nullptr, nullptr, WS_O, (float*)WS_H, p.wg, p.wu, WS_CS, p.wd, p.wo, WS_SY, (float*)WS_A, warm DBG(0)},
    GemmArgs{WS_O, p.wo, nullptr, p.x, WS_H, WS_ST, nullptr, nullptr, nullptr, p.wd, nullptr, WS_SY, nullptr, 0u DBG(1)},
    GemmArgs{WS_H, p.wg, p.wu, p.out, WS_A, WS_ST, nullptr, nullptr, WS_CS, p.wd, nullptr, WS_SY, WS_SL, 0u DBG(2)},
    GemmArgs{WS_A, p.wd, nullptr, WS_H, p.out, nullptr, nullptr, nullptr, nullptr, nullptr, nullptr, WS_SY, WS_SL, 0u DBG(3)}};
  k1_phase(a.q, [&](auto lo, auto hi) { wo_phase<1, decltype(lo)::value, decltype(hi)::value>(a.b, a.c); });
  PAD(1);
  wo_phase<2, 0, 0>(a.b, a.c);
  PAD(2);
  Late late;
  glu_phase(a.c, [&](auto tc, unsigned w) { dn_phase<1, decltype(tc)::value>(a.d, late, w); }, late);
  PAD(3);
  dn_phase<2, 0>(a.d, late);
  PAD(4);
}

extern "C" __global__ void k_zend() {}

extern "C" void kernel_launch(void* const* d_in, const int* in_sizes, int n_in, void* d_out, int out_size, void* d_ws,
                              size_t ws_size, hipStream_t stream) {
  const Ptrs p{(const bf16_t*)d_in[0], (const bf16_t*)d_in[1], (const bf16_t*)d_in[2], (const bf16_t*)d_in[4], (const bf16_t*)d_in[5], (const bf16_t*)d_in[6], (bf16_t*)d_out, (char*)d_ws};
  constexpr int L_all = SNAP + 64;
  static_assert(SNAP >= 3 * 256 * 128 + 4 * 14592 + 4096 + 1024 + 256 + 512 + 128 + 64 && L_all <= 163840, "LDS");
  (void)hipFuncSetAttribute((const void*)k_all, hipFuncAttributeMaxDynamicSharedMemorySize, L_all);
  k_all<<<256, 512, L_all, stream>>>(p);
}
```

```cpp
#include <hip/hip_runtime.h>
#include <hip/hip_bf16.h>
#include <stdint.h>
#include <type_traits>

typedef unsigned short bf16_t;
typedef short bf16x8 __attribute__((ext_vector_type(8)));
typedef short s16x4 __attribute__((ext_vector_type(4)));
typedef float f32x4 __attribute__((ext_vector_type(4)));
typedef float f32x2 __attribute__((ext_vector_type(2)));
typedef unsigned u32x2 __attribute__((ext_vector_type(2)));
typedef unsigned u32x4 __attribute__((ext_vector_type(4)));
typedef __bf16 bf16x2_t __attribute__((ext_vector_type(2)));
typedef __attribute__((address_space(3))) void* lds_void_ptr;
typedef __attribute__((address_space(3))) char* lds_char_ptr;

constexpr int S_ = 2048, D_ = 1024, DF_ = 3584;
constexpr int RBO = D_ / 8, RBH = D_ / 8, RBA = DF_ / 8;

__device__ __forceinline__ float bflo(unsigned v) { return __uint_as_float(v << 16); }
__device__ __forceinline__ float bfhi(unsigned v) { return __uint_as_float(v & 0xffff0000u); }
__device__ __forceinline__ unsigned pk_bf16(float lo, float hi) {
  f32x2 v = {lo, hi};
  bf16x2_t b = __builtin_convertvector(v, bf16x2_t);
  return __builtin_bit_cast(unsigned, b);
}
__device__ __forceinline__ int wswz(int row) { return (((row >> 1) & 1) | (((row >> 3) & 1) << 1)) << 1; }

template <int OFF> __device__ __forceinline__ s16x4 ds_tr(unsigned addr) {
  s16x4 r; asm volatile("ds_read_b64_tr_b16 %0, %1 offset:%2" : "=v"(r) : "v"(addr), "n"(OFF)); return r;
}
template <int OFF> __device__ __forceinline__ bf16x8 ds_b128(unsigned addr) {
  bf16x8 r; asm volatile("ds_read_b128 %0, %1 offset:%2" : "=v"(r) : "v"(addr), "n"(OFF)); return r;
}
template <int OFF> __device__ __forceinline__ f32x2 ds_b64(unsigned addr) {
  f32x2 r; asm volatile("ds_read_b64 %0, %1 offset:%2" : "=v"(r) : "v"(addr), "n"(OFF)); return r;
}
__device__ __forceinline__ float sum8(float v) {
  v += __int_as_float(__builtin_amdgcn_update_dpp(0, __float_as_int(v), 0x128, 0xf, 0xf, false));
  auto a = __builtin_amdgcn_permlane16_swap(__float_as_uint(v), __float_as_uint(v), false, false);
  v = __uint_as_float(a[0]) + __uint_as_float(a[1]);
  auto b = __builtin_amdgcn_permlane32_swap(__float_as_uint(v), __float_as_uint(v), false, false);
  return __uint_as_float(b[0]) + __uint_as_float(b[1]);
}
__device__ __forceinline__ float sum8l(float v) {
  v += __int_as_float(__builtin_amdgcn_update_dpp(0, __float_as_int(v), 0xb1, 0xf, 0xf, false));
  v += __int_as_float(__builtin_amdgcn_update_dpp(0, __float_as_int(v), 0x4e, 0xf, 0xf, false));
  return v + __int_as_float(__builtin_amdgcn_update_dpp(0, __float_as_int(v), 0x141, 0xf, 0xf, false));
}
template <int OFF> __device__ __forceinline__ void ds_w128(unsigned addr, u32x4 v) { asm volatile("ds_write_b128 %0, %1 offset:%2" :: "v"(addr), "v"(v), "n"(OFF) : "memory"); }
__device__ __forceinline__ int ds_r32(unsigned addr) { int r; asm volatile("ds_read_b32 %0, %1" : "=v"(r) : "v"(addr)); return r; }
__device__ __forceinline__ void ds_w32(unsigned addr, int v) { asm volatile("ds_write_b32 %0, %1" :: "v"(addr), "v"(v) : "memory"); }
template <int N> __device__ __forceinline__ void wait_vm() { asm volatile("s_waitcnt vmcnt(%0)" ::"n"(N) : "memory"); }
__device__ __forceinline__ void raw_barrier() { asm volatile("" ::: "memory"); __builtin_amdgcn_s_barrier(); asm volatile("" ::: "memory"); }
__device__ __forceinline__ void lds_barrier() { asm volatile("s_waitcnt lgkmcnt(0)" ::: "memory"); __builtin_amdgcn_s_barrier(); asm volatile("" ::: "memory"); }

template <int I, int N, class F> __device__ __forceinline__ void static_for(F&& f) {
  if constexpr (I < N) { f(std::integral_constant<int, I>{}); static_for<I + 1, N>(f); }
}
template <int V> using IC = std::integral_constant<int, V>;
#define RSRC(p) __builtin_amdgcn_make_buffer_rsrc((void*)(p), 0, 0x7fffffff, 0x00020000)
#define RSRCN(p, n) __builtin_amdgcn_make_buffer_rsrc((void*)(p), 0, (n), 0x00020000)
#define LDSP(a) ((lds_void_ptr)(size_t)(a))
#define MFMA(a, b, c) __builtin_amdgcn_mfma_f32_16x16x32_bf16(a, b, c, 0, 0, 0)
#define CODE_ADDR(v, sym) asm volatile("s_getpc_b64 s[40:41]\n\ts_add_u32 s40, s40, " sym "@rel32@lo+4\n\ts_addc_u32 s41, s41, " sym "@rel32@hi+12\n\ts_mov_b64 %0, s[40:41]" : "=s"(v) :: "s40", "s41", "scc")
#define MERGED 1
constexpr int SNAP = 162752;
constexpr int STAB1 = 786432;
constexpr int EPOCH = 0;
constexpr int F_MARK = 4096, F_H = 4096 + 2048, F_ROW = 8192, F_COL = 16384;
#define F_O(t) (49152 + (t) * 128 + (((((t) >> 3) ^ 1) & 1) << 13))
constexpr int F_ACT = 65536;
constexpr int F_PART = 131072;
constexpr int KN_TRY = 6;
struct Tag { unsigned lo, hi; };
__device__ __forceinline__ Tag tag_get(unsigned lds0) {
  f32x2 v = ds_b64<0>(lds0 + SNAP);
  asm volatile("s_waitcnt lgkmcnt(0)" : "+v"(v) :: "memory");
  return Tag{(unsigned)__builtin_amdgcn_readfirstlane(__float_as_int(v[0])) + 8u, (unsigned)__builtin_amdgcn_readfirstlane(__float_as_int(v[1]))};
}
__device__ __forceinline__ bool in_time(unsigned lds0) {
  int v = ds_r32(lds0 + SNAP + 28);
  asm volatile("s_waitcnt lgkmcnt(0)" : "+v"(v) :: "memory");
  return (unsigned)__builtin_amdgcn_s_memrealtime() - (unsigned)__builtin_amdgcn_readfirstlane(v) < 20000000u;
}
__device__ __forceinline__ int lane_id() { return __builtin_amdgcn_mbcnt_hi(~0u, __builtin_amdgcn_mbcnt_lo(~0u, 0u)); }
#if defined(__HIP_DEVICE_COMPILE__)
__device__ __forceinline__ void flag_set(const unsigned* sync, int off, Tag t, unsigned state) {
  __builtin_amdgcn_raw_buffer_store_b64((u32x2){t.lo + state, t.hi}, RSRC(sync), lane_id() == 0 ? 0u : 0x80000000u, off, 16);
}
__device__ __forceinline__ u32x2 flag_get(const unsigned* sync, int off, int n) { return __builtin_amdgcn_raw_buffer_load_b64(RSRC(sync), lane_id() < n ? lane_id() * 8u : 0x80000000u, off, 16); }
__device__ __forceinline__ bool flags_up(u32x2 v, int n, Tag t, unsigned state) { return __builtin_amdgcn_ballot_w64(lane_id() < n && (v[0] - t.lo - state > 7u - state || v[1] != t.hi)) == 0; }
__device__ __forceinline__ bool words_up(unsigned v, Tag t) { return __builtin_amdgcn_ballot_w64(v != ((lane_id() & 1) ? t.hi : t.lo)) == 0; }
__device__ __forceinline__ void wait_flags(unsigned lds0, const unsigned* sync, int off, int n, unsigned state) {
  const Tag t = tag_get(lds0);
  while (!flags_up(flag_get(sync, off, n), n, t, state) && in_time(lds0)) {}
}
#endif
__device__ __forceinline__ void lds_wait8(unsigned addr, int n = 8) {
  int dv; unsigned polls = 0;
  do { dv = ds_r32(addr); asm volatile("s_waitcnt lgkmcnt(0)" : "+v"(dv) :: "memory"); } while (__builtin_amdgcn_readfirstlane(dv) < n && ++polls < 40000000u);
}
template <int N> __device__ __forceinline__ bool marked(unsigned addr) {
  bf16x8 v[N];
  static_for<0, N>([&](auto ic) { constexpr int i = decltype(ic)::value; v[i] = ds_b128<i * 4096>(addr); });
  asm volatile("s_waitcnt lgkmcnt(0)" ::: "memory");
  unsigned m = 0;
#pragma unroll
  for (int i = 0; i < N; ++i) {
    asm volatile("" : "+v"(v[i]));
    const u32x4 u = __builtin_bit_cast(u32x4, v[i]);
    m = max(max(m, u[0]), u[1]); m = max(max(m, u[2]), u[3]);
  }
  return __builtin_amdgcn_ballot_w64(m == 0xffffffffu) != 0;
}

struct GemmArgs {
  const bf16_t* X;
  const bf16_t* W0;
  const bf16_t* W1;
  const bf16_t* R;
  bf16_t* O;
  float* st;
  const bf16_t* CW0;
  const bf16_t* CW1;
  float* cs;
  const bf16_t* PF0;
  const bf16_t* PF1;
  unsigned* sync;
  float* slab;
  unsigned warm;
};
#define STAMP(i)
#define STAMPW(i, w)
template <int MF, int NF> struct Frags { bf16x8 x[MF]; s16x4 lo[NF], hi[NF]; };

__device__ __forceinline__ unsigned dn_wsrc(int lane, int wave) { return (unsigned)((lane >> 3) * (D_ * 2) + (((lane & 7) ^ wswz((wave & 3) * 8 + (lane >> 3))) << 4)); }
constexpr int OWNE = 0, OWNM = 32768;
#define KN_P0 2
#define KN_P1 3
#define KN_P2 0
#define KN_P3 1
constexpr int WOB = 57600;
constexpr int wo_pos(int s) { return s < 6 ? WOB + s * 16384 : (s - 6) * 16384; }
template <class HK>
__device__ __forceinline__ void k1_phase(const GemmArgs& a, HK hook) {
#if defined(__HIP_DEVICE_COMPILE__)
  constexpr int K = D_, LDW = 3 * D_, WN = 4;
  constexpr int HR = 16, STRIDE = 128;
  constexpr int XT = 144 * 128, WU = 24576;
  constexpr int XBASE = 0, WBASE = 3 * XT, RING = WBASE + 4 * WU;
  constexpr int MF = 4, NF = 3, MFX = 5;
  constexpr int NT = 16, NXI = 18, NLW = 4, XI = 5, XIMIN = 4, WI = 6;
  extern __shared__ __attribute__((aligned(16))) char smem[];
  STAMP(0);
  const unsigned lds0 = (unsigned)(size_t)(lds_char_ptr)smem;
  const int tid = threadIdx.x, lane = tid & 63;
  const int wave = __builtin_amdgcn_readfirstlane(tid >> 6);
  const int wm = wave / WN, wn = wave % WN;
  const int bid = blockIdx.x, xcd = bid & 7, idx = bid >> 3;
  const int tm = (xcd / 4) * 8 + idx / 4, tn = (xcd % 4) * 4 + idx % 4;
  const int m0 = tm * 128;
  const int fr = lane & 15, fq = lane >> 4;
  const int wq = (lane >> 2) & 3, wp = lane & 3;
  constexpr int TCH = RING + 2304;
  auto touch = [&](const void* base, unsigned voff, unsigned soff, unsigned lim = 0x7fffffffu) {
    __builtin_amdgcn_raw_ptr_buffer_load_lds(RSRCN(base, lim), LDSP(lds0 + TCH), 4, voff, soff, 0, 0);
  };
  const auto rO = RSRC(a.O);

  unsigned xsrc[XI], wsrc[WI];
  {
    const int w4 = wave & 3, lr = lane >> 3;
    const unsigned xv = (unsigned)(lr * (K * 2) + (((lane & 7) ^ ((w4 * 4 + (lr >> 1)) & 7)) << 4));
    const int r0 = m0 - HR + w4 * 8;
#pragma unroll
    for (int i = 0; i < XI; ++i) xsrc[i] = xv + (unsigned)((i == 0 ? (r0 < 0 ? 0 : r0) : r0 + 32 * (i < 4 ? i : 4)) * (K * 2));
    const unsigned wv = (unsigned)((w4 * 8 + lr) * (LDW * 2) + tn * 128 + (((lane & 7) ^ wswz(w4 * 8 + lr)) << 4));
#pragma unroll
    for (int i = 0; i < WI; ++i) wsrc[i] = wv + (unsigned)((i & 1) * 32 * (LDW * 2) + (i >> 1) * 2048);
  }
  auto ldX = [&](int i, int tile, int slot) {
    const int j = wave + NLW * i;
    if (j >= NXI) return;
    __builtin_amdgcn_raw_ptr_buffer_load_lds(RSRC(a.X), LDSP(lds0 + XBASE + slot * XT + j * 1024), 16, xsrc[i], tile * 128, 0, 0);
  };
  auto ldW = [&](int i, int unit, int slot) {
    const int j = wave + NLW * i;
    __builtin_amdgcn_raw_ptr_buffer_load_lds(RSRC(a.W0), LDSP(lds0 + WBASE + slot * WU + j * 1024), 16, wsrc[i], unit * 64 * LDW * 2, 0, 0);
  };
  auto issueX = [&](int tile, int slot) {
    if (wave >= NLW) return;
#pragma unroll
    for (int i = 0; i < XI; ++i) ldX(i, tile, slot);
  };
  auto issueW = [&](int unit, int slot) {
    if (wave >= NLW) return;
#pragma unroll
    for (int i = 0; i < WI; ++i) ldW(i, unit, slot);
  };

  f32x4 acc[NF][MF];
#pragma unroll
  for (int n = 0; n < NF; ++n)
#pragma unroll
    for (int m = 0; m < MF; ++m) acc[n][m] = (f32x4){0.f, 0.f, 0.f, 0.f};
  f32x4 acch = {0.f, 0.f, 0.f, 0.f};
  f32x4 accs = {0.f, 0.f, 0.f, 0.f};
  constexpr int ONES = RING;

  unsigned xa0, xa1, xh0, xh1;
  { const unsigned o = fr * 128 + ((fq ^ ((fr >> 1) & 7)) << 4), mo = (HR + wm * MF * 16) * 128;
    xa0 = lds0 + XBASE + mo + o; xa1 = lds0 + XBASE + mo + (o ^ 64); xh0 = lds0 + XBASE + o; xh1 = lds0 + XBASE + (o ^ 64); }
  unsigned wa;
  { const int row = 8 * fq + wq; wa = lds0 + WBASE + row * STRIDE + (((wn << 1) ^ wswz(row)) << 4) + wp * 8; }

  using I0 = IC<0>; using I1 = IC<1>;
  using T = std::true_type; using F = std::false_type;
  typedef Frags<MFX, NF> FR;
  struct RdCtx { unsigned xa, xh, wb; };
  auto mkctx = [&](bool ks1, unsigned xoffs, unsigned woffs) {
    RdCtx c; c.xa = (ks1 ? xa1 : xa0) + xoffs; c.xh = fr == 0 ? lds0 + ONES : (ks1 ? xh1 : xh0) + xoffs; c.wb = wa + woffs;
    return c;
  };
  auto rd_item = [&](FR& f, const RdCtx& c, auto KSc, auto kc) {
    constexpr int KS = decltype(KSc)::value, k = decltype(kc)::value;
    if constexpr (k < MF) f.x[k] = ds_b128<k * 2048>(c.xa);
    else if constexpr (k < MFX) f.x[k] = ds_b128<0>(c.xh);
    else {
      constexpr int n = k - MFX, o = n * 8192 + KS * 32 * STRIDE;
      f.lo[n] = ds_tr<o>(c.wb); f.hi[n] = ds_tr<o + 4 * STRIDE>(c.wb);
    }
  };
  auto wait_frags = [&](FR& f) {
    asm volatile("s_waitcnt lgkmcnt(0)" ::: "memory");
#pragma unroll
    for (int m = 0; m < MFX; ++m) asm volatile("" : "+v"(f.x[m]));
#pragma unroll
    for (int n = 0; n < NF; ++n) asm volatile("" : "+v"(f.lo[n]), "+v"(f.hi[n]));
  };
  auto step = [&](auto CSc, auto STc, FR& cur, FR& nxt, const RdCtx& c, auto KSc, auto PFc, auto NLc, auto&& ld) {
    constexpr bool PF = decltype(PFc)::value != 0; constexpr int NL = decltype(NLc)::value;
    constexpr int R = MFX + NF, RG = NF - 1;
    constexpr bool CS = decltype(CSc)::value; constexpr int ST = decltype(STc)::value;
    static_for<0, NF>([&](auto gc) {
      constexpr int g = decltype(gc)::value;
      if constexpr (PF) static_for<0, R>([&](auto kc) { constexpr int k = decltype(kc)::value; if constexpr (k * RG / R == g) rd_item(nxt, c, KSc, kc); });
      static_for<0, NL>([&](auto ic) { constexpr int i = decltype(ic)::value; if constexpr (i * NF / NL == g) ld(i); });
      const bf16x8 w = __builtin_shufflevector(cur.lo[g], cur.hi[g], 0, 1, 2, 3, 4, 5, 6, 7);
#pragma unroll
      for (int m = 0; m < MF; ++m) acc[g][m] = MFMA(w, cur.x[m], acc[g][m]);
      if constexpr (g == 1 + ST / WN) acch = MFMA(w, cur.x[MF], acch);
      if constexpr (CS && g == 0) accs = MFMA(w, cur.x[MF], accs);
      __builtin_amdgcn_sched_barrier(0);
    });
    if constexpr (PF) wait_frags(nxt);
  };
  auto step1 = [&](auto CSc, auto STc, FR& cur, FR& nxt, const RdCtx& c, auto KSc, auto R0c, auto RLc, auto IBc, auto&& sync, auto NLc, auto&& ld) {
    constexpr bool CS = decltype(CSc)::value; constexpr int ST = decltype(STc)::value, NL = decltype(NLc)::value, R0 = decltype(R0c)::value;
    constexpr int E0 = CS ? 1 : 0, E1 = 1 == 1 + ST / WN ? 1 : 0, E2 = 2 == 1 + ST / WN ? 1 : 0;
    constexpr int NM = 12 + E0 + E1 + E2, NR = MFX + 2 * NF;
    constexpr int IB = decltype(IBc)::value < 0 && decltype(IBc)::value > -100 ? NM + decltype(IBc)::value : decltype(IBc)::value;
    constexpr int RL = decltype(RLc)::value < 0 ? NM + decltype(RLc)::value : decltype(RLc)::value;
    static_for<0, NM>([&](auto ic) {
      constexpr int I = decltype(ic)::value;
      constexpr int g = I < 4 + E0 ? 0 : (I < 8 + E0 + E1 ? 1 : 2), q = I - (g == 0 ? 0 : (g == 1 ? 4 + E0 : 8 + E0 + E1));
      const bf16x8 w = __builtin_shufflevector(cur.lo[g], cur.hi[g], 0, 1, 2, 3, 4, 5, 6, 7);
      if constexpr (q < 4) acc[g][q] = MFMA(w, cur.x[q], acc[g][q]);
      else if constexpr (g == 0) accs = MFMA(w, cur.x[MF], accs);
      else acch = MFMA(w, cur.x[MF], acch);
      __builtin_amdgcn_sched_barrier(0);
      if constexpr (I == IB) sync();
      static_for<0, NR>([&](auto rc) {
        constexpr int r = decltype(rc)::value;
        if constexpr (R0 + r * (RL - R0) / (NR - 1) == I) {
          if constexpr (r < MFX) rd_item(nxt, c, KSc, rc);
          else {
            constexpr int n = (r - MFX) >> 1, o = n * 8192 + decltype(KSc)::value * 32 * STRIDE;
            if constexpr ((r - MFX) & 1) nxt.hi[n] = ds_tr<o + 4 * STRIDE>(c.wb); else nxt.lo[n] = ds_tr<o>(c.wb);
          }
        }
      });
      static_for<0, NL>([&](auto lc) { constexpr int i = decltype(lc)::value; if constexpr (R0 + i * (NM - R0) / NL == I) ld(i); });
      __builtin_amdgcn_sched_barrier(0);
    });
    wait_frags(nxt);
  };
  FR fa, fb;
  using IM1 = IC<-1>;

  asm volatile("s_waitcnt lgkmcnt(0)" :: "s"(a.warm), "v"(wsrc[WI - 1]), "v"(xsrc[XI - 1]));
  issueW(0, 0); issueX(0, 0); issueW(1, 1); issueX(1, 1);
  u32x4 sx[4];
  constexpr int TNEW = RING + 3072;
  auto ldT = [&](int dst) {
    int lx = lane; asm volatile("" : "+v"(lx));
    const auto rT = RSRCN((const char*)a.cs + STAB1, 2048 * 16);
    const unsigned to = (unsigned)((m0 - 16) * 16 + lx * 16);
    __builtin_amdgcn_raw_ptr_buffer_load_lds(rT, LDSP(lds0 + dst), 16, to, 0, 0, 16);
    __builtin_amdgcn_raw_ptr_buffer_load_lds(rT, LDSP(lds0 + dst + 1024), 16, to + 1024u, 0, 0, 16);
    if (lx < 16) __builtin_amdgcn_raw_ptr_buffer_load_lds(rT, LDSP(lds0 + dst + 2048), 16, to + 2048u, 0, 0, 16);
  };
  auto ldS = [&]() {
    int lx = lane; asm volatile("" : "+v"(lx));
    const unsigned so = (unsigned)((m0 + tn * 8 + wave * 2 + (lx >> 5)) * (K * 2) + (lx & 31) * 16);
#pragma unroll
    for (int i = 0; i < 4; ++i) sx[i] = __builtin_amdgcn_raw_buffer_load_b128(RSRC(a.X), so, i * 512, 0);
  };
  __builtin_amdgcn_sched_barrier(0);
  if (wave == 4) {
    if (lane < 4) *(unsigned*)(smem + ONES + lane * 4) = 0x3f803f80u;
  }
  if (wave == 5) {
    if (lane < 2) __builtin_amdgcn_raw_ptr_buffer_load_lds(RSRC(a.sync), LDSP(lds0 + SNAP), 4, lane * 4, EPOCH, 0, 16);
    ds_w32(lds0 + SNAP + 28, (int)__builtin_amdgcn_s_memrealtime());
    const int p2 = (xcd >> 1) * 2 + (idx >> 4), mb = (xcd & 1) * 16 + (idx & 15);
#pragma unroll
    for (int i = 0; i < 3; ++i)
      __builtin_amdgcn_raw_buffer_store_b128((u32x4){~0u, ~0u, ~0u, ~0u}, RSRC(a.st), lane * 16 + i * 1024, ((p2 * 16 + (mb >> 1)) * RBH + (xcd >> 2) * 64 + (mb & 1) * 12) * 256, 16);
  }
  constexpr int HQ = 5;
  const int slc = (3 - ((unsigned)(size_t)a.W0 >> 7) - (xcd & 3) * 4) & 7;
  const int hid = (idx >> 2) * 3 + (idx & 3) - ((idx & 3) > slc ? 1 : 0);
  const bool hlp = slc < 4 && (idx & 3) != slc;
  const int hj1 = hlp ? hid / 3 - HQ : -100, hj2 = hlp ? hid / 3 + 8 - HQ : -100;
  const unsigned htv = (unsigned)(lane * (LDW * 2) + (hid % 3) * 2048 + ((xcd & 3) * 4 + slc) * 128);
  if (wave == 6 && hlp && hj1 <= 0) touch(a.W0, htv, (hid / 3) * (64 * LDW * 2));
  auto code_touch = [&]() {
    unsigned long long c0, c1; CODE_ADDR(c0, "k_all"); CODE_ADDR(c1, "k_zend");
    const unsigned len = c1 - c0 < 262144ull ? (unsigned)(c1 - c0) : 0u;
    touch((const void*)c0, (unsigned)((lane * 32 + idx) * 128), 0, len);
  };
  if (wave == 7) code_touch();
  if (wave < NLW) wait_vm<WI + XIMIN>();
  lds_barrier();
  { const RdCtx c = mkctx(false, 0, 0); static_for<0, MFX + NF>([&](auto kc) { rd_item(fa, c, I0{}, kc); }); }
  wait_frags(fa);
  STAMP(1);
  auto run = [&](auto ROLEc) __attribute__((always_inline)) {
    constexpr int ROLE = decltype(ROLEc)::value;
    constexpr bool LDR = ROLE < NLW;
    using CS0 = std::integral_constant<bool, ROLE / WN == 0>; using CS1 = std::integral_constant<bool, ROLE / WN == 1>;
    using STt = IC<ROLE>;
    auto nold = [&](int) {};
    { const RdCtx c = mkctx(true, 0, 0); step(CS0{}, STt{}, fa, fb, c, I1{}, T{}, IC<LDR ? WI + XI : 0>{}, [&](int i) { if (i < WI) ldW(i, 2, 2); else ldX(i - WI, 2, 2); }); }
    if constexpr (LDR) ldS();
    int slx = 1;
    if constexpr (ROLE == 5) wait_vm<3>();
    auto iter = [&](int j, auto LXc, auto LWc, auto WAITc, auto TIc) {
      constexpr bool LX = decltype(LXc)::value && LDR, LW = decltype(LWc)::value && LDR;
      constexpr int WAIT = decltype(WAITc)::value, TI = decltype(TIc)::value;
      constexpr int EX = (LDR && TI < 0) ? WI : 0;
      constexpr int OL = TI == 1 ? 0 : 2, OH = (LDR && TI > 0) ? 2 : OL;
      constexpr int NLT = EX + (LX ? XI : 0) + (LW ? WI : 0) + 2 * (OH - OL), N0 = (NLT + 1) / 2, N1 = NLT - N0;
      if constexpr (LDR) wait_vm<WAIT + (TI > 0 ? 2 * OL : 0)>();
      unsigned osrc = 0;
      if constexpr (OH > OL) { int lx = lane; asm volatile("" : "+v"(lx)); const int row = ROLE * 8 + (lx >> 3); osrc = (unsigned)(row * D_ + ((xcd & 1) * 8 + (idx & 7)) * 64 + ((lx & 7) ^ wswz(row)) * 8) * 2u; }
      if constexpr (ROLE == 6 && TI == 0) { if (j == hj1 || j == hj2) touch(a.W0, htv, (j + HQ) * (64 * LDW * 2)); }
      constexpr int S1 = 2, S3 = 11;
      if constexpr (LDR && TI <= 0) {
        if (j == S1) {
          float sm = 0.f, sq = 0.f;
          const bf16x2_t one2 = __builtin_bit_cast(bf16x2_t, 0x3f803f80u);
#pragma unroll
          for (int i = 0; i < 4; ++i)
#pragma unroll
            for (int e = 0; e < 4; ++e) { const unsigned u = sx[i][e]; const bf16x2_t v = __builtin_bit_cast(bf16x2_t, u); sm = __builtin_amdgcn_fdot2_f32_bf16(v, one2, sm, false); sq = __builtin_amdgcn_fdot2_f32_bf16(v, v, sq, false); }
          sm = sum8l(sm); sq = sum8l(sq);
          sm += __int_as_float(__builtin_amdgcn_update_dpp(0, __float_as_int(sm), 0x128, 0xf, 0xf, false));
          sq += __int_as_float(__builtin_amdgcn_update_dpp(0, __float_as_int(sq), 0x128, 0xf, 0xf, false));
          { auto p = __builtin_amdgcn_permlane16_swap(__float_as_uint(sm), __float_as_uint(sm), false, false); sm = __uint_as_float(p[0]) + __uint_as_float(p[1]); }
          { auto p = __builtin_amdgcn_permlane16_swap(__float_as_uint(sq), __float_as_uint(sq), false, false); sq = __uint_as_float(p[0]) + __uint_as_float(p[1]); }
          const float mu = sm * (1.f / 1024.f);
          float var = sq * (1.f / 1024.f) - mu * mu; var = var > 0.f ? var : 0.f;
          int lx = lane; asm volatile("" : "+v"(lx));
          const Tag tg = tag_get(lds0);
          __builtin_amdgcn_raw_buffer_store_b128((u32x4){__float_as_uint(mu), tg.lo, __float_as_uint(__builtin_amdgcn_rsqf(var + 1e-5f)), tg.lo + tg.hi}, RSRC(a.cs),
                                                 (lx & 31) == 0 ? (unsigned)(STAB1 + (m0 + tn * 8 + ROLE * 2 + (lx >> 5)) * 16) : 0x80000000u, 0, 16);
          __builtin_amdgcn_sched_barrier(0);
          asm volatile("s_nop 1" ::: "memory");
        }
      }
      if constexpr (ROLE == 7 && TI == 0) {
        if (j == S3) ldT(TNEW);
        if (j == S3 + 1) {
          for (;;) {
            wait_vm<0>();
            int lx = lane; asm volatile("" : "+v"(lx));
            const unsigned ad = lds0 + TNEW + lx * 16;
            u32x4 v[3];
            v[0] = __builtin_bit_cast(u32x4, ds_b128<0>(ad)); v[1] = __builtin_bit_cast(u32x4, ds_b128<1024>(ad)); v[2] = __builtin_bit_cast(u32x4, ds_b128<2048>(ad));
            const Tag tg = tag_get(lds0);
            asm volatile("" : "+v"(v[0]), "+v"(v[1]), "+v"(v[2]) :: "memory");
            auto good = [&](const u32x4& e) { return e[1] == tg.lo && e[3] == tg.lo + tg.hi; };
            const bool bad = (!good(v[0]) && (tm > 0 || lx >= 16)) || !good(v[1]) || (!good(v[2]) && lx < 16);
            if (__builtin_amdgcn_ballot_w64(bad) == 0 || !in_time(lds0)) break;
            ldT(TNEW);
          }
        }
      }
      if constexpr (ROLE == 5 && TI == 1) wait_vm<0>();
      if constexpr (ROLE == 7 && TI == 1) code_touch();
      if constexpr (ROLE == 4 && TI == 0) touch(a.PF1, (bid * 64 + lane) * 128, 0, j == 1 + idx % 12 ? 0x7fffffffu : 0u);
      if constexpr (ROLE >= 4 && TI == 2) {
        const bf16_t* cw = ROLE < 6 ? a.CW0 : a.CW1;
        touch(cw, lane < 56 ? (unsigned)(((ROLE & 1) * 512 + bid) * (DF_ * 2) + lane * 128) : 0x80000000u, 0);
      }
      int xp = slx + 2; if (xp >= 3) xp -= 3;
      auto ld = [&](int i) { if (i < EX) { ldW(i, j + 2, (j + 2) & 3); return; } i -= EX;
                             if (LX && i < XI) ldX(i, j + 2, xp);
                             else if constexpr (LW) ldW(i - (LX ? XI : 0), j + 3, (j + 3) & 3);
                             else { const int q = i - (LX ? XI : 0), t = OL + (q >> 1), h = q & 1;
                               __builtin_amdgcn_raw_ptr_buffer_load_lds(RSRC(a.PF1), LDSP(lds0 + wo_pos(t >> 1) + (t & 1) * 8192 + (ROLE + 4 * h) * 1024), 16,
                                                                        osrc, ((((t & 1) * 8 + (t >> 1)) ^ ((xcd & 3) * 4)) * 2 + h) * (32 * D_ * 2), 0, 0); } };
      { const RdCtx c = mkctx(false, slx * XT, (j & 3) * WU); step1(CS1{}, STt{}, fb, fa, c, I0{}, IC<LDR ? 1 : 0>{}, IC<-4>{}, IC<LDR ? 1 : -100>{}, [&]() { raw_barrier(); }, IC<N0>{}, ld); }
      { const RdCtx c = mkctx(true, slx * XT, (j & 3) * WU); step1(CS0{}, STt{}, fa, fb, c, I1{}, I0{}, IC<LDR ? -4 : -6>{},
          IC<LDR ? -100 : -3>{}, [&]() { if constexpr (TI != 3) { asm volatile("s_waitcnt lgkmcnt(0)" ::: "memory"); raw_barrier(); } }, IC<N1>{}, [&](int i) { ld(i + N0); }); }
      slx = slx + 1; if (slx >= 3) slx = 0;
    };
    using WS_ = IC<2 * WI + XIMIN>;
    if constexpr (!LDR) raw_barrier();
    if constexpr (LDR) iter(1, T{}, T{}, IC<WI + XIMIN + 4>{}, IM1{});
    for (int j = LDR ? 2 : 1; j <= NT - 4; ++j) iter(j, T{}, T{}, WS_{}, I0{});
    iter(NT - 3, T{}, F{}, WS_{}, I1{});
    iter(NT - 2, F{}, F{}, IC<WI + XIMIN>{}, IC<2>{});
    iter(NT - 1, F{}, F{}, I0{}, IC<3>{});
    if constexpr (ROLE == 5) flag_set(a.sync, F_MARK + ((xcd >> 1) * 2 + (idx >> 4)) * 256 + ((xcd & 1) * 16 + (idx & 15)) * 8, tag_get(lds0), 0);
    { const RdCtx c = mkctx(false, 0, 0); step(CS1{}, STt{}, fb, fa, c, I0{}, F{}, I0{}, nold); }
  };
  switch (wave) {
    case 0: run(IC<0>{}); break; case 1: run(IC<1>{}); break;
    case 2: run(IC<2>{}); break; case 3: run(IC<3>{}); break;
    case 4: run(IC<4>{}); break; case 5: run(IC<5>{}); break;
    case 6: run(IC<6>{}); break; default: run(IC<7>{}); break;
  }
  STAMP(2);
  {
    float* tcs = (float*)(smem + RING + 64);
    float* tkv = tcs + 128;
    if (fr == 0) { *(f32x4*)(tcs + wm * 64 + wn * 16 + fq * 4) = accs; *(f32x4*)(tkv + wave * 16 + fq * 4) = acch; }
    const int srow = m0 + wave * 16 + fr;
    const bool par = fr & 1;
    float bias[4];
    {
      const unsigned mk = 0x5555u & ((2u << (srow < 15 ? srow : 15)) - 1u), sft = (fr & 14) - fq * 4 + 14;
      const unsigned b0 = ~(mk >> sft), b1 = ~((mk << 16) >> sft);
#pragma unroll
      for (int j = 0; j < 4; ++j) bias[j] = __uint_as_float((unsigned)__builtin_amdgcn_sbfe((int)(j < 2 ? b0 : b1), 2 - 2 * (j & 1), 1) & 0xff61b1e6u);
    }
    unsigned pl = par ? 0x01000c0cu : 0x0c0c0100u, ph = par ? 0x03020c0cu : 0x0c0c0302u;
    asm volatile("" : "+v"(bias[0]), "+v"(bias[1]), "+v"(bias[2]), "+v"(bias[3]), "+v"(pl), "+v"(ph));
    STAMP(9);
    lds_barrier();
    STAMP(6);
    constexpr int RS = 400;
    char* qt = smem;
    f32x4 csn[NF];
    const f32x4 c0a = *(const f32x4*)(tcs + wn * 16 + fq * 4), c0b = *(const f32x4*)(tcs + 64 + wn * 16 + fq * 4);
    csn[1] = *(const f32x4*)(tkv + wn * 16 + fq * 4); csn[2] = *(const f32x4*)(tkv + 64 + wn * 16 + fq * 4);
    f32x2 st[MF + 1];
#pragma unroll
    for (int m = 0; m <= MF; ++m) {
      const int r = m < MF ? 16 + (wm * MF + m) * 16 + fr : fr;
      const f32x4 t4 = *(const f32x4*)(smem + TNEW + r * 16); st[m] = (f32x2){t4[0], t4[2]};
    }
    const f32x4 csh = *(const f32x4*)(tkv + wave * 16 + fq * 4);
    asm volatile("" ::: "memory");
    csn[0] = c0a + c0b;
#pragma unroll
    for (int m = 0; m < MF; ++m) {
      const int r = 16 + (wm * MF + m) * 16 + fr;
      const float mu = st[m][0], rs = st[m][1];
#pragma unroll
      for (int n = 0; n < NF; ++n) {
        const int col = (n * 4 + wn) * 16 + fq * 4;
        const f32x4 cs = csn[n];
        const u32x2 v = {pk_bf16(rs * (acc[n][m][0] - mu * cs[0]), rs * (acc[n][m][1] - mu * cs[1])),
                         pk_bf16(rs * (acc[n][m][2] - mu * cs[2]), rs * (acc[n][m][3] - mu * cs[3]))};
        *(u32x2*)(qt + r * RS + col * 2) = v;
      }
    }
    {
      const float mu = st[MF][0], rs = st[MF][1];
      const int col = (4 + wave) * 16 + fq * 4;
      const u32x2 v = {pk_bf16(rs * (acch[0] - mu * csh[0]), rs * (acch[1] - mu * csh[1])),
                       pk_bf16(rs * (acch[2] - mu * csh[2]), rs * (acch[3] - mu * csh[3]))};
      *(u32x2*)(qt + fr * RS + col * 2) = v;
    }
    STAMP(4);
    lds_barrier();
    STAMP(5);
    const unsigned qa = lds0 + (16 + wave * 16 + fr) * RS + fq * 16;
    const unsigned ka = lds0 + (wave * 16 + fr) * RS + 128 + fq * 16;
    const unsigned va = lds0 + (wave * 16 + fq * 4 + wq) * RS + 256 + wp * 16;
    bf16x8 qf[2], kf[2][2]; s16x4 vf[2][2][2];
    qf[0] = ds_b128<0>(qa); kf[0][0] = ds_b128<0>(ka); kf[0][1] = ds_b128<16 * RS>(ka);
    qf[1] = ds_b128<64>(qa); kf[1][0] = ds_b128<64>(ka); kf[1][1] = ds_b128<16 * RS + 64>(ka);
    vf[0][0][0] = ds_tr<0>(va); vf[0][0][1] = ds_tr<16 * RS>(va); vf[0][1][0] = ds_tr<8>(va); vf[0][1][1] = ds_tr<16 * RS + 8>(va);
    vf[1][0][0] = ds_tr<64>(va); vf[1][0][1] = ds_tr<16 * RS + 64>(va); vf[1][1][0] = ds_tr<72>(va); vf[1][1][1] = ds_tr<16 * RS + 72>(va);
    asm volatile("s_waitcnt lgkmcnt(8)" ::: "memory");
#pragma unroll
    for (int hh = 0; hh < 2; ++hh) asm volatile("" : "+v"(qf[hh]), "+v"(kf[hh][0]), "+v"(kf[hh][1]));
    const float sc2 = 0.17677669529663687f * 1.4426950408889634f;
    u32x4 ov[2];
    const f32x4 z = {0.f, 0.f, 0.f, 0.f};
    f32x4 sc[2][2]; float lg[2][4], mx[2], sum[2];
    auto x16 = [](float v) { auto p = __builtin_amdgcn_permlane16_swap(__float_as_uint(v), __float_as_uint(v), false, false); return (f32x2){__uint_as_float(p[0]), __uint_as_float(p[1])}; };
    auto x32 = [](float v) { auto p = __builtin_amdgcn_permlane32_swap(__float_as_uint(v), __float_as_uint(v), false, false); return (f32x2){__uint_as_float(p[0]), __uint_as_float(p[1])}; };
#pragma unroll
    for (int hh = 0; hh < 2; ++hh) {
      sc[hh][0] = MFMA(kf[hh][0], qf[hh], z);
      sc[hh][1] = MFMA(kf[hh][1], qf[hh], z);
    }
    __builtin_amdgcn_sched_barrier(0);
#pragma unroll
    for (int hh = 0; hh < 2; ++hh) {
#pragma unroll
      for (int j = 0; j < 4; ++j) lg[hh][j] = __builtin_fmaf(par ? sc[hh][j >> 1][(j & 1) * 2 + 1] : sc[hh][j >> 1][(j & 1) * 2], sc2, bias[j]);
      mx[hh] = fmaxf(fmaxf(lg[hh][0], lg[hh][1]), fmaxf(lg[hh][2], lg[hh][3]));
    }
    __builtin_amdgcn_sched_barrier(0);
    { const f32x2 p = x16(mx[0]), q = x16(mx[1]); mx[0] = fmaxf(p[0], p[1]); mx[1] = fmaxf(q[0], q[1]); }
    { const f32x2 p = x32(mx[0]), q = x32(mx[1]); mx[0] = fmaxf(p[0], p[1]); mx[1] = fmaxf(q[0], q[1]); }
    __builtin_amdgcn_sched_barrier(0);
#pragma unroll
    for (int hh = 0; hh < 2; ++hh) {
#pragma unroll
      for (int j = 0; j < 4; ++j) lg[hh][j] = __builtin_amdgcn_exp2f(lg[hh][j] - mx[hh]);
      sum[hh] = (lg[hh][0] + lg[hh][1]) + (lg[hh][2] + lg[hh][3]);
    }
    __builtin_amdgcn_sched_barrier(0);
    asm volatile("s_waitcnt lgkmcnt(0)" ::: "memory");
#pragma unroll
    for (int hh = 0; hh < 2; ++hh) asm volatile("" : "+v"(vf[hh][0][0]), "+v"(vf[hh][0][1]), "+v"(vf[hh][1][0]), "+v"(vf[hh][1][1]));
    { const f32x2 p = x16(sum[0]), q = x16(sum[1]); sum[0] = p[0] + p[1]; sum[1] = q[0] + q[1]; }
    { const f32x2 p = x32(sum[0]), q = x32(sum[1]); sum[0] = __builtin_amdgcn_rcpf(p[0] + p[1]); sum[1] = __builtin_amdgcn_rcpf(q[0] + q[1]); }
    __builtin_amdgcn_sched_barrier(0);
#pragma unroll
    for (int hh = 0; hh < 2; ++hh) {
      const float inv = sum[hh];
      const unsigned c0 = pk_bf16(lg[hh][0] * inv, lg[hh][1] * inv), c1 = pk_bf16(lg[hh][2] * inv, lg[hh][3] * inv);
      const u32x4 pp = {__builtin_amdgcn_perm(0u, c0, pl), __builtin_amdgcn_perm(0u, c0, ph), __builtin_amdgcn_perm(0u, c1, pl), __builtin_amdgcn_perm(0u, c1, ph)};
      const bf16x8 pf = __builtin_bit_cast(bf16x8, pp);
      sc[hh][0] = MFMA(__builtin_shufflevector(vf[hh][0][0], vf[hh][0][1], 0, 1, 2, 3, 4, 5, 6, 7), pf, z);
      sc[hh][1] = MFMA(__builtin_shufflevector(vf[hh][1][0], vf[hh][1][1], 0, 1, 2, 3, 4, 5, 6, 7), pf, z);
    }
    __builtin_amdgcn_sched_barrier(0);
#pragma unroll
    for (int hh = 0; hh < 2; ++hh) ov[hh] = (u32x4){pk_bf16(sc[hh][0][0], sc[hh][0][1]), pk_bf16(sc[hh][0][2], sc[hh][0][3]), pk_bf16(sc[hh][1][0], sc[hh][1][1]), pk_bf16(sc[hh][1][2], sc[hh][1][3])};
    STAMP(10);
#pragma unroll
    for (int hh = 0; hh < 2; ++hh) __builtin_amdgcn_raw_buffer_store_b128(ov[hh], rO, (((m0 >> 4) + wave) * RBO + tn * 8 + hh * 4) * 256 + lane * 16, 0, 16);
    STAMP(11);
  }
  STAMP(7);
  STAMP(8);
  const Tag tg = tag_get(lds0);
  asm volatile("s_waitcnt vmcnt(0)" ::: "memory");
  raw_barrier();
  if (wave == 0) flag_set(a.sync, F_O(tm) + tn * 8, tg, 0);
  hook();
  STAMP(3);
#endif
}

struct Late { u32x4 v[3]; };
template <class HK>
__device__ __forceinline__ void glu_phase(const GemmArgs& a, HK hook, Late& late) {
#if defined(__HIP_DEVICE_COMPILE__)
  constexpr int LDW = DF_, WN = 2;
  constexpr int UPOFF = 32 * 224 + 16;
  constexpr int XT = 32768, WU = 14592;
  constexpr int XBASE = 0, WBASE = 3 * XT, RING = WBASE + 4 * WU;
  constexpr int MF = 4, NF = 7;
  constexpr int NT = 16, NXI = 16, NWI = 14, NLW = 4, XI = 4, WI = 4;
  extern __shared__ __attribute__((aligned(16))) char smem[];
  STAMP(0);
  const unsigned lds0 = (unsigned)(size_t)(lds_char_ptr)smem;
  const int tid = threadIdx.x, lane = tid & 63;
  const int wave = __builtin_amdgcn_readfirstlane(tid >> 6);
  const int wm = wave / WN, wn = wave % WN;
  const int bid = blockIdx.x, xcd = bid & 7, idx = bid >> 3;
  const int tm = (xcd / 4) * 4 + idx / 8, tn = (xcd % 4) * 8 + idx % 8;
  const int m0 = tm * 256;
  const int krot = (tm >> 2) * 8;
  const int fr = lane & 15, fq = lane >> 4;
  const int wq = (lane >> 2) & 3, wp = lane & 3;
  const int panel = m0 >> 8;
  constexpr int TCH = RING + 5120;
  constexpr int DONE = TCH + 896;
  auto done_inc = [&]() { if (lane == 0) asm volatile("ds_add_u32 %0, %1" :: "v"(lds0 + DONE), "v"(1) : "memory"); };
  auto touch = [&](const void* base, unsigned voff, unsigned soff, unsigned lim = 0x7fffffffu) {
    __builtin_amdgcn_raw_ptr_buffer_load_lds(RSRCN(base, lim), LDSP(lds0 + TCH), 4, voff, soff, 0, 0);
  };
  const auto rO = RSRC(a.O);

  unsigned xsrc[XI], wsrc[WI];
#pragma unroll
  for (int i = 0; i < XI; ++i) {
    const int j = (wave % NLW) + NLW * i;
    xsrc[i] = (unsigned)((((m0 >> 4) + (j >> 1)) * RBH + (j & 1) * 4) * 256 + lane * 16);
  }
#pragma unroll
  for (int i = 0; i < WI; ++i) {
    int j = (wave % NLW) + NLW * i; if (j >= NWI) j -= NLW;
    const int jj = j >= 7 ? j - 7 : j;
    const int b = jj * 1024 + lane * 16, p = b / 224, pos = (b % 224) >> 4;
    const int row = 8 * ((p & 15) >> 2) + (p & 3) + 4 * (p >> 4);
    wsrc[i] = (unsigned)(row * LDW + tn * 112 + pos * 8) * 2u;
  }
  auto ldX = [&](int i, int tile, int part, int slot) {
    tile = (tile + krot) & 15;
    const int j = wave + NLW * i;
    __builtin_amdgcn_raw_ptr_buffer_load_lds(RSRC(a.X), LDSP(lds0 + XBASE + slot * XT + (part * NXI + j) * 1024), 16, xsrc[i], tile * 2048 + part * (NXI / 2) * RBH * 256, 0, 16);
  };
  auto ldW = [&](int i, int unit, int slot) {
    const auto rW0 = RSRC(a.W0);
    const auto rW1 = RSRC(a.W1);
    int j = wave + NLW * i; if (j >= NWI) j -= NLW;
    unit = (unit + 2 * krot) & 31;
    const bool up = j >= 7;
    __builtin_amdgcn_raw_ptr_buffer_load_lds(up ? rW1 : rW0, LDSP(lds0 + WBASE + slot * WU + (up ? UPOFF + (j - 7) * 1024 : j * 1024)), 16, wsrc[i], unit * 32 * LDW * 2, 0, 0);
  };
  auto issueX = [&](int tile, int part, int slot) {
    if (wave >= NLW) return;
#pragma unroll
    for (int i = 0; i < XI; ++i) ldX(i, tile, part, slot);
  };
  auto issueW = [&](int unit, int slot) {
    if (wave >= NLW) return;
#pragma unroll
    for (int i = 0; i < WI; ++i) ldW(i, unit, slot);
  };

  f32x4 acc[NF][MF];
#pragma unroll
  for (int n = 0; n < NF; ++n)
#pragma unroll
    for (int m = 0; m < MF; ++m) acc[n][m] = (f32x4){0.f, 0.f, 0.f, 0.f};
  f32x4 accs = {0.f, 0.f, 0.f, 0.f};

  const unsigned xa0 = lds0 + XBASE + wm * MF * 16 * 128 + lane * 16, xa1 = xa0 + 1024;
  unsigned wa[2];
#pragma unroll
  for (int c = 0; c < 2; ++c) wa[c] = lds0 + WBASE + (4 * fq + wq) * 224 + wn * 112 + (c == 0 ? wp * 8 : 96 + (wp & 1) * 8 + (wp >> 1) * UPOFF);

  using I0 = IC<0>; using I1 = IC<1>;
  using T = std::true_type; using F = std::false_type;
  typedef Frags<MF, NF> FR;
  struct RdCtx { unsigned xa, wb[2]; };
  auto mkctx = [&](bool ks1, unsigned xoffs, unsigned woffs) {
    RdCtx c; c.xa = (ks1 ? xa1 : xa0) + xoffs; c.wb[0] = wa[0] + woffs; c.wb[1] = wa[1] + woffs;
    return c;
  };
  auto rd_item = [&](FR& f, const RdCtx& c, auto kc) {
    constexpr int k = decltype(kc)::value;
    if constexpr (k < MF) f.x[k] = ds_b128<k * 2048>(c.xa);
    else {
      constexpr int n = k - MF, cc = n == 6, o = n == 6 ? 0 : (n >> 1) * 32 + (n & 1) * UPOFF;
      f.lo[n] = ds_tr<o>(c.wb[cc]); f.hi[n] = ds_tr<o + 16 * 224>(c.wb[cc]);
    }
  };
  auto wait_frags = [&](FR& f) {
    asm volatile("s_waitcnt lgkmcnt(0)" ::: "memory");
#pragma unroll
    for (int m = 0; m < MF; ++m) asm volatile("" : "+v"(f.x[m]));
#pragma unroll
    for (int n = 0; n < NF; ++n) asm volatile("" : "+v"(f.lo[n]), "+v"(f.hi[n]));
  };
  auto step = [&](FR& cur, FR& nxt, const RdCtx& c, auto PFc, auto NLc, auto&& ld) {
    constexpr bool PF = decltype(PFc)::value != 0; constexpr int NL = decltype(NLc)::value;
    constexpr int R = MF + NF, RG = NF - 1;
    static_for<0, NF>([&](auto gc) {
      constexpr int g = decltype(gc)::value;
      if constexpr (PF) static_for<0, R>([&](auto kc) { constexpr int k = decltype(kc)::value; if constexpr (k * RG / R == g) rd_item(nxt, c, kc); });
      static_for<0, NL>([&](auto ic) { constexpr int i = decltype(ic)::value; if constexpr (i * NF / NL == g) ld(i); });
      const bf16x8 w = __builtin_shufflevector(cur.lo[g], cur.hi[g], 0, 1, 2, 3, 4, 5, 6, 7);
#pragma unroll
      for (int m = 0; m < MF; ++m) acc[g][m] = MFMA(w, cur.x[m], acc[g][m]);
      __builtin_amdgcn_sched_barrier(0);
    });
    if constexpr (PF) wait_frags(nxt);
  };
  auto stepg = [&](auto CSc, FR& cur, FR& nxt, const RdCtx& c, auto R0c, auto IBc, auto&& sync, auto NLc, auto&& ld) {
    constexpr bool CS = decltype(CSc)::value; constexpr int R0 = decltype(R0c)::value, IB = decltype(IBc)::value, NL = decltype(NLc)::value;
    constexpr int NM = NF * MF, NR = MF + 2 * NF;
    static_for<0, NM>([&](auto ic) {
      constexpr int I = decltype(ic)::value, g = I / MF, m = I % MF;
      constexpr int RL = IB > 8 ? 20 : 22;
      const bf16x8 w = __builtin_shufflevector(cur.lo[g], cur.hi[g], 0, 1, 2, 3, 4, 5, 6, 7);
      acc[g][m] = MFMA(w, cur.x[m], acc[g][m]);
      __builtin_amdgcn_sched_barrier(0);
      if constexpr (I == IB) sync();
      static_for<0, NR>([&](auto rc) {
        constexpr int r = decltype(rc)::value;
        if constexpr (R0 + r * (RL - R0) / (NR - 1) == I) {
          if constexpr (r < MF) nxt.x[r] = ds_b128<r * 2048>(c.xa);
          else {
            constexpr int n = (r - MF) >> 1, cc = n == 6, o = n == 6 ? 0 : (n >> 1) * 32 + (n & 1) * UPOFF;
            if constexpr ((r - MF) & 1) nxt.hi[n] = ds_tr<o + 16 * 224>(c.wb[cc]); else nxt.lo[n] = ds_tr<o>(c.wb[cc]);
          }
        }
      });
      static_for<0, NL>([&](auto lc) { constexpr int i = decltype(lc)::value; if constexpr (R0 + i * (NM - 1 - R0) / NL == I) ld(i); });
      if constexpr (CS && m == MF - 1) {
        const unsigned sv = fr == g ? 0x3f803f80u : 0u;
        const u32x4 s4 = {sv, sv, sv, sv};
        accs = MFMA(w, __builtin_bit_cast(bf16x8, s4), accs);
      }
      __builtin_amdgcn_sched_barrier(0);
    });
    wait_frags(nxt);
  };
  FR fa, fb;

  f32x4* tb_row = (f32x4*)(smem + RING);
  float* tb_cs = (float*)(tb_row + 256);
  constexpr int RTAB = 524288;
  auto glu_stats_issue = [&]() {
    if (wave == 7) __builtin_amdgcn_raw_ptr_buffer_load_lds(RSRC(a.st), LDSP(lds0 + RING + 3072), 16, ((lane >> 2) * S_ + m0 + tn * 8 + (lane & 3) * 2) * 8, 0, 0, 16);
  };
  auto glu_stats = [&]() {
    int lx = lane; asm volatile("" : "+v"(lx));
    const unsigned sa = lds0 + RING + 3072 + lx * 8;
    f32x2 p0 = ds_b64<0>(sa), p1 = ds_b64<512>(sa);
    asm volatile("s_waitcnt lgkmcnt(0)" : "+v"(p0), "+v"(p1) :: "memory");
    const float mu = sum8(p0[0] + p1[0]) * (1.f / 16.f);
    const float v = sum8((p0[1] + p1[1]) + 64.f * ((p0[0] - mu) * (p0[0] - mu) + (p1[0] - mu) * (p1[0] - mu))) * (1.f / 1024.f) + 1e-5f;
    const float rs = 1.0f / sqrtf(v);
    const f32x4 rv = {mu, -0.10294324f * rs * rs * rs, -2.3022082f * rs, v};
    __builtin_amdgcn_raw_buffer_store_b128(__builtin_bit_cast(u32x4, rv), RSRC(a.cs), lx < 8 ? (unsigned)(RTAB + (m0 + tn * 8 + lx) * 16) : 0x80000000u, 0, 16);
  };
  auto epilogue = [&](auto EAc) __attribute__((always_inline)) {
    STAMP(2);
    constexpr bool EARLY = decltype(EAc)::value;
    if constexpr (!EARLY) {
      lds_wait8(lds0 + DONE);
      __builtin_amdgcn_s_setprio(KN_P0);
      STAMPW(9, 4);
    } else __builtin_amdgcn_s_setprio(KN_P1);
    asm volatile("" ::: "memory");
    f32x4 cg[4], cu[4], rw[MF], rw6[2];
    int lz = lane; asm volatile("" : "+v"(lz));
    const int fr = lz & 15, fq = lz >> 4;
#pragma unroll
    for (int p = 0; p < 4; ++p) {
      const int o = wn * 56 + p * 16 + (p == 3 ? (fq & 1) : fq) * 4;
      cg[p] = *(const f32x4*)(tb_cs + o); cu[p] = *(const f32x4*)(tb_cs + 112 + o);
    }
#pragma unroll
    for (int m = 0; m < MF; ++m) rw[m] = tb_row[(wm * MF + m) * 16 + fr];
#pragma unroll
    for (int q = 0; q < 2; ++q) rw6[q] = tb_row[(wm * MF + 2 * q + (fq >> 1)) * 16 + fr];
    const int gz = ((m0 >> 4) + wm * 4) * RBA * 256 + fr * 16;
    const int go0 = gz + ((fq & 1) * RBA + (fq >> 1)) * 256;
    const unsigned le = lds0 + OWNE + (wm * 4 + (fq & 1)) * 2048 + (wn * 4 + (fq >> 1)) * 256 + fr * 16, lm = lds0 + OWNM + (wm * 4 + (fq & 1)) * 1024 + (wn * 2 + (fq >> 1)) * 256 + fr * 16;
    unsigned hv[MF][NF];
    unsigned dw = dn_wsrc(lz, wave); asm volatile("" : "+v"(dw));
    const Tag tg = tag_get(lds0);
    auto put = [&](unsigned a0, unsigned a1, unsigned b0, unsigned b1, int vo, int so, unsigned la, auto LOc) {
      const auto s0 = __builtin_amdgcn_permlane16_swap(a0, b0, false, false), s1 = __builtin_amdgcn_permlane16_swap(a1, b1, false, false);
      const u32x4 v = {s0[0], s1[0], s0[1], s1[1]};
      __builtin_amdgcn_raw_buffer_store_b128(v, rO, vo, so, 16);
      if constexpr (decltype(LOc)::value >= 0) ds_w128<decltype(LOc)::value>(la, v);
      __builtin_amdgcn_sched_barrier(0);
      asm volatile("s_nop 1" ::: "memory");
      __builtin_amdgcn_sched_barrier(0);
    };
#define STAGE(body) _Pragma("unroll") for (int i = 0; i < 2 * NN; ++i) { const int m = MB + i / NN, n = N0 + i % NN; const f32x4 k = n == 6 ? rw6[m >> 1] : rw[m]; body; } __builtin_amdgcn_sched_barrier(0)
    auto grp = [&](auto MBc, auto N0c, auto NNc, auto&& mid, auto&& at) {
      constexpr int MB = decltype(MBc)::value, N0 = decltype(N0c)::value, NN = decltype(NNc)::value;
      f32x2 G[2 * NN], U[2 * NN], P[2 * NN];
      __builtin_amdgcn_sched_barrier(0);
      STAGE({ const int e = (n == 6 ? m & 1 : n & 1) * 2; const f32x4 x = n == 6 ? acc[6][m & 2] : acc[n & 6][m]; G[i] = ((f32x2){x[e], x[e + 1]}) - k[0] * ((f32x2){cg[n >> 1][e], cg[n >> 1][e + 1]}); });
      STAGE(P[i] = G[i] * G[i]);
      at(I0{});
      STAGE(P[i] = P[i] * k[1] + k[2]);
      STAGE(P[i] = G[i] * P[i]);
      at(I1{});
      STAGE({ const int e = (n == 6 ? m & 1 : n & 1) * 2; const f32x4 x = n == 6 ? acc[6][m | 1] : acc[n | 1][m]; U[i] = ((f32x2){x[e], x[e + 1]}) - k[0] * ((f32x2){cu[n >> 1][e], cu[n >> 1][e + 1]}); });
      STAGE(P[i] = ((f32x2){__builtin_amdgcn_exp2f(P[i][0]), __builtin_amdgcn_exp2f(P[i][1])}));
      mid();
      at(IC<2>{});
      __builtin_amdgcn_sched_barrier(0);
      STAGE(P[i] = P[i] * k[3] + k[3]);
      STAGE(P[i] = ((f32x2){__builtin_amdgcn_rcpf(P[i][0]), __builtin_amdgcn_rcpf(P[i][1])}));
      at(IC<3>{});
      STAGE(U[i] = G[i] * U[i]);
      STAGE(U[i] = U[i] * P[i]);
      STAGE({ hv[m][n] = pk_bf16(U[i][0], U[i][1]); asm volatile("" : "+v"(hv[m][n])); });
    };
#undef STAGE
    using I2 = IC<2>; using I3 = IC<3>; using I4 = IC<4>;
    const int se = (tn * 12 + wn * 4) * 256;
#define KN_PE 2
    auto none = [&](auto) {};
    auto put01 = [&]() { put(hv[0][0], hv[0][1], hv[1][0], hv[1][1], go0, se, le, I0{}); put(hv[0][2], hv[0][3], hv[1][2], hv[1][3], go0 + 512, se, le, IC<512>{}); };
    grp(I0{}, I0{}, I4{}, [&]() { if constexpr (EARLY) lds_wait8(lds0 + DONE); }, [&](auto tc) { constexpr int t = decltype(tc)::value;
      if constexpr (EARLY) { if constexpr (t >= 2) { hook(IC<3 + 2 * t>{}, dw); hook(IC<4 + 2 * t>{}, dw); } } else hook(IC<7 + t>{}, dw); });
    hook(IC<11>{}, dw);
    grp(I2{}, I0{}, I4{}, [&]() {}, [&](auto tc) { constexpr int t = decltype(tc)::value; if constexpr (t == 0) hook(IC<12>{}, dw); if constexpr (t == KN_PE) put01(); });
    put(hv[2][0], hv[2][1], hv[3][0], hv[3][1], go0, se + 2 * RBA * 256, le, IC<4096>{}); put(hv[2][2], hv[2][3], hv[3][2], hv[3][3], go0 + 512, se + 2 * RBA * 256, le, IC<4096 + 512>{});
    asm volatile("" ::: "memory");
    __builtin_amdgcn_sched_barrier(0);
    STAMP(4);
    STAMPW(11, 4);
    __builtin_amdgcn_s_setprio(EARLY ? KN_P3 : KN_P2);
#pragma unroll
    for (int q = 0; q < 2; ++q)
#pragma unroll
      for (int e = 0; e < 4; ++e) {
        const float x0 = acc[6][2 * q][e], x1 = acc[6][2 * q + 1][e];
        auto sw = __builtin_amdgcn_permlane32_swap(__float_as_uint(x0), __float_as_uint(x1), false, false);
        acc[6][2 * q][e] = __uint_as_float(sw[0]); acc[6][2 * q + 1][e] = __uint_as_float(sw[1]);
      }
    grp(I0{}, I4{}, I3{}, [&]() {}, none);
    { wait_vm<0>(); flag_set(a.sync, F_ACT + ((panel * 4 + wm) * 64 + tn * 2 + wn) * 8, tg, 1); STAMPW(6, 4); STAMP(10); }
    auto keep = [&](unsigned a0, unsigned a1, unsigned b0, unsigned b1) {
      const auto s0 = __builtin_amdgcn_permlane16_swap(a0, b0, false, false), s1 = __builtin_amdgcn_permlane16_swap(a1, b1, false, false);
      return (u32x4){s0[0], s1[0], s0[1], s1[1]}; };
    grp(I2{}, I4{}, I3{}, [&]() { late.v[0] = keep(hv[0][4], hv[0][5], hv[1][4], hv[1][5]); ds_w128<0>(lm, late.v[0]); }, none);
    late.v[1] = keep(hv[2][4], hv[2][5], hv[3][4], hv[3][5]); ds_w128<2048>(lm, late.v[1]);
    late.v[2] = keep(hv[0][6], hv[1][6], hv[2][6], hv[3][6]);
    asm volatile("" ::: "memory");
    __builtin_amdgcn_s_setprio(0);
  };

  constexpr int WR = 2;
  if (wave == 5) ds_w32(lds0 + DONE, 0);
  STAMP(9); STAMP(6);
  auto check_tile = [&](int t) {
    int lx = lane; asm volatile("" : "+v"(lx));
    const unsigned ad = lds0 + XBASE + t * XT + wave * 1024 + lx * 16;
    if (__builtin_expect(marked<8>(ad), 0)) {
      for (int tries = 0;; ++tries) {
        if (tries >= KN_TRY) wait_flags(lds0, a.sync, F_H + panel * 256, 32, 0);
#pragma unroll 1
        for (int u = t; u < t + 1 + (t == 0); ++u) { issueX(u, 0, u); issueX(u, 1, u); }
        if (t == 0) wait_vm<2 * XI>(); else wait_vm<0>();
        if (tries >= KN_TRY || !marked<8>(ad)) break;
      }
    }
  };
  auto run = [&](auto LDRc) __attribute__((always_inline)) {
    constexpr bool LDR = decltype(LDRc)::value;
    if constexpr (LDR) {
      issueX(0, 0, 0); issueX(0, 1, 0); issueX(1, 0, 1); issueX(1, 1, 1);
      issueW(2, 2 ^ WR); issueW(3, 3 ^ WR); wait_vm<2 * XI + 2 * WI>();
      check_tile(0);
    }
    lds_barrier();
    if constexpr (!LDR) { if (wave == 4) __builtin_amdgcn_raw_ptr_buffer_load_lds(RSRC(a.sync), LDSP(lds0 + TCH + 256), 4, lane * 4, F_H + panel * 256, 0, 16); }
    { const RdCtx c = mkctx(false, 0, WR * WU); static_for<0, MF + NF>([&](auto kc) { rd_item(fa, c, kc); }); }
    wait_frags(fa);
    STAMP(1);
    { const RdCtx c = mkctx(true, 0, (WR + 1) * WU); step(fa, fb, c, T{}, IC<LDR ? 2 * XI : 0>{}, [&](int i) { ldX(i % XI, 2, i / XI, 2); }); }
    int xs = 1;
    unsigned long long tb = (unsigned long long)a.PF0; unsigned tv = 0, tl = 0x7fffffffu, ts = 14336u, tmask = 0; int tj = 1;
    if constexpr (!LDR) {
      unsigned long long c0, c1; CODE_ADDR(c0, "k_all"); CODE_ADDR(c1, "k_zend");
#define KN_DJ 10
      constexpr int CTJ = 11, WJ = 1, WE = 8, DJ = KN_DJ, KT = 4;
      tj = DJ;
      if (wave == 4) { tb = c0; tl = c1 - c0 < 262144ull ? (unsigned)(c1 - c0) : 0u; ts = 0; tmask = 1u << CTJ; }
      if (wave == 5) tmask = 0xfu << DJ;
      if (wave == 6) { tb = (unsigned long long)((idx & 16) ? a.W1 : a.W0); ts = 64 * LDW * 2; tmask = ((1u << (WE - KT)) - 1u) << WJ; tj = WJ - krot - KT; }
      const int l4 = lane / 14;
      tv = wave == 4 ? (unsigned)((lane * 32 + idx) * 128)
         : (lane >= 56 ? 0x80000000u : (wave == 5 ? (unsigned)(((xcd & 3) * 32 + idx) * 57344 + lane * 128) : (unsigned)(((idx & 15) * 4 + l4) * (LDW * 2) + (xcd & 3) * 1792 + (lane - l4 * 14) * 128)));
    }
    const int js = 2 * (tm & 3) + 1;
    const auto rC = RSRC(a.cs);
    constexpr int JR = 10, JS = 2;
    const int jf = wave == 4 ? NT - 4 : (wave == 7 ? JR : 64);
    const int fof = wave == 4 ? F_COL + tn * 256 : F_ROW + panel * 256;
    const unsigned evm = tmask | (8u << js) | (wave == 4 ? 3u << (NT - 4) : 0u) | (wave == 7 ? ((5u << JS) | 2u | (11u << JR)) : 0u);
    constexpr int CFL = TCH + 256, CPB = XBASE + ((NT - 3) % 3) * XT;
    auto iter = [&](int j, auto LWc, auto LXc, auto WAITc, auto CAc, auto CBc) {
      constexpr bool LW = decltype(LWc)::value && LDR, LX = decltype(LXc)::value && LDR, LP = LW && !LX;
      if constexpr (LDR) {
        wait_vm<decltype(WAITc)::value>();
        if constexpr (LX) { if (__builtin_expect(j <= 2, 0)) check_tile(j); }
      }
      if constexpr (!LDR) {
        if (__builtin_expect((evm >> j) & 1, 0)) {
          if (j == js + 3) {
            if ((tmask >> (j - 1)) & 1) wait_vm<1>(); else wait_vm<0>();
            flag_set(a.sync, F_COL + tn * 256 + (tm * 4 + wave - 4) * 8, tag_get(lds0), 0);
          }
          if (j == 1) glu_stats_issue();
          if (j == JS && wave == 7) { wait_vm<0>(); glu_stats(); }
          if (j == JS + 2 && wave == 7) {
            if (j == js + 2) wait_vm<1>(); else wait_vm<0>();
            flag_set(a.sync, fof + tn * 8, tag_get(lds0), 0);
          }
          {
            int lx = lane; asm volatile("" : "+v"(lx));
            if (j == jf) __builtin_amdgcn_raw_ptr_buffer_load_lds(RSRC(a.sync), LDSP(lds0 + CFL + (wave & 1) * 256), 4, lx * 4, fof, 0, 16);
            if (j == jf + 1) {
              wait_vm<0>();
              int fv = ds_r32(lds0 + CFL + (wave & 1) * 256 + lx * 4);
              const Tag tg = tag_get(lds0);
              asm volatile("" : "+v"(fv));
              while (!words_up((unsigned)fv, tg) && in_time(lds0))
                asm volatile("global_load_dword %0, %1, %2 sc1\n\ts_waitcnt vmcnt(0)" : "=v"(fv) : "v"(lx * 4), "s"((const char*)a.sync + fof) : "memory");
              if (wave == 7) {
#pragma unroll
                for (int i = 0; i < 4; ++i) __builtin_amdgcn_raw_ptr_buffer_load_lds(rC, LDSP(lds0 + RING + i * 1024), 16, lx * 16, RTAB + m0 * 16 + i * 1024, 0, 16);
              }
            }
            if (j == jf + 3) wait_vm<0>();
          }
          if ((tmask >> j) & 1) { touch((const void*)tb, tv, ((j - tj) & 15) * ts, tl); if (wave == 5) touch((const void*)tb, tv, ((j - tj) & 15) * ts + 7168, tl); }
        }
        __builtin_amdgcn_sched_barrier(0);
      }
      int xp = xs + 2; if (xp >= 3) xp -= 3;
      const int ws = ((j & 1) * 2) ^ WR;
      constexpr bool LD = LDR && !LW;
      unsigned dw = 0;
      if constexpr (LD) { int lx = lane; asm volatile("" : "+v"(lx)); dw = dn_wsrc(lx, wave); }
      auto ldd = [&](int i) { static_for<0, 14>([&](auto kc) { if (i == decltype(kc)::value) hook(IC<64 + decltype(kc)::value>{}, dw); }); };
      auto lda = [&](int i) { if constexpr (LD) ldd(i); else ldW(i % WI, 2 * j + 2 + i / WI, (ws ^ 2) + i / WI); };
      auto ldb = [&](int i) {
          if constexpr (LX) ldX(i % XI, j + 2, i / XI, xp);
          else if constexpr (LD) ldd(7 + i);
          else __builtin_amdgcn_raw_ptr_buffer_load_lds(rC, LDSP(lds0 + CPB + wave * 8192 + i * 1024), 16, lane * 16, (tn * 2 + wn) * 7168 + i * 1024, 0, 16); };
      using NLA = IC<LW ? 2 * WI : (LD ? 7 : 0)>; using NLB = IC<LX ? 2 * XI : (LP || LD ? 7 : 0)>;
      { const RdCtx c = mkctx(false, xs * XT, ws * WU); stepg(CAc, fb, fa, c, IC<LDR ? 2 : 0>{}, IC<LDR ? 1 : -1>{}, [&]() { raw_barrier(); }, NLA{}, lda); }
      { const RdCtx c = mkctx(true, xs * XT, (ws + 1) * WU); stepg(CBc, fa, fb, c, I0{}, IC<LDR ? -1 : 25>{},
          [&]() { asm volatile("s_waitcnt lgkmcnt(0)" ::: "memory"); if (decltype(WAITc)::value == 0 || j + 1 < NT) raw_barrier(); }, NLB{}, ldb); }
      xs = xs + 1; if (xs >= 3) xs = 0;
    };
    using WX_ = IC<2 * XI>;
    bf16x8 pv[8];
    if constexpr (LDR) {
      for (int j = 1; j < NT - 2; ++j) iter(j, T{}, T{}, WX_{}, F{}, F{});
      iter(NT - 2, T{}, F{}, WX_{}, F{}, F{});
      __builtin_amdgcn_s_setprio(3);
      iter(NT - 1, F{}, F{}, IC<7>{}, F{}, F{});
      wait_vm<14>();
      const unsigned ca = lds0 + CPB + wave * 8192 + fq * 1792 + (fr < 14 ? fr : 0) * 16;
      static_for<0, 8>([&](auto ic) { constexpr int i = decltype(ic)::value; pv[i] = ds_b128<i * 224>(ca); });
    } else {
      int j = 1;
      if (wave == 4) {
        wait_vm<0>();
        int fv = ds_r32(lds0 + TCH + 256 + lane * 4);
        const Tag tg = tag_get(lds0);
        asm volatile("" : "+v"(fv));
        if (!words_up((unsigned)fv, tg)) wait_flags(lds0, a.sync, F_H + panel * 256, 32, 0);
      }
      raw_barrier();
#pragma unroll 1
      for (; j < js; ++j) iter(j, F{}, F{}, I0{}, F{}, F{});
      const int ja = wm == 2 ? js + 2 : js;
#pragma unroll 1
      for (; j < ja; ++j) iter(j, F{}, F{}, I0{}, T{}, F{});
#pragma unroll 1
      for (; j < js + 2; ++j) iter(j, F{}, F{}, I0{}, F{}, T{});
      { int lx = lane; asm volatile("" : "+v"(lx));
        __builtin_amdgcn_raw_buffer_store_b128(__builtin_bit_cast(u32x4, accs), rC, (lx & 15) < NF ? (unsigned)((tn * 4 + wn * 2) * 3584 + (tm * 2 + (wm & 1)) * 224
                + ((lx & 15) < 6 ? (lx & 1) * 3584 + ((lx & 15) >> 1) * 64 + (lx >> 4) * 16 : (lx >> 5) * 3584 + 192 + (lx & 16))) : 0x80000000u, 0, 16); }
#pragma unroll 1
      for (; j < NT; ++j) iter(j, F{}, F{}, I1{}, F{}, F{});
      wait_vm<0>();
      done_inc();
    }
    { const RdCtx c = mkctx(false, 0, 0); step(fb, fa, c, F{}, IC<LDR ? 2 : 0>{}, [&](int i) {
        if (i == 1) {
          asm volatile("s_waitcnt lgkmcnt(0)" ::: "memory");
#pragma unroll
          for (int k = 0; k < 8; ++k) asm volatile("" : "+v"(pv[k]));
          f32x4 cv = ((__builtin_bit_cast(f32x4, pv[0]) + __builtin_bit_cast(f32x4, pv[1])) + (__builtin_bit_cast(f32x4, pv[2]) + __builtin_bit_cast(f32x4, pv[3])))
                   + ((__builtin_bit_cast(f32x4, pv[4]) + __builtin_bit_cast(f32x4, pv[5])) + (__builtin_bit_cast(f32x4, pv[6]) + __builtin_bit_cast(f32x4, pv[7])));
#pragma unroll
          for (int k = 0; k < 4; ++k) { const float t = cv[k]; auto sw = __builtin_amdgcn_permlane16_swap(__float_as_uint(t), __float_as_uint(t), false, false); cv[k] = __uint_as_float(sw[0]) + __uint_as_float(sw[1]); }
          if ((lane & 31) < 14) *(f32x4*)(tb_cs + (lane >> 5) * 112 + wn * 56 + fr * 4) = cv;
          done_inc();
        } }); }
    if constexpr (LDR) __builtin_amdgcn_s_setprio(0);
    epilogue(LDRc);
  };
  if (wave < NLW) run(T{}); else run(F{});
  STAMP(7);
  STAMP(8);
  STAMPW(12, 4);
  STAMP(3);
#endif
}

#define KN_CT 7
#define KN_E 6
#define KN_B3 0
#define KN_WB 2
#define KN_GW 1
#define XAUXW 16
#define XAUXD 16
#define KN_WS 2
#define KN_HE 0
#define KN_FL 22
#define KN_LD 0
#define KN_DM 0
#define KN_LX 0
#define STAMPV(i) STAMP(i)
struct FragW { s16x4 lo[4], hi[4]; };
template <int MODE, int LO, int HI>
__device__ __forceinline__ void wo_phase(const GemmArgs& a, const GemmArgs& nx) {
#if defined(__HIP_DEVICE_COMPILE__)
  extern __shared__ __attribute__((aligned(16))) char smem[];
  if constexpr (MODE != 1) STAMP(0);
  const unsigned lds0 = (unsigned)(size_t)(lds_char_ptr)smem;
  const int tid = threadIdx.x, lane = tid & 63;
  const int wave = __builtin_amdgcn_readfirstlane(tid >> 6);
  const int bid = blockIdx.x, xcd = bid & 7, idx = bid >> 3;
  const int tm = (xcd >> 1) * 4 + (idx >> 3), tn = (xcd & 1) * 8 + (idx & 7);
  const int m0 = tm * 128, panel = m0 >> 8, rot = (xcd & 3) * 4;
  if constexpr (MODE == 1) {
    const auto rW = __builtin_amdgcn_make_buffer_rsrc((void*)a.W0, 0, 0x7fffffff, 0x00020000);
    const int row = wave * 8 + (lane >> 3);
    const unsigned wsrc = (unsigned)(row * D_ + tn * 64 + ((lane & 7) ^ wswz(row)) * 8) * 2u;
    static_for<LO, HI>([&](auto ic) { constexpr int i = decltype(ic)::value;
      __builtin_amdgcn_raw_ptr_buffer_load_lds(rW, (lds_void_ptr)(size_t)(lds0 + wo_pos(i >> 1) + (i & 1) * 8192 + wave * 1024), 16, wsrc, (((i & 1) * 8 + (i >> 1)) ^ rot) * (64 * D_ * 2), 0, 0); });
  } else {
    constexpr int PAD = 57344;
    const int g = wave >> 2, rw = wave & 3;
    const int fr = lane & 15, fq = lane >> 4, wq = (lane >> 2) & 3, wp = lane & 3;
    unsigned wa[4];
#pragma unroll
    for (int c = 0; c < 4; ++c) { const int row = 8 * fq + wq; wa[c] = lds0 + g * 8192 + row * 128 + (((c << 1) ^ wswz(row)) << 4) + wp * 8; }
    FragW f[3];
    auto rd = [&](auto ic, auto rc) {
      constexpr int i = decltype(ic)::value, r = decltype(rc)::value, P = wo_pos(i >> 1) + (i & 1) * 4096, B = P & ~32767, o = P & 32767;
      if constexpr (r & 1) f[i % 3].hi[r >> 1] = ds_tr<o + 512>(wa[r >> 1] + B); else f[i % 3].lo[r >> 1] = ds_tr<o>(wa[r >> 1] + B);
    };
    auto pin = [&](FragW& q) {
#pragma unroll
      for (int n = 0; n < 4; ++n) asm volatile("" : "+v"(q.lo[n]), "+v"(q.hi[n]));
    };
    static_for<0, 16>([&](auto rc) { constexpr int r = decltype(rc)::value; rd(std::integral_constant<int, r / 8>{}, std::integral_constant<int, r % 8>{}); });
    if constexpr (KN_CT & 4) { if (wave == 2) {
      unsigned long long c0, c1; CODE_ADDR(c0, "k_all"); CODE_ADDR(c1, "k_zend");
      wait_vm<0>();
      __builtin_amdgcn_raw_ptr_buffer_load_lds(__builtin_amdgcn_make_buffer_rsrc((void*)c0, 0, c1 - c0 < 262144ull ? (unsigned)(c1 - c0) : 0u, 0x00020000), (lds_void_ptr)(size_t)(lds0 + PAD), 4, (unsigned)((lane * 32 + idx) * 128), 0, 0, 0); } }
    if (wave == 1) wait_flags(lds0, a.sync, F_O(tm), 16, 0);
    if constexpr (KN_E & 2) {
      if (wave == 3) {
        const Tag t = tag_get(lds0);
        const unsigned fo = (unsigned)(((lane & 32) ? (xcd >> 2) * 4 + (idx >> 3) : panel) * 256 + (lane & 31) * 8);
        while (!flags_up(__builtin_amdgcn_raw_buffer_load_b64(RSRC(a.sync), fo, F_MARK, 16), 64, t, 0) && in_time(lds0)) {}
      }
    }
    const auto rX = __builtin_amdgcn_make_buffer_rsrc((void*)a.X, 0, 0x7fffffff, 0x00020000);
    const int rb = (m0 >> 4) + rw * 2 + g;
    unsigned xs[2] = {(unsigned)(rb * RBO * 256 + lane * 16), (unsigned)((rb ^ 1) * RBO * 256 + lane * 16)};
    const int ck = (fq & 1) * 2 + (fq >> 1);
    unsigned orow = (unsigned)((rb * 16 + fr) * (D_ * 2) + tn * 128 + ck * 16), ofrg = (unsigned)((rb * RBH + tn * 8 + ck) * 256 + fr * 16);
    asm volatile("" : "+v"(xs[0]), "+v"(xs[1]), "+v"(orow), "+v"(ofrg));
    STAMP(9);
    if constexpr (!KN_B3) { if (!(KN_CT & 4) || wave != 2) wait_vm<0>(); }
    lds_barrier();
    STAMP(6);
    pin(f[0]); pin(f[1]);
    u32x4 xr[16][2], res[2];
#define KN_XD 4
    auto ldX = [&](int i, int m) {
      if (i < 16) xr[i][m] = __builtin_amdgcn_raw_buffer_load_b128(rX, xs[m] + (i & 1) * 1024, ((g * 8 + (i >> 1)) ^ rot) * 2048, XAUXW);
      else if (i == 16) res[m] = __builtin_amdgcn_raw_buffer_load_b128(__builtin_amdgcn_make_buffer_rsrc((void*)a.R, 0, 0x7fffffff, 0x00020000), orow + m * 64, 0, 0);
    };
#pragma unroll
    for (int i = 0; i < KN_XD; ++i) { ldX(i, 0); ldX(i, 1); __builtin_amdgcn_sched_barrier(0); }
    unsigned gsrc[2];
#pragma unroll
    for (int i = 0; i < 2; ++i) {
      const int j = wave + 8 * i, jj = j >= 7 ? j - 7 : j;
      const int b = jj * 1024 + lane * 16, q = b / 224, pos = (b % 224) >> 4;
      gsrc[i] = (unsigned)((8 * ((q & 15) >> 2) + (q & 3) + 4 * (q >> 4)) * DF_ + ((xcd & 3) * 8 + (idx & 7)) * 112 + pos * 8) * 2u;
      asm volatile("" : "+v"(gsrc[i]));
    }
    f32x4 acc[4][2];
#pragma unroll
    for (int n = 0; n < 4; ++n) { acc[n][0] = (f32x4){0.f, 0.f, 0.f, 0.f}; acc[n][1] = acc[n][0]; }
    __builtin_amdgcn_sched_barrier(0);
    static_for<0, 16>([&](auto ic) {
      constexpr int i = decltype(ic)::value;
      if constexpr (i >= 2) { asm volatile("s_waitcnt lgkmcnt(%0)" :: "n"(i < 15 ? 8 : 0) : "memory"); pin(f[i % 3]); }
      if constexpr (KN_B3 && i == (KN_WB & ~1) - 2) raw_barrier();
      static_for<0, 8>([&](auto jc) {
        constexpr int j = decltype(jc)::value, n = j >> 1, m = j & 1;
        const bf16x8 w = __builtin_shufflevector(f[i % 3].lo[n], f[i % 3].hi[n], 0, 1, 2, 3, 4, 5, 6, 7);
        acc[n][m] = __builtin_amdgcn_mfma_f32_16x16x32_bf16(w, __builtin_bit_cast(bf16x8, xr[i][m]), acc[n][m], 0, 0, 0);
        if constexpr (i + 2 < 16) rd(std::integral_constant<int, i + 2>{}, jc);
        if constexpr (j == 1 || j == 5) ldX(i + KN_XD, j >> 2);
        __builtin_amdgcn_sched_barrier(0);
      });
      if constexpr (i == 0) STAMP(1);
    });
    STAMP(2);
    auto slot = [&](int w) { return (w < 6 ? 32768 : WOB - 6 * 4096) + w * 4096 + lane * 16; };
#pragma unroll
    for (int n = 0; n < 4; ++n) *(f32x4*)(smem + slot(wave) + n * 1024) = acc[n][1];
    asm volatile("" : "+v"(res[0]), "+v"(res[1]));
    lds_barrier();
    STAMP(4);
    f32x4 pa[4];
    static_for<0, 4>([&](auto nc) { constexpr int n = decltype(nc)::value; pa[n] = __builtin_bit_cast(f32x4, ds_b128<n * 1024>(lds0 + slot(wave ^ 4))); });
    if constexpr (KN_GW) {
      auto ldG = [&](int i, int u) { const int j = wave + 8 * i;
        __builtin_amdgcn_raw_ptr_buffer_load_lds(__builtin_amdgcn_make_buffer_rsrc((void*)(j >= 7 ? nx.W1 : nx.W0), 0, 0x7fffffff, 0x00020000),
                          (lds_void_ptr)(size_t)(lds0 + 98304 + (u ^ 2) * 14592 + (j >= 7 ? 7184 + (j - 7) * 1024 : j * 1024)), 16, gsrc[i], (u + (xcd >> 2) * 16) * 32 * DF_ * 2, 0, 0); };
      ldG(0, 0); ldG(0, 1);
      if (wave < 6) { ldG(1, 0); ldG(1, 1); }
    }
    asm volatile("s_waitcnt lgkmcnt(0)" : "+v"(pa[0]), "+v"(pa[1]), "+v"(pa[2]), "+v"(pa[3]) :: "memory");
    const auto rO = __builtin_amdgcn_make_buffer_rsrc((void*)a.O, 0, 0x7fffffff, 0x00020000);
    u32x4 o[2];
#pragma unroll
    for (int i = 0; i < 2; ++i) {
      const f32x4 u = acc[2 * i][0] + pa[2 * i], v = acc[2 * i + 1][0] + pa[2 * i + 1];
      const auto s0 = __builtin_amdgcn_permlane16_swap(pk_bf16(u[0], u[1]), pk_bf16(v[0], v[1]), false, false);
      const auto s1 = __builtin_amdgcn_permlane16_swap(pk_bf16(u[2], u[3]), pk_bf16(v[2], v[3]), false, false);
      const u32x4 q4 = {s0[0], s1[0], s0[1], s1[1]};
#pragma unroll
      for (int j = 0; j < 4; ++j) { const unsigned r = res[i][j], q = q4[j]; o[i][j] = pk_bf16(bflo(r) + bflo(q), bfhi(r) + bfhi(q)); }
      __builtin_amdgcn_raw_buffer_store_b128(o[i], rO, ofrg + i * 1024, 0, 16);
    }
    auto sum4 = [](float v) {
      auto p = __builtin_amdgcn_permlane16_swap(__float_as_uint(v), __float_as_uint(v), false, false); v = __uint_as_float(p[0]) + __uint_as_float(p[1]);
      auto q = __builtin_amdgcn_permlane32_swap(__float_as_uint(v), __float_as_uint(v), false, false); return __uint_as_float(q[0]) + __uint_as_float(q[1]); };
    f32x2 e[8], sm = {0.f, 0.f};
#pragma unroll
    for (int j = 0; j < 8; ++j) { const unsigned q = o[j >> 2][j & 3]; e[j] = (f32x2){bflo(q), bfhi(q)}; sm += e[j]; }
    const float mu = sum4(sm[0] + sm[1]) * (1.f / 64.f);
    f32x2 t2 = {0.f, 0.f};
#pragma unroll
    for (int j = 0; j < 8; ++j) { const f32x2 d = e[j] - mu; t2 += d * d; }
    const float q2 = sum4(t2[0] + t2[1]);
    __builtin_amdgcn_raw_buffer_store_b64((u32x2){__float_as_uint(mu), __float_as_uint(q2)}, __builtin_amdgcn_make_buffer_rsrc((void*)a.st, 0, 0x7fffffff, 0x00020000),
                                          lane < 16 ? (unsigned)((tn * S_ + rb * 16 + lane) * 8) : 0x80000000u, 0, 16);
    STAMP(7);
    STAMP(8);
    const Tag tg = tag_get(lds0);
    asm volatile("s_waitcnt vmcnt(0) lgkmcnt(0)" ::: "memory");
    raw_barrier();
    if (wave == 0) flag_set(a.sync, F_H + panel * 256 + ((tm & 1) * 16 + tn) * 8, tg, 0);
    STAMP(3);
  }
#endif
}

struct FragD { s16x4 lo[8], hi[8]; };
enum { O_X, O_W, O_SL, O_SP, O_FA, O_FP, O_QC, O_QD, O_QP, O_RES, O_LP, O_CE, O_CC, O_CD, O_CP, O_BAR };
constexpr int DN_A = 16, DN_PRE = 13;
constexpr unsigned char DN_OPS[][4] = {
 {1,0,O_CE,2}, {1,0,O_QC,0}, {1,2,O_X,6}, {1,5,O_X,7}, {1,9,O_X,8}, {1,13,O_X,9}, {2,2,O_X,10}, {2,5,O_X,11}, {2,9,O_X,12}, {2,13,O_SL,0},
 {3,0,O_CE,7}, {3,2,O_X,13}, {3,5,O_X,14}, {3,9,O_X,15}, {3,13,O_SL,1}, {4,2,O_BAR,12}, {4,3,O_X,16}, {4,6,O_X,17}, {4,10,O_X,18}, {4,13,O_SL,2},
 {5,2,O_X,19}, {5,5,O_X,20}, {5,9,O_W,14}, {5,13,O_W,13}, {6,2,O_X,21}, {6,5,O_X,22}, {6,9,O_W,16}, {6,13,O_W,15}, {7,0,O_FA,0}, {7,2,O_X,23}, {7,5,O_X,24}, {7,9,O_X,25}, {7,13,O_W,17},
 {8,2,O_X,26}, {8,5,O_X,27}, {8,9,O_X,28}, {8,13,O_W,18}, {9,2,O_X,29}, {9,5,O_X,30}, {9,9,O_X,31}, {9,13,O_W,19}, {9,16,O_QC,0}, {10,2,O_X,32}, {10,7,O_X,33}, {10,12,O_W,20},
 {11,0,O_CC,0}, {11,2,O_X,34}, {11,5,O_X,35}, {11,9,O_X,36}, {11,13,O_W,21}, {12,2,O_BAR,17}, {12,3,O_X,37}, {12,6,O_X,38}, {12,10,O_X,39}, {12,13,O_W,22},
 {13,2,O_X,40}, {13,5,O_X,41}, {13,9,O_X,42}, {13,13,O_W,23}, {14,2,O_X,43}, {14,5,O_X,44}, {14,9,O_X,45}, {14,13,O_W,24}, {14,16,O_QD,0},
 {15,2,O_X,46}, {15,7,O_X,47}, {15,12,O_W,25}, {16,0,O_CD,0}, {16,2,O_BAR,23}, {16,3,O_X,48}, {16,6,O_X,49}, {16,10,O_X,50}, {16,13,O_W,26},
 {17,2,O_X,51}, {17,5,O_X,52}, {17,9,O_X,53}, {17,13,O_W,27}, {18,2,O_X,54}, {18,5,O_X,55}, {18,9,O_W,28}, {18,13,O_RES,0}, {19,2,O_W,30}, {19,9,O_W,29},
 {20,2,O_W,31}, {20,5,O_SP,0}, {20,9,O_SP,1}, {20,13,O_RES,1}, {21,2,O_X,56}, {21,5,O_X,57}, {21,9,O_SP,2}, {21,13,O_SP,3}, {22,2,O_X,58}, {22,5,O_X,59}, {22,9,O_X,60}, {22,13,O_X,61},
 {23,2,O_BAR,31}, {23,3,O_X,62}, {23,10,O_X,63}, {24,0,O_FP,0}, {24,0,O_FP,1}, {24,2,O_SP,4}, {24,5,O_SP,5}, {26,0,O_FP,2}, {27,8,O_QP,2},
 {30,0,O_CP,0}, {30,0,O_CP,1}, {30,2,O_LP,0}, {30,3,O_LP,1}, {30,5,O_LP,2}, {30,7,O_LP,3}, {31,0,O_CP,2}, {31,2,O_LP,4}, {31,5,O_LP,5},
};
constexpr int DN_N = sizeof(DN_OPS) / 4, DN_H = (24 - DN_A) / 2, DN_B = DN_A + DN_H, NS = 32;
constexpr int dn_nm(int v) { return v < 24 ? 16 : 8; }
constexpr int dn_unit(int v, int n) { return (v < DN_A || v >= 24) ? v : DN_A + 2 * ((v - DN_A) % DN_H) + (n >> 2); }
constexpr int dn_wpos(int u, int h) { constexpr int P[19] = {8, 9, 10, 11, 16, 17, 18, 6, 7, 12, 13, 14, 15, 0, 1, 2, 3, 4, 5}; return P[u % 19] * 8192 + h * 4096; }
constexpr int dn_first(int v, int g) { int i = 0; while (i < DN_N && (DN_OPS[i][0] < v || (DN_OPS[i][0] == v && DN_OPS[i][1] < g))) ++i; return i; }
constexpr int dn_find(int kind, int arg) { for (int i = 0; i < DN_N; ++i) if (DN_OPS[i][2] == kind && DN_OPS[i][3] == arg) return i; return -1; }
constexpr int dn_vm(int lo, int hi) { int c = 0; for (int i = lo; i < hi; ++i) c += DN_OPS[i][2] <= O_LP; return c; }
constexpr bool dn_bar(int v) { for (int i = 0; i < DN_N; ++i) if (DN_OPS[i][0] == v && DN_OPS[i][2] == O_BAR) return true; return false; }
constexpr int dn_cnt(int w, int i) { int p = -1; for (int u = DN_PRE; u <= w; ++u) if (dn_find(O_W, u) > p) p = dn_find(O_W, u); return p >= 0 ? dn_vm(p + 1, i) : 63; }
constexpr bool dn_ok() {
  int have = DN_PRE - 1, bstep = 0;
  for (int v = 0; v + 1 < NS; ++v) {
    for (int i = dn_first(v, 0); i < dn_first(v + 1, 0); ++i) {
      if (DN_OPS[i][2] == O_BAR) { if (DN_OPS[i][3] > have) have = DN_OPS[i][3]; bstep = v; for (int u = DN_PRE; u <= have; ++u) if (dn_find(O_W, u) < 0 || dn_find(O_W, u) > i) return false; }
      if (DN_OPS[i][2] == O_W) {
        if (bstep < 1) return false;
        for (int t = bstep + 1; t < NS; ++t) for (int n = 0; n < 8; n += 4) if (dn_unit(t, n) < DN_OPS[i][3] && dn_wpos(dn_unit(t, n), 0) == dn_wpos(DN_OPS[i][3], 0)) return false;
      }
    }
    if (dn_unit(v + 1, 7) > have) return false;
  }
  for (int x = 6; x < 64; ++x) {
    int n = 0; for (int i = 0; i < dn_find(O_X, x); ++i) if (DN_OPS[i][2] == O_CE) n = DN_OPS[i][3];
    if (x < 34 ? n < (x - 2) / 4 : dn_find(O_X, x) < dn_find(x < 48 ? O_CC : O_CD, 0)) return false;
  }
  return dn_find(O_SP, 0) >= dn_first(DN_B, 0) && dn_find(O_SP, 4) >= dn_first(24, 0);
}
static_assert(dn_ok(), "schedule");
constexpr int dn_rg(int v, int r) { const int r0 = dn_bar(v) ? 2 : 1; return r0 + r * (dn_nm(v) - r0 + 1) / 16; }
constexpr int dn_rd(int v, int I) { int c = 0; if (v + 1 < NS) for (int r = 0; r < 16; ++r) c += dn_rg(v, r) <= I; return c; }
#define KN_SLP 5
#define KN_PRI 1
template <int MODE, int U>
__device__ __forceinline__ void dn_phase(const GemmArgs& a, const Late& late, unsigned wsrc = 0) {
#if defined(__HIP_DEVICE_COMPILE__)
  extern __shared__ __attribute__((aligned(16))) char smem[];
  if constexpr (MODE != 1) STAMP(0);
  const unsigned lds0 = (unsigned)(size_t)(lds_char_ptr)smem;
  const int tid = threadIdx.x;
  int lane = tid & 63; asm volatile("" : "+v"(lane));
  const int wave = __builtin_amdgcn_readfirstlane(tid >> 6);
  const int bid = blockIdx.x, xcd = bid & 7, idx = bid >> 3;
  const int panel = (xcd >> 2) * 4 + (idx >> 3), c = idx & 7, q = xcd & 3;
  const int g4 = wave & 3, hh = U >= 64 ? U & 1 : wave >> 2;
#define KN_ABL 0
  const auto rW = RSRCN(a.W0, (KN_ABL & 1) ? 0 : 0x7fffffff);
  if constexpr (MODE != 1) wsrc = dn_wsrc(lane, wave);
  auto uj = [&](int v) { return q * 8 + ((c + (v < 3 ? 0 : (v < 17 ? (v - 1) >> 1 : v - 16))) & 7); };
  auto us = [](int v) { return v < 3 ? v : (v < 17 ? (v - 1) & 1 : 2); };
  auto ldW = [&](int v) {
    const int row = v < 24 ? uj(v) * 112 + (us(v) < 2 ? us(v) * 56 + g4 * 8 : 32 + (g4 & 1) * 8 + (g4 >> 1) * 56) : ((((v + c) & 7) * 2 + hh) * 4 + g4) * 56 + 48;
    __builtin_amdgcn_raw_ptr_buffer_load_lds(rW, LDSP(lds0 + dn_wpos(v, 0) + hh * 4096 + g4 * 1024), 16, wsrc, row * (D_ * 2) + c * 256 + (((v < 24 ? hh : 0) ^ (q >> 1)) << 7), 0, 0);
  };
  const int fa_off = F_ACT + (panel * 4 + (wave >> 1)) * 512;
  if constexpr (MODE == 1) {
    if constexpr (U < 32) { if constexpr (U < DN_PRE) ldW(U); }
    else if constexpr (U >= 64) ldW((U - 64) >> 1);
    return;
  }
  const Tag tg = tag_get(lds0);
  const int fr = lane & 15, fq = lane >> 4, wq = (lane >> 2) & 3, wp = lane & 3;
  const int rb = wave * 2 + (q & 1);
  f32x4 acc[8][2];
#pragma unroll
  for (int n = 0; n < 8; ++n) { acc[n][0] = (f32x4){0.f, 0.f, 0.f, 0.f}; acc[n][1] = acc[n][0]; }
  unsigned wa[3][4];
#pragma unroll
  for (int i = 0; i < 4; ++i) { const int row = 8 * fq + wq; wa[0][i] = lds0 + row * 128 + (((i << 1) ^ wswz(row)) << 4) + wp * 8; wa[1][i] = wa[0][i] + 65536; wa[2][i] = wa[0][i] + 131072; }
  const auto rX = RSRCN(a.X, (KN_ABL & 2) ? 0 : 0x7fffffff);
  const unsigned xs[2] = {(unsigned)((panel * 16 + rb) * RBA * 256 + lane * 16), (unsigned)((panel * 16 + (rb ^ 1)) * RBA * 256 + lane * 16)};
  FragD fa, fb;
  u32x4 xr[32][2];
  auto rdw = [&](FragD& f, auto vc, auto rc) {
    constexpr int v = decltype(vc)::value, r = decltype(rc)::value, n = r >> 1, u = dn_unit(v, n);
    constexpr int p = dn_wpos(u, (v < DN_A || v >= 24) ? n >> 2 : (v < DN_B ? 1 : 0)), o = (p & 65535) + (r & 1) * 512, b = p >> 16;
    if constexpr (r & 1) f.hi[n] = ds_tr<o>(wa[b][n & 3]); else f.lo[n] = ds_tr<o>(wa[b][n & 3]);
  };
  auto ldX = [&](int v, int m) {
    if (v < 24) xr[v][m] = __builtin_amdgcn_raw_buffer_load_b128(rX, xs[m], uj(v) * 3072 + us(v) * 1024, 16);
    else xr[v][m] = __builtin_amdgcn_raw_buffer_load_b128(rX, xs[0] + m * 1024, 384 * 256 + ((v + c) & 7) * 2048, 16);
  };
  const auto rS = RSRC(a.slab);
  const int ck = (fq & 1) * 2 + (fq >> 1);
  u32x4 pp[3][2] = {}, res[2];
  auto ldP = [&](int j, bool ok) { pp[j >> 1][j & 1] = __builtin_amdgcn_raw_buffer_load_b128(RSRCN(a.slab, ok ? 0x7fffffff : 0), lane * 16 + (j & 1) * 1024, ((bid * 8 + wave) * 3 + (j >> 1)) * 2048, 16); };
  auto st128 = [&](u32x4 v, auto rs, unsigned vo, int so) {
    __builtin_amdgcn_raw_buffer_store_b128(v, rs, vo, so, 16);
    __builtin_amdgcn_sched_barrier(0);
    asm volatile("s_nop 1" ::: "memory");
    __builtin_amdgcn_sched_barrier(0); };
  auto stP = [&](int j) {
    const int k = 3 - (j >> 1), i = j & 1;
    const f32x4 s0 = acc[(k >> 1) * 4 + 2 * i][k & 1], s1 = acc[(k >> 1) * 4 + 2 * i + 1][k & 1];
    st128((u32x4){pk_bf16(s0[0], s0[1]), pk_bf16(s0[2], s0[3]), pk_bf16(s1[0], s1[1]), pk_bf16(s1[2], s1[3])}, rS, lane * 16 + i * 1024, (((bid ^ k) * 8 + wave) * 3 + (j >> 1)) * 2048); };
  auto stL = [&](int i) {
    const int so = (panel * 16 + (wave >> 1) * 4 + (i == 1 ? 2 : 0)) * RBA * 256 + (i < 2 ? (q * 8 + c) * 12 + 8 + (wave & 1) * 2 : 384 + (q * 8 + c) * 2 + (wave & 1)) * 256;
    st128(late.v[i], RSRC(a.X), (unsigned)((i < 2 ? (fq & 1) * RBA + (fq >> 1) : ck * RBA) * 256 + fr * 16), so); };
  const int fp_off = F_PART + (bid * 8 + wave) * 64;
  unsigned got = 0;
  u32x2 fl = {0u, 0u};
  auto rot16 = [&](int n) { const unsigned m = ((1u << (2 * n)) - 1u) << (2 * c + 2); return (unsigned long long)((m | (m >> 16)) & 0xffffu); };
#define TS5(i)
#define KN_Q0 0
  if constexpr (KN_Q0) fl = flag_get(a.sync, fa_off + q * 128, 16);
  STAMP(9);
  lds_barrier();
  STAMP(6);
  if constexpr (!KN_Q0) fl = flag_get(a.sync, fa_off + q * 128, 16);
  {
    const unsigned e0 = lds0 + OWNE + rb * 2048 + lane * 16, e1 = lds0 + OWNE + (rb ^ 1) * 2048 + lane * 16, m0 = lds0 + OWNM + rb * 1024 + lane * 16, m1 = lds0 + OWNM + (rb ^ 1) * 1024 + lane * 16;
    xr[0][0] = __builtin_bit_cast(u32x4, ds_b128<0>(e0)); xr[0][1] = __builtin_bit_cast(u32x4, ds_b128<0>(e1));
    xr[1][0] = __builtin_bit_cast(u32x4, ds_b128<1024>(e0)); xr[1][1] = __builtin_bit_cast(u32x4, ds_b128<1024>(e1));
    xr[2][0] = __builtin_bit_cast(u32x4, ds_b128<0>(m0)); xr[2][1] = __builtin_bit_cast(u32x4, ds_b128<0>(m1));
    static_for<0, 16>([&](auto rc) { rdw(fa, IC<0>{}, rc); });
  }
  auto op = [&](auto ic) {
    constexpr int i = decltype(ic)::value, kind = DN_OPS[i][2], arg = DN_OPS[i][3];
    if constexpr (kind == O_X) ldX(arg >> 1, arg & 1);
    if constexpr (kind == O_W) ldW(arg);
    if constexpr (kind == O_SL) stL(arg);
    if constexpr (kind == O_SP) stP(arg);
    if constexpr (kind == O_FA) { wait_vm<dn_vm(dn_find(O_SL, 2) + 1, i)>(); flag_set(a.sync, F_ACT + ((panel * 4 + (wave >> 1)) * 64 + (q * 8 + c) * 2 + (wave & 1)) * 8, tg, 2); }
    if constexpr (kind == O_FP) { wait_vm<dn_vm(dn_find(O_SP, 2 * arg + 1) + 1, i)>(); if constexpr (arg) STAMP(10 + arg); flag_set(a.sync, F_PART + ((bid ^ (3 - arg)) * 8 + wave) * 64 + arg * 8, tg, 0); }
    if constexpr (kind == O_QC) fl = flag_get(a.sync, fa_off + q * 128, 16);
    if constexpr (kind == O_QD) fl = flag_get(a.sync, fa_off, 64);
    if constexpr (kind == O_QP) fl = flag_get(a.sync, fp_off, 3);
    if constexpr (kind == O_RES) res[arg] = __builtin_amdgcn_raw_buffer_load_b128(RSRC(a.R), (unsigned)(((panel * 16 + rb) * RBH + c * 16 + (q >> 1) * 8 + ck) * 256 + fr * 16), arg * 1024, 16);
    if constexpr (kind == O_LP) ldP(arg, (got >> (arg >> 1)) & 1);
    if constexpr (kind == O_CE) {
      const unsigned long long need = rot16(arg);
      auto up = [&]() { return (__builtin_amdgcn_ballot_w64(fl[0] - tg.lo - 1u > 6u || fl[1] != tg.hi) & need) == 0; };
      if (__builtin_expect(!up(), 0)) { do fl = flag_get(a.sync, fa_off + q * 128, 16); while (!up() && in_time(lds0)); }
      STAMP(4);
    }
    if constexpr (kind == O_CC) { if (__builtin_expect(!flags_up(fl, 16, tg, 2), 0)) wait_flags(lds0, a.sync, fa_off + q * 128, 16, 2); }
    if constexpr (kind == O_CD) { if (__builtin_expect(!flags_up(fl, 64, tg, 2), 0)) wait_flags(lds0, a.sync, fa_off, 64, 2); }
    if constexpr (kind == O_CP) got |= (unsigned)__builtin_amdgcn_ballot_w64(fl[0] == tg.lo && fl[1] == tg.hi) & (1u << arg);
    if constexpr (kind == O_BAR) { if constexpr (!(KN_ABL & 4)) { wait_vm<dn_cnt(arg, i)>(); lds_barrier(); } }
    __builtin_amdgcn_sched_barrier(0);
  };
  static_for<0, NS>([&](auto vc) {
    constexpr int v = decltype(vc)::value, NM = dn_nm(v), NRD = v + 1 < NS ? 16 : 0;
    FragD& cur = (v & 1) ? fb : fa; FragD& nxt = (v & 1) ? fa : fb;
    TS5(v);
    if constexpr (KN_PRI == 1) __builtin_amdgcn_s_setprio(3 - (v & 3));
    if constexpr (KN_PRI == 2) { if (wave >= 4) __builtin_amdgcn_s_setprio(v & 1); else __builtin_amdgcn_s_setprio(1 - (v & 1)); }
    if constexpr (KN_PRI == 3) { if constexpr (!(v & 1)) __builtin_amdgcn_s_setprio(3 - ((v >> 1) & 3)); }
    static_for<dn_first(v, 0), dn_first(v, 1)>(op);
    __builtin_amdgcn_sched_barrier(0);
    static_for<0, NM>([&](auto ic) {
      constexpr int I = decltype(ic)::value, n = NM == 16 ? I >> 1 : I, m = NM == 16 ? I & 1 : I >> 2;
      if constexpr (I % (NM / 4) == 0) {
        constexpr int left = 12 - 2 * n + dn_rd(v, I);
        asm volatile("s_waitcnt lgkmcnt(%0)" :: "n"(left < 15 ? left : 15) : "memory");
        asm volatile("" : "+v"(cur.lo[n]), "+v"(cur.hi[n]), "+v"(cur.lo[n + 1]), "+v"(cur.hi[n + 1]));
        if constexpr (v == 0 && I == 0) {
#pragma unroll
          for (int i = 0; i < 3; ++i) asm volatile("" : "+v"(xr[i][0]), "+v"(xr[i][1]));
          STAMP(1);
        }
        __builtin_amdgcn_sched_barrier(0);
      }
      const bf16x8 w = __builtin_shufflevector(cur.lo[n], cur.hi[n], 0, 1, 2, 3, 4, 5, 6, 7);
      constexpr int an = v < DN_A ? n : (v < DN_B ? 4 : 0) + (n & 3), am = v < 24 ? m : 0;
      acc[an][am] = MFMA(w, __builtin_bit_cast(bf16x8, xr[dn_unit(v, n)][m]), acc[an][am]);
      __builtin_amdgcn_sched_barrier(0);
      static_for<dn_first(v, I + 1), dn_first(v, I + 2)>(op);
      static_for<0, NRD>([&](auto rc) { constexpr int r = decltype(rc)::value; if constexpr (dn_rg(v, r) == I + 1) rdw(nxt, IC<v + 1>{}, rc); });
      __builtin_amdgcn_sched_barrier(0);
    });
    if constexpr (v == 23) STAMP(10);
  });
  STAMP(2);
  TS5(NS);
  static_for<dn_first(NS, 0), DN_N>(op);
  if (got != 7u) {
    for (;;) {
      const u32x2 f0 = flag_get(a.sync, fp_off, 3);
      __builtin_amdgcn_s_sleep(KN_SLP);
      const u32x2 f1 = flag_get(a.sync, fp_off, 3);
      if (flags_up(f0, 3, tg, 0)) break;
      if (flags_up(f1, 3, tg, 0) || !in_time(lds0)) break;
    }
    u32x4 pe[6];
#pragma unroll
    for (int j = 5; j >= 0; --j) { pe[j] = pp[j >> 1][j & 1]; ldP(j, !((got >> (j >> 1)) & 1)); }
#pragma unroll
    for (int j = 5; j >= 0; --j) pp[j >> 1][j & 1] |= pe[j];
  }
  TS5(NS + 1);
  STAMP(5);
  const auto rO = RSRC(a.O);
  const unsigned orow = (unsigned)(((panel * 16 + rb) * 16 + fr) * (D_ * 2) + c * 256 + (q >> 1) * 128 + ck * 16);
#define KN_OAUX 0
#pragma unroll
  for (int i = 0; i < 2; ++i) {
    f32x4 u = acc[2 * i][0], w = acc[2 * i + 1][0];
#pragma unroll
    for (int k = 0; k < 3; ++k) {
      const unsigned q0 = pp[k][i][0], q1 = pp[k][i][1], q2 = pp[k][i][2], q3 = pp[k][i][3];
      u += (f32x4){bflo(q0), bfhi(q0), bflo(q1), bfhi(q1)}; w += (f32x4){bflo(q2), bfhi(q2), bflo(q3), bfhi(q3)};
    }
    const auto s0 = __builtin_amdgcn_permlane16_swap(pk_bf16(u[0], u[1]), pk_bf16(w[0], w[1]), false, false);
    const auto s1 = __builtin_amdgcn_permlane16_swap(pk_bf16(u[2], u[3]), pk_bf16(w[2], w[3]), false, false);
    const u32x4 q4 = {s0[0], s1[0], s0[1], s1[1]};
    u32x4 o;
#pragma unroll
    for (int j = 0; j < 4; ++j) { const unsigned r = res[i][j], x = q4[j]; o[j] = pk_bf16(bflo(r) + bflo(x), bfhi(r) + bfhi(x)); }
    __builtin_amdgcn_raw_buffer_store_b128(o, rO, orow + i * 64, 0, KN_OAUX);
  }
  if (bid == 0 && wave == 0) flag_set(a.sync, EPOCH, tg, 0);
  STAMP(7);
  STAMP(8);
  STAMP(3);
#endif
}

struct Ptrs { const bf16_t *x, *wqkv, *wo, *wg, *wu, *wd; bf16_t* out; char* ws; };
#define DBG(i)
#define WS_O ((bf16_t*)(p.ws + (8u << 20)))
#define WS_SL ((float*)(p.ws + (42u << 20)))
#define WS_H ((bf16_t*)(p.ws + (16u << 20)))
#define WS_ST ((float*)(p.ws + (24u << 20)))
#define WS_CS ((float*)(p.ws + (25u << 20)))
#define WS_SY ((unsigned*)(p.ws + (26u << 20)))
#define WS_A ((bf16_t*)(p.ws + (28u << 20)))
#if defined(__HIP_DEVICE_COMPILE__)
#define ARG_WARM(v) do { asm volatile("s_load_dword %0, %1, 0x0" : "=s"(v) : "s"(__builtin_amdgcn_kernarg_segment_ptr())); __builtin_amdgcn_sched_barrier(0); } while (0)
#else
#define ARG_WARM(v)
#endif
struct Args4 { GemmArgs q, b, c, d; };
extern "C" __global__ __launch_bounds__(512, 1) void k_all(Ptrs p) {
  unsigned warm = 0;
  ARG_WARM(warm);
#define PAD(k)
  const Args4 a{GemmArgs{p.x, p.wqkv, nullptr, nullptr, WS_O, (float*)WS_H, p.wg, p.wu, WS_CS, p.wd, p.wo, WS_SY, (float*)WS_A, warm DBG(0)},
    GemmArgs{WS_O, p.wo, nullptr, p.x, WS_H, WS_ST, nullptr, nullptr, nullptr, p.wd, nullptr, WS_SY, nullptr, 0u DBG(1)},
    GemmArgs{WS_H, p.wg, p.wu, p.out, WS_A, WS_ST, nullptr, nullptr, WS_CS, p.wd, nullptr, WS_SY, WS_SL, 0u DBG(2)},
    GemmArgs{WS_A, p.wd, nullptr, WS_H, p.out, nullptr, nullptr, nullptr, nullptr, nullptr, nullptr, WS_SY, WS_SL, 0u DBG(3)}};
  k1_phase(a.q, [&]() { wo_phase<1, 2, 16>(a.b, a.c); });
  PAD(1);
  wo_phase<2, 0, 0>(a.b, a.c);
  PAD(2);
  Late late;
  glu_phase(a.c, [&](auto tc, unsigned w) { dn_phase<1, decltype(tc)::value>(a.d, late, w); }, late);
  PAD(3);
  dn_phase<2, 0>(a.d, late);
  PAD(4);
}

extern "C" __global__ void k_zend() {}

extern "C" void kernel_launch(void* const* d_in, const int* in_sizes, int n_in, void* d_out, int out_size, void* d_ws,
                              size_t ws_size, hipStream_t stream) {
  const Ptrs p{(const bf16_t*)d_in[0], (const bf16_t*)d_in[1], (const bf16_t*)d_in[2], (const bf16_t*)d_in[4], (const bf16_t*)d_in[5], (const bf16_t*)d_in[6], (bf16_t*)d_out, (char*)d_ws};
  constexpr int L_all = SNAP + 64;
  static_assert(SNAP >= 3 * 256 * 128 + 4 * 14592 + 4096 + 1024 + 256 + 512 + 128 + 64 && L_all <= 163840, "LDS");
  (void)hipFuncSetAttribute((const void*)k_all, hipFuncAttributeMaxDynamicSharedMemorySize, L_all);
  k_all<<<256, 512, L_all, stream>>>(p);
}
```

```cpp
#include <hip/hip_runtime.h>
#include <hip/hip_bf16.h>
#include <stdint.h>
#include <type_traits>

typedef unsigned short bf16_t;
typedef short bf16x8 __attribute__((ext_vector_type(8)));
typedef short s16x4 __attribute__((ext_vector_type(4)));
typedef float f32x4 __attribute__((ext_vector_type(4)));
typedef float f32x2 __attribute__((ext_vector_type(2)));
typedef unsigned u32x2 __attribute__((ext_vector_type(2)));
typedef unsigned u32x4 __attribute__((ext_vector_type(4)));
typedef __bf16 bf16x2_t __attribute__((ext_vector_type(2)));
typedef __attribute__((address_space(3))) void* lds_void_ptr;
typedef __attribute__((address_space(3))) char* lds_char_ptr;

#define KN_NT 0
constexpr int S_ = 2048, D_ = 1024, DF_ = 3584;
constexpr int RBO = D_ / 8, RBH = D_ / 8, RBA = DF_ / 8;

__device__ __forceinline__ float bflo(unsigned v) { return __uint_as_float(v << 16); }
__device__ __forceinline__ float bfhi(unsigned v) { return __uint_as_float(v & 0xffff0000u); }
__device__ __forceinline__ unsigned pk_bf16(float lo, float hi) {
  f32x2 v = {lo, hi};
  bf16x2_t b = __builtin_convertvector(v, bf16x2_t);
  return __builtin_bit_cast(unsigned, b);
}
__device__ __forceinline__ int wswz(int row) { return (((row >> 1) & 1) | (((row >> 3) & 1) << 1)) << 1; }

template <int OFF> __device__ __forceinline__ s16x4 ds_tr(unsigned addr) {
  s16x4 r; asm volatile("ds_read_b64_tr_b16 %0, %1 offset:%2" : "=v"(r) : "v"(addr), "n"(OFF)); return r;
}
template <int OFF> __device__ __forceinline__ bf16x8 ds_b128(unsigned addr) {
  bf16x8 r; asm volatile("ds_read_b128 %0, %1 offset:%2" : "=v"(r) : "v"(addr), "n"(OFF)); return r;
}
template <int OFF> __device__ __forceinline__ f32x2 ds_b64(unsigned addr) {
  f32x2 r; asm volatile("ds_read_b64 %0, %1 offset:%2" : "=v"(r) : "v"(addr), "n"(OFF)); return r;
}
__device__ __forceinline__ float sum8(float v) {
  v += __int_as_float(__builtin_amdgcn_update_dpp(0, __float_as_int(v), 0x128, 0xf, 0xf, false));
  auto a = __builtin_amdgcn_permlane16_swap(__float_as_uint(v), __float_as_uint(v), false, false);
  v = __uint_as_float(a[0]) + __uint_as_float(a[1]);
  auto b = __builtin_amdgcn_permlane32_swap(__float_as_uint(v), __float_as_uint(v), false, false);
  return __uint_as_float(b[0]) + __uint_as_float(b[1]);
}
__device__ __forceinline__ float sum8l(float v) {
  v += __int_as_float(__builtin_amdgcn_update_dpp(0, __float_as_int(v), 0xb1, 0xf, 0xf, false));
  v += __int_as_float(__builtin_amdgcn_update_dpp(0, __float_as_int(v), 0x4e, 0xf, 0xf, false));
  return v + __int_as_float(__builtin_amdgcn_update_dpp(0, __float_as_int(v), 0x141, 0xf, 0xf, false));
}
template <int OFF> __device__ __forceinline__ void ds_w128(unsigned addr, u32x4 v) { asm volatile("ds_write_b128 %0, %1 offset:%2" :: "v"(addr), "v"(v), "n"(OFF) : "memory"); }
__device__ __forceinline__ int ds_r32(unsigned addr) { int r; asm volatile("ds_read_b32 %0, %1" : "=v"(r) : "v"(addr)); return r; }
__device__ __forceinline__ void ds_w32(unsigned addr, int v) { asm volatile("ds_write_b32 %0, %1" :: "v"(addr), "v"(v) : "memory"); }
template <int N> __device__ __forceinline__ void wait_vm() { asm volatile("s_waitcnt vmcnt(%0)" ::"n"(N) : "memory"); }
__device__ __forceinline__ void raw_barrier() { asm volatile("" ::: "memory"); __builtin_amdgcn_s_barrier(); asm volatile("" ::: "memory"); }
__device__ __forceinline__ void lds_barrier() { asm volatile("s_waitcnt lgkmcnt(0)" ::: "memory"); __builtin_amdgcn_s_barrier(); asm volatile("" ::: "memory"); }

template <int I, int N, class F> __device__ __forceinline__ void static_for(F&& f) {
  if constexpr (I < N) { f(std::integral_constant<int, I>{}); static_for<I + 1, N>(f); }
}
template <int V> using IC = std::integral_constant<int, V>;
constexpr int first_at(int g, int r0, int num, int den, int n) { int i = 0; while (i < n && r0 + i * num / den < g) ++i; return i; }
#define RSRC(p) __builtin_amdgcn_make_buffer_rsrc((void*)(p), 0, 0x7fffffff, 0x00020000)
#define RSRCN(p, n) __builtin_amdgcn_make_buffer_rsrc((void*)(p), 0, (n), 0x00020000)
#define LDSP(a) ((lds_void_ptr)(size_t)(a))
#define MFMA(a, b, c) __builtin_amdgcn_mfma_f32_16x16x32_bf16(a, b, c, 0, 0, 0)
#define CODE_ADDR(v, sym) asm volatile("s_getpc_b64 s[40:41]\n\ts_add_u32 s40, s40, " sym "@rel32@lo+4\n\ts_addc_u32 s41, s41, " sym "@rel32@hi+12\n\ts_mov_b64 %0, s[40:41]" : "=s"(v) :: "s40", "s41", "scc")
#define MERGED 1
constexpr int SNAP = 162752;
constexpr int STAB1 = 786432;
constexpr int EPOCH = 0;
constexpr int F_MARK = 4096, F_H = 4096 + 2048, F_ROW = 8192, F_COL = 16384;
#define F_O(t) (32768 + ((t) & 7) * 1024 + (((((t) >> 3) ^ 1) & 1) << 13))
#define KN_RM 3
#define WO_ROT (((xcd & 3) * 4) ^ (idx & KN_RM))
#define KN_WA 6
#define KN_WE 10

constexpr int F_ACT = 65536;
constexpr int F_PART = 131072;
constexpr int KN_TRY = 6;
struct Tag { unsigned lo, hi; };
__device__ __forceinline__ Tag tag_get(unsigned lds0) {
  f32x2 v = ds_b64<0>(lds0 + SNAP);
  asm volatile("s_waitcnt lgkmcnt(0)" : "+v"(v) :: "memory");
  return Tag{(unsigned)__builtin_amdgcn_readfirstlane(__float_as_int(v[0])) + 8u, (unsigned)__builtin_amdgcn_readfirstlane(__float_as_int(v[1]))};
}
__device__ __forceinline__ bool in_time(unsigned lds0) {
  int v = ds_r32(lds0 + SNAP + 28);
  asm volatile("s_waitcnt lgkmcnt(0)" : "+v"(v) :: "memory");
  return (unsigned)__builtin_amdgcn_s_memrealtime() - (unsigned)__builtin_amdgcn_readfirstlane(v) < 20000000u;
}
__device__ __forceinline__ int lane_id() { return __builtin_amdgcn_mbcnt_hi(~0u, __builtin_amdgcn_mbcnt_lo(~0u, 0u)); }
#if defined(__HIP_DEVICE_COMPILE__)
__device__ __forceinline__ void flag_set(const unsigned* sync, int off, Tag t, unsigned state) {
  __builtin_amdgcn_raw_buffer_store_b64((u32x2){t.lo + state, t.hi}, RSRC(sync), lane_id() == 0 ? 0u : 0x80000000u, off, 16);
}
__device__ __forceinline__ u32x2 flag_get(const unsigned* sync, int off, int n) { return __builtin_amdgcn_raw_buffer_load_b64(RSRC(sync), lane_id() < n ? lane_id() * 8u : 0x80000000u, off, 16); }
__device__ __forceinline__ bool flags_up(u32x2 v, int n, Tag t, unsigned state) { return __builtin_amdgcn_ballot_w64(lane_id() < n && (v[0] - t.lo - state > 7u - state || v[1] != t.hi)) == 0; }
__device__ __forceinline__ bool words_up(unsigned v, Tag t) { return __builtin_amdgcn_ballot_w64(v != ((lane_id() & 1) ? t.hi : t.lo)) == 0; }
__device__ __forceinline__ void wait_flags(unsigned lds0, const unsigned* sync, int off, int n, unsigned state) {
  const Tag t = tag_get(lds0);
  while (!flags_up(flag_get(sync, off, n), n, t, state) && in_time(lds0)) {}
}
#endif
__device__ __forceinline__ void lds_wait8(unsigned addr, int n = 8) {
  int dv; unsigned polls = 0;
  do { dv = ds_r32(addr); asm volatile("s_waitcnt lgkmcnt(0)" : "+v"(dv) :: "memory"); } while (__builtin_amdgcn_readfirstlane(dv) < n && ++polls < 40000000u);
}
template <int N> __device__ __forceinline__ bool marked(unsigned addr) {
  bf16x8 v[N];
  static_for<0, N>([&](auto ic) { constexpr int i = decltype(ic)::value; v[i] = ds_b128<i * 4096>(addr); });
  asm volatile("s_waitcnt lgkmcnt(0)" ::: "memory");
  unsigned m = 0;
#pragma unroll
  for (int i = 0; i < N; ++i) {
    asm volatile("" : "+v"(v[i]));
    const u32x4 u = __builtin_bit_cast(u32x4, v[i]);
    m = max(max(m, u[0]), u[1]); m = max(max(m, u[2]), u[3]);
  }
  return __builtin_amdgcn_ballot_w64(m == 0xffffffffu) != 0;
}

struct GemmArgs {
  const bf16_t* X;
  const bf16_t* W0;
  const bf16_t* W1;
  const bf16_t* R;
  bf16_t* O;
  float* st;
  const bf16_t* CW0;
  const bf16_t* CW1;
  float* cs;
  const bf16_t* PF0;
  const bf16_t* PF1;
  unsigned* sync;
  float* slab;
  unsigned warm;
};
#define STAMP(i)
#define STAMPW(i, w)
#define TS6(i)
#define TS4(i)
template <int MF, int NF> struct Frags { bf16x8 x[MF]; s16x4 lo[NF], hi[NF]; };

__device__ __forceinline__ unsigned dn_wsrc(int lane, int wave) { return (unsigned)((lane >> 3) * (D_ * 2) + (((lane & 7) ^ wswz((wave & 3) * 8 + (lane >> 3))) << 4)); }
constexpr int OWNE = 0, OWNM = 32768;
#define KN_P0 2
#define KN_P1 3
#define KN_P2 0
#define KN_P3 1
constexpr int wo_pos(int s) { return s < 3 ? 18432 + s * 16384 : (s == 3 ? 129024 : (s == 4 ? 0 : 71424 + (s - 5) * 16384)); }
template <class HK>
__device__ __forceinline__ void k1_phase(const GemmArgs& a, HK hook) {
#if defined(__HIP_DEVICE_COMPILE__)
  constexpr int K = D_, LDW = 3 * D_, WN = 4;
  constexpr int HR = 16, STRIDE = 128;
  constexpr int XT = 144 * 128, WU = 24576;
  constexpr int XBASE = 0, WBASE = 3 * XT, RING = WBASE + 4 * WU;
  constexpr int MF = 4, NF = 3, MFX = 5;
  constexpr int NT = 16, NXI = 18, NLW = 4, XI = 5, XIMIN = 4, WI = 6;
  extern __shared__ __attribute__((aligned(16))) char smem[];
  STAMP(0);
  const unsigned lds0 = (unsigned)(size_t)(lds_char_ptr)smem;
  const int tid = threadIdx.x, lane = tid & 63;
  const int wave = __builtin_amdgcn_readfirstlane(tid >> 6);
  const int wm = wave / WN, wn = wave % WN;
  const int bid = blockIdx.x, xcd = bid & 7, idx = bid >> 3;
  const int tm = (xcd / 4) * 8 + idx / 4, tn = (xcd % 4) * 4 + idx % 4;
  const int m0 = tm * 128;
  const int fr = lane & 15, fq = lane >> 4;
  const int wq = (lane >> 2) & 3, wp = lane & 3;
  constexpr int TCH = RING + 2304;
  auto touch = [&](const void* base, unsigned voff, unsigned soff, unsigned lim = 0x7fffffffu) {
    __builtin_amdgcn_raw_ptr_buffer_load_lds(RSRCN(base, lim), LDSP(lds0 + TCH), 4, voff, soff, 0, 0);
  };
  const auto rO = RSRC(a.O);

  unsigned xsrc[XI], wsrc[WI];
  {
    const int w4 = wave & 3, lr = lane >> 3;
    const unsigned xv = (unsigned)(lr * (K * 2) + (((lane & 7) ^ ((w4 * 4 + (lr >> 1)) & 7)) << 4));
    const int r0 = m0 - HR + w4 * 8;
#pragma unroll
    for (int i = 0; i < XI; ++i) xsrc[i] = xv + (unsigned)((i == 0 ? (r0 < 0 ? 0 : r0) : r0 + 32 * (i < 4 ? i : 4)) * (K * 2));
    const unsigned wv = (unsigned)((w4 * 8 + lr) * (LDW * 2) + tn * 128 + (((lane & 7) ^ wswz(w4 * 8 + lr)) << 4));
#pragma unroll
    for (int i = 0; i < WI; ++i) wsrc[i] = wv + (unsigned)((i & 1) * 32 * (LDW * 2) + (i >> 1) * 2048);
  }
  auto ldX = [&](int i, int tile, int slot) {
    const int j = wave + NLW * i;
    if (j >= NXI) return;
    __builtin_amdgcn_raw_ptr_buffer_load_lds(RSRC(a.X), LDSP(lds0 + XBASE + slot * XT + j * 1024), 16, xsrc[i], tile * 128, 0, (KN_NT & 1) ? 2 : 0);
  };
  auto ldW = [&](int i, int unit, int slot) {
    const int j = wave + NLW * i;
    __builtin_amdgcn_raw_ptr_buffer_load_lds(RSRC(a.W0), LDSP(lds0 + WBASE + slot * WU + j * 1024), 16, wsrc[i], unit * 64 * LDW * 2, 0, (KN_NT & 2) ? 2 : 0);
  };
  auto issueX = [&](int tile, int slot) {
    if (wave >= NLW) return;
#pragma unroll
    for (int i = 0; i < XI; ++i) ldX(i, tile, slot);
  };
  auto issueW = [&](int unit, int slot) {
    if (wave >= NLW) return;
#pragma unroll
    for (int i = 0; i < WI; ++i) ldW(i, unit, slot);
  };

  f32x4 acc[NF][MF];
#pragma unroll
  for (int n = 0; n < NF; ++n)
#pragma unroll
    for (int m = 0; m < MF; ++m) acc[n][m] = (f32x4){0.f, 0.f, 0.f, 0.f};
  f32x4 acch[NF];
#pragma unroll
  for (int n = 0; n < NF; ++n) acch[n] = (f32x4){0.f, 0.f, 0.f, 0.f};
  constexpr int ONES = RING;

  unsigned xa0, xa1, xh0, xh1;
  { const unsigned o = fr * 128 + ((fq ^ ((fr >> 1) & 7)) << 4), mo = (HR + wm * MF * 16) * 128;
    xa0 = lds0 + XBASE + mo + o; xa1 = lds0 + XBASE + mo + (o ^ 64); xh0 = lds0 + XBASE + o; xh1 = lds0 + XBASE + (o ^ 64); }
  unsigned wa;
  { const int row = 8 * fq + wq; wa = lds0 + WBASE + row * STRIDE + (((wn << 1) ^ wswz(row)) << 4) + wp * 8; }

  using I0 = IC<0>; using I1 = IC<1>;
  using T = std::true_type; using F = std::false_type;
  typedef Frags<MFX, NF> FR;
  struct RdCtx { unsigned xa, xh, wb; };
  auto mkctx = [&](bool ks1, unsigned xoffs, unsigned woffs) {
    RdCtx c; c.xa = (ks1 ? xa1 : xa0) + xoffs; c.xh = fr == 0 ? lds0 + ONES : (ks1 ? xh1 : xh0) + xoffs; c.wb = wa + woffs;
    return c;
  };
  auto rd_item = [&](FR& f, const RdCtx& c, auto KSc, auto kc) {
    constexpr int KS = decltype(KSc)::value, k = decltype(kc)::value;
    if constexpr (k < MF) f.x[k] = ds_b128<k * 2048>(c.xa);
    else if constexpr (k < MFX) f.x[k] = ds_b128<0>(c.xh);
    else {
      constexpr int n = k - MFX, o = n * 8192 + KS * 32 * STRIDE;
      f.lo[n] = ds_tr<o>(c.wb); f.hi[n] = ds_tr<o + 4 * STRIDE>(c.wb);
    }
  };
  auto wait_frags = [&](FR& f, auto Hc) {
    asm volatile("s_waitcnt lgkmcnt(0)" ::: "memory");
#pragma unroll
    for (int m = 0; m < MF + decltype(Hc)::value; ++m) asm volatile("" : "+v"(f.x[m]));
#pragma unroll
    for (int n = 0; n < NF; ++n) asm volatile("" : "+v"(f.lo[n]), "+v"(f.hi[n]));
  };
  auto step1 = [&](auto Hc, FR& cur, FR& nxt, const RdCtx& c, auto KSc, auto R0c, auto RLc, auto IBc, auto&& sync, auto NLc, auto&& ld) {
    constexpr int H = decltype(Hc)::value, NL = decltype(NLc)::value, R0 = decltype(R0c)::value;
    constexpr int MH = MF + H, NM = NF * MH, NR = decltype(RLc)::value == -100 ? 0 : MH + 2 * NF;
    constexpr int IB = decltype(IBc)::value < 0 && decltype(IBc)::value > -100 ? NM + decltype(IBc)::value : decltype(IBc)::value;
    constexpr int RL = NM + decltype(RLc)::value;
    static_for<0, NM>([&](auto ic) {
      constexpr int I = decltype(ic)::value, g = I / MH, q = I % MH;
      const bf16x8 w = __builtin_shufflevector(cur.lo[g], cur.hi[g], 0, 1, 2, 3, 4, 5, 6, 7);
      if constexpr (q < MF) acc[g][q] = MFMA(w, cur.x[q], acc[g][q]);
      else acch[g] = MFMA(w, cur.x[MF], acch[g]);
      __builtin_amdgcn_sched_barrier(0);
      if constexpr (I == IB) sync();
      static_for<first_at(I, R0, RL - R0, NR - 1, NR), first_at(I + 1, R0, RL - R0, NR - 1, NR)>([&](auto rc) {
        constexpr int r = decltype(rc)::value;
        if constexpr (r < MH) rd_item(nxt, c, KSc, rc);
        else {
          constexpr int n = (r - MH) >> 1, o = n * 8192 + decltype(KSc)::value * 32 * STRIDE;
          if constexpr ((r - MH) & 1) nxt.hi[n] = ds_tr<o + 4 * STRIDE>(c.wb); else nxt.lo[n] = ds_tr<o>(c.wb);
        }
      });
#pragma unroll
      for (int i = first_at(I, R0, NM - R0, NL, NL); i < first_at(I + 1, R0, NM - R0, NL, NL); ++i) ld(i);
      __builtin_amdgcn_sched_barrier(0);
    });
    if constexpr (NR > 0) wait_frags(nxt, Hc);
  };
  FR fa, fb;
  using IM1 = IC<-1>;
  unsigned tv6 = 0;

  asm volatile("s_waitcnt lgkmcnt(0)" :: "s"(a.warm), "v"(wsrc[WI - 1]), "v"(xsrc[XI - 1]));
  issueW(0, 0); issueX(0, 0); issueW(1, 1); issueX(1, 1);
  u32x4 sx[4];
  constexpr int TNEW = RING + 3072;
  auto ldT = [&](int dst) {
    int lx = lane; asm volatile("" : "+v"(lx));
    const auto rT = RSRCN((const char*)a.cs + STAB1, 2048 * 16);
    const unsigned to = (unsigned)((m0 - 16) * 16 + lx * 16);
    __builtin_amdgcn_raw_ptr_buffer_load_lds(rT, LDSP(lds0 + dst), 16, to, 0, 0, 16);
    __builtin_amdgcn_raw_ptr_buffer_load_lds(rT, LDSP(lds0 + dst + 1024), 16, to + 1024u, 0, 0, 16);
    if (lx < 16) __builtin_amdgcn_raw_ptr_buffer_load_lds(rT, LDSP(lds0 + dst + 2048), 16, to + 2048u, 0, 0, 16);
  };
  auto ldS = [&]() {
    int lx = lane; asm volatile("" : "+v"(lx));
    const unsigned so = (unsigned)((m0 + tn * 8 + (wave & 3) * 2 + (lx >> 5)) * (K * 2) + (lx & 31) * 16);
#pragma unroll
    for (int i = 0; i < 4; ++i) sx[i] = __builtin_amdgcn_raw_buffer_load_b128(RSRC(a.X), so, i * 512, 0);
  };
  __builtin_amdgcn_sched_barrier(0);
  if (wave == 4) {
    if (lane < 8) *(unsigned*)(smem + ONES + lane * 4) = lane < 4 ? 0x3f803f80u : 0u;
  }
  if (wave == 5) {
    if (lane < 2) __builtin_amdgcn_raw_ptr_buffer_load_lds(RSRC(a.sync), LDSP(lds0 + SNAP), 4, lane * 4, EPOCH, 0, 16);
    ds_w32(lds0 + SNAP + 28, (int)__builtin_amdgcn_s_memrealtime());
#define KN_PT 1
    if constexpr (KN_PT) touch(a.cs, lane < 8 ? (unsigned)((const char*)a.sync - (const char*)a.cs + bid * 1024 + lane * 128) : (lane == 8 ? (unsigned)(STAB1 + (m0 + tn * 8) * 16) : (lane < 23 ? (unsigned)(bid * 1792 + (lane - 9) * 128) : 0x80000000u)), 0);
    const int p2 = (xcd >> 1) * 2 + (idx >> 4), mb = (xcd & 1) * 16 + (idx & 15);
#pragma unroll
    for (int i = 0; i < 3; ++i)
      __builtin_amdgcn_raw_buffer_store_b128((u32x4){~0u, ~0u, ~0u, ~0u}, RSRC(a.st), lane * 16 + i * 1024, ((p2 * 16 + (mb >> 1)) * RBH + (xcd >> 2) * 64 + (mb & 1) * 12) * 256, 16);
  }
  constexpr int HQ = 5;
  const int slc = (3 - ((unsigned)(size_t)a.W0 >> 7) - (xcd & 3) * 4) & 7;
  const int hid = (idx >> 2) * 3 + (idx & 3) - ((idx & 3) > slc ? 1 : 0);
  const bool hlp = slc < 4 && (idx & 3) != slc;
  const int hj1 = hlp ? hid / 3 - HQ : -100, hj2 = hlp ? hid / 3 + 8 - HQ : -100;
  const unsigned htv = (unsigned)(lane * (LDW * 2) + (hid % 3) * 2048 + ((xcd & 3) * 4 + slc) * 128);
  if (wave == 6 && hlp && hj1 <= 0) touch(a.W0, htv, (hid / 3) * (64 * LDW * 2));
  auto code_touch = [&]() {
    unsigned long long c0, c1; CODE_ADDR(c0, "k_all"); CODE_ADDR(c1, "k_zend");
    const unsigned len = c1 - c0 < 262144ull ? (unsigned)(c1 - c0) : 0u;
    touch((const void*)c0, (unsigned)((lane * 32 + idx) * 128), 0, len);
  };
#define KN_MP 4
#define KN_MF 3
  const int jp = (bid >> 4) < KN_MF ? 10 + (bid >> 4) : (bid >> 4) - KN_MP;
  auto batch = [&]() {
    int lx = lane; asm volatile("" : "+v"(lx));
#define KN_MX 0
    if (KN_MX && (bid >> 4) >= KN_MF) { touch(a.X, lx * 2048, (bid & 15) * 262144 + (bid >> 4) * 128); touch(a.X, lx * 2048, (bid & 15) * 262144 + 131072 + (bid >> 4) * 128); }
    if ((bid >> 4) >= KN_MF) { touch(a.W0, lx * 128, bid * 24576); touch(a.W0, lx * 128, bid * 24576 + 8192); touch(a.W0, lx * 128, bid * 24576 + 16384); }
    touch(a.PF1, lx * 128, bid * 8192);
  };
  if (KN_MP && wave == 4 && jp <= 0) batch();
  if (wave == 7) code_touch();
  const int srow = m0 + wave * 16 + fr;
  const bool par = fr & 1;
  float bias[4];
  {
    const unsigned mk = 0x5555u & ((2u << (srow < 15 ? srow : 15)) - 1u), sft = (fr & 14) - fq * 4 + 14;
    const unsigned b0 = ~(mk >> sft), b1 = ~((mk << 16) >> sft);
#pragma unroll
    for (int j = 0; j < 4; ++j) bias[j] = __uint_as_float((unsigned)__builtin_amdgcn_sbfe((int)(j < 2 ? b0 : b1), 2 - 2 * (j & 1), 1) & 0xff61b1e6u);
  }
  unsigned pl = par ? 0x01000c0cu : 0x0c0c0100u, ph = par ? 0x03020c0cu : 0x0c0c0302u;
  asm volatile("" : "+v"(bias[0]), "+v"(bias[1]), "+v"(bias[2]), "+v"(bias[3]), "+v"(pl), "+v"(ph));
  constexpr int RS = 400, QT = WBASE + 3 * WU - 144 * RS;
  static_assert(wo_pos(0) >= XT && wo_pos(1) <= 2 * XT && wo_pos(2) + 16384 <= QT && wo_pos(3) >= QT + 144 * RS && KN_WA <= 6 && KN_WE <= 10 && KN_WA <= KN_WE, "LDS");
  int qw = (16 + wm * 64 + fr) * RS + wn * 32 + fq * 8;
  unsigned ka = lds0 + QT + (wave * 16 + fr) * RS + fq * 16, va = lds0 + QT + (wave * 16 + fq * 4 + wq) * RS + 256 + wp * 16;
  asm volatile("" : "+v"(qw), "+v"(ka), "+v"(va));
#define KN_CI 2
#define KN_SL 1
#define KN_S1 2
  if (wave < NLW) wait_vm<WI + XIMIN>();
  lds_barrier();
  { const RdCtx c = mkctx(false, 0, 0); static_for<0, MFX + NF>([&](auto kc) { rd_item(fa, c, I0{}, kc); }); }
  wait_frags(fa, I1{});
  STAMP(1);
  f32x4 t4[MFX], csn[NF];
  int up = 0;
  const unsigned csa = lds0 + RING + 64 + wn * 192 + fq * 16, cfa = lds0 + ONES + 16 + wn * 4;
  auto ldC = [&]() { up = ds_r32(cfa); csn[0] = __builtin_bit_cast(f32x4, ds_b128<0>(csa)); csn[1] = __builtin_bit_cast(f32x4, ds_b128<64>(csa)); csn[2] = __builtin_bit_cast(f32x4, ds_b128<128>(csa)); };
  auto pinC = [&]() { asm volatile("s_waitcnt lgkmcnt(0)" : "+v"(up), "+v"(csn[0]), "+v"(csn[1]), "+v"(csn[2]) :: "memory"); };
  auto pinT = [&](bool h) { asm volatile("s_waitcnt lgkmcnt(0)" : "+v"(t4[0]), "+v"(t4[1]), "+v"(t4[2]), "+v"(t4[3]) :: "memory"); if (h) asm volatile("" : "+v"(t4[4])); };
  auto run = [&](auto ROLEc) __attribute__((always_inline)) {
    constexpr int ROLE = decltype(ROLEc)::value;
    constexpr bool LDR = ROLE < NLW;
    using HL = IC<LDR ? 1 : 0>;
    auto nos = [&]() {};
    TS6(0);
    if constexpr (!LDR && KN_SL == 1) { ldS(); __builtin_amdgcn_sched_barrier(0); }
    { const RdCtx c = mkctx(true, 0, 0); step1(HL{}, fa, fb, c, I1{}, I0{}, IC<-4>{}, IC<-100>{}, nos, IC<LDR ? WI + XI : 0>{}, [&](int i) { if (i < WI) ldW(i, 2, 2); else ldX(i - WI, 2, 2); }); }
#define KN_WLD 2
    if constexpr (!LDR && KN_SL == 2) ldS();
    int slx = 1;
    if constexpr (ROLE == 5) wait_vm<7 + KN_PT>();
    auto iter = [&](int j, auto LXc, auto LWc, auto WAITc, auto TIc) {
      constexpr bool LX = decltype(LXc)::value && LDR, LW = decltype(LWc)::value && LDR;
      constexpr int WAIT = decltype(WAITc)::value, TI = decltype(TIc)::value;
      TS6(j);
      constexpr int EX = (LDR && TI < 0 && KN_WLD == 3) ? WI : 0;
      constexpr int TO = 2, OL = TI <= TO ? 0 : 2, OH = (LDR && TI >= TO) ? 2 : OL;
      constexpr int NLT = EX + (LX ? XI : 0) + (LW ? WI : 0) + 2 * (OH - OL), N0 = (NLT + 1) / 2, N1 = NLT - N0;
      if constexpr (LDR) wait_vm<WAIT + (TI > 0 ? 2 * OL : 0)>();
      unsigned osrc = 0;
      if constexpr (OH > OL) { int lx = lane; asm volatile("" : "+v"(lx)); const int row = ROLE * 8 + (lx >> 3); osrc = (unsigned)(row * D_ + ((xcd & 1) * 8 + (idx & 7)) * 64 + ((lx & 7) ^ wswz(row)) * 8) * 2u; }
      if constexpr (ROLE == 6 && TI == 0) { if (j == hj1 || j == hj2) touch(a.W0, htv, (j + HQ) * (64 * LDW * 2)); }
      constexpr int S3 = 11;
      if constexpr (!LDR && TI == 0) {
        if (j == KN_S1) {
#define KN_PIN 0
          float sm = 0.f, sq = 0.f;
          if constexpr (KN_PIN) asm volatile("" : "+v"(sm), "+v"(sq));
          const bf16x2_t one2 = __builtin_bit_cast(bf16x2_t, 0x3f803f80u);
#pragma unroll
          for (int i = 0; i < 4; ++i)
#pragma unroll
            for (int e = 0; e < 4; ++e) { const unsigned u = sx[i][e]; const bf16x2_t v = __builtin_bit_cast(bf16x2_t, u); sm = __builtin_amdgcn_fdot2_f32_bf16(v, one2, sm, false); sq = __builtin_amdgcn_fdot2_f32_bf16(v, v, sq, false); }
          sm = sum8l(sm); sq = sum8l(sq);
          sm += __int_as_float(__builtin_amdgcn_update_dpp(0, __float_as_int(sm), 0x128, 0xf, 0xf, false));
          sq += __int_as_float(__builtin_amdgcn_update_dpp(0, __float_as_int(sq), 0x128, 0xf, 0xf, false));
          { auto p = __builtin_amdgcn_permlane16_swap(__float_as_uint(sm), __float_as_uint(sm), false, false); sm = __uint_as_float(p[0]) + __uint_as_float(p[1]); }
          { auto p = __builtin_amdgcn_permlane16_swap(__float_as_uint(sq), __float_as_uint(sq), false, false); sq = __uint_as_float(p[0]) + __uint_as_float(p[1]); }
          const float mu = sm * (1.f / 1024.f);
          float var = sq * (1.f / 1024.f) - mu * mu; var = var > 0.f ? var : 0.f;
          int lx = lane; asm volatile("" : "+v"(lx));
          const Tag tg = tag_get(lds0);
          if constexpr (ROLE == 5) { wait_vm<0>(); flag_set(a.sync, F_MARK + ((xcd >> 1) * 2 + (idx >> 4)) * 256 + ((xcd & 1) * 16 + (idx & 15)) * 8, tg, 0); }
          __builtin_amdgcn_raw_buffer_store_b128((u32x4){__float_as_uint(mu), tg.lo, __float_as_uint(__builtin_amdgcn_rsqf(var + 1e-5f)), tg.lo + tg.hi}, RSRC(a.cs),
                                                 (lx & 31) == 0 ? (unsigned)(STAB1 + (m0 + tn * 8 + (ROLE & 3) * 2 + (lx >> 5)) * 16) : 0x80000000u, 0, 16);
          __builtin_amdgcn_sched_barrier(0);
          asm volatile("s_nop 1" ::: "memory");
        }
      }
      if constexpr (ROLE == 7 && TI == 0) {
        if (j == S3) ldT(TNEW);
        if (j == S3 + 1) {
          for (;;) {
            wait_vm<0>();
            int lx = lane; asm volatile("" : "+v"(lx));
            const unsigned ad = lds0 + TNEW + lx * 16;
            u32x4 v[3];
            v[0] = __builtin_bit_cast(u32x4, ds_b128<0>(ad)); v[1] = __builtin_bit_cast(u32x4, ds_b128<1024>(ad)); v[2] = __builtin_bit_cast(u32x4, ds_b128<2048>(ad));
            const Tag tg = tag_get(lds0);
            asm volatile("" : "+v"(v[0]), "+v"(v[1]), "+v"(v[2]) :: "memory");
            auto good = [&](const u32x4& e) { return e[1] == tg.lo && e[3] == tg.lo + tg.hi; };
            const bool bad = (!good(v[0]) && (tm > 0 || lx >= 16)) || !good(v[1]) || (!good(v[2]) && lx < 16);
            if (__builtin_amdgcn_ballot_w64(bad) == 0 || !in_time(lds0)) break;
            ldT(TNEW);
          }
        }
      }
      if constexpr (ROLE == 7 && TI == 1) code_touch();
      if constexpr (ROLE == 4 && TI == 0 && !KN_MP) touch(a.PF1, (bid * 64 + lane) * 128, 0, j == 1 + idx % 12 ? 0x7fffffffu : 0u);
      if constexpr (ROLE == 4 && TI == 0 && KN_MP) { if (j == jp) batch(); }
      if constexpr (ROLE >= 4 && TI == 2) {
        const bf16_t* cw = ROLE < 6 ? a.CW0 : a.CW1;
        touch(cw, lane < 56 ? (unsigned)(((ROLE & 1) * 512 + bid) * (DF_ * 2) + lane * 128) : 0x80000000u, 0);
      }
      int xp = slx + 2; if (xp >= 3) xp -= 3;
      auto ld = [&](int i) { if (i < EX) { ldW(i, j + 2, (j + 2) & 3); return; } i -= EX;
                             if (LX && i < XI) ldX(i, j + 2, xp);
                             else if constexpr (LW) ldW(i - (LX ? XI : 0), j + KN_WLD, (j + KN_WLD) & 3);
                             else { const int q = i - (LX ? XI : 0), t = OL + (q >> 1), h = q & 1;
                               __builtin_amdgcn_raw_ptr_buffer_load_lds(RSRC(a.PF1), LDSP(lds0 + wo_pos(t >> 1) + (t & 1) * 8192 + (ROLE + 4 * h) * 1024), 16,
                                                                        osrc, ((((t & 1) * 8 + (t >> 1)) ^ WO_ROT) * 2 + h) * (32 * D_ * 2), 0, 0); } };
      { const RdCtx c = mkctx(false, slx * XT, (j & 3) * WU); step1(HL{}, fb, fa, c, I0{}, IC<LDR ? 1 : 0>{}, IC<-4>{}, IC<LDR ? 1 : -100>{}, [&]() { raw_barrier(); }, IC<N0>{}, ld); }
      { const RdCtx c = mkctx(true, slx * XT, (j & 3) * WU); step1(HL{}, fa, fb, c, I1{}, I0{}, IC<LDR ? -4 : -6>{},
          IC<LDR ? -100 : -3>{}, [&]() { if constexpr (TI != 3) { asm volatile("s_waitcnt lgkmcnt(0)" ::: "memory"); raw_barrier(); } }, IC<N1>{}, [&](int i) { ld(i + N0); }); }
      slx = slx + 1; if (slx >= 3) slx = 0;
    };
    using WS_ = IC<(KN_WLD - 1) * WI + XIMIN>;
    if constexpr (!LDR) raw_barrier();
    if constexpr (LDR) iter(1, T{}, T{}, IC<WI + XIMIN>{}, IM1{});
    for (int j = LDR ? 2 : 1; j <= NT - 4; ++j) iter(j, T{}, T{}, WS_{}, I0{});
    iter(NT - 3, T{}, std::integral_constant<bool, KN_WLD == 2>{}, WS_{}, I1{});
    iter(NT - 2, F{}, F{}, IC<WI + XIMIN>{}, IC<2>{});
    iter(NT - 1, F{}, F{}, I0{}, IC<3>{});
    TS6(16);
    { const RdCtx c = mkctx(false, 0, 0); const unsigned ta = lds0 + TNEW + fr * 16;
      step1(HL{}, fb, fa, c, I0{}, I0{}, IC<-100>{}, IC<-100>{}, nos, IC<MFX>{}, [&](int i) {
        if (i == 0) static_for<0, MF + (LDR ? 1 : 0)>([&](auto mc) { constexpr int m = decltype(mc)::value; t4[m] = __builtin_bit_cast(f32x4, ds_b128<(m < MF ? 16 + (ROLE / WN * MF + m) * 16 : 0) * 16>(ta)); });
        if constexpr (!LDR) { if (i == KN_CI) ldC(); } });
      pinT(LDR); if constexpr (!LDR) pinC(); }
    TS6(17);
  };
  switch (wave) {
    case 0: run(IC<0>{}); break; case 1: run(IC<1>{}); break;
    case 2: run(IC<2>{}); break; case 3: run(IC<3>{}); break;
    case 4: run(IC<4>{}); break; case 5: run(IC<5>{}); break;
    case 6: run(IC<6>{}); break; default: run(IC<7>{}); break;
  }
  STAMP(2);
  {
    if (wave < 4) {
      if (fr == 0) {
#pragma unroll
        for (int n = 0; n < NF; ++n) *(f32x4*)(smem + RING + 64 + wn * 192 + fq * 16 + n * 64) = acch[n];
      }
      ds_w32(cfa, 1);
#pragma unroll
      for (int n = 0; n < NF; ++n)
#pragma unroll
        for (int e = 0; e < 4; ++e) { const float h = acch[n][e]; csn[n][e] = __int_as_float(__builtin_amdgcn_update_dpp(0, __float_as_int(h), 0x150, 0xf, 0xf, false)); }
    } else {
      unsigned polls = 0;
      while (__builtin_expect(__builtin_amdgcn_readfirstlane(up) == 0, 0) && ++polls < 40000000u) {
        ldC(); pinC();
      }
    }
    STAMP(9);
    TS6(18);
    TS6(19);
    STAMP(6);
    auto nrm = [&](const f32x4& v, const f32x4& cs, const f32x4& t) {
      const float mu = t[0], rs = t[2];
      return (u32x2){pk_bf16(rs * (v[0] - mu * cs[0]), rs * (v[1] - mu * cs[1])), pk_bf16(rs * (v[2] - mu * cs[2]), rs * (v[3] - mu * cs[3]))}; };
    u32x2 ov2[MF][NF];
#pragma unroll
    for (int m = 0; m < MF; ++m) {
#pragma unroll
      for (int n = 0; n < NF; ++n) { ov2[m][n] = nrm(acc[n][m], csn[n], t4[m]); asm volatile("" : "+v"(ov2[m][n])); }
      __builtin_amdgcn_sched_barrier(0);
#pragma unroll
      for (int n = 0; n < NF; ++n) *(u32x2*)(smem + QT + qw + m * 16 * RS + n * 128) = ov2[m][n];
      __builtin_amdgcn_sched_barrier(0);
    }
    if (wave < 4) {
#pragma unroll
      for (int n = 1; n < NF; ++n) *(u32x2*)(smem + QT + qw - 16 * RS + n * 128) = nrm(acch[n], csn[n], t4[MF]);
      __builtin_amdgcn_sched_barrier(0);
      hook(IC<3>{}, IC<2>{}, IC<KN_WA>{});
    }
    STAMP(4);
    TS6(20);
    lds_barrier();
    TS6(21);
    STAMP(5);
    bf16x8 qf[2], kf[2][2]; s16x4 vf[2][2][2];
    qf[0] = ds_b128<16 * RS>(ka); kf[0][0] = ds_b128<128>(ka); kf[0][1] = ds_b128<16 * RS + 128>(ka);
    qf[1] = ds_b128<16 * RS + 64>(ka); kf[1][0] = ds_b128<192>(ka); kf[1][1] = ds_b128<16 * RS + 192>(ka);
    vf[0][0][0] = ds_tr<0>(va); vf[0][0][1] = ds_tr<16 * RS>(va); vf[0][1][0] = ds_tr<8>(va); vf[0][1][1] = ds_tr<16 * RS + 8>(va);
    vf[1][0][0] = ds_tr<64>(va); vf[1][0][1] = ds_tr<16 * RS + 64>(va); vf[1][1][0] = ds_tr<72>(va); vf[1][1][1] = ds_tr<16 * RS + 72>(va);
    hook(IC<1>{}, IC<KN_WA>{}, IC<KN_WE>{});
    asm volatile("s_waitcnt lgkmcnt(8)" ::: "memory");
#pragma unroll
    for (int hh = 0; hh < 2; ++hh) asm volatile("" : "+v"(qf[hh]), "+v"(kf[hh][0]), "+v"(kf[hh][1]));
    const float sc2 = 0.17677669529663687f * 1.4426950408889634f;
    unsigned pm = (unsigned)__builtin_amdgcn_sbfe((int)pl, 24, 1); asm volatile("" : "+v"(pm));
    auto vmax = [](float x, float y) { float r; asm("v_max_f32 %0, %1, %2" : "=v"(r) : "v"(x), "v"(y)); return r; };
    u32x4 ov[2];
    const f32x4 z = {0.f, 0.f, 0.f, 0.f};
    f32x4 sc[2][2]; float lg[2][4], mx[2], sum[2];
    auto x16 = [](float v) { auto p = __builtin_amdgcn_permlane16_swap(__float_as_uint(v), __float_as_uint(v), false, false); return (f32x2){__uint_as_float(p[0]), __uint_as_float(p[1])}; };
    auto x32 = [](float v) { auto p = __builtin_amdgcn_permlane32_swap(__float_as_uint(v), __float_as_uint(v), false, false); return (f32x2){__uint_as_float(p[0]), __uint_as_float(p[1])}; };
#pragma unroll
    for (int hh = 0; hh < 2; ++hh) {
      sc[hh][0] = MFMA(kf[hh][0], qf[hh], z);
      sc[hh][1] = MFMA(kf[hh][1], qf[hh], z);
    }
    __builtin_amdgcn_sched_barrier(0);
#pragma unroll
    for (int hh = 0; hh < 2; ++hh) {
#pragma unroll
      for (int j = 0; j < 4; ++j) { const float e0 = sc[hh][j >> 1][(j & 1) * 2], e1 = sc[hh][j >> 1][(j & 1) * 2 + 1];
        lg[hh][j] = __builtin_fmaf(__uint_as_float((__float_as_uint(e1) & pm) | (__float_as_uint(e0) & ~pm)), sc2, bias[j]); }
      mx[hh] = fmaxf(fmaxf(lg[hh][0], lg[hh][1]), fmaxf(lg[hh][2], lg[hh][3]));
    }
    __builtin_amdgcn_sched_barrier(0);
    { auto p = __builtin_amdgcn_permlane16_swap(__float_as_uint(mx[0]), __float_as_uint(mx[1]), false, false);
      const f32x2 q = x32(vmax(__uint_as_float(p[0]), __uint_as_float(p[1]))), r = x16(vmax(q[0], q[1])); mx[0] = r[0]; mx[1] = r[1]; }
    __builtin_amdgcn_sched_barrier(0);
#pragma unroll
    for (int hh = 0; hh < 2; ++hh) {
#pragma unroll
      for (int j = 0; j < 4; ++j) lg[hh][j] = __builtin_amdgcn_exp2f(lg[hh][j] - mx[hh]);
      sum[hh] = (lg[hh][0] + lg[hh][1]) + (lg[hh][2] + lg[hh][3]);
    }
    __builtin_amdgcn_sched_barrier(0);
    asm volatile("s_waitcnt lgkmcnt(0)" ::: "memory");
#pragma unroll
    for (int hh = 0; hh < 2; ++hh) asm volatile("" : "+v"(vf[hh][0][0]), "+v"(vf[hh][0][1]), "+v"(vf[hh][1][0]), "+v"(vf[hh][1][1]));
    { auto p = __builtin_amdgcn_permlane16_swap(__float_as_uint(sum[0]), __float_as_uint(sum[1]), false, false);
      const f32x2 q = x32(__uint_as_float(p[0]) + __uint_as_float(p[1])), r = x16(__builtin_amdgcn_rcpf(q[0] + q[1])); sum[0] = r[0]; sum[1] = r[1]; }
    __builtin_amdgcn_sched_barrier(0);
#pragma unroll
    for (int hh = 0; hh < 2; ++hh) {
      const float inv = sum[hh];
      const unsigned c0 = pk_bf16(lg[hh][0] * inv, lg[hh][1] * inv), c1 = pk_bf16(lg[hh][2] * inv, lg[hh][3] * inv);
      const u32x4 pp = {__builtin_amdgcn_perm(0u, c0, pl), __builtin_amdgcn_perm(0u, c0, ph), __builtin_amdgcn_perm(0u, c1, pl), __builtin_amdgcn_perm(0u, c1, ph)};
      const bf16x8 pf = __builtin_bit_cast(bf16x8, pp);
      sc[hh][0] = MFMA(__builtin_shufflevector(vf[hh][0][0], vf[hh][0][1], 0, 1, 2, 3, 4, 5, 6, 7), pf, z);
      sc[hh][1] = MFMA(__builtin_shufflevector(vf[hh][1][0], vf[hh][1][1], 0, 1, 2, 3, 4, 5, 6, 7), pf, z);
    }
    __builtin_amdgcn_sched_barrier(0);
#pragma unroll
    for (int hh = 0; hh < 2; ++hh) ov[hh] = (u32x4){pk_bf16(sc[hh][0][0], sc[hh][0][1]), pk_bf16(sc[hh][0][2], sc[hh][0][3]), pk_bf16(sc[hh][1][0], sc[hh][1][1]), pk_bf16(sc[hh][1][2], sc[hh][1][3])};
    STAMP(10);
#pragma unroll
    for (int hh = 0; hh < 2; ++hh) __builtin_amdgcn_raw_buffer_store_b128(ov[hh], rO, (((m0 >> 4) + wave) * RBO + tn * 8 + hh * 4) * 256 + lane * 16, 0, 16);
    STAMP(11);
  }
  STAMP(7);
  STAMP(8);
  TS6(22);
  const Tag tg = tag_get(lds0);
  asm volatile("s_waitcnt vmcnt(0)" ::: "memory");
  flag_set(a.sync, F_O(tm) + (wave >> 1) * 256 + tn * 16 + (wave & 1) * 8, tg, 0);
  raw_barrier();
  hook(IC<1>{}, IC<KN_WE>{}, IC<16>{});
  STAMP(3);
#endif
}

struct Late { u32x4 v[3]; };
template <class HK>
__device__ __forceinline__ void glu_phase(const GemmArgs& a, HK hook, Late& late) {
#if defined(__HIP_DEVICE_COMPILE__)
  constexpr int LDW = DF_, WN = 2;
  constexpr int UPOFF = 32 * 224 + 16;
  constexpr int XT = 32768, WU = 14592;
  constexpr int XBASE = 0, WBASE = 3 * XT, RING = WBASE + 4 * WU;
  constexpr int MF = 4, NF = 7;
  constexpr int NT = 16, NXI = 16, NWI = 14, NLW = 4, XI = 4, WI = 4;
  extern __shared__ __attribute__((aligned(16))) char smem[];
  STAMP(0);
  const unsigned lds0 = (unsigned)(size_t)(lds_char_ptr)smem;
  const int tid = threadIdx.x, lane = tid & 63;
  const int wave = __builtin_amdgcn_readfirstlane(tid >> 6);
  const int wm = wave / WN, wn = wave % WN;
  const int bid = blockIdx.x, xcd = bid & 7, idx = bid >> 3;
  const int tm = (xcd / 4) * 4 + idx / 8, tn = (xcd % 4) * 8 + idx % 8;
  const int m0 = tm * 256;
  const int krot = (tm >> 2) * 8;
  const int fr = lane & 15, fq = lane >> 4;
  const int wq = (lane >> 2) & 3, wp = lane & 3;
  const int panel = m0 >> 8;
  constexpr int TCH = RING + 5120;
  constexpr int DONE = TCH + 896;
  auto done_inc = [&]() { if (lane == 0) asm volatile("ds_add_u32 %0, %1" :: "v"(lds0 + DONE), "v"(1) : "memory"); };
  auto touch = [&](const void* base, unsigned voff, unsigned soff, unsigned lim = 0x7fffffffu) {
    __builtin_amdgcn_raw_ptr_buffer_load_lds(RSRCN(base, lim), LDSP(lds0 + TCH), 4, voff, soff, 0, 0);
  };
  const auto rO = RSRC(a.O);

  unsigned xsrc[XI], wsrc[WI];
#pragma unroll
  for (int i = 0; i < XI; ++i) {
    const int j = (wave % NLW) + NLW * i;
    xsrc[i] = (unsigned)((((m0 >> 4) + (j >> 1)) * RBH + (j & 1) * 4) * 256 + lane * 16);
  }
#pragma unroll
  for (int i = 0; i < WI; ++i) {
    int j = (wave % NLW) + NLW * i; if (j >= NWI) j -= NLW;
    const int jj = j >= 7 ? j - 7 : j;
    const int b = jj * 1024 + lane * 16, p = b / 224, pos = (b % 224) >> 4;
    const int row = 8 * ((p & 15) >> 2) + (p & 3) + 4 * (p >> 4);
    wsrc[i] = (unsigned)(row * LDW + tn * 112 + pos * 8) * 2u;
  }
  auto ldX = [&](int i, int tile, int part, int slot) {
    tile = (tile + krot) & 15;
    const int j = wave + NLW * i;
    __builtin_amdgcn_raw_ptr_buffer_load_lds(RSRC(a.X), LDSP(lds0 + XBASE + slot * XT + (part * NXI + j) * 1024), 16, xsrc[i], tile * 2048 + part * (NXI / 2) * RBH * 256, 0, 16);
  };
  auto ldW = [&](int i, int unit, int slot) {
    const auto rW0 = RSRC(a.W0);
    const auto rW1 = RSRC(a.W1);
    int j = wave + NLW * i; if (j >= NWI) j -= NLW;
    unit = (unit + 2 * krot) & 31;
    const bool up = j >= 7;
    __builtin_amdgcn_raw_ptr_buffer_load_lds(up ? rW1 : rW0, LDSP(lds0 + WBASE + slot * WU + (up ? UPOFF + (j - 7) * 1024 : j * 1024)), 16, wsrc[i], unit * 32 * LDW * 2, 0, 0);
  };
  auto issueX = [&](int tile, int part, int slot) {
    if (wave >= NLW) return;
#pragma unroll
    for (int i = 0; i < XI; ++i) ldX(i, tile, part, slot);
  };
  auto issueW = [&](int unit, int slot) {
    if (wave >= NLW) return;
#pragma unroll
    for (int i = 0; i < WI; ++i) ldW(i, unit, slot);
  };

  f32x4 acc[NF][MF];
#pragma unroll
  for (int n = 0; n < NF; ++n)
#pragma unroll
    for (int m = 0; m < MF; ++m) acc[n][m] = (f32x4){0.f, 0.f, 0.f, 0.f};
  f32x4 accs = {0.f, 0.f, 0.f, 0.f};

  const unsigned xa0 = lds0 + XBASE + wm * MF * 16 * 128 + lane * 16, xa1 = xa0 + 1024;
  unsigned wa[2];
#pragma unroll
  for (int c = 0; c < 2; ++c) wa[c] = lds0 + WBASE + (4 * fq + wq) * 224 + wn * 112 + (c == 0 ? wp * 8 : 96 + (wp & 1) * 8 + (wp >> 1) * UPOFF);

  using I0 = IC<0>; using I1 = IC<1>;
  using T = std::true_type; using F = std::false_type;
  typedef Frags<MF, NF> FR;
  struct RdCtx { unsigned xa, wb[2]; };
  auto mkctx = [&](bool ks1, unsigned xoffs, unsigned woffs) {
    RdCtx c; c.xa = (ks1 ? xa1 : xa0) + xoffs; c.wb[0] = wa[0] + woffs; c.wb[1] = wa[1] + woffs;
    return c;
  };
  auto rd_item = [&](FR& f, const RdCtx& c, auto kc) {
    constexpr int k = decltype(kc)::value;
    if constexpr (k < MF) f.x[k] = ds_b128<k * 2048>(c.xa);
    else {
      constexpr int n = k - MF, cc = n == 6, o = n == 6 ? 0 : (n >> 1) * 32 + (n & 1) * UPOFF;
      f.lo[n] = ds_tr<o>(c.wb[cc]); f.hi[n] = ds_tr<o + 16 * 224>(c.wb[cc]);
    }
  };
  auto wait_frags = [&](FR& f) {
    asm volatile("s_waitcnt lgkmcnt(0)" ::: "memory");
#pragma unroll
    for (int m = 0; m < MF; ++m) asm volatile("" : "+v"(f.x[m]));
#pragma unroll
    for (int n = 0; n < NF; ++n) asm volatile("" : "+v"(f.lo[n]), "+v"(f.hi[n]));
  };
  auto step = [&](FR& cur, FR& nxt, const RdCtx& c, auto PFc, auto NLc, auto&& ld) {
    constexpr bool PF = decltype(PFc)::value != 0; constexpr int NL = decltype(NLc)::value;
    constexpr int R = MF + NF, RG = NF - 1;
    static_for<0, NF>([&](auto gc) {
      constexpr int g = decltype(gc)::value;
      if constexpr (PF) static_for<0, R>([&](auto kc) { constexpr int k = decltype(kc)::value; if constexpr (k * RG / R == g) rd_item(nxt, c, kc); });
      static_for<0, NL>([&](auto ic) { constexpr int i = decltype(ic)::value; if constexpr (i * NF / NL == g) ld(i); });
      const bf16x8 w = __builtin_shufflevector(cur.lo[g], cur.hi[g], 0, 1, 2, 3, 4, 5, 6, 7);
#pragma unroll
      for (int m = 0; m < MF; ++m) acc[g][m] = MFMA(w, cur.x[m], acc[g][m]);
      __builtin_amdgcn_sched_barrier(0);
    });
    if constexpr (PF) wait_frags(nxt);
  };
  auto stepg = [&](auto CSc, FR& cur, FR& nxt, const RdCtx& c, auto R0c, auto IBc, auto&& sync, auto NLc, auto&& ld) {
    constexpr bool CS = decltype(CSc)::value; constexpr int R0 = decltype(R0c)::value, IB = decltype(IBc)::value, NL = decltype(NLc)::value;
    constexpr int NM = NF * MF, NR = MF + 2 * NF;
    static_for<0, NM>([&](auto ic) {
      constexpr int I = decltype(ic)::value, g = I / MF, m = I % MF;
      constexpr int RL = IB > 8 ? 20 : 22;
      const bf16x8 w = __builtin_shufflevector(cur.lo[g], cur.hi[g], 0, 1, 2, 3, 4, 5, 6, 7);
      acc[g][m] = MFMA(w, cur.x[m], acc[g][m]);
      __builtin_amdgcn_sched_barrier(0);
      if constexpr (I == IB) sync();
      static_for<first_at(I, R0, RL - R0, NR - 1, NR), first_at(I + 1, R0, RL - R0, NR - 1, NR)>([&](auto rc) {
        constexpr int r = decltype(rc)::value;
        if constexpr (r < MF) nxt.x[r] = ds_b128<r * 2048>(c.xa);
        else {
          constexpr int n = (r - MF) >> 1, cc = n == 6, o = n == 6 ? 0 : (n >> 1) * 32 + (n & 1) * UPOFF;
          if constexpr ((r - MF) & 1) nxt.hi[n] = ds_tr<o + 16 * 224>(c.wb[cc]); else nxt.lo[n] = ds_tr<o>(c.wb[cc]);
        }
      });
#pragma unroll
      for (int i = first_at(I, R0, NM - 1 - R0, NL, NL); i < first_at(I + 1, R0, NM - 1 - R0, NL, NL); ++i) ld(i);
      if constexpr (CS && m == MF - 1) {
        const unsigned sv = fr == g ? 0x3f803f80u : 0u;
        const u32x4 s4 = {sv, sv, sv, sv};
        accs = MFMA(w, __builtin_bit_cast(bf16x8, s4), accs);
      }
      __builtin_amdgcn_sched_barrier(0);
    });
    wait_frags(nxt);
  };
  FR fa, fb;
  unsigned tv4 = 0;

  f32x4* tb_row = (f32x4*)(smem + RING);
  float* tb_cs = (float*)(tb_row + 256);
  constexpr int RTAB = 524288;
  auto glu_stats_issue = [&]() {
    if (wave == 7) __builtin_amdgcn_raw_ptr_buffer_load_lds(RSRC(a.st), LDSP(lds0 + RING + 3072), 16, ((lane >> 2) * S_ + m0 + tn * 8 + (lane & 3) * 2) * 8, 0, 0, 16);
  };
  auto glu_stats = [&]() {
    int lx = lane; asm volatile("" : "+v"(lx));
    const unsigned sa = lds0 + RING + 3072 + lx * 8;
    f32x2 p0 = ds_b64<0>(sa), p1 = ds_b64<512>(sa);
    asm volatile("s_waitcnt lgkmcnt(0)" : "+v"(p0), "+v"(p1) :: "memory");
    const float mu = sum8(p0[0] + p1[0]) * (1.f / 16.f);
    const float v = sum8((p0[1] + p1[1]) + 64.f * ((p0[0] - mu) * (p0[0] - mu) + (p1[0] - mu) * (p1[0] - mu))) * (1.f / 1024.f) + 1e-5f;
    const float rs = 1.0f / sqrtf(v);
    const f32x4 rv = {mu, -0.10294324f * rs * rs * rs, -2.3022082f * rs, v};
    __builtin_amdgcn_raw_buffer_store_b128(__builtin_bit_cast(u32x4, rv), RSRC(a.cs), lx < 8 ? (unsigned)(RTAB + (m0 + tn * 8 + lx) * 16) : 0x80000000u, 0, 16);
  };
  auto epilogue = [&](auto EAc) __attribute__((always_inline)) {
    STAMP(2);
    constexpr bool EARLY = decltype(EAc)::value;
    if constexpr (!EARLY) {
      lds_wait8(lds0 + DONE);
      __builtin_amdgcn_s_setprio(KN_P0);
      STAMPW(9, 4);
    } else __builtin_amdgcn_s_setprio(KN_P1);
    asm volatile("" ::: "memory");
    f32x4 cg[4], cu[4], rw[MF], rw6[2];
    int lz = lane; asm volatile("" : "+v"(lz));
    const int fr = lz & 15, fq = lz >> 4;
#pragma unroll
    for (int p = 0; p < 4; ++p) {
      const int o = wn * 56 + p * 16 + (p == 3 ? (fq & 1) : fq) * 4;
      cg[p] = *(const f32x4*)(tb_cs + o); cu[p] = *(const f32x4*)(tb_cs + 112 + o);
    }
#pragma unroll
    for (int m = 0; m < MF; ++m) rw[m] = tb_row[(wm * MF + m) * 16 + fr];
#pragma unroll
    for (int q = 0; q < 2; ++q) rw6[q] = tb_row[(wm * MF + 2 * q + (fq >> 1)) * 16 + fr];
    const int gz = ((m0 >> 4) + wm * 4) * RBA * 256 + fr * 16;
    const int go0 = gz + ((fq & 1) * RBA + (fq >> 1)) * 256;
    const unsigned le = lds0 + OWNE + (wm * 4 + (fq & 1)) * 2048 + (wn * 4 + (fq >> 1)) * 256 + fr * 16, lm = lds0 + OWNM + (wm * 4 + (fq & 1)) * 1024 + (wn * 2 + (fq >> 1)) * 256 + fr * 16;
    unsigned hv[MF][NF];
    unsigned dw = dn_wsrc(lz, wave); asm volatile("" : "+v"(dw));
    const Tag tg = tag_get(lds0);
    auto put = [&](unsigned a0, unsigned a1, unsigned b0, unsigned b1, int vo, int so, unsigned la, auto LOc) {
      const auto s0 = __builtin_amdgcn_permlane16_swap(a0, b0, false, false), s1 = __builtin_amdgcn_permlane16_swap(a1, b1, false, false);
      const u32x4 v = {s0[0], s1[0], s0[1], s1[1]};
      __builtin_amdgcn_raw_buffer_store_b128(v, rO, vo, so, 16);
      if constexpr (decltype(LOc)::value >= 0) ds_w128<decltype(LOc)::value>(la, v);
      __builtin_amdgcn_sched_barrier(0);
      asm volatile("s_nop 1" ::: "memory");
      __builtin_amdgcn_sched_barrier(0);
    };
#define STAGE(body) _Pragma("unroll") for (int i = 0; i < 2 * NN; ++i) { const int m = MB + i / NN, n = N0 + i % NN; const f32x4 k = n == 6 ? rw6[m >> 1] : rw[m]; body; } __builtin_amdgcn_sched_barrier(0)
    auto grp = [&](auto MBc, auto N0c, auto NNc, auto&& mid, auto&& at) {
      constexpr int MB = decltype(MBc)::value, N0 = decltype(N0c)::value, NN = decltype(NNc)::value;
      f32x2 G[2 * NN], U[2 * NN], P[2 * NN];
      __builtin_amdgcn_sched_barrier(0);
      STAGE({ const int e = (n == 6 ? m & 1 : n & 1) * 2; const f32x4 x = n == 6 ? acc[6][m & 2] : acc[n & 6][m]; G[i] = ((f32x2){x[e], x[e + 1]}) - k[0] * ((f32x2){cg[n >> 1][e], cg[n >> 1][e + 1]}); });
      STAGE(P[i] = G[i] * G[i]);
      at(I0{});
      STAGE(P[i] = P[i] * k[1] + k[2]);
      STAGE(P[i] = G[i] * P[i]);
      at(I1{});
      STAGE({ const int e = (n == 6 ? m & 1 : n & 1) * 2; const f32x4 x = n == 6 ? acc[6][m | 1] : acc[n | 1][m]; U[i] = ((f32x2){x[e], x[e + 1]}) - k[0] * ((f32x2){cu[n >> 1][e], cu[n >> 1][e + 1]}); });
      STAGE(P[i] = ((f32x2){__builtin_amdgcn_exp2f(P[i][0]), __builtin_amdgcn_exp2f(P[i][1])}));
      mid();
      at(IC<2>{});
      __builtin_amdgcn_sched_barrier(0);
      STAGE(P[i] = P[i] * k[3] + k[3]);
      STAGE(P[i] = ((f32x2){__builtin_amdgcn_rcpf(P[i][0]), __builtin_amdgcn_rcpf(P[i][1])}));
      at(IC<3>{});
      STAGE(U[i] = G[i] * U[i]);
      STAGE(U[i] = U[i] * P[i]);
      STAGE({ hv[m][n] = pk_bf16(U[i][0], U[i][1]); asm volatile("" : "+v"(hv[m][n])); });
    };
#undef STAGE
    using I2 = IC<2>; using I3 = IC<3>; using I4 = IC<4>;
    const int se = (tn * 12 + wn * 4) * 256;
#define KN_PE 2
    auto none = [&](auto) {};
    auto put01 = [&]() { put(hv[0][0], hv[0][1], hv[1][0], hv[1][1], go0, se, le, I0{}); put(hv[0][2], hv[0][3], hv[1][2], hv[1][3], go0 + 512, se, le, IC<512>{}); };
    grp(I0{}, I0{}, I4{}, [&]() { if constexpr (EARLY) lds_wait8(lds0 + DONE); }, [&](auto tc) { constexpr int t = decltype(tc)::value;
      if constexpr (EARLY) { if constexpr (t >= 2) { hook(IC<3 + 2 * t>{}, dw); hook(IC<4 + 2 * t>{}, dw); } } else hook(IC<7 + t>{}, dw); });
    hook(IC<11>{}, dw);
    grp(I2{}, I0{}, I4{}, [&]() {}, [&](auto tc) { constexpr int t = decltype(tc)::value; if constexpr (t == 0) hook(IC<12>{}, dw); if constexpr (t == KN_PE) put01(); });
    put(hv[2][0], hv[2][1], hv[3][0], hv[3][1], go0, se + 2 * RBA * 256, le, IC<4096>{}); put(hv[2][2], hv[2][3], hv[3][2], hv[3][3], go0 + 512, se + 2 * RBA * 256, le, IC<4096 + 512>{});
    asm volatile("" ::: "memory");
    __builtin_amdgcn_sched_barrier(0);
    STAMP(4);
    STAMPW(11, 4);
    __builtin_amdgcn_s_setprio(EARLY ? KN_P3 : KN_P2);
#pragma unroll
    for (int q = 0; q < 2; ++q)
#pragma unroll
      for (int e = 0; e < 4; ++e) {
        const float x0 = acc[6][2 * q][e], x1 = acc[6][2 * q + 1][e];
        auto sw = __builtin_amdgcn_permlane32_swap(__float_as_uint(x0), __float_as_uint(x1), false, false);
        acc[6][2 * q][e] = __uint_as_float(sw[0]); acc[6][2 * q + 1][e] = __uint_as_float(sw[1]);
      }
    grp(I0{}, I4{}, I3{}, [&]() {}, none);
    { wait_vm<0>(); flag_set(a.sync, F_ACT + ((panel * 4 + wm) * 64 + tn * 2 + wn) * 8, tg, 1); STAMPW(6, 4); STAMP(10); }
    auto keep = [&](unsigned a0, unsigned a1, unsigned b0, unsigned b1) {
      const auto s0 = __builtin_amdgcn_permlane16_swap(a0, b0, false, false), s1 = __builtin_amdgcn_permlane16_swap(a1, b1, false, false);
      return (u32x4){s0[0], s1[0], s0[1], s1[1]}; };
    grp(I2{}, I4{}, I3{}, [&]() { late.v[0] = keep(hv[0][4], hv[0][5], hv[1][4], hv[1][5]); ds_w128<0>(lm, late.v[0]); }, none);
    late.v[1] = keep(hv[2][4], hv[2][5], hv[3][4], hv[3][5]); ds_w128<2048>(lm, late.v[1]);
    late.v[2] = keep(hv[0][6], hv[1][6], hv[2][6], hv[3][6]);
    asm volatile("" ::: "memory");
    __builtin_amdgcn_s_setprio(0);
  };

  TS4(0);
  constexpr int WR = 2;
  if (wave == 5) ds_w32(lds0 + DONE, 0);
  STAMP(9); STAMP(6);
  auto check_tile = [&](int t) {
    int lx = lane; asm volatile("" : "+v"(lx));
    const unsigned ad = lds0 + XBASE + t * XT + wave * 1024 + lx * 16;
    if (__builtin_expect(marked<8>(ad), 0)) {
      for (int tries = 0;; ++tries) {
        if (tries >= KN_TRY) wait_flags(lds0, a.sync, F_H + panel * 256, 32, 0);
#pragma unroll 1
        for (int u = t; u < t + 1 + (t == 0); ++u) { issueX(u, 0, u); issueX(u, 1, u); }
        if (t == 0) wait_vm<2 * XI>(); else wait_vm<0>();
        if (tries >= KN_TRY || !marked<8>(ad)) break;
      }
    }
  };
  auto run = [&](auto LDRc) __attribute__((always_inline)) {
    constexpr bool LDR = decltype(LDRc)::value;
    if constexpr (LDR) {
      issueX(0, 0, 0); issueX(0, 1, 0); issueX(1, 0, 1); issueX(1, 1, 1);
      issueW(2, 2 ^ WR); issueW(3, 3 ^ WR); wait_vm<2 * XI + 2 * WI>();
      check_tile(0);
    }
    lds_barrier();
    if constexpr (!LDR) { if (wave == 4) __builtin_amdgcn_raw_ptr_buffer_load_lds(RSRC(a.sync), LDSP(lds0 + TCH + 256), 4, lane * 4, F_H + panel * 256, 0, 16); }
    { const RdCtx c = mkctx(false, 0, WR * WU); static_for<0, MF + NF>([&](auto kc) { rd_item(fa, c, kc); }); }
    wait_frags(fa);
    STAMP(1);
    TS4(1);
    { const RdCtx c = mkctx(true, 0, (WR + 1) * WU); step(fa, fb, c, T{}, IC<LDR ? 2 * XI : 0>{}, [&](int i) { ldX(i % XI, 2, i / XI, 2); }); }
    int xs = 1;
    unsigned long long tb = (unsigned long long)a.PF0; unsigned tv = 0, tl = 0x7fffffffu, ts = 7168u, to = 0, tmask = 0;
    if constexpr (!LDR) {
      unsigned long long c0, c1; CODE_ADDR(c0, "k_all"); CODE_ADDR(c1, "k_zend");
#define KN_WDM 0x55
#define KN_DA 6
#define KN_DN 2
      if (wave == 4) { tb = c0; tl = c1 - c0 < 262144ull ? (unsigned)(c1 - c0) : 0u; tmask = 1u << 11; }
      if (wave == 5) { tmask = KN_WDM << (KN_DA + idx % KN_DN); to = ((xcd & 3) * 32 + idx) * 57344 + (xcd >> 2) * 28672; }
      if (wave == 6) { tb = (unsigned long long)((idx & 16) ? a.W1 : a.W0); ts = 64 * LDW * 2; tmask = 0x1eu; to = (krot + 4) * ts; }
      const int l4 = lane / 14;
      tv = wave == 4 ? (unsigned)((lane * 32 + idx) * 128)
         : (lane >= 56 ? 0x80000000u : (wave == 5 ? (unsigned)(lane * 128) : (unsigned)(((idx & 15) * 4 + l4) * (LDW * 2) + (xcd & 3) * 1792 + (lane - l4 * 14) * 128)));
    }
    const int js = 2 * (tm & 3) + 1;
    const auto rC = RSRC(a.cs);
    constexpr int JR = 10, JS = 2;
    const int jf = wave == 4 ? NT - 4 : (wave == 7 ? JR : 64);
    const int fof = wave == 4 ? F_COL + tn * 256 : F_ROW + panel * 256;
    const unsigned evm = tmask | (8u << js) | (wave == 4 ? 3u << (NT - 4) : 0u) | (wave == 7 ? ((5u << JS) | 2u | (11u << JR)) : 0u);
    constexpr int CFL = TCH + 256, CPB = XBASE + ((NT - 3) % 3) * XT;
    auto iter = [&](int j, auto LWc, auto LXc, auto WAITc, auto CAc, auto CBc) {
      constexpr bool LW = decltype(LWc)::value && LDR, LX = decltype(LXc)::value && LDR, LP = LW && !LX;
      TS4(j + 1);
      if constexpr (LDR) {
        wait_vm<decltype(WAITc)::value>();
        if constexpr (LX) { if (__builtin_expect(j <= 2, 0)) check_tile(j); }
      }
      if constexpr (!LDR) {
        if (__builtin_expect((evm >> j) & 1, 0)) {
          if (j == js + 3) {
            if ((tmask >> (j - 1)) & 1) wait_vm<1>(); else wait_vm<0>();
            flag_set(a.sync, F_COL + tn * 256 + (tm * 4 + wave - 4) * 8, tag_get(lds0), 0);
          }
          if (j == 1) glu_stats_issue();
          if (j == JS && wave == 7) { wait_vm<0>(); glu_stats(); }
          if (j == JS + 2 && wave == 7) {
            if (j == js + 2) wait_vm<1>(); else wait_vm<0>();
            flag_set(a.sync, fof + tn * 8, tag_get(lds0), 0);
          }
          {
            int lx = lane; asm volatile("" : "+v"(lx));
            if (j == jf) __builtin_amdgcn_raw_ptr_buffer_load_lds(RSRC(a.sync), LDSP(lds0 + CFL + (wave & 1) * 256), 4, lx * 4, fof, 0, 16);
            if (j == jf + 1) {
              wait_vm<0>();
              int fv = ds_r32(lds0 + CFL + (wave & 1) * 256 + lx * 4);
              const Tag tg = tag_get(lds0);
              asm volatile("" : "+v"(fv));
              while (!words_up((unsigned)fv, tg) && in_time(lds0))
                asm volatile("global_load_dword %0, %1, %2 sc1\n\ts_waitcnt vmcnt(0)" : "=v"(fv) : "v"(lx * 4), "s"((const char*)a.sync + fof) : "memory");
              if (wave == 7) {
#pragma unroll
                for (int i = 0; i < 4; ++i) __builtin_amdgcn_raw_ptr_buffer_load_lds(rC, LDSP(lds0 + RING + i * 1024), 16, lx * 16, RTAB + m0 * 16 + i * 1024, 0, 16);
              }
            }
            if (j == jf + 3) wait_vm<0>();
          }
          if ((tmask >> j) & 1) { touch((const void*)tb, tv, to, tl); to += ts; }
        }
        __builtin_amdgcn_sched_barrier(0);
      }
      int xp = xs + 2; if (xp >= 3) xp -= 3;
      const int ws = ((j & 1) * 2) ^ WR;
      constexpr bool LD = LDR && !LW;
      unsigned dw = 0;
      if constexpr (LD) { int lx = lane; asm volatile("" : "+v"(lx)); dw = dn_wsrc(lx, wave); }
      auto ldd = [&](int i) { static_for<0, 14>([&](auto kc) { if (i == decltype(kc)::value) hook(IC<64 + decltype(kc)::value>{}, dw); }); };
      auto lda = [&](int i) { if constexpr (LD) ldd(i); else ldW(i % WI, 2 * j + 2 + i / WI, (ws ^ 2) + i / WI); };
      auto ldb = [&](int i) {
          if constexpr (LX) ldX(i % XI, j + 2, i / XI, xp);
          else if constexpr (LD) ldd(7 + i);
          else __builtin_amdgcn_raw_ptr_buffer_load_lds(rC, LDSP(lds0 + CPB + wave * 8192 + i * 1024), 16, lane * 16, (tn * 2 + wn) * 7168 + i * 1024, 0, 16); };
      using NLA = IC<LW ? 2 * WI : (LD ? 7 : 0)>; using NLB = IC<LX ? 2 * XI : (LP || LD ? 7 : 0)>;
      { const RdCtx c = mkctx(false, xs * XT, ws * WU); stepg(CAc, fb, fa, c, IC<LDR ? 2 : 0>{}, IC<LDR ? 1 : -1>{}, [&]() { raw_barrier(); }, NLA{}, lda); }
      { const RdCtx c = mkctx(true, xs * XT, (ws + 1) * WU); stepg(CBc, fa, fb, c, I0{}, IC<LDR ? -1 : 25>{},
          [&]() { asm volatile("s_waitcnt lgkmcnt(0)" ::: "memory"); if (decltype(WAITc)::value == 0 || j + 1 < NT) raw_barrier(); }, NLB{}, ldb); }
      xs = xs + 1; if (xs >= 3) xs = 0;
    };
    using WX_ = IC<2 * XI>;
    bf16x8 pv[8];
    if constexpr (LDR) {
      for (int j = 1; j < NT - 2; ++j) iter(j, T{}, T{}, WX_{}, F{}, F{});
      iter(NT - 2, T{}, F{}, WX_{}, F{}, F{});
      __builtin_amdgcn_s_setprio(3);
      iter(NT - 1, F{}, F{}, IC<7>{}, F{}, F{});
      wait_vm<14>();
      const unsigned ca = lds0 + CPB + wave * 8192 + fq * 1792 + (fr < 14 ? fr : 0) * 16;
      static_for<0, 8>([&](auto ic) { constexpr int i = decltype(ic)::value; pv[i] = ds_b128<i * 224>(ca); });
    } else {
      int j = 1;
      if (wave == 4) {
        wait_vm<0>();
        int fv = ds_r32(lds0 + TCH + 256 + lane * 4);
        const Tag tg = tag_get(lds0);
        asm volatile("" : "+v"(fv));
        if (!words_up((unsigned)fv, tg)) wait_flags(lds0, a.sync, F_H + panel * 256, 32, 0);
      }
      raw_barrier();
#pragma unroll 1
      for (; j < js; ++j) iter(j, F{}, F{}, I0{}, F{}, F{});
      const int ja = wm == 2 ? js + 2 : js;
#pragma unroll 1
      for (; j < ja; ++j) iter(j, F{}, F{}, I0{}, T{}, F{});
#pragma unroll 1
      for (; j < js + 2; ++j) iter(j, F{}, F{}, I0{}, F{}, T{});
      { int lx = lane; asm volatile("" : "+v"(lx));
        __builtin_amdgcn_raw_buffer_store_b128(__builtin_bit_cast(u32x4, accs), rC, (lx & 15) < NF ? (unsigned)((tn * 4 + wn * 2) * 3584 + (tm * 2 + (wm & 1)) * 224
                + ((lx & 15) < 6 ? (lx & 1) * 3584 + ((lx & 15) >> 1) * 64 + (lx >> 4) * 16 : (lx >> 5) * 3584 + 192 + (lx & 16))) : 0x80000000u, 0, 16); }
#pragma unroll 1
      for (; j < NT; ++j) iter(j, F{}, F{}, I1{}, F{}, F{});
      wait_vm<0>();
      done_inc();
    }
    TS4(17);
    { const RdCtx c = mkctx(false, 0, 0); step(fb, fa, c, F{}, IC<LDR ? 2 : 0>{}, [&](int i) {
        if (i == 1) {
          asm volatile("s_waitcnt lgkmcnt(0)" ::: "memory");
#pragma unroll
          for (int k = 0; k < 8; ++k) asm volatile("" : "+v"(pv[k]));
          f32x4 cv = ((__builtin_bit_cast(f32x4, pv[0]) + __builtin_bit_cast(f32x4, pv[1])) + (__builtin_bit_cast(f32x4, pv[2]) + __builtin_bit_cast(f32x4, pv[3])))
                   + ((__builtin_bit_cast(f32x4, pv[4]) + __builtin_bit_cast(f32x4, pv[5])) + (__builtin_bit_cast(f32x4, pv[6]) + __builtin_bit_cast(f32x4, pv[7])));
#pragma unroll
          for (int k = 0; k < 4; ++k) { const float t = cv[k]; auto sw = __builtin_amdgcn_permlane16_swap(__float_as_uint(t), __float_as_uint(t), false, false); cv[k] = __uint_as_float(sw[0]) + __uint_as_float(sw[1]); }
          if ((lane & 31) < 14) *(f32x4*)(tb_cs + (lane >> 5) * 112 + wn * 56 + fr * 4) = cv;
          done_inc();
        } }); }
    if constexpr (LDR) __builtin_amdgcn_s_setprio(0);
    epilogue(LDRc);
  };
  if (wave < NLW) run(T{}); else run(F{});
  STAMP(7);
  STAMP(8);
  STAMPW(12, 4);
  STAMP(3);
#endif
}

#define KN_CT 7
#define KN_E 6
#define KN_B3 0
#define KN_WB 2
#define KN_GW 1
#define XAUXW 16
#define XAUXD 16
#define KN_WS 2
#define KN_HE 0
#define KN_FL 22
#define KN_LD 0
#define KN_DM 0
#define KN_LX 0
#define STAMPV(i) STAMP(i)
struct FragW { s16x4 lo[4], hi[4]; };
template <int MODE, int LO, int HI>
__device__ __forceinline__ void wo_phase(const GemmArgs& a, const GemmArgs& nx) {
#if defined(__HIP_DEVICE_COMPILE__)
  extern __shared__ __attribute__((aligned(16))) char smem[];
  if constexpr (MODE == 2) STAMP(0);
  const unsigned lds0 = (unsigned)(size_t)(lds_char_ptr)smem;
  const int tid = threadIdx.x, lane = tid & 63;
  const int wave = __builtin_amdgcn_readfirstlane(tid >> 6);
  const int bid = blockIdx.x, xcd = bid & 7, idx = bid >> 3;
  const int tm = (xcd >> 1) * 4 + (idx >> 3), tn = (xcd & 1) * 8 + (idx & 7);
  const int m0 = tm * 128, panel = m0 >> 8, rot = WO_ROT;
  if constexpr (MODE & 1) {
    const auto rW = __builtin_amdgcn_make_buffer_rsrc((void*)a.W0, 0, 0x7fffffff, 0x00020000);
    const int row = wave * 8 + (lane >> 3);
    const unsigned wsrc = (unsigned)(row * D_ + tn * 64 + ((lane & 7) ^ wswz(row)) * 8) * 2u;
    static_for<LO, HI>([&](auto ic) { constexpr int i = decltype(ic)::value;
      __builtin_amdgcn_raw_ptr_buffer_load_lds(rW, (lds_void_ptr)(size_t)(lds0 + wo_pos(i >> 1) + (i & 1) * 8192 + wave * 1024), 16, wsrc, (((i & 1) * 8 + (i >> 1)) ^ rot) * (64 * D_ * 2), 0, 0);
      if constexpr (MODE == 3) __builtin_amdgcn_raw_ptr_buffer_load_lds(rW, LDSP(lds0 + wo_pos(i >> 1) + (i & 1) * 8192 + 4096 + wave * 1024), 16, wsrc, (((i & 1) * 8 + (i >> 1)) ^ rot) * (64 * D_ * 2) + 32 * D_ * 2, 0, 0); });
  } else {
    constexpr int PAD = 149504;
    const int g = wave >> 2, rw = wave & 3;
    const int fr = lane & 15, fq = lane >> 4, wq = (lane >> 2) & 3, wp = lane & 3;
    unsigned wa[4];
#pragma unroll
    for (int c = 0; c < 4; ++c) { const int row = 8 * fq + wq; wa[c] = lds0 + g * 8192 + row * 128 + (((c << 1) ^ wswz(row)) << 4) + wp * 8; }
    FragW f[3];
    auto rd = [&](auto ic, auto rc) {
      constexpr int i = decltype(ic)::value, r = decltype(rc)::value, P = wo_pos(i >> 1) + (i & 1) * 4096, B = P & ~32767, o = P & 32767;
      if constexpr (r & 1) f[i % 3].hi[r >> 1] = ds_tr<o + 512>(wa[r >> 1] + B); else f[i % 3].lo[r >> 1] = ds_tr<o>(wa[r >> 1] + B);
    };
    auto pin = [&](FragW& q) {
#pragma unroll
      for (int n = 0; n < 4; ++n) asm volatile("" : "+v"(q.lo[n]), "+v"(q.hi[n]));
    };
    static_for<0, 16>([&](auto rc) { constexpr int r = decltype(rc)::value; rd(IC<r / 8>{}, IC<r % 8>{}); });
    const Tag tg = tag_get(lds0);
    pin(f[0]); pin(f[1]);
    if (wave == 2) {
      unsigned long long c0, c1; CODE_ADDR(c0, "k_all"); CODE_ADDR(c1, "k_zend");
      __builtin_amdgcn_raw_ptr_buffer_load_lds(RSRCN(c0, c1 - c0 < 262144ull ? (unsigned)(c1 - c0) : 0u), LDSP(lds0 + PAD), 4, (unsigned)((lane * 32 + idx) * 128), 0, 0, 0); }
    if (wave == 3) {
      const unsigned fo = (unsigned)(((lane & 32) ? (xcd >> 2) * 4 + (idx >> 3) : panel) * 256 + (lane & 31) * 8);
      while (!flags_up(__builtin_amdgcn_raw_buffer_load_b64(RSRC(a.sync), fo, F_MARK, 16), 64, tg, 0) && in_time(lds0)) {}
    }
    const auto rX = __builtin_amdgcn_make_buffer_rsrc((void*)a.X, 0, 0x7fffffff, 0x00020000);
    const int rb = (m0 >> 4) + rw * 2 + g;
    unsigned xs[2] = {(unsigned)(rb * RBO * 256 + lane * 16), (unsigned)((rb ^ 1) * RBO * 256 + lane * 16)};
    const int ck = (fq & 1) * 2 + (fq >> 1);
    unsigned orow = (unsigned)((rb * 16 + fr) * (D_ * 2) + tn * 128 + ck * 16), ofrg = (unsigned)((rb * RBH + tn * 8 + ck) * 256 + fr * 16);
    asm volatile("" : "+v"(xs[0]), "+v"(xs[1]), "+v"(orow), "+v"(ofrg));
#define KN_RE 0
    u32x4 res[2];
    if constexpr (KN_RE) {
#pragma unroll
      for (int m = 0; m < 2; ++m) res[m] = __builtin_amdgcn_raw_buffer_load_b128(RSRC(a.R), orow + m * 64, 0, 0);
    }
    STAMP(9);
    const int fo_off = F_O(tm) + rw * 256 + ((g ^ (rot >> 3)) & 1) * 128;
    const unsigned first = (rot & 4) ? 0xff00u : 0x00ffu;
    auto down = [&](u32x2 v) { return (unsigned)__builtin_amdgcn_ballot_w64(v[0] != tg.lo || v[1] != tg.hi) & 0xffffu; };
    unsigned dn, polls = 0;
    do { dn = down(flag_get(a.sync, fo_off, 16)); asm volatile("" ::: "memory"); } while ((dn & first) && ((++polls & 1023u) || in_time(lds0)));
    STAMP(6);
    u32x2 fl = {0u, 0u};
    u32x4 xr[16][2];
#define KN_XD 4
    auto ldX = [&](int i, int m) {
      if (i < 16) xr[i][m] = __builtin_amdgcn_raw_buffer_load_b128(rX, xs[m] + (i & 1) * 1024, ((g * 8 + (i >> 1)) ^ rot) * 2048, XAUXW);
      else if (i == 16 && !KN_RE) res[m] = __builtin_amdgcn_raw_buffer_load_b128(__builtin_amdgcn_make_buffer_rsrc((void*)a.R, 0, 0x7fffffff, 0x00020000), orow + m * 64, 0, 0);
    };
#pragma unroll
    for (int i = 0; i < KN_XD; ++i) { ldX(i, 0); ldX(i, 1); __builtin_amdgcn_sched_barrier(0); }
    unsigned gsrc[2];
#pragma unroll
    for (int i = 0; i < 2; ++i) {
      const int j = wave + 8 * i, jj = j >= 7 ? j - 7 : j;
      const int b = jj * 1024 + lane * 16, q = b / 224, pos = (b % 224) >> 4;
      gsrc[i] = (unsigned)((8 * ((q & 15) >> 2) + (q & 3) + 4 * (q >> 4)) * DF_ + ((xcd & 3) * 8 + (idx & 7)) * 112 + pos * 8) * 2u;
      asm volatile("" : "+v"(gsrc[i]));
    }
    f32x4 acc[4][2];
#pragma unroll
    for (int n = 0; n < 4; ++n) { acc[n][0] = (f32x4){0.f, 0.f, 0.f, 0.f}; acc[n][1] = acc[n][0]; }
    __builtin_amdgcn_sched_barrier(0);
    static_for<0, 16>([&](auto ic) {
      constexpr int i = decltype(ic)::value;
      if constexpr (i >= 2) { asm volatile("s_waitcnt lgkmcnt(%0)" :: "n"(i < 15 ? 8 : 0) : "memory"); pin(f[i % 3]); }
      if constexpr (KN_WE < 6 && i == 0) { wait_vm<2 * KN_XD>(); raw_barrier(); }
      if constexpr (i == (KN_WE < 6 ? 4 : (KN_WE & ~1) - 2)) raw_barrier();
      if constexpr (i == 8 - KN_XD - 2) fl = flag_get(a.sync, fo_off, 16);
      if constexpr (i == 8 - KN_XD) { if (__builtin_expect(dn && down(fl), 0)) wait_flags(lds0, a.sync, fo_off, 16, 0); }
      static_for<0, 8>([&](auto jc) {
        constexpr int j = decltype(jc)::value, n = j >> 1, m = j & 1;
        const bf16x8 w = __builtin_shufflevector(f[i % 3].lo[n], f[i % 3].hi[n], 0, 1, 2, 3, 4, 5, 6, 7);
        acc[n][m] = __builtin_amdgcn_mfma_f32_16x16x32_bf16(w, __builtin_bit_cast(bf16x8, xr[i][m]), acc[n][m], 0, 0, 0);
        if constexpr (i + 2 < 16) rd(std::integral_constant<int, i + 2>{}, jc);
        if constexpr (j == 1 || j == 5) ldX(i + KN_XD, j >> 2);
        __builtin_amdgcn_sched_barrier(0);
      });
      if constexpr (i == 0) STAMP(1);
    });
    STAMP(2);
    auto slot = [&](int w) { return wo_pos(0) + w * 4096 + lane * 16; };
#pragma unroll
    for (int n = 0; n < 4; ++n) *(f32x4*)(smem + slot(wave) + n * 1024) = acc[n][1];
    asm volatile("" : "+v"(res[0]), "+v"(res[1]));
    lds_barrier();
    STAMP(4);
    f32x4 pa[4];
    static_for<0, 4>([&](auto nc) { constexpr int n = decltype(nc)::value; pa[n] = __builtin_bit_cast(f32x4, ds_b128<n * 1024>(lds0 + slot(wave ^ 4))); });
    if constexpr (KN_GW) {
      auto ldG = [&](int i, int u) { const int j = wave + 8 * i;
        __builtin_amdgcn_raw_ptr_buffer_load_lds(__builtin_amdgcn_make_buffer_rsrc((void*)(j >= 7 ? nx.W1 : nx.W0), 0, 0x7fffffff, 0x00020000),
                          (lds_void_ptr)(size_t)(lds0 + 98304 + (u ^ 2) * 14592 + (j >= 7 ? 7184 + (j - 7) * 1024 : j * 1024)), 16, gsrc[i], (u + (xcd >> 2) * 16) * 32 * DF_ * 2, 0, 0); };
      ldG(0, 0); ldG(0, 1);
      if (wave < 6) { ldG(1, 0); ldG(1, 1); }
    }
    asm volatile("s_waitcnt lgkmcnt(0)" : "+v"(pa[0]), "+v"(pa[1]), "+v"(pa[2]), "+v"(pa[3]) :: "memory");
    const auto rO = __builtin_amdgcn_make_buffer_rsrc((void*)a.O, 0, 0x7fffffff, 0x00020000);
    u32x4 o[2];
#pragma unroll
    for (int i = 0; i < 2; ++i) {
      const f32x4 u = acc[2 * i][0] + pa[2 * i], v = acc[2 * i + 1][0] + pa[2 * i + 1];
      const auto s0 = __builtin_amdgcn_permlane16_swap(pk_bf16(u[0], u[1]), pk_bf16(v[0], v[1]), false, false);
      const auto s1 = __builtin_amdgcn_permlane16_swap(pk_bf16(u[2], u[3]), pk_bf16(v[2], v[3]), false, false);
      const u32x4 q4 = {s0[0], s1[0], s0[1], s1[1]};
#pragma unroll
      for (int j = 0; j < 4; ++j) { const unsigned r = res[i][j], q = q4[j]; o[i][j] = pk_bf16(bflo(r) + bflo(q), bfhi(r) + bfhi(q)); }
      __builtin_amdgcn_raw_buffer_store_b128(o[i], rO, ofrg + i * 1024, 0, 16);
    }
    auto sum4 = [](float v) {
      auto p = __builtin_amdgcn_permlane16_swap(__float_as_uint(v), __float_as_uint(v), false, false); v = __uint_as_float(p[0]) + __uint_as_float(p[1]);
      auto q = __builtin_amdgcn_permlane32_swap(__float_as_uint(v), __float_as_uint(v), false, false); return __uint_as_float(q[0]) + __uint_as_float(q[1]); };
    f32x2 e[8], sm = {0.f, 0.f};
#pragma unroll
    for (int j = 0; j < 8; ++j) { const unsigned q = o[j >> 2][j & 3]; e[j] = (f32x2){bflo(q), bfhi(q)}; sm += e[j]; }
    const float mu = sum4(sm[0] + sm[1]) * (1.f / 64.f);
    f32x2 t2 = {0.f, 0.f};
#pragma unroll
    for (int j = 0; j < 8; ++j) { const f32x2 d = e[j] - mu; t2 += d * d; }
    const float q2 = sum4(t2[0] + t2[1]);
    __builtin_amdgcn_raw_buffer_store_b64((u32x2){__float_as_uint(mu), __float_as_uint(q2)}, __builtin_amdgcn_make_buffer_rsrc((void*)a.st, 0, 0x7fffffff, 0x00020000),
                                          lane < 16 ? (unsigned)((tn * S_ + rb * 16 + lane) * 8) : 0x80000000u, 0, 16);
    STAMP(7);
    STAMP(8);
    asm volatile("s_waitcnt vmcnt(0) lgkmcnt(0)" ::: "memory");
    raw_barrier();
    if (wave == 0) flag_set(a.sync, F_H + panel * 256 + ((tm & 1) * 16 + tn) * 8, tg, 0);
    STAMP(3);
  }
#endif
}

struct FragD { s16x4 lo[8], hi[8]; };
enum { O_X, O_W, O_SL, O_SP, O_FA, O_FP, O_QC, O_QD, O_QP, O_RES, O_LP, O_CE, O_CC, O_CD, O_CP, O_BAR };
constexpr int DN_A = 16, DN_PRE = 13;
constexpr unsigned char DN_OPS[][4] = {
 {1,0,O_CE,2}, {1,0,O_QC,0}, {1,2,O_X,6}, {1,5,O_X,7}, {1,9,O_X,8}, {1,13,O_X,9}, {2,2,O_X,10}, {2,5,O_X,11}, {2,9,O_X,12}, {2,13,O_SL,0},
 {3,0,O_CE,7}, {3,2,O_X,13}, {3,5,O_X,14}, {3,9,O_X,15}, {3,13,O_SL,1}, {4,2,O_BAR,12}, {4,3,O_X,16}, {4,6,O_X,17}, {4,10,O_X,18}, {4,13,O_SL,2},
 {5,2,O_X,19}, {5,5,O_X,20}, {5,9,O_W,14}, {5,13,O_W,13}, {6,2,O_X,21}, {6,5,O_X,22}, {6,9,O_W,16}, {6,13,O_W,15}, {7,0,O_FA,0}, {7,2,O_X,23}, {7,5,O_X,24}, {7,9,O_X,25}, {7,13,O_W,17},
 {8,2,O_X,26}, {8,5,O_X,27}, {8,9,O_X,28}, {8,13,O_W,18}, {9,2,O_X,29}, {9,5,O_X,30}, {9,9,O_X,31}, {9,13,O_W,19}, {9,16,O_QC,0}, {10,2,O_X,32}, {10,7,O_X,33}, {10,12,O_W,20},
 {11,0,O_CC,0}, {11,2,O_X,34}, {11,5,O_X,35}, {11,9,O_X,36}, {11,13,O_W,21}, {12,2,O_BAR,17}, {12,3,O_X,37}, {12,6,O_X,38}, {12,10,O_X,39}, {12,13,O_W,22},
 {13,2,O_X,40}, {13,5,O_X,41}, {13,9,O_X,42}, {13,13,O_W,23}, {14,2,O_X,43}, {14,5,O_X,44}, {14,9,O_X,45}, {14,13,O_W,24}, {14,16,O_QD,0},
 {15,2,O_X,46}, {15,7,O_X,47}, {15,12,O_W,25}, {16,0,O_CD,0}, {16,2,O_BAR,23}, {16,3,O_X,48}, {16,6,O_X,49}, {16,10,O_X,50}, {16,13,O_W,26},
 {17,2,O_X,51}, {17,5,O_X,52}, {17,9,O_X,53}, {17,13,O_W,27}, {18,2,O_X,54}, {18,5,O_X,55}, {18,9,O_W,28}, {18,13,O_RES,0}, {19,2,O_W,30}, {19,9,O_W,29},
 {20,2,O_W,31}, {20,5,O_SP,0}, {20,9,O_SP,1}, {20,13,O_RES,1}, {21,2,O_X,56}, {21,5,O_X,57}, {21,9,O_SP,2}, {21,13,O_SP,3}, {22,2,O_X,58}, {22,5,O_X,59}, {22,9,O_X,60}, {22,13,O_X,61},
 {23,2,O_BAR,31}, {23,3,O_X,62}, {23,10,O_X,63}, {24,0,O_FP,0}, {24,0,O_FP,1}, {24,2,O_SP,4}, {24,5,O_SP,5}, {26,0,O_FP,2}, {27,8,O_QP,2},
 {30,0,O_CP,0}, {30,0,O_CP,1}, {30,2,O_LP,0}, {30,3,O_LP,1}, {30,5,O_LP,2}, {30,7,O_LP,3}, {31,0,O_CP,2}, {31,2,O_LP,4}, {31,5,O_LP,5},
};
constexpr int DN_N = sizeof(DN_OPS) / 4, DN_H = (24 - DN_A) / 2, DN_B = DN_A + DN_H, NS = 32;
constexpr int dn_nm(int v) { return v < 24 ? 16 : 8; }
constexpr int dn_unit(int v, int n) { return (v < DN_A || v >= 24) ? v : DN_A + 2 * ((v - DN_A) % DN_H) + (n >> 2); }
constexpr int dn_wpos(int u, int h) { constexpr int P[19] = {8, 9, 10, 11, 16, 17, 18, 6, 7, 12, 13, 14, 15, 0, 1, 2, 3, 4, 5}; return P[u % 19] * 8192 + h * 4096; }
constexpr int dn_first(int v, int g) { int i = 0; while (i < DN_N && (DN_OPS[i][0] < v || (DN_OPS[i][0] == v && DN_OPS[i][1] < g))) ++i; return i; }
constexpr int dn_find(int kind, int arg) { for (int i = 0; i < DN_N; ++i) if (DN_OPS[i][2] == kind && DN_OPS[i][3] == arg) return i; return -1; }
constexpr int dn_vm(int lo, int hi) { int c = 0; for (int i = lo; i < hi; ++i) c += DN_OPS[i][2] <= O_LP; return c; }
constexpr bool dn_bar(int v) { for (int i = 0; i < DN_N; ++i) if (DN_OPS[i][0] == v && DN_OPS[i][2] == O_BAR) return true; return false; }
constexpr int dn_cnt(int w, int i) { int p = -1; for (int u = DN_PRE; u <= w; ++u) if (dn_find(O_W, u) > p) p = dn_find(O_W, u); return p >= 0 ? dn_vm(p + 1, i) : 63; }
constexpr bool dn_ok() {
  int have = DN_PRE - 1, bstep = 0;
  for (int v = 0; v + 1 < NS; ++v) {
    for (int i = dn_first(v, 0); i < dn_first(v + 1, 0); ++i) {
      if (DN_OPS[i][2] == O_BAR) { if (DN_OPS[i][3] > have) have = DN_OPS[i][3]; bstep = v; for (int u = DN_PRE; u <= have; ++u) if (dn_find(O_W, u) < 0 || dn_find(O_W, u) > i) return false; }
      if (DN_OPS[i][2] == O_W) {
        if (bstep < 1) return false;
        for (int t = bstep + 1; t < NS; ++t) for (int n = 0; n < 8; n += 4) if (dn_unit(t, n) < DN_OPS[i][3] && dn_wpos(dn_unit(t, n), 0) == dn_wpos(DN_OPS[i][3], 0)) return false;
      }
    }
    if (dn_unit(v + 1, 7) > have) return false;
  }
  for (int x = 6; x < 64; ++x) {
    int n = 0; for (int i = 0; i < dn_find(O_X, x); ++i) if (DN_OPS[i][2] == O_CE) n = DN_OPS[i][3];
    if (x < 34 ? n < (x - 2) / 4 : dn_find(O_X, x) < dn_find(x < 48 ? O_CC : O_CD, 0)) return false;
  }
  return dn_find(O_SP, 0) >= dn_first(DN_B, 0) && dn_find(O_SP, 4) >= dn_first(24, 0);
}
static_assert(dn_ok(), "schedule");
constexpr int dn_rd(int v, int I) { int c = 0; const int r0 = dn_bar(v) ? 2 : 1; if (v + 1 < NS) for (int r = 0; r < 16; ++r) c += r0 + r * (dn_nm(v) - r0 + 1) / 16 <= I; return c; }
struct DnTab { short first[NS + 1][18], rd[NS][18], wait[DN_N]; };
constexpr DnTab dn_tab() {
  DnTab t = {};
  for (int v = 0; v <= NS; ++v) for (int g = 0; g < 18; ++g) { t.first[v][g] = dn_first(v, g); if (v < NS) t.rd[v][g] = dn_rd(v, g); }
  for (int i = 0; i < DN_N; ++i) {
    const int kind = DN_OPS[i][2], arg = DN_OPS[i][3];
    t.wait[i] = kind == O_FA ? dn_vm(dn_find(O_SL, 2) + 1, i) : (kind == O_FP ? dn_vm(dn_find(O_SP, 2 * arg + 1) + 1, i) : (kind == O_BAR ? dn_cnt(arg, i) : 0));
  }
  return t;
}
constexpr DnTab DT = dn_tab();
#define KN_SLP 5
#define KN_PRI 1
template <int MODE, int U>
__device__ __forceinline__ void dn_phase(const GemmArgs& a, const Late& late, unsigned wsrc = 0) {
#if defined(__HIP_DEVICE_COMPILE__)
  extern __shared__ __attribute__((aligned(16))) char smem[];
  if constexpr (MODE != 1) STAMP(0);
  const unsigned lds0 = (unsigned)(size_t)(lds_char_ptr)smem;
  const int tid = threadIdx.x;
  int lane = tid & 63; asm volatile("" : "+v"(lane));
  const int wave = __builtin_amdgcn_readfirstlane(tid >> 6);
  const int bid = blockIdx.x, xcd = bid & 7, idx = bid >> 3;
  const int panel = (xcd >> 2) * 4 + (idx >> 3), c = idx & 7, q = xcd & 3;
  const int g4 = wave & 3, hh = U >= 64 ? U & 1 : wave >> 2;
#define KN_ABL 0
  const auto rW = RSRCN(a.W0, (KN_ABL & 1) ? 0 : 0x7fffffff);
  if constexpr (MODE != 1) wsrc = dn_wsrc(lane, wave);
  auto uj = [&](int v) { return q * 8 + ((c + (v < 3 ? 0 : (v < 17 ? (v - 1) >> 1 : v - 16))) & 7); };
  auto us = [](int v) { return v < 3 ? v : (v < 17 ? (v - 1) & 1 : 2); };
  auto ldW = [&](int v) {
    const int row = v < 24 ? uj(v) * 112 + (us(v) < 2 ? us(v) * 56 + g4 * 8 : 32 + (g4 & 1) * 8 + (g4 >> 1) * 56) : ((((v + c) & 7) * 2 + hh) * 4 + g4) * 56 + 48;
    __builtin_amdgcn_raw_ptr_buffer_load_lds(rW, LDSP(lds0 + dn_wpos(v, 0) + hh * 4096 + g4 * 1024), 16, wsrc, row * (D_ * 2) + c * 256 + (((v < 24 ? hh : 0) ^ (q >> 1)) << 7), 0, 0);
  };
  const int fa_off = F_ACT + (panel * 4 + (wave >> 1)) * 512;
  if constexpr (MODE == 1) {
    if constexpr (U < 32) { if constexpr (U < DN_PRE) ldW(U); }
    else if constexpr (U >= 64) ldW((U - 64) >> 1);
    return;
  }
  const Tag tg = tag_get(lds0);
  const int fr = lane & 15, fq = lane >> 4, wq = (lane >> 2) & 3, wp = lane & 3;
  const int rb = wave * 2 + (q & 1);
  f32x4 acc[8][2];
#pragma unroll
  for (int n = 0; n < 8; ++n) { acc[n][0] = (f32x4){0.f, 0.f, 0.f, 0.f}; acc[n][1] = acc[n][0]; }
  unsigned wa[3][4];
#pragma unroll
  for (int i = 0; i < 4; ++i) { const int row = 8 * fq + wq; wa[0][i] = lds0 + row * 128 + (((i << 1) ^ wswz(row)) << 4) + wp * 8; wa[1][i] = wa[0][i] + 65536; wa[2][i] = wa[0][i] + 131072; }
  const auto rX = RSRCN(a.X, (KN_ABL & 2) ? 0 : 0x7fffffff);
  const unsigned xs[2] = {(unsigned)((panel * 16 + rb) * RBA * 256 + lane * 16), (unsigned)((panel * 16 + (rb ^ 1)) * RBA * 256 + lane * 16)};
  FragD fa, fb;
  u32x4 xr[32][2];
  auto rdw = [&](FragD& f, auto vc, auto rc) {
    constexpr int v = decltype(vc)::value, r = decltype(rc)::value, n = r >> 1, u = dn_unit(v, n);
    constexpr int p = dn_wpos(u, (v < DN_A || v >= 24) ? n >> 2 : (v < DN_B ? 1 : 0)), o = (p & 65535) + (r & 1) * 512, b = p >> 16;
    if constexpr (r & 1) f.hi[n] = ds_tr<o>(wa[b][n & 3]); else f.lo[n] = ds_tr<o>(wa[b][n & 3]);
  };
  auto ldX = [&](int v, int m) {
    if (v < 24) xr[v][m] = __builtin_amdgcn_raw_buffer_load_b128(rX, xs[m], uj(v) * 3072 + us(v) * 1024, 16);
    else xr[v][m] = __builtin_amdgcn_raw_buffer_load_b128(rX, xs[0] + m * 1024, 384 * 256 + ((v + c) & 7) * 2048, 16);
  };
  const auto rS = RSRC(a.slab);
  const int ck = (fq & 1) * 2 + (fq >> 1);
  u32x4 pp[3][2] = {}, res[2];
  auto ldP = [&](int j, bool ok) { pp[j >> 1][j & 1] = __builtin_amdgcn_raw_buffer_load_b128(RSRCN(a.slab, ok ? 0x7fffffff : 0), lane * 16 + (j & 1) * 1024, ((bid * 8 + wave) * 3 + (j >> 1)) * 2048, 16); };
  auto st128 = [&](u32x4 v, auto rs, unsigned vo, int so) {
    __builtin_amdgcn_raw_buffer_store_b128(v, rs, vo, so, 16);
    __builtin_amdgcn_sched_barrier(0);
    asm volatile("s_nop 1" ::: "memory");
    __builtin_amdgcn_sched_barrier(0); };
  auto stP = [&](int j) {
    const int k = 3 - (j >> 1), i = j & 1;
    const f32x4 s0 = acc[(k >> 1) * 4 + 2 * i][k & 1], s1 = acc[(k >> 1) * 4 + 2 * i + 1][k & 1];
    st128((u32x4){pk_bf16(s0[0], s0[1]), pk_bf16(s0[2], s0[3]), pk_bf16(s1[0], s1[1]), pk_bf16(s1[2], s1[3])}, rS, lane * 16 + i * 1024, (((bid ^ k) * 8 + wave) * 3 + (j >> 1)) * 2048); };
  auto stL = [&](int i) {
    const int so = (panel * 16 + (wave >> 1) * 4 + (i == 1 ? 2 : 0)) * RBA * 256 + (i < 2 ? (q * 8 + c) * 12 + 8 + (wave & 1) * 2 : 384 + (q * 8 + c) * 2 + (wave & 1)) * 256;
    st128(late.v[i], RSRC(a.X), (unsigned)((i < 2 ? (fq & 1) * RBA + (fq >> 1) : ck * RBA) * 256 + fr * 16), so); };
  const int fp_off = F_PART + (bid * 8 + wave) * 64;
  unsigned got = 0;
  u32x2 fl = {0u, 0u};
  auto rot16 = [&](int n) { const unsigned m = ((1u << (2 * n)) - 1u) << (2 * c + 2); return (unsigned long long)((m | (m >> 16)) & 0xffffu); };
#define TS5(i)
#define KN_Q0 0
  if constexpr (KN_Q0) fl = flag_get(a.sync, fa_off + q * 128, 16);
  STAMP(9);
  lds_barrier();
  STAMP(6);
  if constexpr (!KN_Q0) fl = flag_get(a.sync, fa_off + q * 128, 16);
  {
    const unsigned e0 = lds0 + OWNE + rb * 2048 + lane * 16, e1 = lds0 + OWNE + (rb ^ 1) * 2048 + lane * 16, m0 = lds0 + OWNM + rb * 1024 + lane * 16, m1 = lds0 + OWNM + (rb ^ 1) * 1024 + lane * 16;
    xr[0][0] = __builtin_bit_cast(u32x4, ds_b128<0>(e0)); xr[0][1] = __builtin_bit_cast(u32x4, ds_b128<0>(e1));
    xr[1][0] = __builtin_bit_cast(u32x4, ds_b128<1024>(e0)); xr[1][1] = __builtin_bit_cast(u32x4, ds_b128<1024>(e1));
    xr[2][0] = __builtin_bit_cast(u32x4, ds_b128<0>(m0)); xr[2][1] = __builtin_bit_cast(u32x4, ds_b128<0>(m1));
    static_for<0, 16>([&](auto rc) { rdw(fa, IC<0>{}, rc); });
  }
  auto op = [&](auto ic) {
    constexpr int i = decltype(ic)::value, kind = DN_OPS[i][2], arg = DN_OPS[i][3];
    if constexpr (kind == O_X) ldX(arg >> 1, arg & 1);
    if constexpr (kind == O_W) ldW(arg);
    if constexpr (kind == O_SL) stL(arg);
    if constexpr (kind == O_SP) stP(arg);
    if constexpr (kind == O_FA) { wait_vm<DT.wait[i]>(); flag_set(a.sync, F_ACT + ((panel * 4 + (wave >> 1)) * 64 + (q * 8 + c) * 2 + (wave & 1)) * 8, tg, 2); }
    if constexpr (kind == O_FP) { wait_vm<DT.wait[i]>(); if constexpr (arg) STAMP(10 + arg); flag_set(a.sync, F_PART + ((bid ^ (3 - arg)) * 8 + wave) * 64 + arg * 8, tg, 0); }
    if constexpr (kind == O_QC) fl = flag_get(a.sync, fa_off + q * 128, 16);
    if constexpr (kind == O_QD) fl = flag_get(a.sync, fa_off, 64);
    if constexpr (kind == O_QP) fl = flag_get(a.sync, fp_off, 3);
    if constexpr (kind == O_RES) res[arg] = __builtin_amdgcn_raw_buffer_load_b128(RSRC(a.R), (unsigned)(((panel * 16 + rb) * RBH + c * 16 + (q >> 1) * 8 + ck) * 256 + fr * 16), arg * 1024, 16);
    if constexpr (kind == O_LP) ldP(arg, (got >> (arg >> 1)) & 1);
    if constexpr (kind == O_CE) {
      const unsigned long long need = rot16(arg);
      auto up = [&]() { return (__builtin_amdgcn_ballot_w64(fl[0] - tg.lo - 1u > 6u || fl[1] != tg.hi) & need) == 0; };
      if (__builtin_expect(!up(), 0)) { do fl = flag_get(a.sync, fa_off + q * 128, 16); while (!up() && in_time(lds0)); }
      STAMP(4);
    }
    if constexpr (kind == O_CC) { if (__builtin_expect(!flags_up(fl, 16, tg, 2), 0)) wait_flags(lds0, a.sync, fa_off + q * 128, 16, 2); }
    if constexpr (kind == O_CD) { if (__builtin_expect(!flags_up(fl, 64, tg, 2), 0)) wait_flags(lds0, a.sync, fa_off, 64, 2); }
    if constexpr (kind == O_CP) got |= (unsigned)__builtin_amdgcn_ballot_w64(fl[0] == tg.lo && fl[1] == tg.hi) & (1u << arg);
    if constexpr (kind == O_BAR) { if constexpr (!(KN_ABL & 4)) { wait_vm<DT.wait[i]>(); lds_barrier(); } }
    __builtin_amdgcn_sched_barrier(0);
  };
  static_for<0, NS>([&](auto vc) {
    constexpr int v = decltype(vc)::value, NM = dn_nm(v);
    FragD& cur = (v & 1) ? fb : fa; FragD& nxt = (v & 1) ? fa : fb;
    TS5(v);
    if constexpr (KN_PRI == 1) __builtin_amdgcn_s_setprio(3 - (v & 3));
    if constexpr (KN_PRI == 2) { if (wave >= 4) __builtin_amdgcn_s_setprio(v & 1); else __builtin_amdgcn_s_setprio(1 - (v & 1)); }
    if constexpr (KN_PRI == 3) { if constexpr (!(v & 1)) __builtin_amdgcn_s_setprio(3 - ((v >> 1) & 3)); }
    static_for<DT.first[v][0], DT.first[v][1]>(op);
    __builtin_amdgcn_sched_barrier(0);
    static_for<0, NM>([&](auto ic) {
      constexpr int I = decltype(ic)::value, n = NM == 16 ? I >> 1 : I, m = NM == 16 ? I & 1 : I >> 2;
      if constexpr (I % (NM / 4) == 0) {
        constexpr int left = 12 - 2 * n + DT.rd[v][I];
        asm volatile("s_waitcnt lgkmcnt(%0)" :: "n"(left < 15 ? left : 15) : "memory");
        asm volatile("" : "+v"(cur.lo[n]), "+v"(cur.hi[n]), "+v"(cur.lo[n + 1]), "+v"(cur.hi[n + 1]));
        if constexpr (v == 0 && I == 0) {
#pragma unroll
          for (int i = 0; i < 3; ++i) asm volatile("" : "+v"(xr[i][0]), "+v"(xr[i][1]));
          STAMP(1);
        }
        __builtin_amdgcn_sched_barrier(0);
      }
      const bf16x8 w = __builtin_shufflevector(cur.lo[n], cur.hi[n], 0, 1, 2, 3, 4, 5, 6, 7);
      constexpr int an = v < DN_A ? n : (v < DN_B ? 4 : 0) + (n & 3), am = v < 24 ? m : 0;
      acc[an][am] = MFMA(w, __builtin_bit_cast(bf16x8, xr[dn_unit(v, n)][m]), acc[an][am]);
      __builtin_amdgcn_sched_barrier(0);
      static_for<DT.first[v][I + 1], DT.first[v][I + 2]>(op);
      static_for<DT.rd[v][I], DT.rd[v][I + 1]>([&](auto rc) { rdw(nxt, IC<v + 1>{}, rc); });
      __builtin_amdgcn_sched_barrier(0);
    });
    if constexpr (v == 23) STAMP(10);
  });
  STAMP(2);
  TS5(NS);
  static_for<DT.first[NS][0], DN_N>(op);
  if (got != 7u) {
    for (;;) {
      const u32x2 f0 = flag_get(a.sync, fp_off, 3);
      __builtin_amdgcn_s_sleep(KN_SLP);
      const u32x2 f1 = flag_get(a.sync, fp_off, 3);
      if (flags_up(f0, 3, tg, 0)) break;
      if (flags_up(f1, 3, tg, 0) || !in_time(lds0)) break;
    }
    u32x4 pe[6];
#pragma unroll
    for (int j = 5; j >= 0; --j) { pe[j] = pp[j >> 1][j & 1]; ldP(j, !((got >> (j >> 1)) & 1)); }
#pragma unroll
    for (int j = 5; j >= 0; --j) pp[j >> 1][j & 1] |= pe[j];
  }
  TS5(NS + 1);
  STAMP(5);
  const auto rO = RSRC(a.O);
  const unsigned orow = (unsigned)(((panel * 16 + rb) * 16 + fr) * (D_ * 2) + c * 256 + (q >> 1) * 128 + ck * 16);
#define KN_OAUX 0
#pragma unroll
  for (int i = 0; i < 2; ++i) {
    f32x4 u = acc[2 * i][0], w = acc[2 * i + 1][0];
#pragma unroll
    for (int k = 0; k < 3; ++k) {
      const unsigned q0 = pp[k][i][0], q1 = pp[k][i][1], q2 = pp[k][i][2], q3 = pp[k][i][3];
      u += (f32x4){bflo(q0), bfhi(q0), bflo(q1), bfhi(q1)}; w += (f32x4){bflo(q2), bfhi(q2), bflo(q3), bfhi(q3)};
    }
    const auto s0 = __builtin_amdgcn_permlane16_swap(pk_bf16(u[0], u[1]), pk_bf16(w[0], w[1]), false, false);
    const auto s1 = __builtin_amdgcn_permlane16_swap(pk_bf16(u[2], u[3]), pk_bf16(w[2], w[3]), false, false);
    const u32x4 q4 = {s0[0], s1[0], s0[1], s1[1]};
    u32x4 o;
#pragma unroll
    for (int j = 0; j < 4; ++j) { const unsigned r = res[i][j], x = q4[j]; o[j] = pk_bf16(bflo(r) + bflo(x), bfhi(r) + bfhi(x)); }
    __builtin_amdgcn_raw_buffer_store_b128(o, rO, orow + i * 64, 0, KN_OAUX);
  }
  if (bid == 0 && wave == 0) flag_set(a.sync, EPOCH, tg, 0);
  STAMP(7);
  STAMP(8);
  STAMP(3);
#endif
}

struct Ptrs { const bf16_t *x, *wqkv, *wo, *wg, *wu, *wd; bf16_t* out; char* ws; };
#define DBG(i)
#define WS_O ((bf16_t*)(p.ws + (8u << 20)))
#define WS_SL ((float*)(p.ws + (42u << 20)))
#define WS_H ((bf16_t*)(p.ws + (16u << 20)))
#define WS_ST ((float*)(p.ws + (24u << 20)))
#define WS_CS ((float*)(p.ws + (25u << 20)))
#define WS_SY ((unsigned*)(p.ws + (26u << 20)))
#define WS_A ((bf16_t*)(p.ws + (28u << 20)))
#if defined(__HIP_DEVICE_COMPILE__)
#define ARG_WARM(v) do { asm volatile("s_load_dword %0, %1, 0x0" : "=s"(v) : "s"(__builtin_amdgcn_kernarg_segment_ptr())); __builtin_amdgcn_sched_barrier(0); } while (0)
#else
#define ARG_WARM(v)
#endif
struct Args4 { GemmArgs q, b, c, d; };
extern "C" __global__ __launch_bounds__(512, 1) void k_all(Ptrs p) {
  unsigned warm = 0;
  ARG_WARM(warm);
#define PAD(k)
  const Args4 a{GemmArgs{p.x, p.wqkv, nullptr, nullptr, WS_O, (float*)WS_H, p.wg, p.wu, WS_CS, p.wd, p.wo, WS_SY, (float*)WS_A, warm DBG(0)},
    GemmArgs{WS_O, p.wo, nullptr, p.x, WS_H, WS_ST, nullptr, nullptr, nullptr, p.wd, nullptr, WS_SY, nullptr, 0u DBG(1)},
    GemmArgs{WS_H, p.wg, p.wu, p.out, WS_A, WS_ST, nullptr, nullptr, WS_CS, p.wd, nullptr, WS_SY, WS_SL, 0u DBG(2)},
    GemmArgs{WS_A, p.wd, nullptr, WS_H, p.out, nullptr, nullptr, nullptr, nullptr, nullptr, nullptr, WS_SY, WS_SL, 0u DBG(3)}};
  k1_phase(a.q, [&](auto md, auto lo, auto hi) { wo_phase<decltype(md)::value, decltype(lo)::value, decltype(hi)::value>(a.b, a.c); });
  PAD(1);
  wo_phase<2, 0, 0>(a.b, a.c);
  PAD(2);
  Late late;
  glu_phase(a.c, [&](auto tc, unsigned w) { dn_phase<1, decltype(tc)::value>(a.d, late, w); }, late);
  PAD(3);
  dn_phase<2, 0>(a.d, late);
  PAD(4);
}

extern "C" __global__ void k_zend() {}

extern "C" void kernel_launch(void* const* d_in, const int* in_sizes, int n_in, void* d_out, int out_size, void* d_ws,
                              size_t ws_size, hipStream_t stream) {
  const Ptrs p{(const bf16_t*)d_in[0], (const bf16_t*)d_in[1], (const bf16_t*)d_in[2], (const bf16_t*)d_in[4], (const bf16_t*)d_in[5], (const bf16_t*)d_in[6], (bf16_t*)d_out, (char*)d_ws};
  constexpr int L_all = SNAP + 64;
  static_assert(SNAP >= 3 * 256 * 128 + 4 * 14592 + 4096 + 1024 + 256 + 512 + 128 + 64 && L_all <= 163840, "LDS");
  (void)hipFuncSetAttribute((const void*)k_all, hipFuncAttributeMaxDynamicSharedMemorySize, L_all);
  k_all<<<256, 512, L_all, stream>>>(p);
}
```

```cpp
#include <hip/hip_runtime.h>
#include <hip/hip_bf16.h>
#include <stdint.h>
#include <type_traits>

typedef unsigned short bf16_t;
typedef short bf16x8 __attribute__((ext_vector_type(8)));
typedef short s16x4 __attribute__((ext_vector_type(4)));
typedef float f32x4 __attribute__((ext_vector_type(4)));
typedef float f32x2 __attribute__((ext_vector_type(2)));
typedef unsigned u32x2 __attribute__((ext_vector_type(2)));
typedef unsigned u32x4 __attribute__((ext_vector_type(4)));
typedef __bf16 bf16x2_t __attribute__((ext_vector_type(2)));
typedef __attribute__((address_space(3))) void* lds_void_ptr;
typedef __attribute__((address_space(3))) char* lds_char_ptr;

#define KN_NT 0
constexpr int S_ = 2048, D_ = 1024, DF_ = 3584;
constexpr int RBO = D_ / 8, RBH = D_ / 8, RBA = DF_ / 8;

__device__ __forceinline__ float bflo(unsigned v) { return __uint_as_float(v << 16); }
__device__ __forceinline__ float bfhi(unsigned v) { return __uint_as_float(v & 0xffff0000u); }
__device__ __forceinline__ unsigned pk_bf16(float lo, float hi) {
  f32x2 v = {lo, hi};
  bf16x2_t b = __builtin_convertvector(v, bf16x2_t);
  return __builtin_bit_cast(unsigned, b);
}
__device__ __forceinline__ int wswz(int row) { return (((row >> 1) & 1) | (((row >> 3) & 1) << 1)) << 1; }

template <int OFF> __device__ __forceinline__ s16x4 ds_tr(unsigned addr) {
  s16x4 r; asm volatile("ds_read_b64_tr_b16 %0, %1 offset:%2" : "=v"(r) : "v"(addr), "n"(OFF)); return r;
}
template <int OFF> __device__ __forceinline__ bf16x8 ds_b128(unsigned addr) {
  bf16x8 r; asm volatile("ds_read_b128 %0, %1 offset:%2" : "=v"(r) : "v"(addr), "n"(OFF)); return r;
}
template <int OFF> __device__ __forceinline__ f32x2 ds_b64(unsigned addr) {
  f32x2 r; asm volatile("ds_read_b64 %0, %1 offset:%2" : "=v"(r) : "v"(addr), "n"(OFF)); return r;
}
__device__ __forceinline__ float sum8(float v) {
  v += __int_as_float(__builtin_amdgcn_update_dpp(0, __float_as_int(v), 0x128, 0xf, 0xf, false));
  auto a = __builtin_amdgcn_permlane16_swap(__float_as_uint(v), __float_as_uint(v), false, false);
  v = __uint_as_float(a[0]) + __uint_as_float(a[1]);
  auto b = __builtin_amdgcn_permlane32_swap(__float_as_uint(v), __float_as_uint(v), false, false);
  return __uint_as_float(b[0]) + __uint_as_float(b[1]);
}
__device__ __forceinline__ float sum8l(float v) {
  v += __int_as_float(__builtin_amdgcn_update_dpp(0, __float_as_int(v), 0xb1, 0xf, 0xf, false));
  v += __int_as_float(__builtin_amdgcn_update_dpp(0, __float_as_int(v), 0x4e, 0xf, 0xf, false));
  return v + __int_as_float(__builtin_amdgcn_update_dpp(0, __float_as_int(v), 0x141, 0xf, 0xf, false));
}
template <int OFF> __device__ __forceinline__ void ds_w128(unsigned addr, u32x4 v) { asm volatile("ds_write_b128 %0, %1 offset:%2" :: "v"(addr), "v"(v), "n"(OFF) : "memory"); }
__device__ __forceinline__ int ds_r32(unsigned addr) { int r; asm volatile("ds_read_b32 %0, %1" : "=v"(r) : "v"(addr)); return r; }
__device__ __forceinline__ void ds_w32(unsigned addr, int v) { asm volatile("ds_write_b32 %0, %1" :: "v"(addr), "v"(v) : "memory"); }
template <int N> __device__ __forceinline__ void wait_vm() { asm volatile("s_waitcnt vmcnt(%0)" ::"n"(N) : "memory"); }
__device__ __forceinline__ void raw_barrier() { asm volatile("" ::: "memory"); __builtin_amdgcn_s_barrier(); asm volatile("" ::: "memory"); }
__device__ __forceinline__ void lds_barrier() { asm volatile("s_waitcnt lgkmcnt(0)" ::: "memory"); __builtin_amdgcn_s_barrier(); asm volatile("" ::: "memory"); }

template <int I, int N, class F> __device__ __forceinline__ void static_for(F&& f) {
  if constexpr (I < N) { f(std::integral_constant<int, I>{}); static_for<I + 1, N>(f); }
}
template <int V> using IC = std::integral_constant<int, V>;
constexpr int first_at(int g, int r0, int num, int den, int n) { int i = 0; while (i < n && r0 + i * num / den < g) ++i; return i; }
#define RSRC(p) __builtin_amdgcn_make_buffer_rsrc((void*)(p), 0, 0x7fffffff, 0x00020000)
#define RSRCN(p, n) __builtin_amdgcn_make_buffer_rsrc((void*)(p), 0, (n), 0x00020000)
#define LDSP(a) ((lds_void_ptr)(size_t)(a))
#define MFMA(a, b, c) __builtin_amdgcn_mfma_f32_16x16x32_bf16(a, b, c, 0, 0, 0)
#define CODE_ADDR(v, sym) asm volatile("s_getpc_b64 s[40:41]\n\ts_add_u32 s40, s40, " sym "@rel32@lo+4\n\ts_addc_u32 s41, s41, " sym "@rel32@hi+12\n\ts_mov_b64 %0, s[40:41]" : "=s"(v) :: "s40", "s41", "scc")
#define MERGED 1
constexpr int SNAP = 162752;
constexpr int STAB1 = 786432;
constexpr int EPOCH = 0;
constexpr int F_MARK = 4096, F_H = 4096 + 2048, F_ROW = 8192, F_COL = 16384;
#define F_O(t) (32768 + ((t) & 7) * 1024 + (((((t) >> 3) ^ 1) & 1) << 13))
#define KN_RM 3
#define WO_ROT (((xcd & 3) * 4) ^ (idx & KN_RM))
#define KN_G2 2
#define KN_EV 2
#define KN_WA 6
#define KN_WE 10

constexpr int F_ACT = 65536;
constexpr int F_PART = 131072;
constexpr int KN_TRY = 6;
struct Tag { unsigned lo, hi; };
__device__ __forceinline__ Tag tag_get(unsigned lds0) {
  f32x2 v = ds_b64<0>(lds0 + SNAP);
  asm volatile("s_waitcnt lgkmcnt(0)" : "+v"(v) :: "memory");
  return Tag{(unsigned)__builtin_amdgcn_readfirstlane(__float_as_int(v[0])) + 8u, (unsigned)__builtin_amdgcn_readfirstlane(__float_as_int(v[1]))};
}
__device__ __forceinline__ bool in_time(unsigned lds0) {
  int v = ds_r32(lds0 + SNAP + 28);
  asm volatile("s_waitcnt lgkmcnt(0)" : "+v"(v) :: "memory");
  return (unsigned)__builtin_amdgcn_s_memrealtime() - (unsigned)__builtin_amdgcn_readfirstlane(v) < 20000000u;
}
__device__ __forceinline__ int lane_id() { int l; asm volatile("v_mbcnt_lo_u32_b32 %0, -1, 0\n\tv_mbcnt_hi_u32_b32 %0, -1, %0" : "=v"(l)); return l; }
#if defined(__HIP_DEVICE_COMPILE__)
__device__ __forceinline__ void flag_set(const unsigned* sync, int off, Tag t, unsigned state) {
  __builtin_amdgcn_raw_buffer_store_b64((u32x2){t.lo + state, t.hi}, RSRCN((const char*)sync + off, 8), lane_id() * 8u, 0, 16);
}
__device__ __forceinline__ u32x2 flag_get(const unsigned* sync, int off, int n) { return __builtin_amdgcn_raw_buffer_load_b64(RSRCN((const char*)sync + off, n * 8), lane_id() * 8u, 0, 16); }
__device__ __forceinline__ bool flags_up(u32x2 v, int n, Tag t, unsigned state) { return (__builtin_amdgcn_ballot_w64(v[0] - t.lo - state > 7u - state || v[1] != t.hi) & (~0ull >> (64 - n))) == 0; }
__device__ __forceinline__ bool words_up(unsigned v, Tag t) { return ((__builtin_amdgcn_ballot_w64(v != t.lo) & 0x5555555555555555ull) | (__builtin_amdgcn_ballot_w64(v != t.hi) & 0xaaaaaaaaaaaaaaaaull)) == 0; }
__device__ __forceinline__ void wait_flags(unsigned lds0, const unsigned* sync, int off, int n, unsigned state) {
  const Tag t = tag_get(lds0);
#define KN_WS2 8
  while (!flags_up(flag_get(sync, off, n), n, t, state) && in_time(lds0)) __builtin_amdgcn_s_sleep(KN_WS2);
}
#endif
__device__ __forceinline__ void lds_wait8(unsigned addr, int n = 8) {
  int dv; unsigned polls = 0;
  do { dv = ds_r32(addr); asm volatile("s_waitcnt lgkmcnt(0)" : "+v"(dv) :: "memory"); } while (__builtin_amdgcn_readfirstlane(dv) < n && ++polls < 40000000u);
}
template <int N> __device__ __forceinline__ bool marked(unsigned addr) {
  bf16x8 v[N];
  static_for<0, N>([&](auto ic) { constexpr int i = decltype(ic)::value; v[i] = ds_b128<i * 4096>(addr); });
  asm volatile("s_waitcnt lgkmcnt(0)" ::: "memory");
  unsigned m = 0;
#pragma unroll
  for (int i = 0; i < N; ++i) {
    asm volatile("" : "+v"(v[i]));
    const u32x4 u = __builtin_bit_cast(u32x4, v[i]);
    m = max(max(m, u[0]), u[1]); m = max(max(m, u[2]), u[3]);
  }
  return __builtin_amdgcn_ballot_w64(m == 0xffffffffu) != 0;
}

struct GemmArgs {
  const bf16_t* X;
  const bf16_t* W0;
  const bf16_t* W1;
  const bf16_t* R;
  bf16_t* O;
  float* st;
  const bf16_t* CW0;
  const bf16_t* CW1;
  float* cs;
  const bf16_t* PF0;
  const bf16_t* PF1;
  unsigned* sync;
  float* slab;
  unsigned warm;
};
#define STAMP(i)
#define STAMPW(i, w)
#define TS6(i)
#define TS4(i)
template <int MF, int NF> struct Frags { bf16x8 x[MF]; s16x4 lo[NF], hi[NF]; };

__device__ __forceinline__ unsigned dn_wsrc(int lane, int wave) { return (unsigned)((lane >> 3) * (D_ * 2) + (((lane & 7) ^ wswz((wave & 3) * 8 + (lane >> 3))) << 4)); }
#define KN_QP 2
constexpr int FLP = 155648;
constexpr int OWNE = 0, OWNM = 98304;
#define KN_PA 0x0123
#define KN_PB 0x0123
constexpr int wo_pos(int s) { return s < 3 ? 18432 + s * 16384 : (s == 3 ? 129024 : (s == 4 ? 0 : 71424 + (s - 5) * 16384)); }
template <class HK>
__device__ __forceinline__ void k1_phase(const GemmArgs& a, HK hook) {
#if defined(__HIP_DEVICE_COMPILE__)
  constexpr int K = D_, LDW = 3 * D_, WN = 4;
  constexpr int HR = 16, STRIDE = 128;
  constexpr int XT = 144 * 128, WU = 24576;
  constexpr int XBASE = 0, WBASE = 3 * XT, RING = WBASE + 4 * WU;
  constexpr int MF = 4, NF = 3, MFX = 5;
  constexpr int NT = 16, NXI = 18, NLW = 4, XI = 5, XIMIN = 4, WI = 6;
  extern __shared__ __attribute__((aligned(16))) char smem[];
  STAMP(0);
  const unsigned lds0 = (unsigned)(size_t)(lds_char_ptr)smem;
  const int tid = threadIdx.x, lane = tid & 63;
  const int wave = __builtin_amdgcn_readfirstlane(tid >> 6);
  const int wm = wave / WN, wn = wave % WN;
  const int bid = blockIdx.x, xcd = bid & 7, idx = bid >> 3;
  const int tm = (xcd / 4) * 8 + idx / 4, tn = (xcd % 4) * 4 + idx % 4;
  const int m0 = tm * 128;
  const int fr = lane & 15, fq = lane >> 4;
  const int wq = (lane >> 2) & 3, wp = lane & 3;
  constexpr int TCH = RING + 2304;
  auto touch = [&](const void* base, unsigned voff, unsigned soff, unsigned lim = 0x7fffffffu) {
    __builtin_amdgcn_raw_ptr_buffer_load_lds(RSRCN(base, lim), LDSP(lds0 + TCH), 4, voff, soff, 0, 0);
  };
  const auto rO = RSRC(a.O);

  unsigned xsrc[XI], wsrc[WI];
  {
    const int w4 = wave & 3, lr = lane >> 3;
    const unsigned xv = (unsigned)(lr * (K * 2) + (((lane & 7) ^ ((w4 * 4 + (lr >> 1)) & 7)) << 4));
    const int r0 = m0 - HR + w4 * 8;
#pragma unroll
    for (int i = 0; i < XI; ++i) xsrc[i] = xv + (unsigned)((i == 0 ? (r0 < 0 ? 0 : r0) : r0 + 32 * (i < 4 ? i : 4)) * (K * 2));
    const unsigned wv = (unsigned)((w4 * 8 + lr) * (LDW * 2) + tn * 128 + (((lane & 7) ^ wswz(w4 * 8 + lr)) << 4));
#pragma unroll
    for (int i = 0; i < WI; ++i) wsrc[i] = wv + (unsigned)((i & 1) * 32 * (LDW * 2) + (i >> 1) * 2048);
  }
  auto ldX = [&](int i, int tile, int slot) {
    const int j = wave + NLW * i;
    if (j >= NXI) return;
    __builtin_amdgcn_raw_ptr_buffer_load_lds(RSRC(a.X), LDSP(lds0 + XBASE + slot * XT + j * 1024), 16, xsrc[i], tile * 128, 0, (KN_NT & 1) ? 2 : 0);
  };
  auto ldW = [&](int i, int unit, int slot) {
    const int j = wave + NLW * i;
    __builtin_amdgcn_raw_ptr_buffer_load_lds(RSRC(a.W0), LDSP(lds0 + WBASE + slot * WU + j * 1024), 16, wsrc[i], unit * 64 * LDW * 2, 0, (KN_NT & 2) ? 2 : 0);
  };
  auto issueX = [&](int tile, int slot) {
    if (wave >= NLW) return;
#pragma unroll
    for (int i = 0; i < XI; ++i) ldX(i, tile, slot);
  };
  auto issueW = [&](int unit, int slot) {
    if (wave >= NLW) return;
#pragma unroll
    for (int i = 0; i < WI; ++i) ldW(i, unit, slot);
  };

  f32x4 acc[NF][MF];
#pragma unroll
  for (int n = 0; n < NF; ++n)
#pragma unroll
    for (int m = 0; m < MF; ++m) acc[n][m] = (f32x4){0.f, 0.f, 0.f, 0.f};
  f32x4 acch[NF];
#pragma unroll
  for (int n = 0; n < NF; ++n) acch[n] = (f32x4){0.f, 0.f, 0.f, 0.f};
  constexpr int ONES = RING;

  unsigned xa0, xa1, xh0, xh1;
  { const unsigned o = fr * 128 + ((fq ^ ((fr >> 1) & 7)) << 4), mo = (HR + wm * MF * 16) * 128;
    xa0 = lds0 + XBASE + mo + o; xa1 = lds0 + XBASE + mo + (o ^ 64); xh0 = lds0 + XBASE + o; xh1 = lds0 + XBASE + (o ^ 64); }
  unsigned wa;
  { const int row = 8 * fq + wq; wa = lds0 + WBASE + row * STRIDE + (((wn << 1) ^ wswz(row)) << 4) + wp * 8; }

  using I0 = IC<0>; using I1 = IC<1>;
  using T = std::true_type; using F = std::false_type;
  typedef Frags<MFX, NF> FR;
  struct RdCtx { unsigned xa, xh, wb; };
  auto mkctx = [&](bool ks1, unsigned xoffs, unsigned woffs) {
    RdCtx c; c.xa = (ks1 ? xa1 : xa0) + xoffs; c.xh = fr == 0 ? lds0 + ONES : (ks1 ? xh1 : xh0) + xoffs; c.wb = wa + woffs;
    return c;
  };
  auto rd_item = [&](FR& f, const RdCtx& c, auto KSc, auto kc) {
    constexpr int KS = decltype(KSc)::value, k = decltype(kc)::value;
    if constexpr (k < MF) f.x[k] = ds_b128<k * 2048>(c.xa);
    else if constexpr (k < MFX) f.x[k] = ds_b128<0>(c.xh);
    else {
      constexpr int n = k - MFX, o = n * 8192 + KS * 32 * STRIDE;
      f.lo[n] = ds_tr<o>(c.wb); f.hi[n] = ds_tr<o + 4 * STRIDE>(c.wb);
    }
  };
  auto wait_frags = [&](FR& f, auto Hc) {
    asm volatile("s_waitcnt lgkmcnt(0)" ::: "memory");
#pragma unroll
    for (int m = 0; m < MF + decltype(Hc)::value; ++m) asm volatile("" : "+v"(f.x[m]));
#pragma unroll
    for (int n = 0; n < NF; ++n) asm volatile("" : "+v"(f.lo[n]), "+v"(f.hi[n]));
  };
  auto step1 = [&](auto Hc, FR& cur, FR& nxt, const RdCtx& c, auto KSc, auto R0c, auto RLc, auto IBc, auto&& sync, auto NLc, auto&& ld) {
    constexpr int H = decltype(Hc)::value, NL = decltype(NLc)::value, R0 = decltype(R0c)::value;
    constexpr int MH = MF + H, NM = NF * MH, NR = decltype(RLc)::value == -100 ? 0 : MH + 2 * NF;
    constexpr int IB = decltype(IBc)::value < 0 && decltype(IBc)::value > -100 ? NM + decltype(IBc)::value : decltype(IBc)::value;
    constexpr int RL = NM + decltype(RLc)::value;
    static_for<0, NM>([&](auto ic) {
      constexpr int I = decltype(ic)::value, g = I / MH, q = I % MH;
      const bf16x8 w = __builtin_shufflevector(cur.lo[g], cur.hi[g], 0, 1, 2, 3, 4, 5, 6, 7);
      if constexpr (q < MF) acc[g][q] = MFMA(w, cur.x[q], acc[g][q]);
      else acch[g] = MFMA(w, cur.x[MF], acch[g]);
      __builtin_amdgcn_sched_barrier(0);
      if constexpr (I == IB) sync();
      static_for<first_at(I, R0, RL - R0, NR - 1, NR), first_at(I + 1, R0, RL - R0, NR - 1, NR)>([&](auto rc) {
        constexpr int r = decltype(rc)::value;
        if constexpr (r < MH) rd_item(nxt, c, KSc, rc);
        else {
          constexpr int n = (r - MH) >> 1, o = n * 8192 + decltype(KSc)::value * 32 * STRIDE;
          if constexpr ((r - MH) & 1) nxt.hi[n] = ds_tr<o + 4 * STRIDE>(c.wb); else nxt.lo[n] = ds_tr<o>(c.wb);
        }
      });
#pragma unroll
      for (int i = first_at(I, R0, NM - R0, NL, NL); i < first_at(I + 1, R0, NM - R0, NL, NL); ++i) ld(i);
      __builtin_amdgcn_sched_barrier(0);
    });
    if constexpr (NR > 0) wait_frags(nxt, Hc);
  };
  FR fa, fb;
  using IM1 = IC<-1>;
  unsigned tv6 = 0;

  asm volatile("s_waitcnt lgkmcnt(0)" :: "s"(a.warm), "v"(wsrc[WI - 1]), "v"(xsrc[XI - 1]));
#define KN_T1 1
#define KN_DEF 5
  issueW(0, 0); issueX(0, 0);
  if constexpr (!KN_T1) { issueW(1, 1); issueX(1, 1); }
  u32x4 sx[4];
  constexpr int TNEW = RING + 3072;
  auto ldT = [&](int dst) {
    int lx = lane; asm volatile("" : "+v"(lx));
    const auto rT = RSRCN((const char*)a.cs + STAB1, 2048 * 16);
    const unsigned to = (unsigned)((m0 - 16) * 16 + lx * 16);
    __builtin_amdgcn_raw_ptr_buffer_load_lds(rT, LDSP(lds0 + dst), 16, to, 0, 0, 16);
    __builtin_amdgcn_raw_ptr_buffer_load_lds(rT, LDSP(lds0 + dst + 1024), 16, to + 1024u, 0, 0, 16);
    if (lx < 16) __builtin_amdgcn_raw_ptr_buffer_load_lds(rT, LDSP(lds0 + dst + 2048), 16, to + 2048u, 0, 0, 16);
  };
  auto ldS = [&]() {
    int lx = lane; asm volatile("" : "+v"(lx));
    const unsigned so = (unsigned)((m0 + tn * 8 + (wave & 3) * 2 + (lx >> 5)) * (K * 2) + (lx & 31) * 16);
#pragma unroll
    for (int i = 0; i < 4; ++i) sx[i] = __builtin_amdgcn_raw_buffer_load_b128(RSRC(a.X), so, i * 512, 0);
  };
  float mu = 0.f, rsd = 0.f;
  auto stats = [&]() {
    float sm = 0.f, sq = 0.f;
    const bf16x2_t one2 = __builtin_bit_cast(bf16x2_t, 0x3f803f80u);
#pragma unroll
    for (int i = 0; i < 4; ++i)
#pragma unroll
      for (int e = 0; e < 4; ++e) { const unsigned u = sx[i][e]; const bf16x2_t v = __builtin_bit_cast(bf16x2_t, u); sm = __builtin_amdgcn_fdot2_f32_bf16(v, one2, sm, false); sq = __builtin_amdgcn_fdot2_f32_bf16(v, v, sq, false); }
    sm = sum8l(sm); sq = sum8l(sq);
    sm += __int_as_float(__builtin_amdgcn_update_dpp(0, __float_as_int(sm), 0x128, 0xf, 0xf, false));
    sq += __int_as_float(__builtin_amdgcn_update_dpp(0, __float_as_int(sq), 0x128, 0xf, 0xf, false));
    { auto p = __builtin_amdgcn_permlane16_swap(__float_as_uint(sm), __float_as_uint(sm), false, false); sm = __uint_as_float(p[0]) + __uint_as_float(p[1]); }
    { auto p = __builtin_amdgcn_permlane16_swap(__float_as_uint(sq), __float_as_uint(sq), false, false); sq = __uint_as_float(p[0]) + __uint_as_float(p[1]); }
    mu = sm * (1.f / 1024.f);
    float var = sq * (1.f / 1024.f) - mu * mu; var = var > 0.f ? var : 0.f;
    rsd = __builtin_amdgcn_rsqf(var + 1e-5f);
  };
#define KN_SL 1
  __builtin_amdgcn_sched_barrier(0);
  if (KN_SL == -1 && wave >= NLW) { ldS(); stats(); asm volatile("" : "+v"(mu), "+v"(rsd)); }
  if (wave == 4) {
    if (lane < 8) *(unsigned*)(smem + ONES + lane * 4) = lane < 4 ? 0x3f803f80u : 0u;
  }
  auto marks = [&]() {
#define KN_PT 1
    if constexpr (KN_PT) touch(a.cs, lane < 8 ? (unsigned)((const char*)a.sync - (const char*)a.cs + bid * 1024 + lane * 128) : (lane == 8 ? (unsigned)(STAB1 + (m0 + tn * 8) * 16) : (lane < 23 ? (unsigned)(bid * 1792 + (lane - 9) * 128) : 0x80000000u)), 0);
    const int p2 = (xcd >> 1) * 2 + (idx >> 4), mb = (xcd & 1) * 16 + (idx & 15);
#pragma unroll
    for (int i = 0; i < 3; ++i)
      __builtin_amdgcn_raw_buffer_store_b128((u32x4){~0u, ~0u, ~0u, ~0u}, RSRC(a.st), lane * 16 + i * 1024, ((p2 * 16 + (mb >> 1)) * RBH + (xcd >> 2) * 64 + (mb & 1) * 12) * 256, 16);
  };
  if (wave == 5) {
    if (lane < 2) __builtin_amdgcn_raw_ptr_buffer_load_lds(RSRC(a.sync), LDSP(lds0 + SNAP), 4, lane * 4, EPOCH, 0, 16);
    ds_w32(lds0 + SNAP + 28, (int)__builtin_amdgcn_s_memrealtime());
    if (!(KN_DEF & 1)) marks();
  }
  constexpr int HQ = 5;
  const int slc = (3 - ((unsigned)(size_t)a.W0 >> 7) - (xcd & 3) * 4) & 7;
  const int hid = (idx >> 2) * 3 + (idx & 3) - ((idx & 3) > slc ? 1 : 0);
  const bool hlp = slc < 4 && (idx & 3) != slc;
  const int hj1 = hlp ? hid / 3 - HQ : -100, hj2 = hlp ? hid / 3 + 8 - HQ : -100;
  const unsigned htv = (unsigned)(lane * (LDW * 2) + (hid % 3) * 2048 + ((xcd & 3) * 4 + slc) * 128);
  if (!(KN_DEF & 2) && wave == 6 && hlp && hj1 <= 0) touch(a.W0, htv, (hid / 3) * (64 * LDW * 2));
  auto code_touch = [&]() {
    unsigned long long c0, c1; CODE_ADDR(c0, "k_all"); CODE_ADDR(c1, "k_zend");
    const unsigned len = c1 - c0 < 262144ull ? (unsigned)(c1 - c0) : 0u;
    touch((const void*)c0, (unsigned)((lane * 32 + idx) * 128), 0, len);
  };
#define KN_MP 4
#define KN_MF 3
  const int jp = (bid >> 4) < KN_MF ? 10 + (bid >> 4) : (bid >> 4) - KN_MP;
  auto batch = [&]() {
    int lx = lane; asm volatile("" : "+v"(lx));
#define KN_MX 0
    if (KN_MX && (bid >> 4) >= KN_MF) { touch(a.X, lx * 2048, (bid & 15) * 262144 + (bid >> 4) * 128); touch(a.X, lx * 2048, (bid & 15) * 262144 + 131072 + (bid >> 4) * 128); }
    if ((bid >> 4) >= KN_MF) { touch(a.W0, lx * 128, bid * 24576); touch(a.W0, lx * 128, bid * 24576 + 8192); touch(a.W0, lx * 128, bid * 24576 + 16384); }
    touch(a.PF1, lx * 128, bid * 8192);
  };
  if (!(KN_DEF & 4) && wave == 4 && jp <= 0) batch();
  if (wave == 7) code_touch();
  const int srow = m0 + wave * 16 + fr;
  const bool par = fr & 1;
  float bias[4];
  {
    const unsigned mk = 0x5555u & ((2u << (srow < 15 ? srow : 15)) - 1u), sft = (fr & 14) - fq * 4 + 14;
    const unsigned b0 = ~(mk >> sft), b1 = ~((mk << 16) >> sft);
#pragma unroll
    for (int j = 0; j < 4; ++j) bias[j] = __uint_as_float((unsigned)__builtin_amdgcn_sbfe((int)(j < 2 ? b0 : b1), 2 - 2 * (j & 1), 1) & 0xff61b1e6u);
  }
  unsigned pl = par ? 0x01000c0cu : 0x0c0c0100u, ph = par ? 0x03020c0cu : 0x0c0c0302u;
  asm volatile("" : "+v"(bias[0]), "+v"(bias[1]), "+v"(bias[2]), "+v"(bias[3]), "+v"(pl), "+v"(ph));
  constexpr int RS = 400, QT = WBASE + 3 * WU - 144 * RS;
  static_assert(wo_pos(0) >= XT && wo_pos(1) <= 2 * XT && wo_pos(2) + 16384 <= QT && wo_pos(3) >= QT + 144 * RS && KN_WA <= 6 && KN_WE <= 10 && KN_WA <= KN_WE, "LDS");
  int qw = (16 + wm * 64 + fr) * RS + wn * 32 + fq * 8;
  unsigned ka = lds0 + QT + (wave * 16 + fr) * RS + fq * 16, va = lds0 + QT + (wave * 16 + fq * 4 + wq) * RS + 256 + wp * 16;
  asm volatile("" : "+v"(qw), "+v"(ka), "+v"(va));
#define KN_CI 2
#define KN_S1 2
  if constexpr (KN_T1) {
    asm volatile("" : "+v"(xa0), "+v"(xa1), "+v"(xh0), "+v"(xh1), "+v"(wa));
    __builtin_amdgcn_sched_barrier(0);
    issueW(1, 1); issueX(1, 1);
  }
  if (wave < NLW) wait_vm<WI + XIMIN>();
  else if (KN_SL == 0) { ldS(); stats(); asm volatile("" : "+v"(mu), "+v"(rsd)); }
  lds_barrier();
  if ((KN_DEF & 1) && wave == 5) marks();
  if ((KN_DEF & 2) && wave == 6 && hlp && hj1 <= 0) touch(a.W0, htv, (hid / 3) * (64 * LDW * 2));
  if ((KN_DEF & 4) && wave == 4 && jp <= 0) batch();
  { const RdCtx c = mkctx(false, 0, 0); static_for<0, MFX + NF>([&](auto kc) { rd_item(fa, c, I0{}, kc); }); }
  wait_frags(fa, I1{});
  STAMP(1);
  f32x4 t4[MFX], csn[NF];
  int up = 0;
  const unsigned csa = lds0 + RING + 64 + wn * 192 + fq * 16, cfa = lds0 + ONES + 16 + wn * 4;
  auto ldC = [&]() { up = ds_r32(cfa); csn[0] = __builtin_bit_cast(f32x4, ds_b128<0>(csa)); csn[1] = __builtin_bit_cast(f32x4, ds_b128<64>(csa)); csn[2] = __builtin_bit_cast(f32x4, ds_b128<128>(csa)); };
  auto pinC = [&]() { asm volatile("s_waitcnt lgkmcnt(0)" : "+v"(up), "+v"(csn[0]), "+v"(csn[1]), "+v"(csn[2]) :: "memory"); };
  auto pinT = [&](bool h) { asm volatile("s_waitcnt lgkmcnt(0)" : "+v"(t4[0]), "+v"(t4[1]), "+v"(t4[2]), "+v"(t4[3]) :: "memory"); if (h) asm volatile("" : "+v"(t4[4])); };
  auto run = [&](auto ROLEc) __attribute__((always_inline)) {
    constexpr int ROLE = decltype(ROLEc)::value;
    constexpr bool LDR = ROLE < NLW;
    using HL = IC<LDR ? 1 : 0>;
    auto nos = [&]() {};
    TS6(0);
    if constexpr (!LDR && KN_SL == 1) { ldS(); __builtin_amdgcn_sched_barrier(0); }
    { const RdCtx c = mkctx(true, 0, 0); step1(HL{}, fa, fb, c, I1{}, I0{}, IC<-4>{}, IC<-100>{}, nos, IC<LDR ? WI + XI : 0>{}, [&](int i) { if (i < WI) ldW(i, 2, 2); else ldX(i - WI, 2, 2); }); }
#define KN_WLD 2
    if constexpr (!LDR && KN_SL == 2) ldS();
    int slx = 1;
    if constexpr (ROLE == 5) wait_vm<(KN_SL > 0 ? 7 : 3) + KN_PT>();
    auto iter = [&](int j, auto LXc, auto LWc, auto WAITc, auto TIc) {
      constexpr bool LX = decltype(LXc)::value && LDR, LW = decltype(LWc)::value && LDR;
      constexpr int WAIT = decltype(WAITc)::value, TI = decltype(TIc)::value;
      TS6(j);
      constexpr int EX = (LDR && TI < 0 && KN_WLD == 3) ? WI : 0;
      constexpr int TO = 2, OL = TI <= TO ? 0 : 2, OH = (LDR && TI >= TO) ? 2 : OL;
      constexpr int NLT = EX + (LX ? XI : 0) + (LW ? WI : 0) + 2 * (OH - OL), N0 = (NLT + 1) / 2, N1 = NLT - N0;
      if constexpr (LDR) wait_vm<WAIT + (TI > 0 ? 2 * OL : 0)>();
      unsigned osrc = 0;
      if constexpr (OH > OL) { int lx = lane; asm volatile("" : "+v"(lx)); const int row = ROLE * 8 + (lx >> 3); osrc = (unsigned)(row * D_ + ((xcd & 1) * 8 + (idx & 7)) * 64 + ((lx & 7) ^ wswz(row)) * 8) * 2u; }
      if constexpr (ROLE == 6 && TI == 0) { if (j == hj1 || j == hj2) touch(a.W0, htv, (j + HQ) * (64 * LDW * 2)); }
      constexpr int S3 = 11;
      if constexpr (!LDR && TI == 0) {
        if (j == KN_S1) {
          if (KN_EV & 8) __builtin_amdgcn_s_setprio(3);
          if constexpr (KN_SL > 0) stats();
          int lx = lane; asm volatile("" : "+v"(lx));
          const Tag tg = tag_get(lds0);
          if constexpr (ROLE == 5) { wait_vm<0>(); flag_set(a.sync, F_MARK + ((xcd >> 1) * 2 + (idx >> 4)) * 256 + ((xcd & 1) * 16 + (idx & 15)) * 8, tg, 0); }
          __builtin_amdgcn_raw_buffer_store_b128((u32x4){__float_as_uint(mu), tg.lo, __float_as_uint(rsd), tg.lo + tg.hi}, RSRC(a.cs),
                                                 (lx & 31) == 0 ? (unsigned)(STAB1 + (m0 + tn * 8 + (ROLE & 3) * 2 + (lx >> 5)) * 16) : 0x80000000u, 0, 16);
          __builtin_amdgcn_sched_barrier(0);
          asm volatile("s_nop 1" ::: "memory");
          if (KN_EV & 8) __builtin_amdgcn_s_setprio(0);
        }
      }
      if constexpr (ROLE == 7 && TI == 0) {
        if (j == S3) ldT(TNEW);
        if (j == S3 + 1) {
          if (KN_EV & 8) __builtin_amdgcn_s_setprio(3);
          for (;;) {
            wait_vm<0>();
            int lx = lane; asm volatile("" : "+v"(lx));
            const unsigned ad = lds0 + TNEW + lx * 16;
            u32x4 v[3];
            v[0] = __builtin_bit_cast(u32x4, ds_b128<0>(ad)); v[1] = __builtin_bit_cast(u32x4, ds_b128<1024>(ad)); v[2] = __builtin_bit_cast(u32x4, ds_b128<2048>(ad));
            const Tag tg = tag_get(lds0);
            asm volatile("" : "+v"(v[0]), "+v"(v[1]), "+v"(v[2]) :: "memory");
            auto good = [&](const u32x4& e) { return e[1] == tg.lo && e[3] == tg.lo + tg.hi; };
            const bool bad = (!good(v[0]) && (tm > 0 || lx >= 16)) || !good(v[1]) || (!good(v[2]) && lx < 16);
            if (__builtin_amdgcn_ballot_w64(bad) == 0 || !in_time(lds0)) break;
            ldT(TNEW);
          }
          if (KN_EV & 8) __builtin_amdgcn_s_setprio(0);
        }
      }
      if constexpr (ROLE == 7 && TI == 1) code_touch();
      if constexpr (ROLE == 4 && TI == 0 && !KN_MP) touch(a.PF1, (bid * 64 + lane) * 128, 0, j == 1 + idx % 12 ? 0x7fffffffu : 0u);
      if constexpr (ROLE == 4 && TI == 0 && KN_MP) { if (j == jp) batch(); }
      if constexpr (ROLE >= 4 && TI == 2) {
        const bf16_t* cw = ROLE < 6 ? a.CW0 : a.CW1;
        touch(cw, lane < 56 ? (unsigned)(((ROLE & 1) * 512 + bid) * (DF_ * 2) + lane * 128) : 0x80000000u, 0);
      }
      int xp = slx + 2; if (xp >= 3) xp -= 3;
      auto ld = [&](int i) { if (i < EX) { ldW(i, j + 2, (j + 2) & 3); return; } i -= EX;
                             if (LX && i < XI) ldX(i, j + 2, xp);
                             else if constexpr (LW) ldW(i - (LX ? XI : 0), j + KN_WLD, (j + KN_WLD) & 3);
                             else { const int q = i - (LX ? XI : 0), t = OL + (q >> 1), h = q & 1;
                               __builtin_amdgcn_raw_ptr_buffer_load_lds(RSRC(a.PF1), LDSP(lds0 + wo_pos(t >> 1) + (t & 1) * 8192 + (ROLE + 4 * h) * 1024), 16,
                                                                        osrc, ((((t & 1) * 8 + (t >> 1)) ^ WO_ROT) * 2 + h) * (32 * D_ * 2), 0, 0); } };
      { const RdCtx c = mkctx(false, slx * XT, (j & 3) * WU); step1(HL{}, fb, fa, c, I0{}, IC<LDR ? 1 : 0>{}, IC<-4>{}, IC<LDR ? 1 : -100>{}, [&]() { raw_barrier(); }, IC<N0>{}, ld); }
      { const RdCtx c = mkctx(true, slx * XT, (j & 3) * WU); step1(HL{}, fa, fb, c, I1{}, I0{}, IC<LDR ? -4 : -6>{},
          IC<LDR ? -100 : -3>{}, [&]() { if constexpr (TI != 3) { asm volatile("s_waitcnt lgkmcnt(0)" ::: "memory"); raw_barrier(); } }, IC<N1>{}, [&](int i) { ld(i + N0); }); }
      slx = slx + 1; if (slx >= 3) slx = 0;
    };
    using WS_ = IC<(KN_WLD - 1) * WI + XIMIN>;
    if constexpr (!LDR) raw_barrier();
    if constexpr (LDR) iter(1, T{}, T{}, IC<WI + XIMIN>{}, IM1{});
    for (int j = LDR ? 2 : 1; j <= NT - 4; ++j) iter(j, T{}, T{}, WS_{}, I0{});
    iter(NT - 3, T{}, std::integral_constant<bool, KN_WLD == 2>{}, WS_{}, I1{});
    iter(NT - 2, F{}, F{}, IC<WI + XIMIN>{}, IC<2>{});
    iter(NT - 1, F{}, F{}, I0{}, IC<3>{});
    TS6(16);
    { const RdCtx c = mkctx(false, 0, 0); const unsigned ta = lds0 + TNEW + fr * 16;
      step1(HL{}, fb, fa, c, I0{}, I0{}, IC<-100>{}, IC<-100>{}, nos, IC<MFX>{}, [&](int i) {
        if (i == 0) static_for<0, MF + (LDR ? 1 : 0)>([&](auto mc) { constexpr int m = decltype(mc)::value; t4[m] = __builtin_bit_cast(f32x4, ds_b128<(m < MF ? 16 + (ROLE / WN * MF + m) * 16 : 0) * 16>(ta)); });
        if constexpr (!LDR) { if (i == KN_CI) ldC(); } });
      pinT(LDR); if constexpr (!LDR) pinC(); }
    TS6(17);
  };
  switch (wave) {
    case 0: run(IC<0>{}); break; case 1: run(IC<1>{}); break;
    case 2: run(IC<2>{}); break; case 3: run(IC<3>{}); break;
    case 4: run(IC<4>{}); break; case 5: run(IC<5>{}); break;
    case 6: run(IC<6>{}); break; default: run(IC<7>{}); break;
  }
  STAMP(2);
  {
    if (wave < 4) {
      if (fr == 0) {
#pragma unroll
        for (int n = 0; n < NF; ++n) *(f32x4*)(smem + RING + 64 + wn * 192 + fq * 16 + n * 64) = acch[n];
      }
      ds_w32(cfa, 1);
#pragma unroll
      for (int n = 0; n < NF; ++n)
#pragma unroll
        for (int e = 0; e < 4; ++e) { const float h = acch[n][e]; csn[n][e] = __int_as_float(__builtin_amdgcn_update_dpp(0, __float_as_int(h), 0x150, 0xf, 0xf, false)); }
    } else {
      unsigned polls = 0;
      while (__builtin_expect(__builtin_amdgcn_readfirstlane(up) == 0, 0) && ++polls < 40000000u) {
        ldC(); pinC();
      }
    }
    STAMP(9);
    TS6(18);
    TS6(19);
    STAMP(6);
    auto nrm = [&](const f32x4& v, const f32x4& cs, const f32x4& t) {
      const float mu = t[0], rs = t[2];
      return (u32x2){pk_bf16(rs * (v[0] - mu * cs[0]), rs * (v[1] - mu * cs[1])), pk_bf16(rs * (v[2] - mu * cs[2]), rs * (v[3] - mu * cs[3]))}; };
    u32x2 ov2[MF][NF];
#pragma unroll
    for (int m = 0; m < MF; ++m) {
#pragma unroll
      for (int n = 0; n < NF; ++n) { ov2[m][n] = nrm(acc[n][m], csn[n], t4[m]); asm volatile("" : "+v"(ov2[m][n])); }
      __builtin_amdgcn_sched_barrier(0);
#pragma unroll
      for (int n = 0; n < NF; ++n) *(u32x2*)(smem + QT + qw + m * 16 * RS + n * 128) = ov2[m][n];
      __builtin_amdgcn_sched_barrier(0);
    }
    if (wave < 4) {
#pragma unroll
      for (int n = 1; n < NF; ++n) *(u32x2*)(smem + QT + qw - 16 * RS + n * 128) = nrm(acch[n], csn[n], t4[MF]);
      __builtin_amdgcn_sched_barrier(0);
      hook(IC<3>{}, IC<2>{}, IC<KN_WA>{});
    }
    STAMP(4);
    TS6(20);
    lds_barrier();
    TS6(21);
    STAMP(5);
    bf16x8 qf[2], kf[2][2]; s16x4 vf[2][2][2];
    qf[0] = ds_b128<16 * RS>(ka); kf[0][0] = ds_b128<128>(ka); kf[0][1] = ds_b128<16 * RS + 128>(ka);
    qf[1] = ds_b128<16 * RS + 64>(ka); kf[1][0] = ds_b128<192>(ka); kf[1][1] = ds_b128<16 * RS + 192>(ka);
    vf[0][0][0] = ds_tr<0>(va); vf[0][0][1] = ds_tr<16 * RS>(va); vf[0][1][0] = ds_tr<8>(va); vf[0][1][1] = ds_tr<16 * RS + 8>(va);
    vf[1][0][0] = ds_tr<64>(va); vf[1][0][1] = ds_tr<16 * RS + 64>(va); vf[1][1][0] = ds_tr<72>(va); vf[1][1][1] = ds_tr<16 * RS + 72>(va);
    hook(IC<1>{}, IC<KN_WA>{}, IC<KN_WE>{});
    asm volatile("s_waitcnt lgkmcnt(8)" ::: "memory");
#pragma unroll
    for (int hh = 0; hh < 2; ++hh) asm volatile("" : "+v"(qf[hh]), "+v"(kf[hh][0]), "+v"(kf[hh][1]));
    const float sc2 = 0.17677669529663687f * 1.4426950408889634f;
    unsigned pm = (unsigned)__builtin_amdgcn_sbfe((int)pl, 24, 1); asm volatile("" : "+v"(pm));
    auto vmax = [](float x, float y) { float r; asm("v_max_f32 %0, %1, %2" : "=v"(r) : "v"(x), "v"(y)); return r; };
    u32x4 ov[2];
    const f32x4 z = {0.f, 0.f, 0.f, 0.f};
    f32x4 sc[2][2]; float lg[2][4], mx[2], sum[2];
    auto x16 = [](float v) { auto p = __builtin_amdgcn_permlane16_swap(__float_as_uint(v), __float_as_uint(v), false, false); return (f32x2){__uint_as_float(p[0]), __uint_as_float(p[1])}; };
    auto x32 = [](float v) { auto p = __builtin_amdgcn_permlane32_swap(__float_as_uint(v), __float_as_uint(v), false, false); return (f32x2){__uint_as_float(p[0]), __uint_as_float(p[1])}; };
#pragma unroll
    for (int hh = 0; hh < 2; ++hh) {
      sc[hh][0] = MFMA(kf[hh][0], qf[hh], z);
      sc[hh][1] = MFMA(kf[hh][1], qf[hh], z);
    }
    __builtin_amdgcn_sched_barrier(0);
#pragma unroll
    for (int hh = 0; hh < 2; ++hh) {
#pragma unroll
      for (int j = 0; j < 4; ++j) { const float e0 = sc[hh][j >> 1][(j & 1) * 2], e1 = sc[hh][j >> 1][(j & 1) * 2 + 1];
        lg[hh][j] = __builtin_fmaf(__uint_as_float((__float_as_uint(e1) & pm) | (__float_as_uint(e0) & ~pm)), sc2, bias[j]); }
      mx[hh] = fmaxf(fmaxf(lg[hh][0], lg[hh][1]), fmaxf(lg[hh][2], lg[hh][3]));
    }
    __builtin_amdgcn_sched_barrier(0);
    { auto p = __builtin_amdgcn_permlane16_swap(__float_as_uint(mx[0]), __float_as_uint(mx[1]), false, false);
      const f32x2 q = x32(vmax(__uint_as_float(p[0]), __uint_as_float(p[1]))), r = x16(vmax(q[0], q[1])); mx[0] = r[0]; mx[1] = r[1]; }
    __builtin_amdgcn_sched_barrier(0);
#pragma unroll
    for (int hh = 0; hh < 2; ++hh) {
#pragma unroll
      for (int j = 0; j < 4; ++j) lg[hh][j] = __builtin_amdgcn_exp2f(lg[hh][j] - mx[hh]);
      sum[hh] = (lg[hh][0] + lg[hh][1]) + (lg[hh][2] + lg[hh][3]);
    }
    __builtin_amdgcn_sched_barrier(0);
    asm volatile("s_waitcnt lgkmcnt(0)" ::: "memory");
#pragma unroll
    for (int hh = 0; hh < 2; ++hh) asm volatile("" : "+v"(vf[hh][0][0]), "+v"(vf[hh][0][1]), "+v"(vf[hh][1][0]), "+v"(vf[hh][1][1]));
    { auto p = __builtin_amdgcn_permlane16_swap(__float_as_uint(sum[0]), __float_as_uint(sum[1]), false, false);
      const f32x2 q = x32(__uint_as_float(p[0]) + __uint_as_float(p[1])), r = x16(__builtin_amdgcn_rcpf(q[0] + q[1])); sum[0] = r[0]; sum[1] = r[1]; }
    __builtin_amdgcn_sched_barrier(0);
#pragma unroll
    for (int hh = 0; hh < 2; ++hh) {
      const float inv = sum[hh];
      const unsigned c0 = pk_bf16(lg[hh][0] * inv, lg[hh][1] * inv), c1 = pk_bf16(lg[hh][2] * inv, lg[hh][3] * inv);
      const u32x4 pp = {__builtin_amdgcn_perm(0u, c0, pl), __builtin_amdgcn_perm(0u, c0, ph), __builtin_amdgcn_perm(0u, c1, pl), __builtin_amdgcn_perm(0u, c1, ph)};
      const bf16x8 pf = __builtin_bit_cast(bf16x8, pp);
      sc[hh][0] = MFMA(__builtin_shufflevector(vf[hh][0][0], vf[hh][0][1], 0, 1, 2, 3, 4, 5, 6, 7), pf, z);
      sc[hh][1] = MFMA(__builtin_shufflevector(vf[hh][1][0], vf[hh][1][1], 0, 1, 2, 3, 4, 5, 6, 7), pf, z);
    }
    __builtin_amdgcn_sched_barrier(0);
#pragma unroll
    for (int hh = 0; hh < 2; ++hh) ov[hh] = (u32x4){pk_bf16(sc[hh][0][0], sc[hh][0][1]), pk_bf16(sc[hh][0][2], sc[hh][0][3]), pk_bf16(sc[hh][1][0], sc[hh][1][1]), pk_bf16(sc[hh][1][2], sc[hh][1][3])};
    STAMP(10);
#pragma unroll
    for (int hh = 0; hh < 2; ++hh) __builtin_amdgcn_raw_buffer_store_b128(ov[hh], rO, (((m0 >> 4) + wave) * RBO + tn * 8 + hh * 4) * 256 + lane * 16, 0, 16);
    STAMP(11);
  }
  STAMP(7);
  STAMP(8);
  TS6(22);
  const Tag tg = tag_get(lds0);
  asm volatile("s_waitcnt vmcnt(0)" ::: "memory");
  flag_set(a.sync, F_O(tm) + (wave >> 1) * 256 + tn * 16 + (wave & 1) * 8, tg, 0);
  raw_barrier();
  hook(IC<1>{}, IC<KN_WE>{}, IC<16>{});
  STAMP(3);
#endif
}

struct Late { u32x2 tag; };
template <class HK>
__device__ __forceinline__ void glu_phase(const GemmArgs& a, HK hook, Late& late) {
#if defined(__HIP_DEVICE_COMPILE__)
  constexpr int LDW = DF_, WN = 2;
  constexpr int UPOFF = 32 * 224 + 16;
  constexpr int XT = 32768, WU = 14592;
  constexpr int XBASE = 0, WBASE = 3 * XT, RING = WBASE + 4 * WU;
  constexpr int MF = 4, NF = 7;
  constexpr int NT = 16, NXI = 16, NWI = 14, NLW = 4, XI = 4, WI = 4;
  extern __shared__ __attribute__((aligned(16))) char smem[];
  STAMP(0);
  const unsigned lds0 = (unsigned)(size_t)(lds_char_ptr)smem;
  const int tid = threadIdx.x, lane = tid & 63;
  const int wave = __builtin_amdgcn_readfirstlane(tid >> 6);
  const int wm = wave / WN, wn = wave % WN;
  const int bid = blockIdx.x, xcd = bid & 7, idx = bid >> 3;
  const int tm = (xcd / 4) * 4 + idx / 8, tn = (xcd % 4) * 8 + idx % 8;
  const int m0 = tm * 256;
  const int krot = (tm >> 2) * 8;
  const int fr = lane & 15, fq = lane >> 4;
  const int wq = (lane >> 2) & 3, wp = lane & 3;
  const int panel = m0 >> 8;
  constexpr int TCH = RING + 5120;
  constexpr int DONE = TCH + 896;
  auto done_inc = [&]() { if (lane == 0) asm volatile("ds_add_u32 %0, %1" :: "v"(lds0 + DONE), "v"(1) : "memory"); };
  auto touch = [&](const void* base, unsigned voff, unsigned soff, unsigned lim = 0x7fffffffu) {
    __builtin_amdgcn_raw_ptr_buffer_load_lds(RSRCN(base, lim), LDSP(lds0 + TCH), 4, voff, soff, 0, 0);
  };
  const auto rO = RSRC(a.O);

  unsigned xsrc[XI], wsrc[WI];
#pragma unroll
  for (int i = 0; i < XI; ++i) {
    const int j = (wave % NLW) + NLW * i;
    xsrc[i] = (unsigned)((((m0 >> 4) + (j >> 1)) * RBH + (j & 1) * 4) * 256 + lane * 16);
  }
#pragma unroll
  for (int i = 0; i < WI; ++i) {
    int j = (wave % NLW) + NLW * i; if (j >= NWI) j -= NLW;
    const int jj = j >= 7 ? j - 7 : j;
    const int b = jj * 1024 + lane * 16, p = b / 224, pos = (b % 224) >> 4;
    const int row = 8 * ((p & 15) >> 2) + (p & 3) + 4 * (p >> 4);
    wsrc[i] = (unsigned)(row * LDW + tn * 112 + pos * 8) * 2u;
  }
  auto ldX = [&](int i, int tile, int part, int slot) {
    tile = (tile + krot) & 15;
    const int j = wave + NLW * i;
    __builtin_amdgcn_raw_ptr_buffer_load_lds(RSRC(a.X), LDSP(lds0 + XBASE + slot * XT + (part * NXI + j) * 1024), 16, xsrc[i], tile * 2048 + part * (NXI / 2) * RBH * 256, 0, 16);
  };
  auto ldW = [&](int i, int unit, int slot) {
    const auto rW0 = RSRC(a.W0);
    const auto rW1 = RSRC(a.W1);
    int j = wave + NLW * i; if (j >= NWI) j -= NLW;
    unit = (unit + 2 * krot) & 31;
    const bool up = j >= 7;
    __builtin_amdgcn_raw_ptr_buffer_load_lds(up ? rW1 : rW0, LDSP(lds0 + WBASE + slot * WU + (up ? UPOFF + (j - 7) * 1024 : j * 1024)), 16, wsrc[i], unit * 32 * LDW * 2, 0, 0);
  };
  auto issueX = [&](int tile, int part, int slot) {
    if (wave >= NLW) return;
#pragma unroll
    for (int i = 0; i < XI; ++i) ldX(i, tile, part, slot);
  };
  auto issueW = [&](int unit, int slot) {
    if (wave >= NLW) return;
#pragma unroll
    for (int i = 0; i < WI; ++i) ldW(i, unit, slot);
  };

  f32x4 acc[NF][MF];
#pragma unroll
  for (int n = 0; n < NF; ++n)
#pragma unroll
    for (int m = 0; m < MF; ++m) acc[n][m] = (f32x4){0.f, 0.f, 0.f, 0.f};
  f32x4 accs = {0.f, 0.f, 0.f, 0.f};

  const unsigned xa0 = lds0 + XBASE + wm * MF * 16 * 128 + lane * 16, xa1 = xa0 + 1024;
  unsigned wa[2];
#pragma unroll
  for (int c = 0; c < 2; ++c) wa[c] = lds0 + WBASE + (4 * fq + wq) * 224 + wn * 112 + (c == 0 ? wp * 8 : 96 + (wp & 1) * 8 + (wp >> 1) * UPOFF);

  using I0 = IC<0>; using I1 = IC<1>;
  using T = std::true_type; using F = std::false_type;
  typedef Frags<MF, NF> FR;
  struct RdCtx { unsigned xa, wb[2]; };
  auto mkctx = [&](bool ks1, unsigned xoffs, unsigned woffs) {
    RdCtx c; c.xa = (ks1 ? xa1 : xa0) + xoffs; c.wb[0] = wa[0] + woffs; c.wb[1] = wa[1] + woffs;
    return c;
  };
  auto rd_item = [&](FR& f, const RdCtx& c, auto kc) {
    constexpr int k = decltype(kc)::value;
    if constexpr (k < MF) f.x[k] = ds_b128<k * 2048>(c.xa);
    else {
      constexpr int n = k - MF, cc = n == 6, o = n == 6 ? 0 : (n >> 1) * 32 + (n & 1) * UPOFF;
      f.lo[n] = ds_tr<o>(c.wb[cc]); f.hi[n] = ds_tr<o + 16 * 224>(c.wb[cc]);
    }
  };
  auto wait_frags = [&](FR& f) {
    asm volatile("s_waitcnt lgkmcnt(0)" ::: "memory");
#pragma unroll
    for (int m = 0; m < MF; ++m) asm volatile("" : "+v"(f.x[m]));
#pragma unroll
    for (int n = 0; n < NF; ++n) asm volatile("" : "+v"(f.lo[n]), "+v"(f.hi[n]));
  };
  auto step = [&](FR& cur, FR& nxt, const RdCtx& c, auto PFc, auto NLc, auto&& ld) {
    constexpr bool PF = decltype(PFc)::value != 0; constexpr int NL = decltype(NLc)::value;
    constexpr int R = MF + NF, RG = NF - 1;
    static_for<0, NF>([&](auto gc) {
      constexpr int g = decltype(gc)::value;
      if constexpr (PF) static_for<0, R>([&](auto kc) { constexpr int k = decltype(kc)::value; if constexpr (k * RG / R == g) rd_item(nxt, c, kc); });
      static_for<0, NL>([&](auto ic) { constexpr int i = decltype(ic)::value; if constexpr (i * NF / NL == g) ld(i); });
      const bf16x8 w = __builtin_shufflevector(cur.lo[g], cur.hi[g], 0, 1, 2, 3, 4, 5, 6, 7);
#pragma unroll
      for (int m = 0; m < MF; ++m) acc[g][m] = MFMA(w, cur.x[m], acc[g][m]);
      __builtin_amdgcn_sched_barrier(0);
    });
    if constexpr (PF) wait_frags(nxt);
  };
  auto stepg = [&](auto CSc, FR& cur, FR& nxt, const RdCtx& c, auto Hc, auto R0c, auto IBc, auto&& sync, auto NLc, auto&& ld, auto&& gap) {
    constexpr bool CS = decltype(CSc)::value; constexpr int H = decltype(Hc)::value, R0 = decltype(R0c)::value, IB = decltype(IBc)::value, NL = decltype(NLc)::value;
    constexpr int NM = NF * MF, NR = MF + 2 * NF;
    static_for<0, NM>([&](auto ic) {
      constexpr int I = decltype(ic)::value, g = I / MF, m = I % MF;
      constexpr int RL = IB > 8 ? 20 : 22;
      const bf16x8 w = __builtin_shufflevector(cur.lo[g], cur.hi[g], 0, 1, 2, 3, 4, 5, 6, 7);
      acc[g][m] = MFMA(w, cur.x[m], acc[g][m]);
      __builtin_amdgcn_sched_barrier(0);
      if constexpr (I == IB) sync();
      static_for<first_at(I, R0, RL - R0, NR - 1, NR), first_at(I + 1, R0, RL - R0, NR - 1, NR)>([&](auto rc) {
        constexpr int r = decltype(rc)::value;
        if constexpr (r < MF) nxt.x[r] = ds_b128<r * 2048 + H * 1024>(c.xa);
        else {
          constexpr int n = (r - MF) >> 1, cc = n == 6, o = (n == 6 ? 0 : (n >> 1) * 32 + (n & 1) * UPOFF) + H * WU;
          if constexpr ((r - MF) & 1) nxt.hi[n] = ds_tr<o + 16 * 224>(c.wb[cc]); else nxt.lo[n] = ds_tr<o>(c.wb[cc]);
        }
      });
#pragma unroll
      for (int i = first_at(I, R0, NM - 1 - R0, NL, NL); i < first_at(I + 1, R0, NM - 1 - R0, NL, NL); ++i) ld(i);
      gap(ic);
      if constexpr (CS && m == MF - 1) {
        const unsigned sv = fr == g ? 0x3f803f80u : 0u;
        const u32x4 s4 = {sv, sv, sv, sv};
        accs = MFMA(w, __builtin_bit_cast(bf16x8, s4), accs);
      }
      __builtin_amdgcn_sched_barrier(0);
    });
    wait_frags(nxt);
  };
  FR fa, fb;
  unsigned tv4 = 0;

  f32x4* tb_row = (f32x4*)(smem + RING);
  float* tb_cs = (float*)(tb_row + 256);
  constexpr int RTAB = 524288;
  auto glu_stats_issue = [&]() {
    if (wave == 7) __builtin_amdgcn_raw_ptr_buffer_load_lds(RSRC(a.st), LDSP(lds0 + RING + 3072), 16, ((lane >> 2) * S_ + m0 + tn * 8 + (lane & 3) * 2) * 8, 0, 0, 16);
  };
  auto glu_stats = [&]() {
    int lx = lane; asm volatile("" : "+v"(lx));
    const unsigned sa = lds0 + RING + 3072 + lx * 8;
    f32x2 p0 = ds_b64<0>(sa), p1 = ds_b64<512>(sa);
    asm volatile("s_waitcnt lgkmcnt(0)" : "+v"(p0), "+v"(p1) :: "memory");
    const float mu = sum8(p0[0] + p1[0]) * (1.f / 16.f);
    const float v = sum8((p0[1] + p1[1]) + 64.f * ((p0[0] - mu) * (p0[0] - mu) + (p1[0] - mu) * (p1[0] - mu))) * (1.f / 1024.f) + 1e-5f;
    const float rs = (KN_EV & 2) ? __builtin_amdgcn_rsqf(v) : 1.0f / sqrtf(v);
    const f32x4 rv = {mu, -0.10294324f * rs * rs * rs, -2.3022082f * rs, v};
    __builtin_amdgcn_raw_buffer_store_b128(__builtin_bit_cast(u32x4, rv), RSRC(a.cs), lx < 8 ? (unsigned)(RTAB + (m0 + tn * 8 + lx) * 16) : 0x80000000u, 0, 16);
  };
  f32x4 cg[4], cu[4], rw[MF], rw6[2]; f32x2 tgv; int dnv, go0; unsigned le, ta, t6;
  auto tabs = [&](int i) {
    if (i == 0) { const int lz = lane_id(), fr = lz & 15, fq = lz >> 4;
      ta = lds0 + RING + 4096 + wn * 224 + fq * 16; t6 = lds0 + RING + wm * 1024 + fr * 16;
      go0 = ((m0 >> 4) + wm * 4) * RBA * 256 + fr * 16 + ((fq & 1) * RBA + (fq >> 1)) * 256;
      le = lds0 + (wm * 4 + (fq & 1)) * 1024 + (wn * 2 + (fq >> 1)) * 256 + fr * 16;
      asm volatile("" : "+v"(ta), "+v"(t6), "+v"(go0), "+v"(le)); }
    if (i == 1) { rw[0] = __builtin_bit_cast(f32x4, ds_b128<0>(t6)); rw[1] = __builtin_bit_cast(f32x4, ds_b128<256>(t6)); cg[0] = __builtin_bit_cast(f32x4, ds_b128<0>(ta)); }
    if (i == 2) { cg[1] = __builtin_bit_cast(f32x4, ds_b128<64>(ta)); cu[0] = __builtin_bit_cast(f32x4, ds_b128<448>(ta)); cu[1] = __builtin_bit_cast(f32x4, ds_b128<448 + 64>(ta)); }
    if (i == 3) { rw[2] = __builtin_bit_cast(f32x4, ds_b128<512>(t6)); rw[3] = __builtin_bit_cast(f32x4, ds_b128<768>(t6)); tgv = ds_b64<0>(lds0 + SNAP); }
    if (i == 8) asm volatile("s_waitcnt lgkmcnt(0)" : "+v"(rw[0]), "+v"(rw[1]), "+v"(rw[2]), "+v"(rw[3]), "+v"(cg[0]), "+v"(cg[1]), "+v"(cu[0]), "+v"(cu[1]), "+v"(tgv) :: "memory");
    if (i == 9) { const int lz = lane_id(); const unsigned tc = ta - (lz & 32), tr = t6 + (lz & 32) * 8;
      cg[2] = __builtin_bit_cast(f32x4, ds_b128<128>(ta)); cu[2] = __builtin_bit_cast(f32x4, ds_b128<448 + 128>(ta)); cg[3] = __builtin_bit_cast(f32x4, ds_b128<192>(tc)); cu[3] = __builtin_bit_cast(f32x4, ds_b128<448 + 192>(tc));
      rw6[0] = __builtin_bit_cast(f32x4, ds_b128<0>(tr)); rw6[1] = __builtin_bit_cast(f32x4, ds_b128<512>(tr)); }
    if (i == 10) asm volatile("s_waitcnt lgkmcnt(0)" : "+v"(cg[2]), "+v"(cg[3]), "+v"(cu[2]), "+v"(cu[3]), "+v"(rw6[0]), "+v"(rw6[1]) :: "memory");
    if (i == 11) dnv = ds_r32(lds0 + DONE);
    if (i == 12) asm volatile("s_waitcnt lgkmcnt(0)" : "+v"(dnv) :: "memory");
  };
  auto epilogue = [&](auto EAc) __attribute__((always_inline)) {
    STAMP(2);
    constexpr bool EARLY = decltype(EAc)::value;
    auto prio = [&](auto gc) { constexpr int g = decltype(gc)::value; __builtin_amdgcn_s_setprio(((EARLY ? KN_PA : KN_PB) >> (4 * g)) & 3); };
    prio(I0{});
    STAMPW(9, 4);
    TS4(19);
    unsigned hv[MF][NF];
    auto keep = [&](unsigned a0, unsigned a1, unsigned b0, unsigned b1) {
      const auto s0 = __builtin_amdgcn_permlane16_swap(a0, b0, false, false), s1 = __builtin_amdgcn_permlane16_swap(a1, b1, false, false);
      return (u32x4){s0[0], s1[0], s0[1], s1[1]}; };
    auto put = [&](auto pc, auto hc) {
      constexpr int p = decltype(pc)::value, h = decltype(hc)::value;
      const u32x4 v = keep(hv[2 * h][2 * p], hv[2 * h][2 * p + 1], hv[2 * h + 1][2 * p], hv[2 * h + 1][2 * p + 1]);
      __builtin_amdgcn_raw_buffer_store_b128(v, rO, go0, (tn * 12 + p * 4 + wn * 2 + h * 2 * RBA) * 256, 16);
      if constexpr (p < 2) ds_w128<OWNE + p * 16384 + h * 2048>(le, v); else ds_w128<h * 2048>(le + OWNM, v);
      __builtin_amdgcn_sched_barrier(0);
      asm volatile("s_nop 1" ::: "memory");
      __builtin_amdgcn_sched_barrier(0);
    };
#define STAGE(body) _Pragma("unroll") for (int i = 0; i < 4 * NN; ++i) { const int m = i / NN, n = N0 + i % NN; const f32x4 k = n == 6 ? rw6[m >> 1] : rw[m]; body; } __builtin_amdgcn_sched_barrier(0)
    auto grp = [&](auto N0c, auto NNc, auto&& at) {
      constexpr int N0 = decltype(N0c)::value, NN = decltype(NNc)::value;
      f32x2 G[4 * NN], U[4 * NN], P[4 * NN];
      __builtin_amdgcn_sched_barrier(0);
      STAGE({ const int e = (n == 6 ? m & 1 : n & 1) * 2; const f32x4 x = n == 6 ? acc[6][m & 2] : acc[n & 6][m]; G[i] = ((f32x2){x[e], x[e + 1]}) - k[0] * ((f32x2){cg[n >> 1][e], cg[n >> 1][e + 1]}); });
      STAGE(P[i] = G[i] * G[i]);
      at(I0{});
      STAGE(P[i] = P[i] * k[1] + k[2]);
      STAGE(P[i] = G[i] * P[i]);
      at(I1{});
      STAGE({ const int e = (n == 6 ? m & 1 : n & 1) * 2; const f32x4 x = n == 6 ? acc[6][m | 1] : acc[n | 1][m]; U[i] = ((f32x2){x[e], x[e + 1]}) - k[0] * ((f32x2){cu[n >> 1][e], cu[n >> 1][e + 1]}); });
      STAGE(P[i] = ((f32x2){__builtin_amdgcn_exp2f(P[i][0]), __builtin_amdgcn_exp2f(P[i][1])}));
      at(IC<2>{});
      __builtin_amdgcn_sched_barrier(0);
      STAGE(P[i] = P[i] * k[3] + k[3]);
      STAGE(P[i] = ((f32x2){__builtin_amdgcn_rcpf(P[i][0]), __builtin_amdgcn_rcpf(P[i][1])}));
      at(IC<3>{});
      STAGE(U[i] = G[i] * U[i]);
      STAGE(U[i] = U[i] * P[i]);
      STAGE({ hv[m][n] = pk_bf16(U[i][0], U[i][1]); asm volatile("" : "+v"(hv[m][n])); });
    };
#undef STAGE
    using I2 = IC<2>; using I4 = IC<4>;
    TS4(20);
    grp(I0{}, I2{}, [&](auto tc) { if constexpr (decltype(tc)::value == 3) tabs(11); });
    TS4(21);
    tabs(12);
    if (__builtin_expect(__builtin_amdgcn_readfirstlane(dnv) < 8, 0)) lds_wait8(lds0 + DONE);
    put(I0{}, I0{}); put(I0{}, I1{});
    tabs(9);
    STAMP(4);
    STAMPW(11, 4);
    TS4(22);
    prio(I1{});
    const auto rFl = RSRCN((const char*)a.sync + F_ACT + ((panel * 4 + wm) * 64 + tn * 2 + wn) * 8, 8);
    u32x2 fval = {__float_as_uint(tgv[0]) + 8u, __float_as_uint(tgv[1])}; unsigned foff = lane_id() * 8u;
    asm volatile("" : "+v"(fval), "+v"(foff));
    grp(I2{}, I2{}, [&](auto) {});
    TS4(23);
#define KN_FE -1
    if constexpr (KN_FE < 0) { wait_vm<0>(); __builtin_amdgcn_raw_buffer_store_b64(fval, rFl, foff, 0, 16); STAMPW(6, 4); STAMP(10); }
    put(I1{}, I0{}); put(I1{}, I1{});
    TS4(24);
    prio(I2{});
    tabs(10);
    grp(I4{}, I2{}, [&](auto tc) { if constexpr (decltype(tc)::value == KN_FE) { wait_vm<2>(); __builtin_amdgcn_raw_buffer_store_b64(fval, rFl, foff, 0, 16); STAMPW(6, 4); STAMP(10); } });
    TS4(25);
    auto poll = [&]() { if (foff < 256u) __builtin_amdgcn_raw_ptr_buffer_load_lds(RSRC(a.sync), LDSP(lds0 + FLP + wave * 128), 4, foff >> 1, F_ACT + (panel * 4 + wm) * 512 + (xcd & 3) * 128, 0, 16); };
    if constexpr (KN_QP == 0) poll();
    put(I2{}, I0{}); put(I2{}, I1{});
    prio(IC<3>{});
#pragma unroll
    for (int q = 0; q < 2; ++q)
#pragma unroll
      for (int e = 0; e < 4; ++e) {
        const float x0 = acc[6][2 * q][e], x1 = acc[6][2 * q + 1][e];
        auto sw = __builtin_amdgcn_permlane32_swap(__float_as_uint(x0), __float_as_uint(x1), false, false);
        acc[6][2 * q][e] = __uint_as_float(sw[0]); acc[6][2 * q + 1][e] = __uint_as_float(sw[1]);
      }
    TS4(26);
    int go6 = go0 + (int)((foff >> 7) & 1u) * (RBA * 256) + (int)(foff >> 8) * (RBA * 256 - 256); asm volatile("" : "+v"(go6));
    if constexpr (KN_QP == 1) poll();
    grp(IC<6>{}, I1{}, [&](auto) {});
    if constexpr (KN_QP == 2) poll();
    __builtin_amdgcn_raw_buffer_store_b128(keep(hv[0][6], hv[1][6], hv[2][6], hv[3][6]), rO, go6, (384 + tn * 2 + wn) * 256, 16);
    __builtin_amdgcn_sched_barrier(0);
    asm volatile("s_nop 1" ::: "memory");
    if constexpr (KN_QP == 3) poll();
    late.tag = fval;
    asm volatile("" ::: "memory");
    __builtin_amdgcn_s_setprio(0);
  };

  TS4(0);
  constexpr int WR = 2;
  if (wave == 5) ds_w32(lds0 + DONE, 0);
  STAMP(9); STAMP(6);
  auto check_tile = [&](int t) {
    int lx = lane; asm volatile("" : "+v"(lx));
    const unsigned ad = lds0 + XBASE + t * XT + wave * 1024 + lx * 16;
    if (__builtin_expect(marked<8>(ad), 0)) {
      for (int tries = 0;; ++tries) {
        if (tries >= KN_TRY) wait_flags(lds0, a.sync, F_H + panel * 256, 32, 0);
#pragma unroll 1
        for (int u = t; u < t + 1 + (t == 0); ++u) { issueX(u, 0, u); issueX(u, 1, u); }
        if (t == 0) wait_vm<2 * XI>(); else wait_vm<0>();
        if (tries >= KN_TRY || !marked<8>(ad)) break;
      }
    }
  };
  auto run = [&](auto LDRc) __attribute__((always_inline)) {
    constexpr bool LDR = decltype(LDRc)::value;
    if constexpr (LDR) {
      issueX(0, 0, 0); issueX(0, 1, 0); issueX(1, 0, 1); issueX(1, 1, 1);
      asm volatile("" : "+v"(wsrc[0]), "+v"(wsrc[1]), "+v"(wsrc[2]), "+v"(wsrc[3]));
      if constexpr (KN_G2) wait_vm<2 * XI>(); else { issueW(2, 2 ^ WR); issueW(3, 3 ^ WR); wait_vm<2 * XI + 2 * WI>(); }
      check_tile(0);
    } else {
      wait_vm<0>();
#define KN_HS 0
      if (KN_HS && wave == 4) { __builtin_amdgcn_s_sleep(KN_HS); __builtin_amdgcn_raw_ptr_buffer_load_lds(RSRC(a.sync), LDSP(lds0 + TCH + 256), 4, lane * 4, F_H + panel * 256, 0, 16); }
    }
    unsigned long long tb = (unsigned long long)a.PF0; unsigned tv = 0, tl = 0x7fffffffu, ts = 7168u, to = 0, tmask = 0;
    if constexpr (!LDR) {
      unsigned long long c0, c1; CODE_ADDR(c0, "k_all"); CODE_ADDR(c1, "k_zend");
#define KN_WDM 0x55
#define KN_DA 6
#define KN_DN 2
      if (wave == 4) { tb = c0; tl = c1 - c0 < 262144ull ? (unsigned)(c1 - c0) : 0u; tmask = 1u << 11; }
      if (wave == 5) { tmask = KN_WDM << (KN_DA + idx % KN_DN); to = ((xcd & 3) * 32 + idx) * 57344 + (xcd >> 2) * 28672; }
      if (wave == 6) { tb = (unsigned long long)((idx & 16) ? a.W1 : a.W0); ts = 64 * LDW * 2; tmask = 0x1eu; to = (krot + 4) * ts; }
      const int l4 = lane / 14;
      tv = wave == 4 ? (unsigned)((lane * 32 + idx) * 128)
         : (lane >= 56 ? 0x80000000u : (wave == 5 ? (unsigned)(lane * 128) : (unsigned)(((idx & 15) * 4 + l4) * (LDW * 2) + (xcd & 3) * 1792 + (lane - l4 * 14) * 128)));
    }
    const int js = 2 * (tm & 3) + 1;
    const auto rC = RSRC(a.cs);
    constexpr int JR = 10, JS = 2;
    const int jf = wave == 4 ? NT - 4 : (wave == 7 ? JR : 64);
    const int fof = wave == 4 ? F_COL + tn * 256 : F_ROW + panel * 256;
    unsigned evm = tmask | (8u << js) | (wave == 4 ? 3u << (NT - 4) : 0u) | (wave == 7 ? ((5u << JS) | 2u | (11u << JR)) : 0u);
    Tag tgl = {0u, 0u};
    if constexpr (!LDR && (KN_EV & 4)) tgl = tag_get(lds0);
    if constexpr (!LDR) asm volatile("" : "+v"(tv), "+s"(tb), "+s"(tl), "+s"(ts), "+s"(to), "+s"(tmask), "+s"(evm));
    lds_barrier();
    if constexpr (!LDR && !KN_HS) { if (wave == 4) __builtin_amdgcn_raw_ptr_buffer_load_lds(RSRC(a.sync), LDSP(lds0 + TCH + 256), 4, lane * 4, F_H + panel * 256, 0, 16); }
    { const RdCtx c = mkctx(false, 0, WR * WU); static_for<0, MF + NF>([&](auto kc) { rd_item(fa, c, kc); }); }
    wait_frags(fa);
    STAMP(1);
    TS4(1);
    { const RdCtx c = mkctx(true, 0, (WR + 1) * WU); step(fa, fb, c, T{}, IC<LDR ? 2 * XI : 0>{}, [&](int i) { ldX(i % XI, 2, i / XI, 2); }); }
    RdCtx cc = mkctx(false, XT, 0);
    int xs = 1, dX = XT, dW = 2 * WU;
    int wd = lds0 + WBASE + 2 * WU, wso = ((4 + 2 * krot) & 31) * (32 * LDW * 2), xd = lds0 + XBASE + wave * 1024, xso = ((3 + krot) & 15) * 2048;
    int wc[WI];
#pragma unroll
    for (int i = 0; i < WI; ++i) { int q = wave + NLW * i; if (q >= NWI) q -= NLW; wc[i] = q >= 7 ? UPOFF + (q - 7) * 1024 : q * 1024; }
    unsigned evs = evm, dw = 0;
    auto ldd = [&](int i) { static_for<0, 22>([&](auto kc) { if (i == decltype(kc)::value) hook(IC<64 + decltype(kc)::value>{}, dw); }); };
    constexpr int CFL = TCH + 256, CPB = XBASE + ((NT - 3) % 3) * XT;
    auto iter = [&](int j, auto LWc, auto LXc, auto WAITc, auto CAc, auto CBc) {
      constexpr bool LW = decltype(LWc)::value && LDR, LX = decltype(LXc)::value && LDR, LP = LW && !LX;
      TS4(j + 1);
      if constexpr (LDR) {
        wait_vm<decltype(WAITc)::value>();
        if constexpr (LX) { if (__builtin_expect(j <= 2, 0)) check_tile(j); }
      }
      if constexpr (!LDR) {
        if (__builtin_expect(evs & 2, 0)) {
          if (KN_EV & 1) __builtin_amdgcn_s_setprio(3);
          if (j == js + 3) {
            if ((tmask >> (j - 1)) & 1) wait_vm<1>(); else wait_vm<0>();
            flag_set(a.sync, F_COL + tn * 256 + (tm * 4 + wave - 4) * 8, (KN_EV & 4) ? tgl : tag_get(lds0), 0);
          }
          if (j == 1) glu_stats_issue();
          if (j == JS && wave == 7) { wait_vm<0>(); glu_stats(); }
          if (j == JS + 2 && wave == 7) {
            if (j == js + 2) wait_vm<1>(); else wait_vm<0>();
            flag_set(a.sync, fof + tn * 8, (KN_EV & 4) ? tgl : tag_get(lds0), 0);
          }
          {
            int lx = lane; asm volatile("" : "+v"(lx));
            if (j == jf) __builtin_amdgcn_raw_ptr_buffer_load_lds(RSRC(a.sync), LDSP(lds0 + CFL + (wave & 1) * 256), 4, lx * 4, fof, 0, 16);
            if (j == jf + 1) {
              wait_vm<0>();
              int fv = ds_r32(lds0 + CFL + (wave & 1) * 256 + lx * 4);
              const Tag tg = tag_get(lds0);
              asm volatile("" : "+v"(fv));
              while (!words_up((unsigned)fv, tg) && in_time(lds0))
                asm volatile("global_load_dword %0, %1, %2 sc1\n\ts_waitcnt vmcnt(0)" : "=v"(fv) : "v"(lx * 4), "s"((const char*)a.sync + fof) : "memory");
              if (wave == 7) {
#pragma unroll
                for (int i = 0; i < 4; ++i) __builtin_amdgcn_raw_ptr_buffer_load_lds(rC, LDSP(lds0 + RING + i * 1024), 16, lx * 16, RTAB + m0 * 16 + i * 1024, 0, 16);
              }
            }
            if (j == jf + 3) wait_vm<0>();
          }
          if ((tmask >> j) & 1) { touch((const void*)tb, tv, to, tl); to += ts; }
          if (KN_EV & 1) __builtin_amdgcn_s_setprio(0);
        }
        __builtin_amdgcn_sched_barrier(0);
      }
      constexpr bool LD = LDR && !LW;
      if constexpr (LD) {
        const int lc = lane_id();
        const unsigned ca = lds0 + CPB + wave * 4096 + (lc >> 4) * 896 + ((lc & 15) < 14 ? (lc & 15) : 0) * 16;
        f32x4 p0 = __builtin_bit_cast(f32x4, ds_b128<0>(ca)), p1 = __builtin_bit_cast(f32x4, ds_b128<224>(ca)), p2 = __builtin_bit_cast(f32x4, ds_b128<448>(ca)), p3 = __builtin_bit_cast(f32x4, ds_b128<672>(ca));
        dw = dn_wsrc(lc, wave);
        asm volatile("s_waitcnt lgkmcnt(0)" : "+v"(p0), "+v"(p1), "+v"(p2), "+v"(p3), "+v"(dw) :: "memory");
        f32x4 cv = (p0 + p1) + (p2 + p3);
#pragma unroll
        for (int k = 0; k < 4; ++k) { float t = cv[k]; auto sw = __builtin_amdgcn_permlane16_swap(__float_as_uint(t), __float_as_uint(t), false, false); t = __uint_as_float(sw[0]) + __uint_as_float(sw[1]);
          auto sx = __builtin_amdgcn_permlane32_swap(__float_as_uint(t), __float_as_uint(t), false, false); cv[k] = __uint_as_float(sx[0]) + __uint_as_float(sx[1]); }
        if (lc < 14) *(f32x4*)(tb_cs + (wave >> 1) * 112 + wn * 56 + lc * 4) = cv;
        asm volatile("s_waitcnt lgkmcnt(0)" ::: "memory");
        __builtin_amdgcn_sched_barrier(0);
      }
      auto lda = [&](int i) { if constexpr (LD) ldd(i); else { const int k = i % WI;
          if constexpr (LP) { if (i < 4) __builtin_amdgcn_raw_ptr_buffer_load_lds(rC, LDSP(lds0 + CPB + wave * 4096 + i * 1024), 16, lane * 16, (tn * 4 + wn * 2 + (wave >> 1)) * 3584 + i * 1024, 0, 16); }
          __builtin_amdgcn_raw_ptr_buffer_load_lds((k >= 2 || (k == 1 && wave == 3)) ? RSRC(a.W1) : RSRC(a.W0), LDSP(wd + wc[k]), 16, wsrc[k], wso, 0, 0); } };
      auto ldb = [&](int i) {
          if constexpr (LX) __builtin_amdgcn_raw_ptr_buffer_load_lds(RSRC(a.X), LDSP(xd + i * 4096), 16, xsrc[i % XI], xso + (i / XI) * (NXI / 2) * RBH * 256, 0, 16);
          else if constexpr (LD) ldd(8 + i); };
      using NLA = IC<(LW || LD) ? 8 : 0>; using NLB = IC<(LX || LD) ? 8 : 0>;
      auto pin1 = [](int& v) { asm volatile("" : "+s"(v)); };
      stepg(CAc, fb, fa, cc, I0{}, IC<LDR ? 2 : 0>{}, IC<LDR ? 1 : -1>{}, [&]() { raw_barrier(); }, NLA{}, lda, [&](auto ic) { constexpr int I = decltype(ic)::value;
        if constexpr (LW && I == 12) { wd += WU; wso += 32 * LDW * 2; pin1(wd); pin1(wso); }
        if constexpr (LW && I == 24) { wd += dW > 0 ? -3 * WU : WU; pin1(wd); }
        if constexpr (LW && I == 25) { wso += 32 * LDW * 2; pin1(wso); }
        if constexpr (LW && I == 26) { wso = wso == 1024 * LDW * 2 ? 0 : wso; pin1(wso); }
        if constexpr (!LDR && I == 24) { evs >>= 1; asm volatile("" : "+s"(evs)); }
        if constexpr (I == 7) { dX = xs == 2 ? -2 * XT : XT; pin1(dX); }
        if constexpr (I == 10) { xs = xs == 2 ? 0 : xs + 1; pin1(xs); } });
      stepg(CBc, fa, fb, cc, I1{}, I0{}, IC<LDR ? -1 : 25>{},
          [&]() { asm volatile("s_waitcnt lgkmcnt(0)" ::: "memory"); if (decltype(WAITc)::value == 0 || j + 1 < NT) raw_barrier(); }, NLB{}, ldb, [&](auto ic) { constexpr int I = decltype(ic)::value;
        if constexpr (I == 8) { cc.xa += dX; asm volatile("" : "+v"(cc.xa)); }
        if constexpr (I == (LDR ? 24 : 22)) { cc.wb[0] += dW; cc.wb[1] += dW; asm volatile("" : "+v"(cc.wb[0]), "+v"(cc.wb[1])); }
        if constexpr (I == (LDR ? 25 : 23)) { dW = -dW; pin1(dW); }
        if constexpr (LX && I == 25) { xso = (xso + 2048) & 0x7800; pin1(xso); }
        if constexpr (LX && I == 26) { xd += xs == 1 ? -2 * XT : XT; pin1(xd); } });
    };
    using WX_ = IC<2 * XI>;
    if constexpr (LDR) {
      for (int j = 1; j < NT - 2; ++j) iter(j, T{}, T{}, WX_{}, F{}, F{});
      iter(NT - 2, T{}, F{}, WX_{}, F{}, F{});
      __builtin_amdgcn_s_setprio(3);
      iter(NT - 1, F{}, F{}, I0{}, F{}, F{});
    } else {
      int j = 1;
      if (wave == 4) {
        wait_vm<0>();
        int fv = ds_r32(lds0 + TCH + 256 + lane * 4);
        const Tag tg = tag_get(lds0);
        asm volatile("" : "+v"(fv));
        if (!words_up((unsigned)fv, tg)) wait_flags(lds0, a.sync, F_H + panel * 256, 32, 0);
      }
      raw_barrier();
#pragma unroll 1
      for (; j < js; ++j) iter(j, F{}, F{}, I0{}, F{}, F{});
      const int ja = wm == 2 ? js + 2 : js;
#pragma unroll 1
      for (; j < ja; ++j) iter(j, F{}, F{}, I0{}, T{}, F{});
#pragma unroll 1
      for (; j < js + 2; ++j) iter(j, F{}, F{}, I0{}, F{}, T{});
      { int lx = lane; asm volatile("" : "+v"(lx));
        __builtin_amdgcn_raw_buffer_store_b128(__builtin_bit_cast(u32x4, accs), rC, (lx & 15) < NF ? (unsigned)((tn * 4 + wn * 2) * 3584 + (tm * 2 + (wm & 1)) * 224
                + ((lx & 15) < 6 ? (lx & 1) * 3584 + ((lx & 15) >> 1) * 64 + (lx >> 4) * 16 : (lx >> 5) * 3584 + 192 + (lx & 16))) : 0x80000000u, 0, 16); }
#pragma unroll 1
      for (; j < NT; ++j) iter(j, F{}, F{}, I1{}, F{}, F{});
    }
    done_inc();
    TS4(17);
    step(fb, fa, cc, F{}, IC<NF>{}, [&](int i) { tabs(i); if constexpr (LDR) { if (i > 0) ldd(15 + i); } });
    tabs(8);
    TS4(18);
    epilogue(LDRc);
  };
  if (wave < NLW) run(T{}); else run(F{});
  STAMP(7);
  STAMP(8);
  STAMPW(12, 4);
  STAMP(3);
#endif
}

#define KN_CT 7
#define KN_E 6
#define KN_B3 0
#define KN_WB 2
#define KN_GW 1
#define XAUXW 16
#define XAUXD 16
#define KN_WS 2
#define KN_HE 0
#define KN_FL 22
#define KN_LD 0
#define KN_DM 0
#define KN_LX 0
#define STAMPV(i) STAMP(i)
struct FragW { s16x4 lo[4], hi[4]; };
template <int MODE, int LO, int HI>
__device__ __forceinline__ void wo_phase(const GemmArgs& a, const GemmArgs& nx) {
#if defined(__HIP_DEVICE_COMPILE__)
  extern __shared__ __attribute__((aligned(16))) char smem[];
  if constexpr (MODE == 2) STAMP(0);
  const unsigned lds0 = (unsigned)(size_t)(lds_char_ptr)smem;
  const int tid = threadIdx.x, lane = tid & 63;
  const int wave = __builtin_amdgcn_readfirstlane(tid >> 6);
  const int bid = blockIdx.x, xcd = bid & 7, idx = bid >> 3;
  const int tm = (xcd >> 1) * 4 + (idx >> 3), tn = (xcd & 1) * 8 + (idx & 7);
  const int m0 = tm * 128, panel = m0 >> 8, rot = WO_ROT;
  if constexpr (MODE & 1) {
    const auto rW = __builtin_amdgcn_make_buffer_rsrc((void*)a.W0, 0, 0x7fffffff, 0x00020000);
    const int row = wave * 8 + (lane >> 3);
    const unsigned wsrc = (unsigned)(row * D_ + tn * 64 + ((lane & 7) ^ wswz(row)) * 8) * 2u;
    static_for<LO, HI>([&](auto ic) { constexpr int i = decltype(ic)::value;
      __builtin_amdgcn_raw_ptr_buffer_load_lds(rW, (lds_void_ptr)(size_t)(lds0 + wo_pos(i >> 1) + (i & 1) * 8192 + wave * 1024), 16, wsrc, (((i & 1) * 8 + (i >> 1)) ^ rot) * (64 * D_ * 2), 0, 0);
      if constexpr (MODE == 3) __builtin_amdgcn_raw_ptr_buffer_load_lds(rW, LDSP(lds0 + wo_pos(i >> 1) + (i & 1) * 8192 + 4096 + wave * 1024), 16, wsrc, (((i & 1) * 8 + (i >> 1)) ^ rot) * (64 * D_ * 2) + 32 * D_ * 2, 0, 0); });
  } else {
    constexpr int PAD = 149504;
    const int g = wave >> 2, rw = wave & 3;
    const int fr = lane & 15, fq = lane >> 4, wq = (lane >> 2) & 3, wp = lane & 3;
    unsigned wa[4];
#pragma unroll
    for (int c = 0; c < 4; ++c) { const int row = 8 * fq + wq; wa[c] = lds0 + g * 8192 + row * 128 + (((c << 1) ^ wswz(row)) << 4) + wp * 8; }
    FragW f[3];
    auto rd = [&](auto ic, auto rc) {
      constexpr int i = decltype(ic)::value, r = decltype(rc)::value, P = wo_pos(i >> 1) + (i & 1) * 4096, B = P & ~32767, o = P & 32767;
      if constexpr (r & 1) f[i % 3].hi[r >> 1] = ds_tr<o + 512>(wa[r >> 1] + B); else f[i % 3].lo[r >> 1] = ds_tr<o>(wa[r >> 1] + B);
    };
    auto pin = [&](FragW& q) {
#pragma unroll
      for (int n = 0; n < 4; ++n) asm volatile("" : "+v"(q.lo[n]), "+v"(q.hi[n]));
    };
    static_for<0, 16>([&](auto rc) { constexpr int r = decltype(rc)::value; rd(IC<r / 8>{}, IC<r % 8>{}); });
    const Tag tg = tag_get(lds0);
    pin(f[0]); pin(f[1]);
    if (wave == 2) {
      unsigned long long c0, c1; CODE_ADDR(c0, "k_all"); CODE_ADDR(c1, "k_zend");
      __builtin_amdgcn_raw_ptr_buffer_load_lds(RSRCN(c0, c1 - c0 < 262144ull ? (unsigned)(c1 - c0) : 0u), LDSP(lds0 + PAD), 4, (unsigned)((lane * 32 + idx) * 128), 0, 0, 0); }
    if (wave == 3) {
      const unsigned fo = (unsigned)(((lane & 32) ? (xcd >> 2) * 4 + (idx >> 3) : panel) * 256 + (lane & 31) * 8);
      while (!flags_up(__builtin_amdgcn_raw_buffer_load_b64(RSRC(a.sync), fo, F_MARK, 16), 64, tg, 0) && in_time(lds0)) {}
    }
    const auto rX = __builtin_amdgcn_make_buffer_rsrc((void*)a.X, 0, 0x7fffffff, 0x00020000);
    const int rb = (m0 >> 4) + rw * 2 + g;
    unsigned xs[2] = {(unsigned)(rb * RBO * 256 + lane * 16), (unsigned)((rb ^ 1) * RBO * 256 + lane * 16)};
    const int ck = (fq & 1) * 2 + (fq >> 1);
    unsigned orow = (unsigned)((rb * 16 + fr) * (D_ * 2) + tn * 128 + ck * 16), ofrg = (unsigned)((rb * RBH + tn * 8 + ck) * 256 + fr * 16);
    asm volatile("" : "+v"(xs[0]), "+v"(xs[1]), "+v"(orow), "+v"(ofrg));
#define KN_RE 0
    u32x4 res[2];
    if constexpr (KN_RE) {
#pragma unroll
      for (int m = 0; m < 2; ++m) res[m] = __builtin_amdgcn_raw_buffer_load_b128(RSRC(a.R), orow + m * 64, 0, 0);
    }
    STAMP(9);
    const int fo_off = F_O(tm) + rw * 256 + ((g ^ (rot >> 3)) & 1) * 128;
    const unsigned first = (rot & 4) ? 0xff00u : 0x00ffu;
    auto down = [&](u32x2 v) { return (unsigned)__builtin_amdgcn_ballot_w64(v[0] != tg.lo || v[1] != tg.hi) & 0xffffu; };
    unsigned dn, polls = 0;
    do { dn = down(flag_get(a.sync, fo_off, 16)); asm volatile("" ::: "memory"); } while ((dn & first) && ((++polls & 1023u) || in_time(lds0)));
    STAMP(6);
    u32x2 fl = {0u, 0u};
    u32x4 xr[16][2];
#define KN_XD 4
    auto ldX = [&](int i, int m) {
      if (i < 16) xr[i][m] = __builtin_amdgcn_raw_buffer_load_b128(rX, xs[m] + (i & 1) * 1024, ((g * 8 + (i >> 1)) ^ rot) * 2048, XAUXW);
      else if (i == 16 && !KN_RE) res[m] = __builtin_amdgcn_raw_buffer_load_b128(__builtin_amdgcn_make_buffer_rsrc((void*)a.R, 0, 0x7fffffff, 0x00020000), orow + m * 64, 0, 0);
    };
#pragma unroll
    for (int i = 0; i < KN_XD; ++i) { ldX(i, 0); ldX(i, 1); __builtin_amdgcn_sched_barrier(0); }
    unsigned gsrc[2];
#pragma unroll
    for (int i = 0; i < 2; ++i) {
      const int j = wave + 8 * i, jj = j >= 7 ? j - 7 : j;
      const int b = jj * 1024 + lane * 16, q = b / 224, pos = (b % 224) >> 4;
      gsrc[i] = (unsigned)((8 * ((q & 15) >> 2) + (q & 3) + 4 * (q >> 4)) * DF_ + ((xcd & 3) * 8 + (idx & 7)) * 112 + pos * 8) * 2u;
      asm volatile("" : "+v"(gsrc[i]));
    }
    f32x4 acc[4][2];
#pragma unroll
    for (int n = 0; n < 4; ++n) { acc[n][0] = (f32x4){0.f, 0.f, 0.f, 0.f}; acc[n][1] = acc[n][0]; }
    __builtin_amdgcn_sched_barrier(0);
    static_for<0, 16>([&](auto ic) {
      constexpr int i = decltype(ic)::value;
      if constexpr (i >= 2) { asm volatile("s_waitcnt lgkmcnt(%0)" :: "n"(i < 15 ? 8 : 0) : "memory"); pin(f[i % 3]); }
      if constexpr (KN_WE < 6 && i == 0) { wait_vm<2 * KN_XD>(); raw_barrier(); }
      if constexpr (i == (KN_WE < 6 ? 4 : (KN_WE & ~1) - 2)) raw_barrier();
      if constexpr (i == 8 - KN_XD - 2) fl = flag_get(a.sync, fo_off, 16);
      if constexpr (i == 8 - KN_XD) { if (__builtin_expect(dn && down(fl), 0)) wait_flags(lds0, a.sync, fo_off, 16, 0); }
      static_for<0, 8>([&](auto jc) {
        constexpr int j = decltype(jc)::value, n = j >> 1, m = j & 1;
        const bf16x8 w = __builtin_shufflevector(f[i % 3].lo[n], f[i % 3].hi[n], 0, 1, 2, 3, 4, 5, 6, 7);
        acc[n][m] = __builtin_amdgcn_mfma_f32_16x16x32_bf16(w, __builtin_bit_cast(bf16x8, xr[i][m]), acc[n][m], 0, 0, 0);
        if constexpr (i + 2 < 16) rd(std::integral_constant<int, i + 2>{}, jc);
        if constexpr (j == 1 || j == 5) ldX(i + KN_XD, j >> 2);
        __builtin_amdgcn_sched_barrier(0);
      });
      if constexpr (i == 0) STAMP(1);
    });
    STAMP(2);
    auto slot = [&](int w) { return wo_pos(0) + w * 4096 + lane * 16; };
#pragma unroll
    for (int n = 0; n < 4; ++n) *(f32x4*)(smem + slot(wave) + n * 1024) = acc[n][1];
    asm volatile("" : "+v"(res[0]), "+v"(res[1]));
    lds_barrier();
    STAMP(4);
    f32x4 pa[4];
    static_for<0, 4>([&](auto nc) { constexpr int n = decltype(nc)::value; pa[n] = __builtin_bit_cast(f32x4, ds_b128<n * 1024>(lds0 + slot(wave ^ 4))); });
    auto ldG = [&](int i, int u) { const int j = wave + 8 * i;
      __builtin_amdgcn_raw_ptr_buffer_load_lds(RSRC(j >= 7 ? nx.W1 : nx.W0), LDSP(lds0 + 98304 + (u ^ 2) * 14592 + (j >= 7 ? 7184 + (j - 7) * 1024 : j * 1024)), 16, gsrc[i], (u + (xcd >> 2) * 16) * 32 * DF_ * 2, 0, 0); };
    auto ldG2 = [&](int u) { ldG(0, u); ldG(0, u + 1); if (wave < 6) { ldG(1, u); ldG(1, u + 1); } };
    if constexpr (KN_G2 != 2) ldG2(0);
    asm volatile("s_waitcnt lgkmcnt(0)" : "+v"(pa[0]), "+v"(pa[1]), "+v"(pa[2]), "+v"(pa[3]) :: "memory");
    const auto rO = __builtin_amdgcn_make_buffer_rsrc((void*)a.O, 0, 0x7fffffff, 0x00020000);
    u32x4 o[2];
#pragma unroll
    for (int i = 0; i < 2; ++i) {
      const f32x4 u = acc[2 * i][0] + pa[2 * i], v = acc[2 * i + 1][0] + pa[2 * i + 1];
      const auto s0 = __builtin_amdgcn_permlane16_swap(pk_bf16(u[0], u[1]), pk_bf16(v[0], v[1]), false, false);
      const auto s1 = __builtin_amdgcn_permlane16_swap(pk_bf16(u[2], u[3]), pk_bf16(v[2], v[3]), false, false);
      const u32x4 q4 = {s0[0], s1[0], s0[1], s1[1]};
#pragma unroll
      for (int j = 0; j < 4; ++j) { const unsigned r = res[i][j], q = q4[j]; o[i][j] = pk_bf16(bflo(r) + bflo(q), bfhi(r) + bfhi(q)); }
      __builtin_amdgcn_raw_buffer_store_b128(o[i], rO, ofrg + i * 1024, 0, 16);
    }
    auto sum4 = [](float v) {
      auto p = __builtin_amdgcn_permlane16_swap(__float_as_uint(v), __float_as_uint(v), false, false); v = __uint_as_float(p[0]) + __uint_as_float(p[1]);
      auto q = __builtin_amdgcn_permlane32_swap(__float_as_uint(v), __float_as_uint(v), false, false); return __uint_as_float(q[0]) + __uint_as_float(q[1]); };
    f32x2 e[8], sm = {0.f, 0.f};
#pragma unroll
    for (int j = 0; j < 8; ++j) { const unsigned q = o[j >> 2][j & 3]; e[j] = (f32x2){bflo(q), bfhi(q)}; sm += e[j]; }
    const float mu = sum4(sm[0] + sm[1]) * (1.f / 64.f);
    f32x2 t2 = {0.f, 0.f};
#pragma unroll
    for (int j = 0; j < 8; ++j) { const f32x2 d = e[j] - mu; t2 += d * d; }
    const float q2 = sum4(t2[0] + t2[1]);
    __builtin_amdgcn_raw_buffer_store_b64((u32x2){__float_as_uint(mu), __float_as_uint(q2)}, __builtin_amdgcn_make_buffer_rsrc((void*)a.st, 0, 0x7fffffff, 0x00020000),
                                          lane < 16 ? (unsigned)((tn * S_ + rb * 16 + lane) * 8) : 0x80000000u, 0, 16);
    STAMP(7);
    STAMP(8);
    if constexpr (KN_G2 == 2) ldG2(0);
    if constexpr (KN_G2) { ldG2(2); if (wave < 6) wait_vm<4 * KN_G2>(); else wait_vm<2 * KN_G2>(); } else wait_vm<0>();
    lds_barrier();
    if (wave == 0) flag_set(a.sync, F_H + panel * 256 + ((tm & 1) * 16 + tn) * 8, tg, 0);
    STAMP(3);
  }
#endif
}

struct FragD { s16x4 lo[8], hi[8]; };
enum { O_X, O_W, O_SP, O_FA, O_FP, O_QC, O_QD, O_QP, O_RES, O_LP, O_CC, O_CD, O_CP, O_BAR, O_AR, O_AP };
constexpr int DN_A = 12, DN_E = 0, DN_PRE = 11;
constexpr unsigned char DN_OPS[][4] = {
 {0,10,O_CC,1}, {0,11,O_X,6}, {0,12,O_X,7}, {0,14,O_X,8}, {0,15,O_X,9}, {1,2,O_X,10}, {1,5,O_X,11}, {1,9,O_X,12}, {1,13,O_W,11}, {2,0,O_FA,2}, {2,2,O_X,13}, {2,5,O_X,14}, {2,9,O_X,15}, {2,13,O_W,12},
 {3,2,O_X,16}, {3,5,O_X,17}, {3,9,O_X,18}, {3,13,O_X,19}, {4,2,O_BAR,12}, {4,3,O_W,14}, {4,10,O_W,13}, {4,16,O_QC,2}, {5,2,O_W,17}, {5,7,O_W,16}, {5,12,O_W,15},
 {6,0,O_CC,2}, {6,2,O_X,20}, {6,5,O_X,21}, {6,9,O_X,22}, {6,13,O_W,18}, {7,2,O_X,23}, {7,5,O_X,24}, {7,9,O_X,25}, {7,13,O_W,19}, {8,2,O_X,26}, {8,5,O_X,27}, {8,9,O_X,28}, {8,13,O_W,20},
 {9,2,O_X,29}, {9,5,O_X,30}, {9,9,O_X,31}, {9,13,O_W,21}, {10,2,O_X,32}, {10,5,O_X,33}, {10,9,O_X,34}, {10,13,O_W,22}, {11,2,O_BAR,19}, {11,3,O_X,35}, {11,6,O_X,36}, {11,10,O_X,37}, {11,13,O_W,23}, {11,16,O_QD,0},
 {12,2,O_X,38}, {12,5,O_X,39}, {12,9,O_X,40}, {12,13,O_X,41}, {13,2,O_X,42}, {13,5,O_X,43}, {13,9,O_X,44}, {13,13,O_X,45}, {14,0,O_CD,0}, {14,2,O_X,46}, {14,7,O_X,47}, {14,12,O_RES,0},
 {15,2,O_BAR,23}, {15,3,O_W,25}, {15,10,O_W,24}, {16,2,O_W,27}, {16,7,O_W,26}, {16,12,O_RES,1}, {17,2,O_W,29}, {17,9,O_W,28}, {18,2,O_W,30}, {18,7,O_SP,0}, {18,12,O_SP,1},
 {19,2,O_BAR,23}, {19,3,O_X,48}, {19,6,O_X,49}, {19,10,O_X,50}, {19,13,O_SP,2}, {20,2,O_X,51}, {20,5,O_X,52}, {20,9,O_W,31}, {20,13,O_SP,3},
 {21,2,O_X,53}, {21,5,O_X,54}, {21,9,O_X,55}, {21,13,O_X,56}, {22,0,O_FP,0}, {22,2,O_X,57}, {22,5,O_X,58}, {22,9,O_X,59}, {22,13,O_X,60},
 {23,0,O_FP,1}, {23,2,O_BAR,31}, {23,3,O_X,61}, {23,7,O_X,62}, {23,12,O_X,63}, {24,1,O_SP,4}, {24,2,O_SP,5}, {24,8,O_QP,0}, {25,8,O_AR,0},
 {26,0,O_FP,2}, {27,0,O_CP,0}, {27,0,O_CP,1}, {27,2,O_LP,0}, {27,5,O_LP,1}, {28,2,O_LP,2}, {28,5,O_LP,3}, {28,8,O_QP,2}, {31,0,O_CP,2}, {31,2,O_LP,4}, {31,5,O_LP,5},
 {32,0,O_AP,0}, {32,0,O_AP,1}, {32,0,O_AP,2}, {32,0,O_AP,3}, {32,0,O_AP,4}, {32,0,O_AP,5},
};
constexpr int DN_N = sizeof(DN_OPS) / 4, DN_H = (24 - DN_A) / 2, DN_B = DN_A + DN_H, BB = 24 + DN_E, NS = BB + 8;
constexpr int dn_nm(int v) { return v < 24 - DN_E ? 16 : 8; }
constexpr int dn_unit(int v, int n) { return v < DN_A ? v : (v >= BB ? v - DN_E : DN_A + 2 * (((v < 24 ? v : v - DN_E) - DN_A) % DN_H) + (n >> 2)); }
constexpr int dn_wpos(int u, int h) { constexpr int P[19] = {8, 9, 10, 11, 16, 17, 18, 4, 5, 6, 7, 14, 15, 0, 1, 2, 3, 12, 13}; return P[u % 19] * 8192 + h * 4096; }
constexpr int dn_first(int v, int g) { int i = 0; while (i < DN_N && (DN_OPS[i][0] < v || (DN_OPS[i][0] == v && DN_OPS[i][1] < g))) ++i; return i; }
constexpr int dn_find(int kind, int arg) { for (int i = 0; i < DN_N; ++i) if (DN_OPS[i][2] == kind && DN_OPS[i][3] == arg) return i; return -1; }
constexpr int dn_vm(int lo, int hi) { int c = 0; for (int i = lo; i < hi; ++i) c += DN_OPS[i][2] <= O_LP; return c; }
constexpr bool dn_bar(int v) { for (int i = 0; i < DN_N; ++i) if (DN_OPS[i][0] == v && DN_OPS[i][2] == O_BAR) return true; return false; }
constexpr int dn_cnt(int w, int i) { int p = -1; for (int u = DN_PRE; u <= w; ++u) if (dn_find(O_W, u) > p) p = dn_find(O_W, u); return p >= 0 ? dn_vm(p + 1, i) : 63; }
constexpr bool dn_ok() {
  int have = DN_PRE - 1, bstep = 0;
  for (int v = 0; v + 1 < NS; ++v) {
    for (int i = dn_first(v, 0); i < dn_first(v + 1, 0); ++i) {
      if (DN_OPS[i][2] == O_BAR) { if (DN_OPS[i][3] > have) have = DN_OPS[i][3]; bstep = v; for (int u = DN_PRE; u <= have; ++u) if (dn_find(O_W, u) < 0 || dn_find(O_W, u) > i) return false; }
      if (DN_OPS[i][2] == O_W) {
        if (bstep < 1 && DN_OPS[i][3] > 12) return false;
        for (int t = bstep + 1; t < NS; ++t) for (int n = 0; n < 8; n += 4) if (dn_unit(t, n) < DN_OPS[i][3] && dn_wpos(dn_unit(t, n), 0) == dn_wpos(DN_OPS[i][3], 0)) return false;
      }
    }
    if (dn_unit(v + 1, 7) > have) return false;
  }
  for (int x = 6; x < 64; ++x) {
    int n = 0; for (int i = 0; i < dn_find(O_X, x); ++i) { if (DN_OPS[i][2] == O_CC) n = DN_OPS[i][3]; if (DN_OPS[i][2] == O_CD) n = 4; }
    if (n < (x < 20 ? 1 : (x < 48 ? 2 : 4))) return false;
  }
  if (dn_find(O_FA, 2) < 0) return false;
  return dn_find(O_SP, 0) >= dn_first(DN_B, 0) && dn_find(O_SP, 4) >= dn_first(24, 0);
}
static_assert(dn_ok(), "schedule");
constexpr int dn_rd(int v, int I) { int c = 0; const int r0 = dn_bar(v) ? 2 : 1; if (v + 1 < NS) for (int r = 0; r < 16; ++r) c += r0 + r * (dn_nm(v) - r0 + 1) / 16 <= I; return c; }
struct DnTab { short first[NS + 1][18], rd[NS][18], wait[DN_N]; };
constexpr DnTab dn_tab() {
  DnTab t = {};
  for (int v = 0; v <= NS; ++v) for (int g = 0; g < 18; ++g) { t.first[v][g] = dn_first(v, g); if (v < NS) t.rd[v][g] = dn_rd(v, g); }
  for (int i = 0; i < DN_N; ++i) {
    const int kind = DN_OPS[i][2], arg = DN_OPS[i][3];
    t.wait[i] = kind == O_CC ? (KN_QP == 3 ? 0 : (KN_QP ? 1 : 3)) + dn_vm(0, i) : kind == O_FA ? dn_vm(0, i) : (kind == O_FP ? dn_vm(dn_find(O_SP, 2 * arg + 1) + 1, i) : (kind == O_BAR ? dn_cnt(arg, i) : 0));
  }
  return t;
}
constexpr DnTab DT = dn_tab();
#define KN_SLP 5
#define KN_PRI 1
template <int MODE, int U>
__device__ __forceinline__ void dn_phase(const GemmArgs& a, const Late& late, unsigned wsrc = 0) {
#if defined(__HIP_DEVICE_COMPILE__)
  extern __shared__ __attribute__((aligned(16))) char smem[];
  if constexpr (MODE != 1) STAMP(0);
  const unsigned lds0 = (unsigned)(size_t)(lds_char_ptr)smem;
  const int tid = threadIdx.x;
  int lane = tid & 63; asm volatile("" : "+v"(lane));
  const int wave = __builtin_amdgcn_readfirstlane(tid >> 6);
  const int bid = blockIdx.x, xcd = bid & 7, idx = bid >> 3;
  const int panel = (xcd >> 2) * 4 + (idx >> 3), c = idx & 7, q = xcd & 3;
  const int g4 = wave & 3, hh = U >= 64 ? U & 1 : wave >> 2;
#define KN_ABL 0
  const auto rW = RSRCN(a.W0, (KN_ABL & 1) ? 0 : 0x7fffffff);
  if constexpr (MODE != 1) wsrc = dn_wsrc(lane, wave);
  auto uj = [&](int v) { return q * 8 + ((c + (v < 3 ? 0 : 1 + (v - 3) % 7)) & 7); };
  auto us = [](int v) { return v < 3 ? v : (v - 3) / 7; };
  auto ldW = [&](int v) {
    const int row = v < 24 ? uj(v) * 112 + us(v) * 16 + (g4 & 1) * 8 + (g4 >> 1) * 56 : ((((v + c) & 7) * 2 + hh) * 4 + g4) * 56 + 48;
    __builtin_amdgcn_raw_ptr_buffer_load_lds(rW, LDSP(lds0 + dn_wpos(v, 0) + hh * 4096 + g4 * 1024), 16, wsrc, row * (D_ * 2) + c * 256 + (((v < 24 ? hh : 0) ^ (q >> 1)) << 7), 0, 0);
  };
  const int fa_off = F_ACT + (panel * 4 + (wave >> 1)) * 512;
  if constexpr (MODE == 1) {
    if constexpr (U < 32) { if constexpr (U < DN_PRE) ldW(U); }
    else if constexpr (U >= 64) ldW((U - 64) >> 1);
    return;
  }
  const Tag tg = {(unsigned)__builtin_amdgcn_readfirstlane(late.tag[0]), (unsigned)__builtin_amdgcn_readfirstlane(late.tag[1])};
  const int fr = lane & 15, fq = lane >> 4, wq = (lane >> 2) & 3, wp = lane & 3;
  const int rb = wave * 2 + (q & 1);
  f32x4 acc[8][2];
#pragma unroll
  for (int n = 0; n < 8; ++n) { acc[n][0] = (f32x4){0.f, 0.f, 0.f, 0.f}; acc[n][1] = acc[n][0]; }
  unsigned wa[3][4];
#pragma unroll
  for (int i = 0; i < 4; ++i) { const int row = 8 * fq + wq; wa[0][i] = lds0 + row * 128 + (((i << 1) ^ wswz(row)) << 4) + wp * 8; wa[1][i] = wa[0][i] + 65536; wa[2][i] = wa[0][i] + 131072; }
  const auto rX = RSRCN(a.X, (KN_ABL & 2) ? 0 : 0x7fffffff);
  const unsigned xs[2] = {(unsigned)((panel * 16 + rb) * RBA * 256 + lane * 16), (unsigned)((panel * 16 + (rb ^ 1)) * RBA * 256 + lane * 16)};
  FragD fa, fb;
  u32x4 xr[32][2];
  auto rdw = [&](FragD& f, auto vc, auto rc) {
    constexpr int v = decltype(vc)::value, r = decltype(rc)::value, n = r >> 1, u = dn_unit(v, n);
    constexpr int p = dn_wpos(u, (v < DN_A || v >= BB) ? n >> 2 : (v < DN_B ? 1 : 0)), o = (p & 65535) + (r & 1) * 512, b = p >> 16;
    if constexpr (r & 1) f.hi[n] = ds_tr<o>(wa[b][n & 3]); else f.lo[n] = ds_tr<o>(wa[b][n & 3]);
  };
  auto ldX = [&](int v, int m) {
    if (v < 24) xr[v][m] = __builtin_amdgcn_raw_buffer_load_b128(rX, xs[m], uj(v) * 3072 + us(v) * 1024, 16);
    else xr[v][m] = __builtin_amdgcn_raw_buffer_load_b128(rX, xs[0] + m * 1024, 384 * 256 + ((v + c) & 7) * 2048, 16);
  };
  const auto rS = RSRC(a.slab);
  const int ck = (fq & 1) * 2 + (fq >> 1);
  u32x4 pp[3][2], res[2];
  auto pick = [&](bool on, int e) { const unsigned v = on ? ((e & 1) ? 0x3f800000u : 0x3f80u) : 0u; const int d = e >> 1;
    return __builtin_bit_cast(bf16x8, (u32x4){d == 0 ? v : 0u, d == 1 ? v : 0u, d == 2 ? v : 0u, d == 3 ? v : 0u}); };
  bf16x8 sp0, sp1;
  auto addP = [&](int j) { acc[2 * (j & 1)][0] = MFMA(sp0, __builtin_bit_cast(bf16x8, pp[j >> 1][j & 1]), acc[2 * (j & 1)][0]); acc[2 * (j & 1) + 1][0] = MFMA(sp1, __builtin_bit_cast(bf16x8, pp[j >> 1][j & 1]), acc[2 * (j & 1) + 1][0]); };
  auto ldP = [&](int j, bool ok) { pp[j >> 1][j & 1] = __builtin_amdgcn_raw_buffer_load_b128(RSRCN(a.slab, ok ? 0x7fffffff : 0), lane * 16 + (j & 1) * 1024, ((bid * 8 + wave) * 3 + (j >> 1)) * 2048, 16); };
  auto st128 = [&](u32x4 v, auto rs, unsigned vo, int so) {
    __builtin_amdgcn_raw_buffer_store_b128(v, rs, vo, so, 16);
    __builtin_amdgcn_sched_barrier(0);
    asm volatile("s_nop 1" ::: "memory");
    __builtin_amdgcn_sched_barrier(0); };
  auto stP = [&](int j) {
    const int k = 3 - (j >> 1), i = j & 1;
    const f32x4 s0 = acc[(k >> 1) * 4 + 2 * i][k & 1], s1 = acc[(k >> 1) * 4 + 2 * i + 1][k & 1];
    st128((u32x4){pk_bf16(s0[0], s0[1]), pk_bf16(s0[2], s0[3]), pk_bf16(s1[0], s1[1]), pk_bf16(s1[2], s1[3])}, rS, lane * 16 + i * 1024, (((bid ^ k) * 8 + wave) * 3 + (j >> 1)) * 2048); };
  const int fp_off = F_PART + (bid * 8 + wave) * 64;
  unsigned got = 0;
  unsigned orow = 0;
  u32x2 fl = {0u, 0u};
#define TS5(i)
  auto op = [&](auto ic) {
    constexpr int i = decltype(ic)::value, kind = DN_OPS[i][2], arg = DN_OPS[i][3];
    if constexpr (kind == O_X) ldX(arg >> 1, arg & 1);
    if constexpr (kind == O_W) ldW(arg);
    if constexpr (kind == O_SP) stP(arg);
    if constexpr (kind == O_FA) { wait_vm<DT.wait[i]>(); flag_set(a.sync, F_ACT + ((panel * 4 + (wave >> 1)) * 64 + (q * 8 + c) * 2 + (wave & 1)) * 8 + (arg - 1) * 16384, tg, 0); }
    if constexpr (kind == O_FP) { wait_vm<DT.wait[i]>(); if constexpr (arg) STAMP(10 + arg); flag_set(a.sync, F_PART + ((bid ^ (3 - arg)) * 8 + wave) * 64 + arg * 8, tg, 0); }
    if constexpr (kind == O_QC) fl = flag_get(a.sync, fa_off + q * 128 + (arg - 1) * 16384, 16);
    if constexpr (kind == O_QD) fl = flag_get(a.sync, fa_off + 16384, 64);
    if constexpr (kind == O_QP) fl = flag_get(a.sync, fp_off, 3);
    if constexpr (kind == O_RES) res[arg] = __builtin_amdgcn_raw_buffer_load_b128(RSRC(a.R), lane * 16 + arg * 1024, ((panel * 16 + rb) * RBH + c * 16 + (q >> 1) * 8) * 256, 16);
    if constexpr (kind == O_AR) {
      const bf16x8 s0 = pick(fq == (fr >> 3), fr & 7), s1 = pick(fq == 2 + (fr >> 3), fr & 7);
#pragma unroll
      for (int n = 0; n < 4; ++n) acc[n][0] = MFMA((n & 1) ? s1 : s0, __builtin_bit_cast(bf16x8, res[n >> 1]), acc[n][0]);
      sp0 = pick(fq == (fr >> 2), fr & 3); sp1 = pick(fq == (fr >> 2), 4 + (fr & 3));
      orow = (unsigned)(((panel * 16 + rb) * 16 + fr) * (D_ * 2) + c * 256 + (q >> 1) * 128 + ck * 16);
      asm volatile("" : "+v"(sp0), "+v"(sp1), "+v"(orow));
    }
    if constexpr (kind == O_AP) addP(arg);
    if constexpr (kind == O_LP) ldP(arg, (got >> (arg >> 1)) & 1);
    if constexpr (kind == O_CC && arg == 1) {
      wait_vm<DT.wait[i]>();
      f32x2 f2 = ds_b64<0>(lds0 + FLP + wave * 128 + (lane & 15) * 8);
      asm volatile("s_waitcnt lgkmcnt(0)" : "+v"(f2) :: "memory");
      fl = __builtin_bit_cast(u32x2, f2);
    }
    if constexpr (kind == O_CC) { if (__builtin_expect(!flags_up(fl, 16, tg, 0), 0)) wait_flags(lds0, a.sync, fa_off + q * 128 + (arg - 1) * 16384, 16, 0); if constexpr (arg == 1) STAMP(4); }
    if constexpr (kind == O_CD) { if (__builtin_expect(!flags_up(fl, 64, tg, 0), 0)) wait_flags(lds0, a.sync, fa_off + 16384, 64, 0); }
    if constexpr (kind == O_CP) got |= (unsigned)__builtin_amdgcn_ballot_w64(fl[0] == tg.lo && fl[1] == tg.hi) & (1u << arg) & (got + 1u);
    if constexpr (kind == O_BAR) { if constexpr (!(KN_ABL & 4)) { wait_vm<DT.wait[i]>(); lds_barrier(); } }
    __builtin_amdgcn_sched_barrier(0);
  };
  STAMP(9);
  lds_barrier();
  STAMP(6);
  {
    const unsigned e0 = lds0 + rb * 1024 + lane * 16, e1 = lds0 + (rb ^ 1) * 1024 + lane * 16;
    xr[0][0] = __builtin_bit_cast(u32x4, ds_b128<OWNE>(e0)); xr[0][1] = __builtin_bit_cast(u32x4, ds_b128<OWNE>(e1));
    xr[1][0] = __builtin_bit_cast(u32x4, ds_b128<OWNE + 16384>(e0)); xr[1][1] = __builtin_bit_cast(u32x4, ds_b128<OWNE + 16384>(e1));
    xr[2][0] = __builtin_bit_cast(u32x4, ds_b128<0>(e0 + OWNM)); xr[2][1] = __builtin_bit_cast(u32x4, ds_b128<0>(e1 + OWNM));
    static_for<0, 16>([&](auto rc) { rdw(fa, IC<0>{}, rc); });
  }
  static_for<0, NS>([&](auto vc) {
    constexpr int v = decltype(vc)::value, NM = dn_nm(v);
    FragD& cur = (v & 1) ? fb : fa; FragD& nxt = (v & 1) ? fa : fb;
    TS5(v);
    if constexpr (KN_PRI == 1) __builtin_amdgcn_s_setprio(3 - (v & 3));
    if constexpr (KN_PRI == 2) { if (wave >= 4) __builtin_amdgcn_s_setprio(v & 1); else __builtin_amdgcn_s_setprio(1 - (v & 1)); }
    if constexpr (KN_PRI == 3) { if constexpr (!(v & 1)) __builtin_amdgcn_s_setprio(3 - ((v >> 1) & 3)); }
    static_for<DT.first[v][0], DT.first[v][1]>(op);
    __builtin_amdgcn_sched_barrier(0);
    static_for<0, NM>([&](auto ic) {
      constexpr int I = decltype(ic)::value, n = NM == 16 ? I >> 1 : I, m = NM == 16 ? I & 1 : (v < 24 ? 1 : (v < BB ? 0 : I >> 2));
      if constexpr (I % (NM / 4) == 0) {
        constexpr int left = 12 - 2 * n + DT.rd[v][I];
        asm volatile("s_waitcnt lgkmcnt(%0)" :: "n"(left < 15 ? left : 15) : "memory");
        asm volatile("" : "+v"(cur.lo[n]), "+v"(cur.hi[n]), "+v"(cur.lo[n + 1]), "+v"(cur.hi[n + 1]));
        if constexpr (v == 0 && I == 0) {
#pragma unroll
          for (int i = 0; i < 3; ++i) asm volatile("" : "+v"(xr[i][0]), "+v"(xr[i][1]));
          STAMP(1);
        }
        __builtin_amdgcn_sched_barrier(0);
      }
      const bf16x8 w = __builtin_shufflevector(cur.lo[n], cur.hi[n], 0, 1, 2, 3, 4, 5, 6, 7);
      constexpr int an = v < DN_A ? n : (v < DN_B ? 4 : 0) + (n & 3), am = v < 24 ? m : 0;
      acc[an][am] = MFMA(w, __builtin_bit_cast(bf16x8, xr[dn_unit(v, n)][m]), acc[an][am]);
      __builtin_amdgcn_sched_barrier(0);
      static_for<DT.first[v][I + 1], DT.first[v][I + 2]>(op);
      static_for<DT.rd[v][I], DT.rd[v][I + 1]>([&](auto rc) { rdw(nxt, IC<v + 1>{}, rc); });
      __builtin_amdgcn_sched_barrier(0);
    });
    if constexpr (v == 23) STAMP(10);
  });
  STAMP(2);
  TS5(NS);
  const auto rF = RSRCN((const char*)a.sync + fp_off, got != 7u ? 24 : 0);
  u32x2 f0 = __builtin_amdgcn_raw_buffer_load_b64(rF, lane * 8, 0, 16);
  __builtin_amdgcn_sched_barrier(0);
  static_for<DT.first[NS][0], DN_N>(op);
  if (got != 7u) {
    if (!flags_up(f0, 3, tg, 0)) {
      f0 = __builtin_amdgcn_raw_buffer_load_b64(rF, lane * 8, 0, 16);
      asm volatile("s_sleep %0" :: "n"(KN_SLP) : "memory");
      for (unsigned polls = 1;; ++polls) {
        const u32x2 f1 = __builtin_amdgcn_raw_buffer_load_b64(rF, lane * 8, 0, 16);
        asm volatile("" ::: "memory");
        if (flags_up(f0, 3, tg, 0)) break;
        f0 = __builtin_amdgcn_raw_buffer_load_b64(rF, lane * 8, 0, 16);
        asm volatile("" ::: "memory");
        if (flags_up(f1, 3, tg, 0) || (!(polls & 255u) && !in_time(lds0))) break;
      }
    }
#pragma unroll
    for (int j = 5; j >= 0; --j) ldP(j, !((got >> (j >> 1)) & 1));
    __builtin_amdgcn_sched_barrier(0);
#pragma unroll
    for (int j = 0; j < 6; ++j) addP(j);
  }
  TS5(NS + 1);
  STAMP(5);
#define KN_OAUX 0
#pragma unroll
  for (int i = 0; i < 2; ++i) {
    const f32x4 u = acc[2 * i][0], w = acc[2 * i + 1][0];
    const auto s0 = __builtin_amdgcn_permlane16_swap(pk_bf16(u[0], u[1]), pk_bf16(w[0], w[1]), false, false);
    const auto s1 = __builtin_amdgcn_permlane16_swap(pk_bf16(u[2], u[3]), pk_bf16(w[2], w[3]), false, false);
    __builtin_amdgcn_raw_buffer_store_b128((u32x4){s0[0], s1[0], s0[1], s1[1]}, RSRC(a.O), orow + i * 64, 0, KN_OAUX);
  }
  if (bid == 0 && wave == 0) flag_set(a.sync, EPOCH, tg, 0);
  STAMP(7);
  STAMP(8);
  STAMP(3);
#endif
}

struct Ptrs { const bf16_t *x, *wqkv, *wo, *wg, *wu, *wd; bf16_t* out; char* ws; };
#define DBG(i)
#define WS_O ((bf16_t*)(p.ws + (8u << 20)))
#define WS_SL ((float*)(p.ws + (42u << 20)))
#define WS_H ((bf16_t*)(p.ws + (16u << 20)))
#define WS_ST ((float*)(p.ws + (24u << 20)))
#define WS_CS ((float*)(p.ws + (25u << 20)))
#define WS_SY ((unsigned*)(p.ws + (26u << 20)))
#define WS_A ((bf16_t*)(p.ws + (28u << 20)))
#if defined(__HIP_DEVICE_COMPILE__)
#define ARG_WARM(v) do { asm volatile("s_load_dword %0, %1, 0x0" : "=s"(v) : "s"(__builtin_amdgcn_kernarg_segment_ptr())); __builtin_amdgcn_sched_barrier(0); } while (0)
#else
#define ARG_WARM(v)
#endif
struct Args4 { GemmArgs q, b, c, d; };
extern "C" __global__ __launch_bounds__(512, 1) void k_all(Ptrs p) {
  unsigned warm = 0;
  ARG_WARM(warm);
#define PAD(k)
  const Args4 a{GemmArgs{p.x, p.wqkv, nullptr, nullptr, WS_O, (float*)WS_H, p.wg, p.wu, WS_CS, p.wd, p.wo, WS_SY, (float*)WS_A, warm DBG(0)},
    GemmArgs{WS_O, p.wo, nullptr, p.x, WS_H, WS_ST, nullptr, nullptr, nullptr, p.wd, nullptr, WS_SY, nullptr, 0u DBG(1)},
    GemmArgs{WS_H, p.wg, p.wu, p.out, WS_A, WS_ST, nullptr, nullptr, WS_CS, p.wd, nullptr, WS_SY, WS_SL, 0u DBG(2)},
    GemmArgs{WS_A, p.wd, nullptr, WS_H, p.out, nullptr, nullptr, nullptr, nullptr, nullptr, nullptr, WS_SY, WS_SL, 0u DBG(3)}};
  k1_phase(a.q, [&](auto md, auto lo, auto hi) { wo_phase<decltype(md)::value, decltype(lo)::value, decltype(hi)::value>(a.b, a.c); });
  PAD(1);
  wo_phase<2, 0, 0>(a.b, a.c);
  PAD(2);
  Late late;
  glu_phase(a.c, [&](auto tc, unsigned w) { dn_phase<1, decltype(tc)::value>(a.d, late, w); }, late);
  PAD(3);
  dn_phase<2, 0>(a.d, late);
  PAD(4);
}

extern "C" __global__ void k_zend() {}

extern "C" void kernel_launch(void* const* d_in, const int* in_sizes, int n_in, void* d_out, int out_size, void* d_ws,
                              size_t ws_size, hipStream_t stream) {
  const Ptrs p{(const bf16_t*)d_in[0], (const bf16_t*)d_in[1], (const bf16_t*)d_in[2], (const bf16_t*)d_in[4], (const bf16_t*)d_in[5], (const bf16_t*)d_in[6], (bf16_t*)d_out, (char*)d_ws};
  constexpr int L_all = SNAP + 64;
  static_assert(SNAP >= 3 * 256 * 128 + 4 * 14592 + 4096 + 1024 + 256 + 512 + 128 + 64 && L_all <= 163840, "LDS");
  (void)hipFuncSetAttribute((const void*)k_all, hipFuncAttributeMaxDynamicSharedMemorySize, L_all);
  k_all<<<256, 512, L_all, stream>>>(p);
}
```

```cpp
#include <hip/hip_runtime.h>
#include <hip/hip_bf16.h>
#include <stdint.h>
#include <type_traits>

typedef unsigned short bf16_t;
typedef short bf16x8 __attribute__((ext_vector_type(8)));
typedef short s16x4 __attribute__((ext_vector_type(4)));
typedef float f32x4 __attribute__((ext_vector_type(4)));
typedef float f32x2 __attribute__((ext_vector_type(2)));
typedef unsigned u32x2 __attribute__((ext_vector_type(2)));
typedef unsigned u32x4 __attribute__((ext_vector_type(4)));
typedef __bf16 bf16x2_t __attribute__((ext_vector_type(2)));
typedef __attribute__((address_space(3))) void* lds_void_ptr;
typedef __attribute__((address_space(3))) char* lds_char_ptr;

#define KN_NT 0
constexpr int S_ = 2048, D_ = 1024, DF_ = 3584;
#define KN_NR 31
constexpr int SO = (KN_NR & 1) ? 65536 : 32768, SH = (KN_NR & 2) ? 65536 : 32768, SA = (KN_NR & 4) ? 262144 : DF_ * 32;
__device__ __forceinline__ int rb_o(int rb, int pol) { return (KN_NR & 1) ? (rb & 63) * SO + ((((rb >> 6) ^ (rb >> 5) ^ pol) & 1) << 13) : rb * SO; }
__device__ __forceinline__ int rb_h(int rb, int pol) { return (KN_NR & 2) ? (rb & 63) * SH + ((((rb >> 6) ^ (rb >> 5) ^ pol) & 1) << 13) : rb * SH; }
__device__ __forceinline__ int rb_a(int rb, int pol) { return (KN_NR & 4) ? (rb & 63) * SA + ((((rb >> 6) ^ (rb >> 3) ^ pol) & 1) << 13) : rb * SA; }
template <int B> __device__ __forceinline__ int ko(int off) { return (KN_NR & B) ? off + (off & ~8191) : off; }
#define KN_Z 1
__device__ __forceinline__ int za(int rb) { return !(KN_NR & 4) ? 0 : (KN_Z == 1 ? ((rb >> 1) & 15) << 14 : (KN_Z == 2 ? (((rb >> 1) & 3) << 11) | (((rb >> 3) & 7) << 14) : (KN_Z == 3 ? (((rb >> 1) & 1) << 12) | (((rb >> 2) & 15) << 14) : 0))); }
__device__ __forceinline__ int sl_at(int bid, int wave, int k, int pol) {
  const int l = ((((bid >> 3) * 4 + (bid & 3)) * 8 + wave) * 3 + k) * 2048;
  return (KN_NR & 8) ? l + (l & ~8191) + ((((bid >> 2) ^ (l >> 20) ^ pol) & 1) << 13) : ((bid * 8 + wave) * 3 + k) * 2048;
}

__device__ __forceinline__ float bflo(unsigned v) { return __uint_as_float(v << 16); }
__device__ __forceinline__ float bfhi(unsigned v) { return __uint_as_float(v & 0xffff0000u); }
__device__ __forceinline__ unsigned pk_bf16(float lo, float hi) {
  f32x2 v = {lo, hi};
  bf16x2_t b = __builtin_convertvector(v, bf16x2_t);
  return __builtin_bit_cast(unsigned, b);
}
__device__ __forceinline__ int wswz(int row) { return (((row >> 1) & 1) | (((row >> 3) & 1) << 1)) << 1; }

template <int OFF> __device__ __forceinline__ s16x4 ds_tr(unsigned addr) {
  s16x4 r; asm volatile("ds_read_b64_tr_b16 %0, %1 offset:%2" : "=v"(r) : "v"(addr), "n"(OFF)); return r;
}
template <int OFF> __device__ __forceinline__ bf16x8 ds_b128(unsigned addr) {
  bf16x8 r; asm volatile("ds_read_b128 %0, %1 offset:%2" : "=v"(r) : "v"(addr), "n"(OFF)); return r;
}
template <int OFF> __device__ __forceinline__ f32x2 ds_b64(unsigned addr) {
  f32x2 r; asm volatile("ds_read_b64 %0, %1 offset:%2" : "=v"(r) : "v"(addr), "n"(OFF)); return r;
}
__device__ __forceinline__ float sum8(float v) {
  v += __int_as_float(__builtin_amdgcn_update_dpp(0, __float_as_int(v), 0x128, 0xf, 0xf, false));
  auto a = __builtin_amdgcn_permlane16_swap(__float_as_uint(v), __float_as_uint(v), false, false);
  v = __uint_as_float(a[0]) + __uint_as_float(a[1]);
  auto b = __builtin_amdgcn_permlane32_swap(__float_as_uint(v), __float_as_uint(v), false, false);
  return __uint_as_float(b[0]) + __uint_as_float(b[1]);
}
__device__ __forceinline__ float sum8l(float v) {
  v += __int_as_float(__builtin_amdgcn_update_dpp(0, __float_as_int(v), 0xb1, 0xf, 0xf, false));
  v += __int_as_float(__builtin_amdgcn_update_dpp(0, __float_as_int(v), 0x4e, 0xf, 0xf, false));
  return v + __int_as_float(__builtin_amdgcn_update_dpp(0, __float_as_int(v), 0x141, 0xf, 0xf, false));
}
template <int OFF> __device__ __forceinline__ void ds_w128(unsigned addr, u32x4 v) { asm volatile("ds_write_b128 %0, %1 offset:%2" :: "v"(addr), "v"(v), "n"(OFF) : "memory"); }
__device__ __forceinline__ int ds_r32(unsigned addr) { int r; asm volatile("ds_read_b32 %0, %1" : "=v"(r) : "v"(addr)); return r; }
__device__ __forceinline__ void ds_w32(unsigned addr, int v) { asm volatile("ds_write_b32 %0, %1" :: "v"(addr), "v"(v) : "memory"); }
template <int N> __device__ __forceinline__ void wait_vm() { asm volatile("s_waitcnt vmcnt(%0)" ::"n"(N) : "memory"); }
__device__ __forceinline__ void raw_barrier() { asm volatile("" ::: "memory"); __builtin_amdgcn_s_barrier(); asm volatile("" ::: "memory"); }
__device__ __forceinline__ void lds_barrier() { asm volatile("s_waitcnt lgkmcnt(0)" ::: "memory"); __builtin_amdgcn_s_barrier(); asm volatile("" ::: "memory"); }

template <int I, int N, class F> __device__ __forceinline__ void static_for(F&& f) {
  if constexpr (I < N) { f(std::integral_constant<int, I>{}); static_for<I + 1, N>(f); }
}
template <int V> using IC = std::integral_constant<int, V>;
constexpr int first_at(int g, int r0, int num, int den, int n) { int i = 0; while (i < n && r0 + i * num / den < g) ++i; return i; }
#define RSRC(p) __builtin_amdgcn_make_buffer_rsrc((void*)(p), 0, 0x7fffffff, 0x00020000)
#define RSRCN(p, n) __builtin_amdgcn_make_buffer_rsrc((void*)(p), 0, (n), 0x00020000)
#define LDSP(a) ((lds_void_ptr)(size_t)(a))
#define MFMA(a, b, c) __builtin_amdgcn_mfma_f32_16x16x32_bf16(a, b, c, 0, 0, 0)
#define CODE_ADDR(v, sym) asm volatile("s_getpc_b64 s[40:41]\n\ts_add_u32 s40, s40, " sym "@rel32@lo+4\n\ts_addc_u32 s41, s41, " sym "@rel32@hi+12\n\ts_mov_b64 %0, s[40:41]" : "=s"(v) :: "s40", "s41", "scc")
#define MERGED 1
constexpr int SNAP = 162752;
constexpr int STAB1 = 786432;
constexpr int EPOCH = 0, F_POL = 128, PROBE = 49152;
#define F_SIDE(g) ((((g) ^ 1 ^ pol) & 1) << 13)
#define F_PAN(p) ((KN_NR & 16) ? ((p) & 3) * 256 + F_SIDE((p) >> 2) : (p) * 256)
constexpr int F_MARK = 2048, F_H = 4096, F_ROW = 6144, F_COL = 16384;
#define F_O(t) (32768 + ((t) & 7) * 1024 + F_SIDE((t) >> 3))
#define KN_RM 3
#define WO_ROT (((xcd & 3) * 4) ^ (idx & KN_RM))
#define KN_G2 2
#define KN_EV 2
#define KN_WA 6
#define KN_WE 10

#define F_ACT(p) (65536 + ((KN_NR & 16) ? ((p) & 3) * 2048 + F_SIDE((p) >> 2) : (p) * 2048))
#define F_PART(b) (131072 + ((KN_NR & 16) ? ((b) >> 5) * 16384 + ((((b) >> 3) & 3) * 4 + ((b) & 3)) * 512 + F_SIDE((b) >> 2) : (b) * 512))
constexpr int KN_TRY = 6;
struct Tag { unsigned lo, hi; };
__device__ __forceinline__ Tag tag_get(unsigned lds0) {
  f32x2 v = ds_b64<0>(lds0 + SNAP);
  asm volatile("s_waitcnt lgkmcnt(0)" : "+v"(v) :: "memory");
  return Tag{(unsigned)__builtin_amdgcn_readfirstlane(__float_as_int(v[0])) + 8u, (unsigned)__builtin_amdgcn_readfirstlane(__float_as_int(v[1]))};
}
__device__ __forceinline__ bool in_time(unsigned lds0) {
  int v = ds_r32(lds0 + SNAP + 28);
  asm volatile("s_waitcnt lgkmcnt(0)" : "+v"(v) :: "memory");
  return (unsigned)__builtin_amdgcn_s_memrealtime() - (unsigned)__builtin_amdgcn_readfirstlane(v) < 20000000u;
}
__device__ __forceinline__ int lane_id() { int l; asm volatile("v_mbcnt_lo_u32_b32 %0, -1, 0\n\tv_mbcnt_hi_u32_b32 %0, -1, %0" : "=v"(l)); return l; }
#if defined(__HIP_DEVICE_COMPILE__)
__device__ __forceinline__ void flag_set(const unsigned* sync, int off, Tag t, unsigned state) {
  __builtin_amdgcn_raw_buffer_store_b64((u32x2){t.lo + state, t.hi}, RSRCN((const char*)sync + off, 8), lane_id() * 8u, 0, 16);
}
__device__ __forceinline__ u32x2 flag_get(const unsigned* sync, int off, int n) { return __builtin_amdgcn_raw_buffer_load_b64(RSRCN((const char*)sync + off, n * 8), lane_id() * 8u, 0, 16); }
__device__ __forceinline__ bool flags_up(u32x2 v, int n, Tag t, unsigned state) { return (__builtin_amdgcn_ballot_w64(v[0] - t.lo - state > 7u - state || v[1] != t.hi) & (~0ull >> (64 - n))) == 0; }
__device__ __forceinline__ bool words_up(unsigned v, Tag t) { return ((__builtin_amdgcn_ballot_w64(v != t.lo) & 0x5555555555555555ull) | (__builtin_amdgcn_ballot_w64(v != t.hi) & 0xaaaaaaaaaaaaaaaaull)) == 0; }
__device__ __forceinline__ void wait_flags(unsigned lds0, const unsigned* sync, int off, int n, unsigned state) {
  const Tag t = tag_get(lds0);
#define KN_WS2 8
  while (!flags_up(flag_get(sync, off, n), n, t, state) && in_time(lds0)) __builtin_amdgcn_s_sleep(KN_WS2);
}
#endif
__device__ __forceinline__ void lds_wait8(unsigned addr, int n = 8) {
  int dv; unsigned polls = 0;
  do { dv = ds_r32(addr); asm volatile("s_waitcnt lgkmcnt(0)" : "+v"(dv) :: "memory"); } while (__builtin_amdgcn_readfirstlane(dv) < n && ++polls < 40000000u);
}
template <int N> __device__ __forceinline__ bool marked(unsigned addr) {
  bf16x8 v[N];
  static_for<0, N>([&](auto ic) { constexpr int i = decltype(ic)::value; v[i] = ds_b128<i * 4096>(addr); });
  asm volatile("s_waitcnt lgkmcnt(0)" ::: "memory");
  unsigned m = 0;
#pragma unroll
  for (int i = 0; i < N; ++i) {
    asm volatile("" : "+v"(v[i]));
    const u32x4 u = __builtin_bit_cast(u32x4, v[i]);
    m = max(max(m, u[0]), u[1]); m = max(max(m, u[2]), u[3]);
  }
  return __builtin_amdgcn_ballot_w64(m == 0xffffffffu) != 0;
}

struct GemmArgs {
  const bf16_t* X;
  const bf16_t* W0;
  const bf16_t* W1;
  const bf16_t* R;
  bf16_t* O;
  float* st;
  const bf16_t* CW0;
  const bf16_t* CW1;
  float* cs;
  const bf16_t* PF0;
  const bf16_t* PF1;
  unsigned* sync;
  float* slab;
  unsigned warm;
};
#define STAMP(i)
#define STAMPW(i, w)
#define TS6(i)
#define TS4(i)
__device__ __forceinline__ bf16x8 pick(bool on, int e) {
  const unsigned v = on ? ((e & 1) ? 0x3f800000u : 0x3f80u) : 0u; const int d = e >> 1;
  return __builtin_bit_cast(bf16x8, (u32x4){d == 0 ? v : 0u, d == 1 ? v : 0u, d == 2 ? v : 0u, d == 3 ? v : 0u});
}
template <int MF, int NF> struct Frags { bf16x8 x[MF]; s16x4 lo[NF], hi[NF]; };

__device__ __forceinline__ unsigned dn_wsrc(int lane, int wave) { return (unsigned)((lane >> 3) * (D_ * 2) + (((lane & 7) ^ wswz((wave & 3) * 8 + (lane >> 3))) << 4)); }
#define KN_QP 2
constexpr int FLP = 155648;
constexpr int OWNE = 0, OWNM = 98304;
#define KN_PA 0x0123
#define KN_PB 0x0123
constexpr int wo_pos(int s) { return s < 3 ? 18432 + s * 16384 : (s == 3 ? 129024 : (s == 4 ? 0 : 71424 + (s - 5) * 16384)); }
template <class HK>
__device__ __forceinline__ void k1_phase(const GemmArgs& a, HK hook, int& pol) {
#if defined(__HIP_DEVICE_COMPILE__)
  constexpr int K = D_, LDW = 3 * D_, WN = 4;
  constexpr int HR = 16, STRIDE = 128;
  constexpr int XT = 144 * 128, WU = 24576;
  constexpr int XBASE = 0, WBASE = 3 * XT, RING = WBASE + 4 * WU;
  constexpr int MF = 4, NF = 3, MFX = 5;
  constexpr int NT = 16, NXI = 18, NLW = 4, XI = 5, XIMIN = 4, WI = 6;
  extern __shared__ __attribute__((aligned(16))) char smem[];
  STAMP(0);
  const unsigned lds0 = (unsigned)(size_t)(lds_char_ptr)smem;
  const int tid = threadIdx.x, lane = tid & 63;
  const int wave = __builtin_amdgcn_readfirstlane(tid >> 6);
  const int wm = wave / WN, wn = wave % WN;
  const int bid = blockIdx.x, xcd = bid & 7, idx = bid >> 3;
  const int tm = (xcd / 4) * 8 + idx / 4, tn = (xcd % 4) * 4 + idx % 4;
  const int m0 = tm * 128;
  const int fr = lane & 15, fq = lane >> 4;
  const int wq = (lane >> 2) & 3, wp = lane & 3;
  constexpr int TCH = RING + 2304;
  auto touch = [&](const void* base, unsigned voff, unsigned soff, unsigned lim = 0x7fffffffu) {
    __builtin_amdgcn_raw_ptr_buffer_load_lds(RSRCN(base, lim), LDSP(lds0 + TCH), 4, voff, soff, 0, 0);
  };
  const auto rO = RSRC(a.O);

  unsigned xsrc[XI], wsrc[WI];
  {
    const int w4 = wave & 3, lr = lane >> 3;
    const unsigned xv = (unsigned)(lr * (K * 2) + (((lane & 7) ^ ((w4 * 4 + (lr >> 1)) & 7)) << 4));
    const int r0 = m0 - HR + w4 * 8;
#pragma unroll
    for (int i = 0; i < XI; ++i) xsrc[i] = xv + (unsigned)((i == 0 ? (r0 < 0 ? 0 : r0) : r0 + 32 * (i < 4 ? i : 4)) * (K * 2));
    const unsigned wv = (unsigned)((w4 * 8 + lr) * (LDW * 2) + tn * 128 + (((lane & 7) ^ wswz(w4 * 8 + lr)) << 4));
#pragma unroll
    for (int i = 0; i < WI; ++i) wsrc[i] = wv + (unsigned)((i & 1) * 32 * (LDW * 2) + (i >> 1) * 2048);
  }
  auto ldX = [&](int i, int tile, int slot) {
    const int j = wave + NLW * i;
    if (j >= NXI) return;
    __builtin_amdgcn_raw_ptr_buffer_load_lds(RSRC(a.X), LDSP(lds0 + XBASE + slot * XT + j * 1024), 16, xsrc[i], tile * 128, 0, (KN_NT & 1) ? 2 : 0);
  };
  auto ldW = [&](int i, int unit, int slot) {
    const int j = wave + NLW * i;
    __builtin_amdgcn_raw_ptr_buffer_load_lds(RSRC(a.W0), LDSP(lds0 + WBASE + slot * WU + j * 1024), 16, wsrc[i], unit * 64 * LDW * 2, 0, (KN_NT & 2) ? 2 : 0);
  };
  auto issueX = [&](int tile, int slot) {
    if (wave >= NLW) return;
#pragma unroll
    for (int i = 0; i < XI; ++i) ldX(i, tile, slot);
  };
  auto issueW = [&](int unit, int slot) {
    if (wave >= NLW) return;
#pragma unroll
    for (int i = 0; i < WI; ++i) ldW(i, unit, slot);
  };

  f32x4 acc[NF][MF];
#pragma unroll
  for (int n = 0; n < NF; ++n)
#pragma unroll
    for (int m = 0; m < MF; ++m) acc[n][m] = (f32x4){0.f, 0.f, 0.f, 0.f};
  f32x4 acch[NF];
#pragma unroll
  for (int n = 0; n < NF; ++n) acch[n] = (f32x4){0.f, 0.f, 0.f, 0.f};
  constexpr int ONES = RING;

  unsigned xa0, xa1, xh0, xh1;
  { const unsigned o = fr * 128 + ((fq ^ ((fr >> 1) & 7)) << 4), mo = (HR + wm * MF * 16) * 128;
    xa0 = lds0 + XBASE + mo + o; xa1 = lds0 + XBASE + mo + (o ^ 64); xh0 = lds0 + XBASE + o; xh1 = lds0 + XBASE + (o ^ 64); }
  unsigned wa;
  { const int row = 8 * fq + wq; wa = lds0 + WBASE + row * STRIDE + (((wn << 1) ^ wswz(row)) << 4) + wp * 8; }

  using I0 = IC<0>; using I1 = IC<1>;
  using T = std::true_type; using F = std::false_type;
  typedef Frags<MFX, NF> FR;
  struct RdCtx { unsigned xa, xh, wb; };
  auto mkctx = [&](bool ks1, unsigned xoffs, unsigned woffs) {
    RdCtx c; c.xa = (ks1 ? xa1 : xa0) + xoffs; c.xh = fr == 0 ? lds0 + ONES : (ks1 ? xh1 : xh0) + xoffs; c.wb = wa + woffs;
    return c;
  };
  auto rd_item = [&](FR& f, const RdCtx& c, auto KSc, auto kc) {
    constexpr int KS = decltype(KSc)::value, k = decltype(kc)::value;
    if constexpr (k < MF) f.x[k] = ds_b128<k * 2048>(c.xa);
    else if constexpr (k < MFX) f.x[k] = ds_b128<0>(c.xh);
    else {
      constexpr int n = k - MFX, o = n * 8192 + KS * 32 * STRIDE;
      f.lo[n] = ds_tr<o>(c.wb); f.hi[n] = ds_tr<o + 4 * STRIDE>(c.wb);
    }
  };
  auto wait_frags = [&](FR& f, auto Hc) {
    asm volatile("s_waitcnt lgkmcnt(0)" ::: "memory");
#pragma unroll
    for (int m = 0; m < MF + decltype(Hc)::value; ++m) asm volatile("" : "+v"(f.x[m]));
#pragma unroll
    for (int n = 0; n < NF; ++n) asm volatile("" : "+v"(f.lo[n]), "+v"(f.hi[n]));
  };
  auto step1 = [&](auto Hc, FR& cur, FR& nxt, const RdCtx& c, auto KSc, auto R0c, auto RLc, auto IBc, auto&& sync, auto NLc, auto&& ld) {
    constexpr int H = decltype(Hc)::value, NL = decltype(NLc)::value, R0 = decltype(R0c)::value;
    constexpr int MH = MF + H, NM = NF * MH, NR = decltype(RLc)::value == -100 ? 0 : MH + 2 * NF;
    constexpr int IB = decltype(IBc)::value < 0 && decltype(IBc)::value > -100 ? NM + decltype(IBc)::value : decltype(IBc)::value;
    constexpr int RL = NM + decltype(RLc)::value;
    static_for<0, NM>([&](auto ic) {
      constexpr int I = decltype(ic)::value, g = I / MH, q = I % MH;
      const bf16x8 w = __builtin_shufflevector(cur.lo[g], cur.hi[g], 0, 1, 2, 3, 4, 5, 6, 7);
      if constexpr (q < MF) acc[g][q] = MFMA(w, cur.x[q], acc[g][q]);
      else acch[g] = MFMA(w, cur.x[MF], acch[g]);
      __builtin_amdgcn_sched_barrier(0);
      if constexpr (I == IB) sync();
      static_for<first_at(I, R0, RL - R0, NR - 1, NR), first_at(I + 1, R0, RL - R0, NR - 1, NR)>([&](auto rc) {
        constexpr int r = decltype(rc)::value;
        if constexpr (r < MH) rd_item(nxt, c, KSc, rc);
        else {
          constexpr int n = (r - MH) >> 1, o = n * 8192 + decltype(KSc)::value * 32 * STRIDE;
          if constexpr ((r - MH) & 1) nxt.hi[n] = ds_tr<o + 4 * STRIDE>(c.wb); else nxt.lo[n] = ds_tr<o>(c.wb);
        }
      });
#pragma unroll
      for (int i = first_at(I, R0, NM - R0, NL, NL); i < first_at(I + 1, R0, NM - R0, NL, NL); ++i) ld(i);
      __builtin_amdgcn_sched_barrier(0);
    });
    if constexpr (NR > 0) wait_frags(nxt, Hc);
  };
  FR fa, fb;
  using IM1 = IC<-1>;
  unsigned tv6 = 0;

  asm volatile("s_waitcnt lgkmcnt(0)" :: "s"(a.warm), "v"(wsrc[WI - 1]), "v"(xsrc[XI - 1]));
#define KN_T1 1
#define KN_DEF 5
  issueW(0, 0); issueX(0, 0);
  if constexpr (!KN_T1) { issueW(1, 1); issueX(1, 1); }
  u32x4 sx[4];
  constexpr int TNEW = RING + 3072;
  auto ldT = [&](int dst) {
    int lx = lane; asm volatile("" : "+v"(lx));
    const auto rT = RSRCN((const char*)a.cs + STAB1, 2048 * 16);
    const unsigned to = (unsigned)((m0 - 16) * 16 + lx * 16);
    __builtin_amdgcn_raw_ptr_buffer_load_lds(rT, LDSP(lds0 + dst), 16, to, 0, 0, 16);
    __builtin_amdgcn_raw_ptr_buffer_load_lds(rT, LDSP(lds0 + dst + 1024), 16, to + 1024u, 0, 0, 16);
    if (lx < 16) __builtin_amdgcn_raw_ptr_buffer_load_lds(rT, LDSP(lds0 + dst + 2048), 16, to + 2048u, 0, 0, 16);
  };
  auto ldS = [&]() {
    int lx = lane; asm volatile("" : "+v"(lx));
    const unsigned so = (unsigned)((m0 + tn * 8 + (wave & 3) * 2 + (lx >> 5)) * (K * 2) + (lx & 31) * 16);
#pragma unroll
    for (int i = 0; i < 4; ++i) sx[i] = __builtin_amdgcn_raw_buffer_load_b128(RSRC(a.X), so, i * 512, 0);
  };
  float mu = 0.f, rsd = 0.f;
  auto stats = [&]() {
    float sm = 0.f, sq = 0.f;
    const bf16x2_t one2 = __builtin_bit_cast(bf16x2_t, 0x3f803f80u);
#pragma unroll
    for (int i = 0; i < 4; ++i)
#pragma unroll
      for (int e = 0; e < 4; ++e) { const unsigned u = sx[i][e]; const bf16x2_t v = __builtin_bit_cast(bf16x2_t, u); sm = __builtin_amdgcn_fdot2_f32_bf16(v, one2, sm, false); sq = __builtin_amdgcn_fdot2_f32_bf16(v, v, sq, false); }
    sm = sum8l(sm); sq = sum8l(sq);
    sm += __int_as_float(__builtin_amdgcn_update_dpp(0, __float_as_int(sm), 0x128, 0xf, 0xf, false));
    sq += __int_as_float(__builtin_amdgcn_update_dpp(0, __float_as_int(sq), 0x128, 0xf, 0xf, false));
    { auto p = __builtin_amdgcn_permlane16_swap(__float_as_uint(sm), __float_as_uint(sm), false, false); sm = __uint_as_float(p[0]) + __uint_as_float(p[1]); }
    { auto p = __builtin_amdgcn_permlane16_swap(__float_as_uint(sq), __float_as_uint(sq), false, false); sq = __uint_as_float(p[0]) + __uint_as_float(p[1]); }
    mu = sm * (1.f / 1024.f);
    float var = sq * (1.f / 1024.f) - mu * mu; var = var > 0.f ? var : 0.f;
    rsd = __builtin_amdgcn_rsqf(var + 1e-5f);
  };
#define KN_SL 1
  __builtin_amdgcn_sched_barrier(0);
  if (KN_SL == -1 && wave >= NLW) { ldS(); stats(); asm volatile("" : "+v"(mu), "+v"(rsd)); }
  if (wave == 4) {
    if (lane < 8) *(unsigned*)(smem + ONES + lane * 4) = lane < 4 ? 0x3f803f80u : 0u;
  }
  auto pretouch = [&]() {
    touch(a.cs, lane < 8 ? (unsigned)((const char*)a.sync - (const char*)a.cs + bid * 1024 + lane * 128) : (lane == 8 ? (unsigned)(STAB1 + (m0 + tn * 8) * 16) : (lane < 23 ? (unsigned)(bid * 1792 + (lane - 9) * 128) : 0x80000000u)), 0);
  };
  if (wave == 5) {
    if (lane < 2) __builtin_amdgcn_raw_ptr_buffer_load_lds(RSRC(a.sync), LDSP(lds0 + SNAP), 4, lane * 4, EPOCH, 0, 16);
    ds_w32(lds0 + SNAP + 28, (int)__builtin_amdgcn_s_memrealtime());
  }
  constexpr int HQ = 5;
  const int slc = (3 - ((unsigned)(size_t)a.W0 >> 7) - (xcd & 3) * 4) & 7;
  const int hid = (idx >> 2) * 3 + (idx & 3) - ((idx & 3) > slc ? 1 : 0);
#define KN_HL 1
  const bool hlp = KN_HL && slc < 4 && (idx & 3) != slc;
  const int hj1 = hlp ? hid / 3 - HQ : -100, hj2 = hlp ? hid / 3 + 8 - HQ : -100;
  const unsigned htv = (unsigned)(lane * (LDW * 2) + (hid % 3) * 2048 + ((xcd & 3) * 4 + slc) * 128);
  if (!(KN_DEF & 2) && wave == 6 && hlp && hj1 <= 0) touch(a.W0, htv, (hid / 3) * (64 * LDW * 2));
  auto code_touch = [&]() {
    unsigned long long c0, c1; CODE_ADDR(c0, "k_all"); CODE_ADDR(c1, "k_zend");
    const unsigned len = c1 - c0 < 262144ull ? (unsigned)(c1 - c0) : 0u;
    touch((const void*)c0, (unsigned)((lane * 32 + idx) * 128), 0, len);
  };
#define KN_MP 4
#define KN_MF 3
#ifndef KN_HL
#define KN_HL 1
#endif
#define KN_OJ 0
  auto rowt = [&](int t) { touch(a.W0, lane_id() * 128u, (t * 64 + (bid & 63)) * (LDW * 2), 0x7fffffffu); };
  auto batch = [&](int j) {
    const int t = j + KN_MP;
    if (j == 0) { for (int u = KN_MF; u <= KN_MP; ++u) if ((u & 3) == (bid >> 6) && lane < 48) rowt(u); }
    else if (t < 16 && (t & 3) == (bid >> 6) && lane < 48) rowt(t);
    if (j == 5 + ((bid >> 6) + KN_OJ - KN_MP - 5 + 16) % 4 + 4 * (idx & 1)) touch(a.PF1, lane_id() * 128u, bid * 8192);
  };
  if (!(KN_DEF & 4) && wave == 4) batch(0);
  if (wave == 7) code_touch();
  const int srow = m0 + wave * 16 + fr;
  const bool par = fr & 1;
  float bias[4];
  {
    const unsigned mk = 0x5555u & ((2u << (srow < 15 ? srow : 15)) - 1u), sft = (fr & 14) - fq * 4 + 14;
    const unsigned b0 = ~(mk >> sft), b1 = ~((mk << 16) >> sft);
#pragma unroll
    for (int j = 0; j < 4; ++j) bias[j] = __uint_as_float((unsigned)__builtin_amdgcn_sbfe((int)(j < 2 ? b0 : b1), 2 - 2 * (j & 1), 1) & 0xff61b1e6u);
  }
  unsigned pl = par ? 0x01000c0cu : 0x0c0c0100u, ph = par ? 0x03020c0cu : 0x0c0c0302u;
  asm volatile("" : "+v"(bias[0]), "+v"(bias[1]), "+v"(bias[2]), "+v"(bias[3]), "+v"(pl), "+v"(ph));
  constexpr int RS = 400, QT = WBASE + 3 * WU - 144 * RS;
  static_assert(wo_pos(0) >= XT && wo_pos(1) <= 2 * XT && wo_pos(2) + 16384 <= QT && wo_pos(3) >= QT + 144 * RS && KN_WA <= 6 && KN_WE <= 10 && KN_WA <= KN_WE, "LDS");
  int qw = (16 + wm * 64 + fr) * RS + wn * 32 + fq * 8;
  unsigned ka = lds0 + QT + (wave * 16 + fr) * RS + fq * 16, va = lds0 + QT + (wave * 16 + fq * 4 + wq) * RS + 256 + wp * 16;
  asm volatile("" : "+v"(qw), "+v"(ka), "+v"(va));
#define KN_CI 2
#define KN_S1 2
  if constexpr (KN_T1) {
    asm volatile("" : "+v"(xa0), "+v"(xa1), "+v"(xh0), "+v"(xh1), "+v"(wa));
    __builtin_amdgcn_sched_barrier(0);
    issueW(1, 1); issueX(1, 1);
  }
  if (wave < NLW) wait_vm<WI + XIMIN>();
  else if (KN_SL == 0) { ldS(); stats(); asm volatile("" : "+v"(mu), "+v"(rsd)); }
  lds_barrier();
  if (wave == 5) pretouch();
  if ((KN_DEF & 2) && wave == 6 && hlp && hj1 <= 0) touch(a.W0, htv, (hid / 3) * (64 * LDW * 2));
  if ((KN_DEF & 4) && wave == 4) batch(0);
  { const RdCtx c = mkctx(false, 0, 0); static_for<0, MFX + NF>([&](auto kc) { rd_item(fa, c, I0{}, kc); }); }
  wait_frags(fa, I1{});
  STAMP(1);
  f32x4 t4[MFX], csn[NF];
  int up = 0;
  const unsigned csa = lds0 + RING + 64 + wn * 192 + fq * 16, cfa = lds0 + ONES + 16 + wn * 4;
  auto ldC = [&]() { up = ds_r32(cfa); csn[0] = __builtin_bit_cast(f32x4, ds_b128<0>(csa)); csn[1] = __builtin_bit_cast(f32x4, ds_b128<64>(csa)); csn[2] = __builtin_bit_cast(f32x4, ds_b128<128>(csa)); };
  auto pinC = [&]() { asm volatile("s_waitcnt lgkmcnt(0)" : "+v"(up), "+v"(csn[0]), "+v"(csn[1]), "+v"(csn[2]) :: "memory"); };
  auto pinT = [&](bool h) { asm volatile("s_waitcnt lgkmcnt(0)" : "+v"(t4[0]), "+v"(t4[1]), "+v"(t4[2]), "+v"(t4[3]) :: "memory"); if (h) asm volatile("" : "+v"(t4[4])); };
  auto run = [&](auto ROLEc) __attribute__((always_inline)) {
    constexpr int ROLE = decltype(ROLEc)::value;
    constexpr bool LDR = ROLE < NLW;
    using HL = IC<LDR ? 1 : 0>;
    auto nos = [&]() {};
    TS6(0);
    if constexpr (!LDR && KN_SL == 1) { ldS(); __builtin_amdgcn_sched_barrier(0); }
    { const RdCtx c = mkctx(true, 0, 0); step1(HL{}, fa, fb, c, I1{}, I0{}, IC<-4>{}, IC<-100>{}, nos, IC<LDR ? WI + XI : 0>{}, [&](int i) { if (i < WI) ldW(i, 2, 2); else ldX(i - WI, 2, 2); }); }
#define KN_WLD 2
    if constexpr (!LDR && KN_SL == 2) ldS();
    const int jr = bid == 0 ? 4 : -1;
    int slx = 1;
    if constexpr (ROLE == 5) wait_vm<(KN_SL > 0 ? 5 : 1)>();
    auto iter = [&](int j, auto LXc, auto LWc, auto WAITc, auto TIc) {
      constexpr bool LX = decltype(LXc)::value && LDR, LW = decltype(LWc)::value && LDR;
      constexpr int WAIT = decltype(WAITc)::value, TI = decltype(TIc)::value;
      TS6(j);
      constexpr int EX = (LDR && TI < 0 && KN_WLD == 3) ? WI : 0;
      constexpr int TO = 2, OL = TI <= TO ? 0 : 2, OH = (LDR && TI >= TO) ? 2 : OL;
      constexpr int NLT = EX + (LX ? XI : 0) + (LW ? WI : 0) + 2 * (OH - OL), N0 = (NLT + 1) / 2, N1 = NLT - N0;
      if constexpr (LDR) wait_vm<WAIT + (TI > 0 ? 2 * OL : 0)>();
      unsigned osrc = 0;
      if constexpr (OH > OL) { int lx = lane; asm volatile("" : "+v"(lx)); const int row = ROLE * 8 + (lx >> 3); osrc = (unsigned)(row * D_ + ((xcd & 1) * 8 + (idx & 7)) * 64 + ((lx & 7) ^ wswz(row)) * 8) * 2u; }
      if constexpr (ROLE == 6 && TI == 0) { if (j == hj1 || j == hj2) touch(a.W0, htv, (j + HQ) * (64 * LDW * 2)); }
      constexpr int S3 = 11;
      if constexpr (!LDR && TI == 0) {
        if (j == KN_S1) {
          if (KN_EV & 8) __builtin_amdgcn_s_setprio(3);
          if constexpr (KN_SL > 0) stats();
          int lx = lane; asm volatile("" : "+v"(lx));
          const Tag tg = tag_get(lds0);
          if constexpr (ROLE == 6) {
            const auto rP = RSRCN((const char*)a.sync + PROBE, bid == 0 ? 16384 : 0);
            const unsigned po = (unsigned)(((lx >> 3) & 1) * 8192 + (lx >> 4) * 2048 + (lx & 7) * 16);
            __builtin_amdgcn_raw_buffer_store_b128((u32x4){tg.lo, tg.lo, tg.lo, tg.lo}, rP, po, 0, 16);
            __builtin_amdgcn_raw_buffer_store_b128((u32x4){tg.lo, tg.lo, tg.lo, tg.lo}, rP, po + 1024u, 0, 16);
          }
          __builtin_amdgcn_raw_buffer_store_b128((u32x4){__float_as_uint(mu), tg.lo, __float_as_uint(rsd), tg.lo + tg.hi}, RSRC(a.cs),
                                                 (lx & 31) == 0 ? (unsigned)(STAB1 + (m0 + tn * 8 + (ROLE & 3) * 2 + (lx >> 5)) * 16) : 0x80000000u, 0, 16);
          __builtin_amdgcn_sched_barrier(0);
          asm volatile("s_nop 1" ::: "memory");
          if (KN_EV & 8) __builtin_amdgcn_s_setprio(0);
        }
      }
#define KN_RJ 4
#define KN_PJ 7
      if constexpr (ROLE == 6 && TI == 0) {
        if (j == jr) {
          wait_vm<0>();
          const Tag tg = tag_get(lds0);
          unsigned r0, r1, r2, r3, r4, r5, cnt;
#define KN_RS "s_nop 3"
#define RC1(x) "s_cmp_eq_u32 %" #x ", %8\n\ts_subb_u32 %6, %6, 0\n\t"
          asm volatile("s_not_b32 %1, %8\n\ts_not_b32 %2, %8\n\ts_not_b32 %3, %8\n\ts_not_b32 %4, %8\n\ts_not_b32 %5, %8\n\ts_mov_b32 %6, 3\n\t"
                       "s_load_dword %0, %7, 0\n\ts_load_dword %0, %7, 2048\n\ts_load_dword %0, %7, 5120\n\t"
                       "s_load_dword %1, %7, 8192\n\ts_load_dword %2, %7, 8192+2048\n\ts_load_dword %3, %7, 8192+4096\n\ts_load_dword %4, %7, 8192+6144\n\ts_load_dword %5, %7, 8192+1024\n\t"
                       "s_waitcnt lgkmcnt(5)\n\t" KN_RS "\n\t" RC1(1) RC1(2) RC1(3) RC1(4) RC1(5) "s_waitcnt lgkmcnt(0)"
                       : "=&s"(r0), "=&s"(r1), "=&s"(r2), "=&s"(r3), "=&s"(r4), "=&s"(r5), "=&s"(cnt)
                       : "s"((const char*)a.sync + PROBE), "s"(tg.lo) : "scc", "memory");
          flag_set(a.sync, F_POL, tg, (int)cnt > 0 ? 1u : 0u);
        }
      }
      if constexpr (ROLE == 5 && TI == 0) {
        if (j == KN_PJ) { if (lane < 2) __builtin_amdgcn_raw_ptr_buffer_load_lds(RSRC(a.sync), LDSP(lds0 + SNAP + 8), 4, lane_id() * 4, F_POL, 0, 16); }
        if (j == KN_PJ + 1) {
          wait_vm<0>();
          f32x2 pv = ds_b64<8>(lds0 + SNAP);
          const Tag tg = tag_get(lds0);
          asm volatile("" : "+v"(pv));
          u32x2 pw = __builtin_bit_cast(u32x2, pv);
          if (__builtin_expect(!flags_up(pw, 1, tg, 0), 0)) {
            do pw = flag_get(a.sync, F_POL, 1); while (!flags_up(pw, 1, tg, 0) && in_time(lds0));
            ds_w32(lds0 + SNAP + 8, __builtin_amdgcn_readfirstlane(pw[0]));
          }
          pol = (__builtin_amdgcn_readfirstlane(pw[0]) - tg.lo) & 1;
          const int mb = (xcd & 1) * 16 + (idx & 15);
          unsigned on = 0xffffffffu; asm volatile("" : "+v"(on));
          const unsigned mo = lane_id() * 16u;
#pragma unroll
          for (int i = 0; i < 3; ++i)
            __builtin_amdgcn_raw_buffer_store_b128((u32x4){on, on, on, on}, RSRC(a.st), mo + i * 1024, rb_h(((xcd >> 1) * 2 + (idx >> 4)) * 16 + (mb >> 1), pol) + ko<2>((xcd >> 2) * 16384) + (mb & 1) * 3072, 16);
          __builtin_amdgcn_sched_barrier(0);
          asm volatile("s_nop 1" ::: "memory");
        }
        if (j == KN_PJ + 3) { wait_vm<0>(); flag_set(a.sync, F_MARK + F_PAN((xcd >> 1) * 2 + (idx >> 4)) + ((xcd & 1) * 16 + (idx & 15)) * 8, tag_get(lds0), 0); }
      }
      if constexpr (ROLE == 7 && TI == 0) {
        if (j == S3) ldT(TNEW);
        if (j == S3 + 1) {
          if (KN_EV & 8) __builtin_amdgcn_s_setprio(3);
          for (;;) {
            wait_vm<0>();
            int lx = lane; asm volatile("" : "+v"(lx));
            const unsigned ad = lds0 + TNEW + lx * 16;
            u32x4 v[3];
            v[0] = __builtin_bit_cast(u32x4, ds_b128<0>(ad)); v[1] = __builtin_bit_cast(u32x4, ds_b128<1024>(ad)); v[2] = __builtin_bit_cast(u32x4, ds_b128<2048>(ad));
            const Tag tg = tag_get(lds0);
            asm volatile("" : "+v"(v[0]), "+v"(v[1]), "+v"(v[2]) :: "memory");
            auto good = [&](const u32x4& e) { return e[1] == tg.lo && e[3] == tg.lo + tg.hi; };
            const bool bad = (!good(v[0]) && (tm > 0 || lx >= 16)) || !good(v[1]) || (!good(v[2]) && lx < 16);
            if (__builtin_amdgcn_ballot_w64(bad) == 0 || !in_time(lds0)) break;
            ldT(TNEW);
          }
          if (KN_EV & 8) __builtin_amdgcn_s_setprio(0);
        }
      }
      if constexpr (ROLE == 7 && TI == 1) code_touch();
      if constexpr (ROLE == 4 && TI == 0) batch(j);
      if constexpr (ROLE >= 4 && TI == 2) {
        const bf16_t* cw = ROLE < 6 ? a.CW0 : a.CW1;
        touch(cw, lane < 56 ? (unsigned)(((ROLE & 1) * 512 + bid) * (DF_ * 2) + lane * 128) : 0x80000000u, 0);
      }
      int xp = slx + 2; if (xp >= 3) xp -= 3;
      auto ld = [&](int i) { if (i < EX) { ldW(i, j + 2, (j + 2) & 3); return; } i -= EX;
                             if (LX && i < XI) ldX(i, j + 2, xp);
                             else if constexpr (LW) ldW(i - (LX ? XI : 0), j + KN_WLD, (j + KN_WLD) & 3);
                             else { const int q = i - (LX ? XI : 0), t = OL + (q >> 1), h = q & 1;
                               __builtin_amdgcn_raw_ptr_buffer_load_lds(RSRC(a.PF1), LDSP(lds0 + wo_pos(t >> 1) + (t & 1) * 8192 + (ROLE + 4 * h) * 1024), 16,
                                                                        osrc, ((((t & 1) * 8 + (t >> 1)) ^ WO_ROT) * 2 + h) * (32 * D_ * 2), 0, 0); } };
      { const RdCtx c = mkctx(false, slx * XT, (j & 3) * WU); step1(HL{}, fb, fa, c, I0{}, IC<LDR ? 1 : 0>{}, IC<-4>{}, IC<LDR ? 1 : -100>{}, [&]() { raw_barrier(); }, IC<N0>{}, ld); }
      { const RdCtx c = mkctx(true, slx * XT, (j & 3) * WU); step1(HL{}, fa, fb, c, I1{}, I0{}, IC<LDR ? -4 : -6>{},
          IC<LDR ? -100 : -3>{}, [&]() { if constexpr (TI != 3) { asm volatile("s_waitcnt lgkmcnt(0)" ::: "memory"); raw_barrier(); } }, IC<N1>{}, [&](int i) { ld(i + N0); }); }
      slx = slx + 1; if (slx >= 3) slx = 0;
    };
    using WS_ = IC<(KN_WLD - 1) * WI + XIMIN>;
    if constexpr (!LDR) raw_barrier();
    if constexpr (LDR) iter(1, T{}, T{}, IC<WI + XIMIN>{}, IM1{});
    for (int j = LDR ? 2 : 1; j <= NT - 4; ++j) iter(j, T{}, T{}, WS_{}, I0{});
    iter(NT - 3, T{}, std::integral_constant<bool, KN_WLD == 2>{}, WS_{}, I1{});
    iter(NT - 2, F{}, F{}, IC<WI + XIMIN>{}, IC<2>{});
    iter(NT - 1, F{}, F{}, I0{}, IC<3>{});
    TS6(16);
    { const RdCtx c = mkctx(false, 0, 0); const unsigned ta = lds0 + TNEW + fr * 16;
      step1(HL{}, fb, fa, c, I0{}, I0{}, IC<-100>{}, IC<-100>{}, nos, IC<MFX>{}, [&](int i) {
        if (i == 0) static_for<0, MF + (LDR ? 1 : 0)>([&](auto mc) { constexpr int m = decltype(mc)::value; t4[m] = __builtin_bit_cast(f32x4, ds_b128<(m < MF ? 16 + (ROLE / WN * MF + m) * 16 : 0) * 16>(ta)); });
        if constexpr (!LDR) { if (i == KN_CI) ldC(); } });
      pinT(LDR); if constexpr (!LDR) pinC(); }
    TS6(17);
  };
  switch (wave) {
    case 0: run(IC<0>{}); break; case 1: run(IC<1>{}); break;
    case 2: run(IC<2>{}); break; case 3: run(IC<3>{}); break;
    case 4: run(IC<4>{}); break; case 5: run(IC<5>{}); break;
    case 6: run(IC<6>{}); break; default: run(IC<7>{}); break;
  }
  STAMP(2);
  Tag tg;
  {
    if (wave < 4) {
      if (fr == 0) {
#pragma unroll
        for (int n = 0; n < NF; ++n) *(f32x4*)(smem + RING + 64 + wn * 192 + fq * 16 + n * 64) = acch[n];
      }
      ds_w32(cfa, 1);
#pragma unroll
      for (int n = 0; n < NF; ++n)
#pragma unroll
        for (int e = 0; e < 4; ++e) { const float h = acch[n][e]; csn[n][e] = __int_as_float(__builtin_amdgcn_update_dpp(0, __float_as_int(h), 0x150, 0xf, 0xf, false)); }
    } else {
      unsigned polls = 0;
      while (__builtin_expect(__builtin_amdgcn_readfirstlane(up) == 0, 0) && ++polls < 40000000u) {
        ldC(); pinC();
      }
    }
    STAMP(9);
    TS6(18);
    TS6(19);
    STAMP(6);
    auto nrm = [&](const f32x4& v, const f32x4& cs, const f32x4& t) {
      const float mu = t[0], rs = t[2];
      return (u32x2){pk_bf16(rs * (v[0] - mu * cs[0]), rs * (v[1] - mu * cs[1])), pk_bf16(rs * (v[2] - mu * cs[2]), rs * (v[3] - mu * cs[3]))}; };
    u32x2 ov2[MF][NF];
#pragma unroll
    for (int m = 0; m < MF; ++m) {
#pragma unroll
      for (int n = 0; n < NF; ++n) { ov2[m][n] = nrm(acc[n][m], csn[n], t4[m]); asm volatile("" : "+v"(ov2[m][n])); }
      __builtin_amdgcn_sched_barrier(0);
#pragma unroll
      for (int n = 0; n < NF; ++n) *(u32x2*)(smem + QT + qw + m * 16 * RS + n * 128) = ov2[m][n];
      __builtin_amdgcn_sched_barrier(0);
    }
    if (wave < 4) {
#pragma unroll
      for (int n = 1; n < NF; ++n) *(u32x2*)(smem + QT + qw - 16 * RS + n * 128) = nrm(acch[n], csn[n], t4[MF]);
      __builtin_amdgcn_sched_barrier(0);
      hook(IC<3>{}, IC<2>{}, IC<KN_WA>{});
    }
    STAMP(4);
    TS6(20);
    lds_barrier();
    TS6(21);
    STAMP(5);
    bf16x8 qf[2], kf[2][2]; s16x4 vf[2][2][2];
    qf[0] = ds_b128<16 * RS>(ka); kf[0][0] = ds_b128<128>(ka); kf[0][1] = ds_b128<16 * RS + 128>(ka);
    qf[1] = ds_b128<16 * RS + 64>(ka); kf[1][0] = ds_b128<192>(ka); kf[1][1] = ds_b128<16 * RS + 192>(ka);
    vf[0][0][0] = ds_tr<0>(va); vf[0][0][1] = ds_tr<16 * RS>(va); vf[0][1][0] = ds_tr<8>(va); vf[0][1][1] = ds_tr<16 * RS + 8>(va);
    vf[1][0][0] = ds_tr<64>(va); vf[1][0][1] = ds_tr<16 * RS + 64>(va); vf[1][1][0] = ds_tr<72>(va); vf[1][1][1] = ds_tr<16 * RS + 72>(va);
    u32x4 tp = __builtin_bit_cast(u32x4, ds_b128<0>(lds0 + SNAP));
    hook(IC<1>{}, IC<KN_WA>{}, IC<KN_WE>{});
    asm volatile("s_waitcnt lgkmcnt(9)" ::: "memory");
#pragma unroll
    for (int hh = 0; hh < 2; ++hh) asm volatile("" : "+v"(qf[hh]), "+v"(kf[hh][0]), "+v"(kf[hh][1]));
    const float sc2 = 0.17677669529663687f * 1.4426950408889634f;
    unsigned pm = (unsigned)__builtin_amdgcn_sbfe((int)pl, 24, 1); asm volatile("" : "+v"(pm));
    auto vmax = [](float x, float y) { float r; asm("v_max_f32 %0, %1, %2" : "=v"(r) : "v"(x), "v"(y)); return r; };
    u32x4 ov[2];
    const f32x4 z = {0.f, 0.f, 0.f, 0.f};
    f32x4 sc[2][2]; float lg[2][4], mx[2], sum[2];
    auto x16 = [](float v) { auto p = __builtin_amdgcn_permlane16_swap(__float_as_uint(v), __float_as_uint(v), false, false); return (f32x2){__uint_as_float(p[0]), __uint_as_float(p[1])}; };
    auto x32 = [](float v) { auto p = __builtin_amdgcn_permlane32_swap(__float_as_uint(v), __float_as_uint(v), false, false); return (f32x2){__uint_as_float(p[0]), __uint_as_float(p[1])}; };
#pragma unroll
    for (int hh = 0; hh < 2; ++hh) {
      sc[hh][0] = MFMA(kf[hh][0], qf[hh], z);
      sc[hh][1] = MFMA(kf[hh][1], qf[hh], z);
    }
    __builtin_amdgcn_sched_barrier(0);
#pragma unroll
    for (int hh = 0; hh < 2; ++hh) {
#pragma unroll
      for (int j = 0; j < 4; ++j) { const float e0 = sc[hh][j >> 1][(j & 1) * 2], e1 = sc[hh][j >> 1][(j & 1) * 2 + 1];
        lg[hh][j] = __builtin_fmaf(__uint_as_float((__float_as_uint(e1) & pm) | (__float_as_uint(e0) & ~pm)), sc2, bias[j]); }
      mx[hh] = fmaxf(fmaxf(lg[hh][0], lg[hh][1]), fmaxf(lg[hh][2], lg[hh][3]));
    }
    __builtin_amdgcn_sched_barrier(0);
    { auto p = __builtin_amdgcn_permlane16_swap(__float_as_uint(mx[0]), __float_as_uint(mx[1]), false, false);
      const f32x2 q = x32(vmax(__uint_as_float(p[0]), __uint_as_float(p[1]))), r = x16(vmax(q[0], q[1])); mx[0] = r[0]; mx[1] = r[1]; }
    __builtin_amdgcn_sched_barrier(0);
#pragma unroll
    for (int hh = 0; hh < 2; ++hh) {
#pragma unroll
      for (int j = 0; j < 4; ++j) lg[hh][j] = __builtin_amdgcn_exp2f(lg[hh][j] - mx[hh]);
      sum[hh] = (lg[hh][0] + lg[hh][1]) + (lg[hh][2] + lg[hh][3]);
    }
    __builtin_amdgcn_sched_barrier(0);
    asm volatile("s_waitcnt lgkmcnt(0)" ::: "memory");
#pragma unroll
    for (int hh = 0; hh < 2; ++hh) asm volatile("" : "+v"(vf[hh][0][0]), "+v"(vf[hh][0][1]), "+v"(vf[hh][1][0]), "+v"(vf[hh][1][1]));
    asm volatile("" : "+v"(tp));
    tg = Tag{(unsigned)__builtin_amdgcn_readfirstlane(tp[0]) + 8u, (unsigned)__builtin_amdgcn_readfirstlane(tp[1])};
    pol = (__builtin_amdgcn_readfirstlane(tp[2]) - tg.lo) & 1;
    { auto p = __builtin_amdgcn_permlane16_swap(__float_as_uint(sum[0]), __float_as_uint(sum[1]), false, false);
      const f32x2 q = x32(__uint_as_float(p[0]) + __uint_as_float(p[1])), r = x16(__builtin_amdgcn_rcpf(q[0] + q[1])); sum[0] = r[0]; sum[1] = r[1]; }
    __builtin_amdgcn_sched_barrier(0);
#pragma unroll
    for (int hh = 0; hh < 2; ++hh) {
      const float inv = sum[hh];
      const unsigned c0 = pk_bf16(lg[hh][0] * inv, lg[hh][1] * inv), c1 = pk_bf16(lg[hh][2] * inv, lg[hh][3] * inv);
      const u32x4 pp = {__builtin_amdgcn_perm(0u, c0, pl), __builtin_amdgcn_perm(0u, c0, ph), __builtin_amdgcn_perm(0u, c1, pl), __builtin_amdgcn_perm(0u, c1, ph)};
      const bf16x8 pf = __builtin_bit_cast(bf16x8, pp);
      sc[hh][0] = MFMA(__builtin_shufflevector(vf[hh][0][0], vf[hh][0][1], 0, 1, 2, 3, 4, 5, 6, 7), pf, z);
      sc[hh][1] = MFMA(__builtin_shufflevector(vf[hh][1][0], vf[hh][1][1], 0, 1, 2, 3, 4, 5, 6, 7), pf, z);
    }
    __builtin_amdgcn_sched_barrier(0);
#pragma unroll
    for (int hh = 0; hh < 2; ++hh) ov[hh] = (u32x4){pk_bf16(sc[hh][0][0], sc[hh][0][1]), pk_bf16(sc[hh][0][2], sc[hh][0][3]), pk_bf16(sc[hh][1][0], sc[hh][1][1]), pk_bf16(sc[hh][1][2], sc[hh][1][3])};
    STAMP(10);
#pragma unroll
    for (int hh = 0; hh < 2; ++hh) __builtin_amdgcn_raw_buffer_store_b128(ov[hh], rO, rb_o((m0 >> 4) + wave, pol) + ko<1>(tn * 2048) + hh * 1024 + lane * 16, 0, 16);
    STAMP(11);
  }
  STAMP(7);
  STAMP(8);
  TS6(22);
  asm volatile("s_waitcnt vmcnt(0)" ::: "memory");
  flag_set(a.sync, F_O(tm) + (wave >> 1) * 256 + tn * 16 + (wave & 1) * 8, tg, 0);
  raw_barrier();
  hook(IC<1>{}, IC<KN_WE>{}, IC<16>{});
  STAMP(3);
#endif
}

struct Late { u32x2 tag; };
template <class HK>
__device__ __forceinline__ void glu_phase(const GemmArgs& a, HK hook, Late& late, int pol) {
#if defined(__HIP_DEVICE_COMPILE__)
  constexpr int LDW = DF_, WN = 2;
  constexpr int UPOFF = 32 * 224 + 16;
  constexpr int XT = 32768, WU = 14592;
  constexpr int XBASE = 0, WBASE = 3 * XT, RING = WBASE + 4 * WU;
  constexpr int MF = 4, NF = 7;
  constexpr int NT = 16, NXI = 16, NWI = 14, NLW = 4, XI = 4, WI = 4;
  extern __shared__ __attribute__((aligned(16))) char smem[];
  STAMP(0);
  const unsigned lds0 = (unsigned)(size_t)(lds_char_ptr)smem;
  const int tid = threadIdx.x, lane = tid & 63;
  const int wave = __builtin_amdgcn_readfirstlane(tid >> 6);
  const int wm = wave / WN, wn = wave % WN;
  const int bid = blockIdx.x, xcd = bid & 7, idx = bid >> 3;
  const int tm = (xcd / 4) * 4 + idx / 8, tn = (xcd % 4) * 8 + idx % 8;
  const int m0 = tm * 256;
  const int krot = (tm >> 2) * 8;
  const int fr = lane & 15, fq = lane >> 4;
  const int wq = (lane >> 2) & 3, wp = lane & 3;
  const int panel = m0 >> 8;
  constexpr int TCH = RING + 5120;
  constexpr int DONE = TCH + 896;
  auto done_inc = [&]() { if (lane == 0) asm volatile("ds_add_u32 %0, %1" :: "v"(lds0 + DONE), "v"(1) : "memory"); };
  auto touch = [&](const void* base, unsigned voff, unsigned soff, unsigned lim = 0x7fffffffu) {
    __builtin_amdgcn_raw_ptr_buffer_load_lds(RSRCN(base, lim), LDSP(lds0 + TCH), 4, voff, soff, 0, 0);
  };
  const auto rO = RSRC(a.O);

  unsigned xsrc[XI], wsrc[WI];
#pragma unroll
  for (int i = 0; i < XI; ++i) {
    const int j = (wave % NLW) + NLW * i;
    xsrc[i] = (unsigned)(rb_h((m0 >> 4) + (j >> 1), pol) + (j & 1) * 1024 + lane * 16);
  }
#pragma unroll
  for (int i = 0; i < WI; ++i) {
    int j = (wave % NLW) + NLW * i; if (j >= NWI) j -= NLW;
    const int jj = j >= 7 ? j - 7 : j;
    const int b = jj * 1024 + lane * 16, p = b / 224, pos = (b % 224) >> 4;
    const int row = 8 * ((p & 15) >> 2) + (p & 3) + 4 * (p >> 4);
    wsrc[i] = (unsigned)(row * LDW + tn * 112 + pos * 8) * 2u;
  }
  auto ldX = [&](int i, int tile, int part, int slot) {
    tile = (tile + krot) & 15;
    const int j = wave + NLW * i;
    __builtin_amdgcn_raw_ptr_buffer_load_lds(RSRC(a.X), LDSP(lds0 + XBASE + slot * XT + (part * NXI + j) * 1024), 16, xsrc[i], ko<2>(tile * 2048) + part * (NXI / 2) * SH, 0, 16);
  };
  auto ldW = [&](int i, int unit, int slot) {
    const auto rW0 = RSRC(a.W0);
    const auto rW1 = RSRC(a.W1);
    int j = wave + NLW * i; if (j >= NWI) j -= NLW;
    unit = (unit + 2 * krot) & 31;
    const bool up = j >= 7;
    __builtin_amdgcn_raw_ptr_buffer_load_lds(up ? rW1 : rW0, LDSP(lds0 + WBASE + slot * WU + (up ? UPOFF + (j - 7) * 1024 : j * 1024)), 16, wsrc[i], unit * 32 * LDW * 2, 0, 0);
  };
  auto issueX = [&](int tile, int part, int slot) {
    if (wave >= NLW) return;
#pragma unroll
    for (int i = 0; i < XI; ++i) ldX(i, tile, part, slot);
  };
  auto issueW = [&](int unit, int slot) {
    if (wave >= NLW) return;
#pragma unroll
    for (int i = 0; i < WI; ++i) ldW(i, unit, slot);
  };

  f32x4 acc[NF][MF];
#pragma unroll
  for (int n = 0; n < NF; ++n)
#pragma unroll
    for (int m = 0; m < MF; ++m) acc[n][m] = (f32x4){0.f, 0.f, 0.f, 0.f};
  f32x4 accs = {0.f, 0.f, 0.f, 0.f};

  const unsigned xa0 = lds0 + XBASE + wm * MF * 16 * 128 + lane * 16, xa1 = xa0 + 1024;
  unsigned wa[2];
#pragma unroll
  for (int c = 0; c < 2; ++c) wa[c] = lds0 + WBASE + (4 * fq + wq) * 224 + wn * 112 + (c == 0 ? wp * 8 : 96 + (wp & 1) * 8 + (wp >> 1) * UPOFF);

  using I0 = IC<0>; using I1 = IC<1>;
  using T = std::true_type; using F = std::false_type;
  typedef Frags<MF, NF> FR;
  struct RdCtx { unsigned xa, wb[2]; };
  auto mkctx = [&](bool ks1, unsigned xoffs, unsigned woffs) {
    RdCtx c; c.xa = (ks1 ? xa1 : xa0) + xoffs; c.wb[0] = wa[0] + woffs; c.wb[1] = wa[1] + woffs;
    return c;
  };
  auto rd_item = [&](FR& f, const RdCtx& c, auto kc) {
    constexpr int k = decltype(kc)::value;
    if constexpr (k < MF) f.x[k] = ds_b128<k * 2048>(c.xa);
    else {
      constexpr int n = k - MF, cc = n == 6, o = n == 6 ? 0 : (n >> 1) * 32 + (n & 1) * UPOFF;
      f.lo[n] = ds_tr<o>(c.wb[cc]); f.hi[n] = ds_tr<o + 16 * 224>(c.wb[cc]);
    }
  };
  auto wait_frags = [&](FR& f) {
    asm volatile("s_waitcnt lgkmcnt(0)" ::: "memory");
#pragma unroll
    for (int m = 0; m < MF; ++m) asm volatile("" : "+v"(f.x[m]));
#pragma unroll
    for (int n = 0; n < NF; ++n) asm volatile("" : "+v"(f.lo[n]), "+v"(f.hi[n]));
  };
  auto step = [&](FR& cur, FR& nxt, const RdCtx& c, auto PFc, auto NLc, auto&& ld) {
    constexpr bool PF = decltype(PFc)::value != 0; constexpr int NL = decltype(NLc)::value;
    constexpr int R = MF + NF, RG = NF - 1;
    static_for<0, NF>([&](auto gc) {
      constexpr int g = decltype(gc)::value;
      if constexpr (PF) static_for<0, R>([&](auto kc) { constexpr int k = decltype(kc)::value; if constexpr (k * RG / R == g) rd_item(nxt, c, kc); });
      static_for<0, NL>([&](auto ic) { constexpr int i = decltype(ic)::value; if constexpr (i * NF / NL == g) ld(i); });
      const bf16x8 w = __builtin_shufflevector(cur.lo[g], cur.hi[g], 0, 1, 2, 3, 4, 5, 6, 7);
#pragma unroll
      for (int m = 0; m < MF; ++m) acc[g][m] = MFMA(w, cur.x[m], acc[g][m]);
      __builtin_amdgcn_sched_barrier(0);
    });
    if constexpr (PF) wait_frags(nxt);
  };
  auto stepg = [&](auto CSc, FR& cur, FR& nxt, const RdCtx& c, auto Hc, auto R0c, auto IBc, auto&& sync, auto NLc, auto&& ld, auto&& gap) {
    constexpr bool CS = decltype(CSc)::value; constexpr int H = decltype(Hc)::value, R0 = decltype(R0c)::value, IB = decltype(IBc)::value, NL = decltype(NLc)::value;
    constexpr int NM = NF * MF, NR = MF + 2 * NF;
    static_for<0, NM>([&](auto ic) {
      constexpr int I = decltype(ic)::value, g = I / MF, m = I % MF;
      constexpr int RL = IB > 8 ? 20 : 22;
      const bf16x8 w = __builtin_shufflevector(cur.lo[g], cur.hi[g], 0, 1, 2, 3, 4, 5, 6, 7);
      acc[g][m] = MFMA(w, cur.x[m], acc[g][m]);
      __builtin_amdgcn_sched_barrier(0);
      if constexpr (I == IB) sync();
      static_for<first_at(I, R0, RL - R0, NR - 1, NR), first_at(I + 1, R0, RL - R0, NR - 1, NR)>([&](auto rc) {
        constexpr int r = decltype(rc)::value;
        if constexpr (r < MF) nxt.x[r] = ds_b128<r * 2048 + H * 1024>(c.xa);
        else {
          constexpr int n = (r - MF) >> 1, cc = n == 6, o = (n == 6 ? 0 : (n >> 1) * 32 + (n & 1) * UPOFF) + H * WU;
          if constexpr ((r - MF) & 1) nxt.hi[n] = ds_tr<o + 16 * 224>(c.wb[cc]); else nxt.lo[n] = ds_tr<o>(c.wb[cc]);
        }
      });
#pragma unroll
      for (int i = first_at(I, R0, NM - 1 - R0, NL, NL); i < first_at(I + 1, R0, NM - 1 - R0, NL, NL); ++i) ld(i);
      gap(ic);
      if constexpr (CS && m == MF - 1) {
        const unsigned sv = fr == g ? 0x3f803f80u : 0u;
        const u32x4 s4 = {sv, sv, sv, sv};
        accs = MFMA(w, __builtin_bit_cast(bf16x8, s4), accs);
      }
      __builtin_amdgcn_sched_barrier(0);
    });
    wait_frags(nxt);
  };
  FR fa, fb;
  unsigned tv4 = 0;

  f32x4* tb_row = (f32x4*)(smem + RING);
  float* tb_cs = (float*)(tb_row + 256);
  constexpr int RTAB = 524288;
  auto glu_stats_issue = [&]() {
    if (wave == 7) __builtin_amdgcn_raw_ptr_buffer_load_lds(RSRC(a.st), LDSP(lds0 + RING + 3072), 16, ((lane >> 2) * S_ + m0 + tn * 8 + (lane & 3) * 2) * 8, 0, 0, 16);
  };
  auto glu_stats = [&]() {
    int lx = lane; asm volatile("" : "+v"(lx));
    const unsigned sa = lds0 + RING + 3072 + lx * 8;
    f32x2 p0 = ds_b64<0>(sa), p1 = ds_b64<512>(sa);
    asm volatile("s_waitcnt lgkmcnt(0)" : "+v"(p0), "+v"(p1) :: "memory");
    const float mu = sum8(p0[0] + p1[0]) * (1.f / 16.f);
    const float v = sum8((p0[1] + p1[1]) + 64.f * ((p0[0] - mu) * (p0[0] - mu) + (p1[0] - mu) * (p1[0] - mu))) * (1.f / 1024.f) + 1e-5f;
    const float rs = (KN_EV & 2) ? __builtin_amdgcn_rsqf(v) : 1.0f / sqrtf(v);
    const f32x4 rv = {mu, -0.10294324f * rs * rs * rs, -2.3022082f * rs, v};
    __builtin_amdgcn_raw_buffer_store_b128(__builtin_bit_cast(u32x4, rv), RSRC(a.cs), lx < 8 ? (unsigned)(RTAB + (m0 + tn * 8 + lx) * 16) : 0x80000000u, 0, 16);
  };
  f32x4 cg[4], cu[4], rw[MF], rw6[2]; f32x2 tgv; int dnv, go0; unsigned le, ta, t6;
  auto tabs = [&](int i) {
    if (i == 0) { const int lz = lane_id(), fr = lz & 15, fq = lz >> 4;
      ta = lds0 + RING + 4096 + wn * 224 + fq * 16; t6 = lds0 + RING + wm * 1024 + fr * 16;
      go0 = rb_a((m0 >> 4) + wm * 4, pol) + fr * 16 + (fq & 1) * SA + (fq >> 1) * 256;
      le = lds0 + (wm * 4 + (fq & 1)) * 1024 + (wn * 2 + (fq >> 1)) * 256 + fr * 16;
      asm volatile("" : "+v"(ta), "+v"(t6), "+v"(go0), "+v"(le)); }
    if (i == 1) { rw[0] = __builtin_bit_cast(f32x4, ds_b128<0>(t6)); rw[1] = __builtin_bit_cast(f32x4, ds_b128<256>(t6)); cg[0] = __builtin_bit_cast(f32x4, ds_b128<0>(ta)); }
    if (i == 2) { cg[1] = __builtin_bit_cast(f32x4, ds_b128<64>(ta)); cu[0] = __builtin_bit_cast(f32x4, ds_b128<448>(ta)); cu[1] = __builtin_bit_cast(f32x4, ds_b128<448 + 64>(ta)); }
    if (i == 3) { rw[2] = __builtin_bit_cast(f32x4, ds_b128<512>(t6)); rw[3] = __builtin_bit_cast(f32x4, ds_b128<768>(t6)); tgv = ds_b64<0>(lds0 + SNAP); }
    if (i == 8) asm volatile("s_waitcnt lgkmcnt(0)" : "+v"(rw[0]), "+v"(rw[1]), "+v"(rw[2]), "+v"(rw[3]), "+v"(cg[0]), "+v"(cg[1]), "+v"(cu[0]), "+v"(cu[1]), "+v"(tgv) :: "memory");
    if (i == 9) { const int lz = lane_id(); const unsigned tc = ta - (lz & 32), tr = t6 + (lz & 32) * 8;
      cg[2] = __builtin_bit_cast(f32x4, ds_b128<128>(ta)); cu[2] = __builtin_bit_cast(f32x4, ds_b128<448 + 128>(ta)); cg[3] = __builtin_bit_cast(f32x4, ds_b128<192>(tc)); cu[3] = __builtin_bit_cast(f32x4, ds_b128<448 + 192>(tc));
      rw6[0] = __builtin_bit_cast(f32x4, ds_b128<0>(tr)); rw6[1] = __builtin_bit_cast(f32x4, ds_b128<512>(tr)); }
    if (i == 10) asm volatile("s_waitcnt lgkmcnt(0)" : "+v"(cg[2]), "+v"(cg[3]), "+v"(cu[2]), "+v"(cu[3]), "+v"(rw6[0]), "+v"(rw6[1]) :: "memory");
    if (i == 11) dnv = ds_r32(lds0 + DONE);
    if (i == 12) asm volatile("s_waitcnt lgkmcnt(0)" : "+v"(dnv) :: "memory");
  };
  auto epilogue = [&](auto EAc) __attribute__((always_inline)) {
    STAMP(2);
    constexpr bool EARLY = decltype(EAc)::value;
    auto prio = [&](auto gc) { constexpr int g = decltype(gc)::value; __builtin_amdgcn_s_setprio(((EARLY ? KN_PA : KN_PB) >> (4 * g)) & 3); };
    prio(I0{});
    STAMPW(9, 4);
    TS4(19);
    unsigned hv[MF][NF];
    auto keep = [&](unsigned a0, unsigned a1, unsigned b0, unsigned b1) {
      const auto s0 = __builtin_amdgcn_permlane16_swap(a0, b0, false, false), s1 = __builtin_amdgcn_permlane16_swap(a1, b1, false, false);
      return (u32x4){s0[0], s1[0], s0[1], s1[1]}; };
    auto put = [&](auto pc, auto hc) {
      constexpr int p = decltype(pc)::value, h = decltype(hc)::value;
      const u32x4 v = keep(hv[2 * h][2 * p], hv[2 * h][2 * p + 1], hv[2 * h + 1][2 * p], hv[2 * h + 1][2 * p + 1]);
      __builtin_amdgcn_raw_buffer_store_b128(v, rO, go0, (ko<4>((tn * 12 + p * 4 + wn * 2) * 256) ^ za((m0 >> 4) + wm * 4 + h * 2)) + h * 2 * SA, 16);
      if constexpr (p < 2) ds_w128<OWNE + p * 16384 + h * 2048>(le, v); else ds_w128<h * 2048>(le + OWNM, v);
      __builtin_amdgcn_sched_barrier(0);
      asm volatile("s_nop 1" ::: "memory");
      __builtin_amdgcn_sched_barrier(0);
    };
#define STAGE(body) _Pragma("unroll") for (int i = 0; i < 4 * NN; ++i) { const int m = i / NN, n = N0 + i % NN; const f32x4 k = n == 6 ? rw6[m >> 1] : rw[m]; body; } __builtin_amdgcn_sched_barrier(0)
    auto grp = [&](auto N0c, auto NNc, auto&& at) {
      constexpr int N0 = decltype(N0c)::value, NN = decltype(NNc)::value;
      f32x2 G[4 * NN], U[4 * NN], P[4 * NN];
      __builtin_amdgcn_sched_barrier(0);
      STAGE({ const int e = (n == 6 ? m & 1 : n & 1) * 2; const f32x4 x = n == 6 ? acc[6][m & 2] : acc[n & 6][m]; G[i] = ((f32x2){x[e], x[e + 1]}) - k[0] * ((f32x2){cg[n >> 1][e], cg[n >> 1][e + 1]}); });
      STAGE(P[i] = G[i] * G[i]);
      at(I0{});
      STAGE(P[i] = P[i] * k[1] + k[2]);
      STAGE(P[i] = G[i] * P[i]);
      at(I1{});
      STAGE({ const int e = (n == 6 ? m & 1 : n & 1) * 2; const f32x4 x = n == 6 ? acc[6][m | 1] : acc[n | 1][m]; U[i] = ((f32x2){x[e], x[e + 1]}) - k[0] * ((f32x2){cu[n >> 1][e], cu[n >> 1][e + 1]}); });
      STAGE(P[i] = ((f32x2){__builtin_amdgcn_exp2f(P[i][0]), __builtin_amdgcn_exp2f(P[i][1])}));
      at(IC<2>{});
      __builtin_amdgcn_sched_barrier(0);
      STAGE(P[i] = P[i] * k[3] + k[3]);
      STAGE(P[i] = ((f32x2){__builtin_amdgcn_rcpf(P[i][0]), __builtin_amdgcn_rcpf(P[i][1])}));
      at(IC<3>{});
      STAGE(U[i] = G[i] * U[i]);
      STAGE(U[i] = U[i] * P[i]);
      STAGE({ hv[m][n] = pk_bf16(U[i][0], U[i][1]); asm volatile("" : "+v"(hv[m][n])); });
    };
#undef STAGE
    using I2 = IC<2>; using I4 = IC<4>;
    TS4(20);
    grp(I0{}, I2{}, [&](auto tc) { if constexpr (decltype(tc)::value == 3) tabs(11); });
    TS4(21);
    tabs(12);
    if (__builtin_expect(__builtin_amdgcn_readfirstlane(dnv) < 8, 0)) lds_wait8(lds0 + DONE);
    put(I0{}, I0{}); put(I0{}, I1{});
    tabs(9);
    STAMP(4);
    STAMPW(11, 4);
    TS4(22);
    prio(I1{});
    const auto rFl = RSRCN((const char*)a.sync + F_ACT(panel) + (wm * 64 + tn * 2 + wn) * 8, 8);
    u32x2 fval = {__float_as_uint(tgv[0]) + 8u, __float_as_uint(tgv[1])}; unsigned foff = lane_id() * 8u;
    asm volatile("" : "+v"(fval), "+v"(foff));
    grp(I2{}, I2{}, [&](auto) {});
    TS4(23);
#define KN_FE -1
    if constexpr (KN_FE < 0) { wait_vm<0>(); __builtin_amdgcn_raw_buffer_store_b64(fval, rFl, foff, 0, 16); STAMPW(6, 4); STAMP(10); }
    put(I1{}, I0{}); put(I1{}, I1{});
    TS4(24);
    prio(I2{});
    tabs(10);
    grp(I4{}, I2{}, [&](auto tc) { if constexpr (decltype(tc)::value == KN_FE) { wait_vm<2>(); __builtin_amdgcn_raw_buffer_store_b64(fval, rFl, foff, 0, 16); STAMPW(6, 4); STAMP(10); } });
    TS4(25);
    auto poll = [&]() { if (foff < 256u) __builtin_amdgcn_raw_ptr_buffer_load_lds(RSRC(a.sync), LDSP(lds0 + FLP + wave * 128), 4, foff >> 1, F_ACT(panel) + wm * 512 + (xcd & 3) * 128, 0, 16); };
    if constexpr (KN_QP == 0) poll();
    put(I2{}, I0{}); put(I2{}, I1{});
    prio(IC<3>{});
#pragma unroll
    for (int q = 0; q < 2; ++q)
#pragma unroll
      for (int e = 0; e < 4; ++e) {
        const float x0 = acc[6][2 * q][e], x1 = acc[6][2 * q + 1][e];
        auto sw = __builtin_amdgcn_permlane32_swap(__float_as_uint(x0), __float_as_uint(x1), false, false);
        acc[6][2 * q][e] = __uint_as_float(sw[0]); acc[6][2 * q + 1][e] = __uint_as_float(sw[1]);
      }
    TS4(26);
    int go6 = go0 + (int)((foff >> 7) & 1u) * SA + (int)(foff >> 8) * (SA - 256) + (ko<4>((384 + tn * 2 + wn) * 256) ^ za((m0 >> 4) + wm * 4 + (int)((foff >> 6) & 2u))); asm volatile("" : "+v"(go6));
    if constexpr (KN_QP == 1) poll();
    grp(IC<6>{}, I1{}, [&](auto) {});
    if constexpr (KN_QP == 2) poll();
    __builtin_amdgcn_raw_buffer_store_b128(keep(hv[0][6], hv[1][6], hv[2][6], hv[3][6]), rO, go6, 0, 16);
    __builtin_amdgcn_sched_barrier(0);
    asm volatile("s_nop 1" ::: "memory");
    if constexpr (KN_QP == 3) poll();
    late.tag = fval;
    asm volatile("" ::: "memory");
    __builtin_amdgcn_s_setprio(0);
  };

  TS4(0);
  constexpr int WR = 2;
  if (wave == 5) ds_w32(lds0 + DONE, 0);
  STAMP(9); STAMP(6);
  auto check_tile = [&](int t) {
    int lx = lane; asm volatile("" : "+v"(lx));
    const unsigned ad = lds0 + XBASE + t * XT + wave * 1024 + lx * 16;
    if (__builtin_expect(marked<8>(ad), 0)) {
      for (int tries = 0;; ++tries) {
        if (tries >= KN_TRY) wait_flags(lds0, a.sync, F_H + F_PAN(panel), 32, 0);
#pragma unroll 1
        for (int u = t; u < t + 1 + (t == 0); ++u) { issueX(u, 0, u); issueX(u, 1, u); }
        if (t == 0) wait_vm<2 * XI>(); else wait_vm<0>();
        if (tries >= KN_TRY || !marked<8>(ad)) break;
      }
    }
  };
  auto run = [&](auto LDRc) __attribute__((always_inline)) {
    constexpr bool LDR = decltype(LDRc)::value;
    if constexpr (LDR) {
      issueX(0, 0, 0); issueX(0, 1, 0); issueX(1, 0, 1); issueX(1, 1, 1);
      asm volatile("" : "+v"(wsrc[0]), "+v"(wsrc[1]), "+v"(wsrc[2]), "+v"(wsrc[3]));
      if constexpr (KN_G2) wait_vm<2 * XI>(); else { issueW(2, 2 ^ WR); issueW(3, 3 ^ WR); wait_vm<2 * XI + 2 * WI>(); }
      check_tile(0);
    } else {
      wait_vm<0>();
#define KN_HS 0
      if (KN_HS && wave == 4) { __builtin_amdgcn_s_sleep(KN_HS); __builtin_amdgcn_raw_ptr_buffer_load_lds(RSRC(a.sync), LDSP(lds0 + TCH + 256), 4, lane * 4, F_H + F_PAN(panel), 0, 16); }
    }
    unsigned long long tb = (unsigned long long)a.PF0; unsigned tv = 0, tl = 0x7fffffffu, ts = 7168u, to = 0, tmask = 0;
    if constexpr (!LDR) {
      unsigned long long c0, c1; CODE_ADDR(c0, "k_all"); CODE_ADDR(c1, "k_zend");
#define KN_WDM 0x55
#define KN_DA 6
#define KN_DN 2
      if (wave == 4) { tb = c0; tl = c1 - c0 < 262144ull ? (unsigned)(c1 - c0) : 0u; tmask = 1u << 11; }
      if (wave == 5) { tmask = KN_WDM << (KN_DA + idx % KN_DN); to = ((xcd & 3) * 32 + idx) * 57344 + (xcd >> 2) * 28672; }
      if (wave == 6) { tb = (unsigned long long)((idx & 16) ? a.W1 : a.W0); ts = 64 * LDW * 2; tmask = 0x1eu; to = (krot + 4) * ts; }
      const int l4 = lane / 14;
      tv = wave == 4 ? (unsigned)((lane * 32 + idx) * 128)
         : (lane >= 56 ? 0x80000000u : (wave == 5 ? (unsigned)(lane * 128) : (unsigned)(((idx & 15) * 4 + l4) * (LDW * 2) + (xcd & 3) * 1792 + (lane - l4 * 14) * 128)));
    }
    const int js = 2 * (tm & 3) + 1;
    const auto rC = RSRC(a.cs);
    constexpr int JR = 10, JS = 2;
    const int jf = wave == 4 ? NT - 4 : (wave == 7 ? JR : 64);
    const int fof = wave == 4 ? F_COL + tn * 256 : F_ROW + F_PAN(panel);
    unsigned evm = tmask | (8u << js) | (wave == 4 ? 3u << (NT - 4) : 0u) | (wave == 7 ? ((5u << JS) | 2u | (11u << JR)) : 0u);
    Tag tgl = {0u, 0u};
    if constexpr (!LDR && (KN_EV & 4)) tgl = tag_get(lds0);
    if constexpr (!LDR) asm volatile("" : "+v"(tv), "+s"(tb), "+s"(tl), "+s"(ts), "+s"(to), "+s"(tmask), "+s"(evm));
    lds_barrier();
    if constexpr (!LDR && !KN_HS) { if (wave == 4) __builtin_amdgcn_raw_ptr_buffer_load_lds(RSRC(a.sync), LDSP(lds0 + TCH + 256), 4, lane * 4, F_H + F_PAN(panel), 0, 16); }
    { const RdCtx c = mkctx(false, 0, WR * WU); static_for<0, MF + NF>([&](auto kc) { rd_item(fa, c, kc); }); }
    wait_frags(fa);
    STAMP(1);
    TS4(1);
    { const RdCtx c = mkctx(true, 0, (WR + 1) * WU); step(fa, fb, c, T{}, IC<LDR ? 2 * XI : 0>{}, [&](int i) { ldX(i % XI, 2, i / XI, 2); }); }
    RdCtx cc = mkctx(false, XT, 0);
    int xs = 1, dX = XT, dW = 2 * WU;
    int wd = lds0 + WBASE + 2 * WU, wso = ((4 + 2 * krot) & 31) * (32 * LDW * 2), xd = lds0 + XBASE + wave * 1024, xso = ko<2>(((3 + krot) & 15) * 2048);
    int wc[WI];
#pragma unroll
    for (int i = 0; i < WI; ++i) { int q = wave + NLW * i; if (q >= NWI) q -= NLW; wc[i] = q >= 7 ? UPOFF + (q - 7) * 1024 : q * 1024; }
    unsigned evs = evm, dw = 0;
    auto ldd = [&](int i) { static_for<0, 22>([&](auto kc) { if (i == decltype(kc)::value) hook(IC<64 + decltype(kc)::value>{}, dw); }); };
    constexpr int CFL = TCH + 256, CPB = XBASE + ((NT - 3) % 3) * XT;
    auto iter = [&](int j, auto LWc, auto LXc, auto WAITc, auto CAc, auto CBc) {
      constexpr bool LW = decltype(LWc)::value && LDR, LX = decltype(LXc)::value && LDR, LP = LW && !LX;
      TS4(j + 1);
      if constexpr (LDR) {
        wait_vm<decltype(WAITc)::value>();
        if constexpr (LX) { if (__builtin_expect(j <= 2, 0)) check_tile(j); }
      }
      if constexpr (!LDR) {
        if (__builtin_expect(evs & 2, 0)) {
          if (KN_EV & 1) __builtin_amdgcn_s_setprio(3);
          if (j == js + 3) {
            if ((tmask >> (j - 1)) & 1) wait_vm<1>(); else wait_vm<0>();
            flag_set(a.sync, F_COL + tn * 256 + (tm * 4 + wave - 4) * 8, (KN_EV & 4) ? tgl : tag_get(lds0), 0);
          }
          if (j == 1) glu_stats_issue();
          if (j == JS && wave == 7) { wait_vm<0>(); glu_stats(); }
          if (j == JS + 2 && wave == 7) {
            if (j == js + 2) wait_vm<1>(); else wait_vm<0>();
            flag_set(a.sync, fof + tn * 8, (KN_EV & 4) ? tgl : tag_get(lds0), 0);
          }
          {
            int lx = lane; asm volatile("" : "+v"(lx));
            if (j == jf) __builtin_amdgcn_raw_ptr_buffer_load_lds(RSRC(a.sync), LDSP(lds0 + CFL + (wave & 1) * 256), 4, lx * 4, fof, 0, 16);
            if (j == jf + 1) {
              wait_vm<0>();
              int fv = ds_r32(lds0 + CFL + (wave & 1) * 256 + lx * 4);
              const Tag tg = tag_get(lds0);
              asm volatile("" : "+v"(fv));
              while (!words_up((unsigned)fv, tg) && in_time(lds0))
                asm volatile("global_load_dword %0, %1, %2 sc1\n\ts_waitcnt vmcnt(0)" : "=v"(fv) : "v"(lx * 4), "s"((const char*)a.sync + fof) : "memory");
              if (wave == 7) {
#pragma unroll
                for (int i = 0; i < 4; ++i) __builtin_amdgcn_raw_ptr_buffer_load_lds(rC, LDSP(lds0 + RING + i * 1024), 16, lx * 16, RTAB + m0 * 16 + i * 1024, 0, 16);
              }
            }
            if (j == jf + 3) wait_vm<0>();
          }
          if ((tmask >> j) & 1) { touch((const void*)tb, tv, to, tl); to += ts; }
          if (KN_EV & 1) __builtin_amdgcn_s_setprio(0);
        }
        __builtin_amdgcn_sched_barrier(0);
      }
      constexpr bool LD = LDR && !LW;
      if constexpr (LD) {
        const int lc = lane_id();
        const unsigned ca = lds0 + CPB + wave * 4096 + (lc >> 4) * 896 + ((lc & 15) < 14 ? (lc & 15) : 0) * 16;
        f32x4 p0 = __builtin_bit_cast(f32x4, ds_b128<0>(ca)), p1 = __builtin_bit_cast(f32x4, ds_b128<224>(ca)), p2 = __builtin_bit_cast(f32x4, ds_b128<448>(ca)), p3 = __builtin_bit_cast(f32x4, ds_b128<672>(ca));
        dw = dn_wsrc(lc, wave);
        asm volatile("s_waitcnt lgkmcnt(0)" : "+v"(p0), "+v"(p1), "+v"(p2), "+v"(p3), "+v"(dw) :: "memory");
        f32x4 cv = (p0 + p1) + (p2 + p3);
#pragma unroll
        for (int k = 0; k < 4; ++k) { float t = cv[k]; auto sw = __builtin_amdgcn_permlane16_swap(__float_as_uint(t), __float_as_uint(t), false, false); t = __uint_as_float(sw[0]) + __uint_as_float(sw[1]);
          auto sx = __builtin_amdgcn_permlane32_swap(__float_as_uint(t), __float_as_uint(t), false, false); cv[k] = __uint_as_float(sx[0]) + __uint_as_float(sx[1]); }
        if (lc < 14) *(f32x4*)(tb_cs + (wave >> 1) * 112 + wn * 56 + lc * 4) = cv;
        asm volatile("s_waitcnt lgkmcnt(0)" ::: "memory");
        __builtin_amdgcn_sched_barrier(0);
      }
      auto lda = [&](int i) { if constexpr (LD) ldd(i); else { const int k = i % WI;
          if constexpr (LP) { if (i < 4) __builtin_amdgcn_raw_ptr_buffer_load_lds(rC, LDSP(lds0 + CPB + wave * 4096 + i * 1024), 16, lane * 16, (tn * 4 + wn * 2 + (wave >> 1)) * 3584 + i * 1024, 0, 16); }
#define KN_ND 1
          if (!KN_ND || k < 3 || wave < 2) __builtin_amdgcn_raw_ptr_buffer_load_lds((k >= 2 || (k == 1 && wave == 3)) ? RSRC(a.W1) : RSRC(a.W0), LDSP(wd + wc[k]), 16, wsrc[k], wso, 0, 0); } };
      auto ldb = [&](int i) {
          if constexpr (LX) __builtin_amdgcn_raw_ptr_buffer_load_lds(RSRC(a.X), LDSP(xd + i * 4096), 16, xsrc[i % XI], xso + (i / XI) * (NXI / 2) * SH, 0, 16);
          else if constexpr (LD) ldd(8 + i); };
      using NLA = IC<(LW || LD) ? 8 : 0>; using NLB = IC<(LX || LD) ? 8 : 0>;
      auto pin1 = [](int& v) { asm volatile("" : "+s"(v)); };
      stepg(CAc, fb, fa, cc, I0{}, IC<LDR ? 2 : 0>{}, IC<LDR ? 1 : -1>{}, [&]() { raw_barrier(); }, NLA{}, lda, [&](auto ic) { constexpr int I = decltype(ic)::value;
        if constexpr (LW && I == 12) { wd += WU; wso += 32 * LDW * 2; pin1(wd); pin1(wso); }
        if constexpr (LW && I == 24) { wd += dW > 0 ? -3 * WU : WU; pin1(wd); }
        if constexpr (LW && I == 25) { wso += 32 * LDW * 2; pin1(wso); }
        if constexpr (LW && I == 26) { wso = wso == 1024 * LDW * 2 ? 0 : wso; pin1(wso); }
        if constexpr (!LDR && I == 24) { evs >>= 1; asm volatile("" : "+s"(evs)); }
        if constexpr (I == 7) { dX = xs == 2 ? -2 * XT : XT; pin1(dX); }
        if constexpr (I == 10) { xs = xs == 2 ? 0 : xs + 1; pin1(xs); } });
      stepg(CBc, fa, fb, cc, I1{}, I0{}, IC<LDR ? -1 : 25>{},
          [&]() { asm volatile("s_waitcnt lgkmcnt(0)" ::: "memory"); if (decltype(WAITc)::value == 0 || j + 1 < NT) raw_barrier(); }, NLB{}, ldb, [&](auto ic) { constexpr int I = decltype(ic)::value;
        if constexpr (I == 8) { cc.xa += dX; asm volatile("" : "+v"(cc.xa)); }
        if constexpr (I == (LDR ? 24 : 22)) { cc.wb[0] += dW; cc.wb[1] += dW; asm volatile("" : "+v"(cc.wb[0]), "+v"(cc.wb[1])); }
        if constexpr (I == (LDR ? 25 : 23)) { dW = -dW; pin1(dW); }
        if constexpr (LX && I == 25) { xso = (KN_NR & 2) ? ((xso | 0x2000) + 2048) & 0xd800 : (xso + 2048) & 0x7800; pin1(xso); }
        if constexpr (LX && I == 26) { xd += xs == 1 ? -2 * XT : XT; pin1(xd); } });
    };
    using WX_ = IC<2 * XI>;
    if constexpr (LDR) {
      for (int j = 1; j < NT - 2; ++j) iter(j, T{}, T{}, WX_{}, F{}, F{});
      iter(NT - 2, T{}, F{}, WX_{}, F{}, F{});
      __builtin_amdgcn_s_setprio(3);
      iter(NT - 1, F{}, F{}, I0{}, F{}, F{});
    } else {
      int j = 1;
      if (wave == 4) {
        wait_vm<0>();
        int fv = ds_r32(lds0 + TCH + 256 + lane * 4);
        const Tag tg = tag_get(lds0);
        asm volatile("" : "+v"(fv));
        if (!words_up((unsigned)fv, tg)) wait_flags(lds0, a.sync, F_H + F_PAN(panel), 32, 0);
      }
      raw_barrier();
#pragma unroll 1
      for (; j < js; ++j) iter(j, F{}, F{}, I0{}, F{}, F{});
      const int ja = wm == 2 ? js + 2 : js;
#pragma unroll 1
      for (; j < ja; ++j) iter(j, F{}, F{}, I0{}, T{}, F{});
#pragma unroll 1
      for (; j < js + 2; ++j) iter(j, F{}, F{}, I0{}, F{}, T{});
      { int lx = lane; asm volatile("" : "+v"(lx));
        __builtin_amdgcn_raw_buffer_store_b128(__builtin_bit_cast(u32x4, accs), rC, (lx & 15) < NF ? (unsigned)((tn * 4 + wn * 2) * 3584 + (tm * 2 + (wm & 1)) * 224
                + ((lx & 15) < 6 ? (lx & 1) * 3584 + ((lx & 15) >> 1) * 64 + (lx >> 4) * 16 : (lx >> 5) * 3584 + 192 + (lx & 16))) : 0x80000000u, 0, 16); }
#pragma unroll 1
      for (; j < NT; ++j) iter(j, F{}, F{}, I1{}, F{}, F{});
    }
    done_inc();
    TS4(17);
    step(fb, fa, cc, F{}, IC<NF>{}, [&](int i) { tabs(i); if constexpr (LDR) { if (i > 0) ldd(15 + i); } });
    tabs(8);
    TS4(18);
    epilogue(LDRc);
  };
  if (wave < NLW) run(T{}); else run(F{});
  STAMP(7);
  STAMP(8);
  STAMPW(12, 4);
  STAMP(3);
#endif
}

#define KN_CT 7
#define KN_E 6
#define KN_B3 0
#define KN_WB 2
#define KN_GW 1
#define XAUXW 16
#define XAUXD 16
#define KN_WS 2
#define KN_HE 0
#define KN_FL 22
#define KN_LD 0
#define KN_DM 0
#define KN_LX 0
#define STAMPV(i) STAMP(i)
struct FragW { s16x4 lo[4], hi[4]; };
template <int MODE, int LO, int HI>
__device__ __forceinline__ void wo_phase(const GemmArgs& a, const GemmArgs& nx, int pol = 0) {
#if defined(__HIP_DEVICE_COMPILE__)
  extern __shared__ __attribute__((aligned(16))) char smem[];
  if constexpr (MODE == 2) STAMP(0);
  const unsigned lds0 = (unsigned)(size_t)(lds_char_ptr)smem;
  const int tid = threadIdx.x, lane = tid & 63;
  const int wave = __builtin_amdgcn_readfirstlane(tid >> 6);
  const int bid = blockIdx.x, xcd = bid & 7, idx = bid >> 3;
  const int tm = (xcd >> 1) * 4 + (idx >> 3), tn = (xcd & 1) * 8 + (idx & 7);
  const int m0 = tm * 128, panel = m0 >> 8, rot = WO_ROT;
  if constexpr (MODE & 1) {
    const auto rW = __builtin_amdgcn_make_buffer_rsrc((void*)a.W0, 0, 0x7fffffff, 0x00020000);
    const int row = wave * 8 + (lane >> 3);
    const unsigned wsrc = (unsigned)(row * D_ + tn * 64 + ((lane & 7) ^ wswz(row)) * 8) * 2u;
    static_for<LO, HI>([&](auto ic) { constexpr int i = decltype(ic)::value;
      __builtin_amdgcn_raw_ptr_buffer_load_lds(rW, (lds_void_ptr)(size_t)(lds0 + wo_pos(i >> 1) + (i & 1) * 8192 + wave * 1024), 16, wsrc, (((i & 1) * 8 + (i >> 1)) ^ rot) * (64 * D_ * 2), 0, 0);
      if constexpr (MODE == 3) __builtin_amdgcn_raw_ptr_buffer_load_lds(rW, LDSP(lds0 + wo_pos(i >> 1) + (i & 1) * 8192 + 4096 + wave * 1024), 16, wsrc, (((i & 1) * 8 + (i >> 1)) ^ rot) * (64 * D_ * 2) + 32 * D_ * 2, 0, 0); });
  } else {
    constexpr int PAD = 149504;
    const int g = wave >> 2, rw = wave & 3;
    const int fr = lane & 15, fq = lane >> 4, wq = (lane >> 2) & 3, wp = lane & 3;
    unsigned wa[4];
#pragma unroll
    for (int c = 0; c < 4; ++c) { const int row = 8 * fq + wq; wa[c] = lds0 + g * 8192 + row * 128 + (((c << 1) ^ wswz(row)) << 4) + wp * 8; }
#define KN_SK 0
#define KN_SB 0
    FragW f[16];
    auto rd = [&](auto ic, auto rc) {
      constexpr int i = decltype(ic)::value, r = decltype(rc)::value, P = wo_pos(i >> 1) + (i & 1) * 4096, B = P & ~32767, o = P & 32767;
      if constexpr (r & 1) f[i].hi[r >> 1] = ds_tr<o + 512>(wa[r >> 1] + B); else f[i].lo[r >> 1] = ds_tr<o>(wa[r >> 1] + B);
    };
    auto pin = [&](FragW& q) {
#pragma unroll
      for (int n = 0; n < 4; ++n) asm volatile("" : "+v"(q.lo[n]), "+v"(q.hi[n]));
    };
    static_for<0, 16>([&](auto rc) { constexpr int r = decltype(rc)::value; rd(IC<r / 8>{}, IC<r % 8>{}); });
    const Tag tg = tag_get(lds0);
    pin(f[0]); pin(f[1]);
    if (wave == 2) {
      unsigned long long c0, c1; CODE_ADDR(c0, "k_all"); CODE_ADDR(c1, "k_zend");
      __builtin_amdgcn_raw_ptr_buffer_load_lds(RSRCN(c0, c1 - c0 < 262144ull ? (unsigned)(c1 - c0) : 0u), LDSP(lds0 + PAD), 4, (unsigned)((lane * 32 + idx) * 128), 0, 0, 0); }
    if (wave == 3) {
      const unsigned fo = (unsigned)(((lane & 32) ? F_PAN((xcd >> 2) * 4 + (idx >> 3)) : F_PAN(panel)) + (lane & 31) * 8);
      while (!flags_up(__builtin_amdgcn_raw_buffer_load_b64(RSRC(a.sync), fo, F_MARK, 16), 64, tg, 0) && in_time(lds0)) {}
    }
    const auto rX = __builtin_amdgcn_make_buffer_rsrc((void*)a.X, 0, 0x7fffffff, 0x00020000);
    const int rb = (m0 >> 4) + rw * 2 + g;
    unsigned xs[2] = {(unsigned)(rb_o(rb, pol) + lane * 16), (unsigned)(rb_o(rb ^ 1, pol) + lane * 16)};
    const int ck = (fq & 1) * 2 + (fq >> 1);
    bf16x8 s0 = pick(fq == (fr >> 3), fr & 7), s1 = pick(fq == 2 + (fr >> 3), fr & 7);
    asm volatile("" : "+v"(s0), "+v"(s1));
    unsigned orow = (unsigned)((rb * 16 + fr) * (D_ * 2) + tn * 128 + fq * 16), ofrg = (unsigned)(rb_h(rb, pol) + ko<2>(tn * 2048) + ck * 256 + fr * 16);
    asm volatile("" : "+v"(xs[0]), "+v"(xs[1]), "+v"(orow), "+v"(ofrg));
    u32x4 res[2];
    STAMP(9);
    const int fo_off = F_O(tm) + rw * 256 + ((g ^ (rot >> 3)) & 1) * 128;
    const unsigned first = (rot & 4) ? 0xff00u : 0x00ffu;
    auto down = [&](u32x2 v) { return (unsigned)__builtin_amdgcn_ballot_w64(v[0] != tg.lo || v[1] != tg.hi) & 0xffffu; };
    unsigned dn, polls = 0;
    do { dn = down(flag_get(a.sync, fo_off, 16)); asm volatile("" ::: "memory"); } while ((dn & first) && ((++polls & 1023u) || in_time(lds0)));
    STAMP(6);
    u32x2 fl = {0u, 0u};
    u32x4 xr[16][2];
#define KN_XD 4
    auto ldX = [&](int i, int m) {
      if (i >= 0 && i < 16) xr[i][m] = __builtin_amdgcn_raw_buffer_load_b128(rX, xs[m] + (i & 1) * 1024, ko<1>(((g * 8 + (i >> 1)) ^ rot) * 2048), 16);
      else if (m == 1 && (i == 16 || i == 17)) res[i - 16] = __builtin_amdgcn_raw_buffer_load_b128(RSRC(a.R), orow + (i - 16) * 64, 0, 0);
    };
    constexpr int XD = KN_XD + (KN_SK + 1) / 2;
#pragma unroll
    for (int i = 0; i < XD; ++i) { ldX(i, 1); ldX(i - KN_SK, 0); __builtin_amdgcn_sched_barrier(0); }
    unsigned gsrc[2];
#pragma unroll
    for (int i = 0; i < 2; ++i) {
      const int j = wave + 8 * i, jj = j >= 7 ? j - 7 : j;
      const int b = jj * 1024 + lane * 16, q = b / 224, pos = (b % 224) >> 4;
      gsrc[i] = (unsigned)((8 * ((q & 15) >> 2) + (q & 3) + 4 * (q >> 4)) * DF_ + ((xcd & 3) * 8 + (idx & 7)) * 112 + pos * 8) * 2u;
      asm volatile("" : "+v"(gsrc[i]));
    }
    f32x4 acc[4][2];
#pragma unroll
    for (int n = 0; n < 4; ++n) { acc[n][0] = (f32x4){0.f, 0.f, 0.f, 0.f}; acc[n][1] = acc[n][0]; }
    __builtin_amdgcn_sched_barrier(0);
    f32x4 pa[4];
    auto slot = [&](int w) { return wo_pos(0) + w * 4096 + lane * 16; };
    static_assert(KN_SK + XD >= 2 && KN_SB <= KN_SK && XD <= 6, "order");
    static_for<0, 16 + KN_SK + 1>([&](auto ic) {
      constexpr int i = decltype(ic)::value;
#define KN_WP 3
      if constexpr (KN_WP == 1 && i < 16) __builtin_amdgcn_s_setprio(3 - (i & 3));
      if constexpr (KN_WP == 2 && i < 16 && !(i & 3)) __builtin_amdgcn_s_setprio(3 - (i >> 2));
      if constexpr (KN_WP == 3 && i < 16 && !(i & 1)) __builtin_amdgcn_s_setprio(3 - ((i >> 1) & 3));
      if constexpr (i >= 2 && i < 16) { asm volatile("s_waitcnt lgkmcnt(%0)" :: "n"(i < 15 ? 8 : 0) : "memory"); pin(f[i]); }
      if constexpr (i == (KN_WE & ~1) - 2) raw_barrier();
      if constexpr (i == 8 - XD - 2) fl = flag_get(a.sync, fo_off, 16);
      if constexpr (i == 8 - XD) { if (__builtin_expect(dn && down(fl), 0)) wait_flags(lds0, a.sync, fo_off, 16, 0); }
      if constexpr (i == 16) {
        STAMP(2);
#pragma unroll
        for (int n = 0; n < 4; ++n) *(f32x4*)(smem + slot(wave) + n * 1024) = acc[n][1];
        __builtin_amdgcn_s_setprio(1);
        __builtin_amdgcn_sched_barrier(0);
      }
      if constexpr (i == 16 + KN_SB) {
        lds_barrier();
        STAMP(4);
        static_for<0, 4>([&](auto nc) { constexpr int n = decltype(nc)::value; pa[n] = __builtin_bit_cast(f32x4, ds_b128<n * 1024>(lds0 + slot(wave ^ 4))); });
        __builtin_amdgcn_sched_barrier(0);
      }
      static_for<0, 8>([&](auto jc) {
        constexpr int j = decltype(jc)::value, n = j & 3, m = 1 - (j >> 2), t = m ? i : i - KN_SK;
        if constexpr (t >= 0 && t < 16) acc[n][m] = MFMA(__builtin_shufflevector(f[t].lo[n], f[t].hi[n], 0, 1, 2, 3, 4, 5, 6, 7), __builtin_bit_cast(bf16x8, xr[t][m]), acc[n][m]);
        if constexpr (m == 0 && t == 16) acc[n][0] = MFMA((n & 1) ? s1 : s0, __builtin_bit_cast(bf16x8, res[n >> 1]), acc[n][0]);
        if constexpr (i + 2 < 16) rd(IC<i + 2>{}, jc);
        if constexpr (j == 1) ldX(i + XD, 1);
        if constexpr (j == 5) ldX(i + XD - KN_SK, 0);
        __builtin_amdgcn_sched_barrier(0);
      });
      if constexpr (i == 0) STAMP(1);
    });
    auto ldG = [&](int i, int u) { const int j = wave + 8 * i;
      __builtin_amdgcn_raw_ptr_buffer_load_lds(RSRC(j >= 7 ? nx.W1 : nx.W0), LDSP(lds0 + 98304 + (u ^ 2) * 14592 + (j >= 7 ? 7184 + (j - 7) * 1024 : j * 1024)), 16, gsrc[i], (u + (xcd >> 2) * 16) * 32 * DF_ * 2, 0, 0); };
    auto ldG2 = [&](int u) { ldG(0, u); ldG(0, u + 1); if (wave < 6) { ldG(1, u); ldG(1, u + 1); } };
    if constexpr (KN_G2 != 2) ldG2(0);
    asm volatile("s_waitcnt lgkmcnt(0)" : "+v"(pa[0]), "+v"(pa[1]), "+v"(pa[2]), "+v"(pa[3]) :: "memory");
    const auto rO = __builtin_amdgcn_make_buffer_rsrc((void*)a.O, 0, 0x7fffffff, 0x00020000);
#pragma unroll
    for (int i = 0; i < 2; ++i) {
      pa[2 * i] += acc[2 * i][0]; pa[2 * i + 1] += acc[2 * i + 1][0];
      const f32x4 u = pa[2 * i], v = pa[2 * i + 1];
      const auto t0 = __builtin_amdgcn_permlane16_swap(pk_bf16(u[0], u[1]), pk_bf16(v[0], v[1]), false, false);
      const auto t1 = __builtin_amdgcn_permlane16_swap(pk_bf16(u[2], u[3]), pk_bf16(v[2], v[3]), false, false);
      __builtin_amdgcn_raw_buffer_store_b128((u32x4){t0[0], t1[0], t0[1], t1[1]}, rO, ofrg + i * 1024, 0, 16);
    }
    __builtin_amdgcn_sched_barrier(0);
    __builtin_amdgcn_s_setprio(0);
    auto sum4 = [](float v) {
      auto p = __builtin_amdgcn_permlane16_swap(__float_as_uint(v), __float_as_uint(v), false, false); v = __uint_as_float(p[0]) + __uint_as_float(p[1]);
      auto q = __builtin_amdgcn_permlane32_swap(__float_as_uint(v), __float_as_uint(v), false, false); return __uint_as_float(q[0]) + __uint_as_float(q[1]); };
    f32x2 e[8], sm = {0.f, 0.f};
#pragma unroll
    for (int j = 0; j < 8; ++j) { e[j] = (f32x2){pa[j >> 1][(j & 1) * 2], pa[j >> 1][(j & 1) * 2 + 1]}; sm += e[j]; }
    const float mu = sum4(sm[0] + sm[1]) * (1.f / 64.f);
    f32x2 t2 = {0.f, 0.f};
#pragma unroll
    for (int j = 0; j < 8; ++j) { const f32x2 d = e[j] - mu; t2 += d * d; }
    const float q2 = sum4(t2[0] + t2[1]);
    __builtin_amdgcn_raw_buffer_store_b64((u32x2){__float_as_uint(mu), __float_as_uint(q2)}, __builtin_amdgcn_make_buffer_rsrc((void*)a.st, 0, 0x7fffffff, 0x00020000),
                                          lane < 16 ? (unsigned)((tn * S_ + rb * 16 + lane) * 8) : 0x80000000u, 0, 16);
    STAMP(7);
    STAMP(8);
    if constexpr (KN_G2 == 2) ldG2(0);
    if constexpr (KN_G2) { ldG2(2); if (wave < 6) wait_vm<4 * KN_G2>(); else wait_vm<2 * KN_G2>(); } else wait_vm<0>();
    lds_barrier();
    if (wave == 0) flag_set(a.sync, F_H + F_PAN(panel) + ((tm & 1) * 16 + tn) * 8, tg, 0);
    STAMP(3);
  }
#endif
}

struct FragD { s16x4 lo[8], hi[8]; };
enum { O_X, O_W, O_SP, O_FA, O_FP, O_QC, O_QD, O_QP, O_RES, O_LP, O_CC, O_CD, O_CP, O_BAR, O_AR, O_AP };
constexpr int DN_A = 12, DN_E = 0, DN_PRE = 11;
constexpr unsigned char DN_OPS[][4] = {
 {0,10,O_CC,1}, {0,11,O_X,6}, {0,12,O_X,7}, {0,14,O_X,8}, {0,15,O_X,9}, {1,2,O_X,10}, {1,5,O_X,11}, {1,9,O_X,12}, {1,13,O_W,11}, {2,0,O_FA,2}, {2,2,O_X,13}, {2,5,O_X,14}, {2,9,O_X,15}, {2,13,O_W,12},
 {3,2,O_X,16}, {3,5,O_X,17}, {3,9,O_X,18}, {3,13,O_X,19}, {4,2,O_BAR,12}, {4,3,O_W,14}, {4,10,O_W,13}, {4,16,O_QC,2}, {5,2,O_W,17}, {5,7,O_W,16}, {5,12,O_W,15},
 {6,0,O_CC,2}, {6,2,O_X,20}, {6,5,O_X,21}, {6,9,O_X,22}, {6,13,O_W,18}, {7,2,O_X,23}, {7,5,O_X,24}, {7,9,O_X,25}, {7,13,O_W,19}, {8,2,O_X,26}, {8,5,O_X,27}, {8,9,O_X,28}, {8,13,O_W,20},
 {9,2,O_X,29}, {9,5,O_X,30}, {9,9,O_X,31}, {9,13,O_W,21}, {10,2,O_X,32}, {10,5,O_X,33}, {10,9,O_X,34}, {10,13,O_W,22}, {11,2,O_BAR,19}, {11,3,O_X,35}, {11,6,O_X,36}, {11,10,O_X,37}, {11,13,O_W,23}, {11,16,O_QD,0},
 {12,2,O_X,38}, {12,5,O_X,39}, {12,9,O_X,40}, {12,13,O_X,41}, {13,2,O_X,42}, {13,5,O_X,43}, {13,9,O_X,44}, {13,13,O_X,45}, {14,0,O_CD,0}, {14,2,O_X,46}, {14,7,O_X,47}, {14,12,O_RES,0},
 {15,2,O_BAR,23}, {15,3,O_W,25}, {15,10,O_W,24}, {16,2,O_W,27}, {16,7,O_W,26}, {16,12,O_RES,1}, {17,2,O_W,29}, {17,9,O_W,28}, {18,2,O_W,30}, {18,7,O_SP,0}, {18,12,O_SP,1},
 {19,2,O_BAR,23}, {19,3,O_X,48}, {19,6,O_X,49}, {19,10,O_X,50}, {19,13,O_SP,2}, {20,2,O_X,51}, {20,5,O_X,52}, {20,9,O_W,31}, {20,13,O_SP,3},
 {21,2,O_X,53}, {21,5,O_X,54}, {21,9,O_X,55}, {21,13,O_X,56}, {22,0,O_FP,0}, {22,2,O_X,57}, {22,5,O_X,58}, {22,9,O_X,59}, {22,13,O_X,60},
 {23,0,O_FP,1}, {23,2,O_BAR,31}, {23,3,O_X,61}, {23,7,O_X,62}, {23,12,O_X,63}, {24,1,O_SP,4}, {24,2,O_SP,5}, {24,8,O_QP,0}, {25,8,O_AR,0},
 {26,0,O_FP,2}, {27,0,O_CP,0}, {27,0,O_CP,1}, {27,2,O_LP,0}, {27,5,O_LP,1}, {28,2,O_LP,2}, {28,5,O_LP,3}, {28,8,O_QP,2}, {31,0,O_CP,2}, {31,2,O_LP,4}, {31,5,O_LP,5},
 {32,0,O_AP,0}, {32,0,O_AP,1}, {32,0,O_AP,2}, {32,0,O_AP,3}, {32,0,O_AP,4}, {32,0,O_AP,5},
};
constexpr int DN_N = sizeof(DN_OPS) / 4, DN_H = (24 - DN_A) / 2, DN_B = DN_A + DN_H, BB = 24 + DN_E, NS = BB + 8;
constexpr int dn_nm(int v) { return v < 24 - DN_E ? 16 : 8; }
constexpr int dn_unit(int v, int n) { return v < DN_A ? v : (v >= BB ? v - DN_E : DN_A + 2 * (((v < 24 ? v : v - DN_E) - DN_A) % DN_H) + (n >> 2)); }
constexpr int dn_wpos(int u, int h) { constexpr int P[19] = {8, 9, 10, 11, 16, 17, 18, 4, 5, 6, 7, 14, 15, 0, 1, 2, 3, 12, 13}; return P[u % 19] * 8192 + h * 4096; }
constexpr int dn_first(int v, int g) { int i = 0; while (i < DN_N && (DN_OPS[i][0] < v || (DN_OPS[i][0] == v && DN_OPS[i][1] < g))) ++i; return i; }
constexpr int dn_find(int kind, int arg) { for (int i = 0; i < DN_N; ++i) if (DN_OPS[i][2] == kind && DN_OPS[i][3] == arg) return i; return -1; }
constexpr int dn_vm(int lo, int hi) { int c = 0; for (int i = lo; i < hi; ++i) c += DN_OPS[i][2] <= O_LP; return c; }
constexpr bool dn_bar(int v) { for (int i = 0; i < DN_N; ++i) if (DN_OPS[i][0] == v && DN_OPS[i][2] == O_BAR) return true; return false; }
constexpr int dn_cnt(int w, int i) { int p = -1; for (int u = DN_PRE; u <= w; ++u) if (dn_find(O_W, u) > p) p = dn_find(O_W, u); return p >= 0 ? dn_vm(p + 1, i) : 63; }
constexpr bool dn_ok() {
  int have = DN_PRE - 1, bstep = 0;
  for (int v = 0; v + 1 < NS; ++v) {
    for (int i = dn_first(v, 0); i < dn_first(v + 1, 0); ++i) {
      if (DN_OPS[i][2] == O_BAR) { if (DN_OPS[i][3] > have) have = DN_OPS[i][3]; bstep = v; for (int u = DN_PRE; u <= have; ++u) if (dn_find(O_W, u) < 0 || dn_find(O_W, u) > i) return false; }
      if (DN_OPS[i][2] == O_W) {
        if (bstep < 1 && DN_OPS[i][3] > 12) return false;
        for (int t = bstep + 1; t < NS; ++t) for (int n = 0; n < 8; n += 4) if (dn_unit(t, n) < DN_OPS[i][3] && dn_wpos(dn_unit(t, n), 0) == dn_wpos(DN_OPS[i][3], 0)) return false;
      }
    }
    if (dn_unit(v + 1, 7) > have) return false;
  }
  for (int x = 6; x < 64; ++x) {
    int n = 0; for (int i = 0; i < dn_find(O_X, x); ++i) { if (DN_OPS[i][2] == O_CC) n = DN_OPS[i][3]; if (DN_OPS[i][2] == O_CD) n = 4; }
    if (n < (x < 20 ? 1 : (x < 48 ? 2 : 4))) return false;
  }
  if (dn_find(O_FA, 2) < 0) return false;
  return dn_find(O_SP, 0) >= dn_first(DN_B, 0) && dn_find(O_SP, 4) >= dn_first(24, 0);
}
static_assert(dn_ok(), "schedule");
constexpr int dn_rd(int v, int I) { int c = 0; const int r0 = dn_bar(v) ? 2 : 1; if (v + 1 < NS) for (int r = 0; r < 16; ++r) c += r0 + r * (dn_nm(v) - r0 + 1) / 16 <= I; return c; }
struct DnTab { short first[NS + 1][18], rd[NS][18], wait[DN_N]; };
constexpr DnTab dn_tab() {
  DnTab t = {};
  for (int v = 0; v <= NS; ++v) for (int g = 0; g < 18; ++g) { t.first[v][g] = dn_first(v, g); if (v < NS) t.rd[v][g] = dn_rd(v, g); }
  for (int i = 0; i < DN_N; ++i) {
    const int kind = DN_OPS[i][2], arg = DN_OPS[i][3];
    t.wait[i] = kind == O_CC ? (KN_QP == 3 ? 0 : (KN_QP ? 1 : 3)) + dn_vm(0, i) : kind == O_FA ? dn_vm(0, i) : (kind == O_FP ? dn_vm(dn_find(O_SP, 2 * arg + 1) + 1, i) : (kind == O_BAR ? dn_cnt(arg, i) : 0));
  }
  return t;
}
constexpr DnTab DT = dn_tab();
#define KN_SLP 5
#define KN_PRI 1
template <int MODE, int U>
__device__ __forceinline__ void dn_phase(const GemmArgs& a, const Late& late, unsigned wsrc, int pol = 0) {
#if defined(__HIP_DEVICE_COMPILE__)
  extern __shared__ __attribute__((aligned(16))) char smem[];
  if constexpr (MODE != 1) STAMP(0);
  const unsigned lds0 = (unsigned)(size_t)(lds_char_ptr)smem;
  const int tid = threadIdx.x;
  int lane = tid & 63; asm volatile("" : "+v"(lane));
  const int wave = __builtin_amdgcn_readfirstlane(tid >> 6);
  const int bid = blockIdx.x, xcd = bid & 7, idx = bid >> 3;
  const int panel = (xcd >> 2) * 4 + (idx >> 3), c = idx & 7, q = xcd & 3;
  const int g4 = wave & 3, hh = U >= 64 ? U & 1 : wave >> 2;
#define KN_ABL 0
  const auto rW = RSRCN(a.W0, (KN_ABL & 1) ? 0 : 0x7fffffff);
  if constexpr (MODE != 1) wsrc = dn_wsrc(lane, wave);
  auto uj = [&](int v) { return q * 8 + ((c + (v < 3 ? 0 : 1 + (v - 3) % 7)) & 7); };
  auto us = [](int v) { return v < 3 ? v : (v - 3) / 7; };
  auto ldW = [&](int v) {
    const int row = v < 24 ? uj(v) * 112 + us(v) * 16 + (g4 & 1) * 8 + (g4 >> 1) * 56 : ((((v + c) & 7) * 2 + hh) * 4 + g4) * 56 + 48;
    __builtin_amdgcn_raw_ptr_buffer_load_lds(rW, LDSP(lds0 + dn_wpos(v, 0) + hh * 4096 + g4 * 1024), 16, wsrc, row * (D_ * 2) + c * 256 + (((v < 24 ? hh : 0) ^ (q >> 1)) << 7), 0, 0);
  };
  const int fa_off = F_ACT(panel) + (wave >> 1) * 512;
  if constexpr (MODE == 1) {
    if constexpr (U < 32) { if constexpr (U < DN_PRE) ldW(U); }
    else if constexpr (U >= 64) ldW((U - 64) >> 1);
    return;
  }
  const Tag tg = {(unsigned)__builtin_amdgcn_readfirstlane(late.tag[0]), (unsigned)__builtin_amdgcn_readfirstlane(late.tag[1])};
  const int fr = lane & 15, fq = lane >> 4, wq = (lane >> 2) & 3, wp = lane & 3;
  const int rb = wave * 2 + (q & 1);
  f32x4 acc[8][2];
#pragma unroll
  for (int n = 0; n < 8; ++n) { acc[n][0] = (f32x4){0.f, 0.f, 0.f, 0.f}; acc[n][1] = acc[n][0]; }
  unsigned wa[3][4];
#pragma unroll
  for (int i = 0; i < 4; ++i) { const int row = 8 * fq + wq; wa[0][i] = lds0 + row * 128 + (((i << 1) ^ wswz(row)) << 4) + wp * 8; wa[1][i] = wa[0][i] + 65536; wa[2][i] = wa[0][i] + 131072; }
  const auto rX = RSRCN(a.X, (KN_ABL & 2) ? 0 : 0x7fffffff);
  const unsigned xs[2] = {(unsigned)(rb_a(panel * 16 + rb, pol) + lane * 16), (unsigned)(rb_a(panel * 16 + (rb ^ 1), pol) + lane * 16)};
  FragD fa, fb;
  u32x4 xr[32][2];
  auto rdw = [&](FragD& f, auto vc, auto rc) {
    constexpr int v = decltype(vc)::value, r = decltype(rc)::value, n = r >> 1, u = dn_unit(v, n);
    constexpr int p = dn_wpos(u, (v < DN_A || v >= BB) ? n >> 2 : (v < DN_B ? 1 : 0)), o = (p & 65535) + (r & 1) * 512, b = p >> 16;
    if constexpr (r & 1) f.hi[n] = ds_tr<o>(wa[b][n & 3]); else f.lo[n] = ds_tr<o>(wa[b][n & 3]);
  };
  auto ldX = [&](int v, int m) {
    if (v < 24) xr[v][m] = __builtin_amdgcn_raw_buffer_load_b128(rX, xs[m], ko<4>(uj(v) * 3072 + us(v) * 1024) ^ za(panel * 16 + rb), 16);
    else xr[v][m] = __builtin_amdgcn_raw_buffer_load_b128(rX, xs[0] + m * 1024, ko<4>(384 * 256 + ((v + c) & 7) * 2048) ^ za(panel * 16 + rb), 16);
  };
  const auto rS = RSRC(a.slab);
  const int ck = (fq & 1) * 2 + (fq >> 1);
  u32x4 pp[3][2], res[2];
  bf16x8 sp0, sp1;
  auto addP = [&](int j) { acc[2 * (j & 1)][0] = MFMA(sp0, __builtin_bit_cast(bf16x8, pp[j >> 1][j & 1]), acc[2 * (j & 1)][0]); acc[2 * (j & 1) + 1][0] = MFMA(sp1, __builtin_bit_cast(bf16x8, pp[j >> 1][j & 1]), acc[2 * (j & 1) + 1][0]); };
  auto ldP = [&](int j, bool ok) { pp[j >> 1][j & 1] = __builtin_amdgcn_raw_buffer_load_b128(RSRCN(a.slab, ok ? 0x7fffffff : 0), lane * 16 + (j & 1) * 1024, sl_at(bid, wave, j >> 1, pol), 16); };
  auto st128 = [&](u32x4 v, auto rs, unsigned vo, int so) {
    __builtin_amdgcn_raw_buffer_store_b128(v, rs, vo, so, 16);
    __builtin_amdgcn_sched_barrier(0);
    asm volatile("s_nop 1" ::: "memory");
    __builtin_amdgcn_sched_barrier(0); };
  auto stP = [&](int j) {
    const int k = 3 - (j >> 1), i = j & 1;
    const f32x4 s0 = acc[(k >> 1) * 4 + 2 * i][k & 1], s1 = acc[(k >> 1) * 4 + 2 * i + 1][k & 1];
    st128((u32x4){pk_bf16(s0[0], s0[1]), pk_bf16(s0[2], s0[3]), pk_bf16(s1[0], s1[1]), pk_bf16(s1[2], s1[3])}, rS, lane * 16 + i * 1024, sl_at(bid ^ k, wave, j >> 1, pol)); };
  const int fp_off = F_PART(bid) + wave * 64;
  unsigned got = 0;
  unsigned orow = 0;
  u32x2 fl = {0u, 0u};
#define TS5(i)
  auto op = [&](auto ic) {
    constexpr int i = decltype(ic)::value, kind = DN_OPS[i][2], arg = DN_OPS[i][3];
    if constexpr (kind == O_X) ldX(arg >> 1, arg & 1);
    if constexpr (kind == O_W) ldW(arg);
    if constexpr (kind == O_SP) stP(arg);
    if constexpr (kind == O_FA) { wait_vm<DT.wait[i]>(); flag_set(a.sync, fa_off + ((q * 8 + c) * 2 + (wave & 1)) * 8 + (arg - 1) * 16384, tg, 0); }
    if constexpr (kind == O_FP) { wait_vm<DT.wait[i]>(); if constexpr (arg) STAMP(10 + arg); flag_set(a.sync, F_PART(bid ^ (3 - arg)) + wave * 64 + arg * 8, tg, 0); }
    if constexpr (kind == O_QC) fl = flag_get(a.sync, fa_off + q * 128 + (arg - 1) * 16384, 16);
    if constexpr (kind == O_QD) fl = flag_get(a.sync, fa_off + 16384, 64);
    if constexpr (kind == O_QP) fl = flag_get(a.sync, fp_off, 3);
    if constexpr (kind == O_RES) res[arg] = __builtin_amdgcn_raw_buffer_load_b128(RSRC(a.R), lane * 16 + arg * 1024, rb_h(panel * 16 + rb, pol) + ko<2>(c * 4096 + (q >> 1) * 2048), 16);
    if constexpr (kind == O_AR) {
      const bf16x8 s0 = pick(fq == (fr >> 3), fr & 7), s1 = pick(fq == 2 + (fr >> 3), fr & 7);
#pragma unroll
      for (int n = 0; n < 4; ++n) acc[n][0] = MFMA((n & 1) ? s1 : s0, __builtin_bit_cast(bf16x8, res[n >> 1]), acc[n][0]);
      sp0 = pick(fq == (fr >> 2), fr & 3); sp1 = pick(fq == (fr >> 2), 4 + (fr & 3));
      orow = (unsigned)(((panel * 16 + rb) * 16 + fr) * (D_ * 2) + c * 256 + (q >> 1) * 128 + ck * 16);
      asm volatile("" : "+v"(sp0), "+v"(sp1), "+v"(orow));
    }
    if constexpr (kind == O_AP) addP(arg);
    if constexpr (kind == O_LP) ldP(arg, (got >> (arg >> 1)) & 1);
    if constexpr (kind == O_CC && arg == 1) {
      wait_vm<DT.wait[i]>();
      f32x2 f2 = ds_b64<0>(lds0 + FLP + wave * 128 + (lane & 15) * 8);
      asm volatile("s_waitcnt lgkmcnt(0)" : "+v"(f2) :: "memory");
      fl = __builtin_bit_cast(u32x2, f2);
    }
    if constexpr (kind == O_CC) { if (__builtin_expect(!flags_up(fl, 16, tg, 0), 0)) wait_flags(lds0, a.sync, fa_off + q * 128 + (arg - 1) * 16384, 16, 0); if constexpr (arg == 1) STAMP(4); }
    if constexpr (kind == O_CD) { if (__builtin_expect(!flags_up(fl, 64, tg, 0), 0)) wait_flags(lds0, a.sync, fa_off + 16384, 64, 0); }
    if constexpr (kind == O_CP) got |= (unsigned)__builtin_amdgcn_ballot_w64(fl[0] == tg.lo && fl[1] == tg.hi) & (1u << arg) & (got + 1u);
    if constexpr (kind == O_BAR) { if constexpr (!(KN_ABL & 4)) { wait_vm<DT.wait[i]>(); lds_barrier(); } }
    __builtin_amdgcn_sched_barrier(0);
  };
  STAMP(9);
  lds_barrier();
  STAMP(6);
  {
    const unsigned e0 = lds0 + rb * 1024 + lane * 16, e1 = lds0 + (rb ^ 1) * 1024 + lane * 16;
    xr[0][0] = __builtin_bit_cast(u32x4, ds_b128<OWNE>(e0)); xr[0][1] = __builtin_bit_cast(u32x4, ds_b128<OWNE>(e1));
    xr[1][0] = __builtin_bit_cast(u32x4, ds_b128<OWNE + 16384>(e0)); xr[1][1] = __builtin_bit_cast(u32x4, ds_b128<OWNE + 16384>(e1));
    xr[2][0] = __builtin_bit_cast(u32x4, ds_b128<0>(e0 + OWNM)); xr[2][1] = __builtin_bit_cast(u32x4, ds_b128<0>(e1 + OWNM));
    static_for<0, 16>([&](auto rc) { rdw(fa, IC<0>{}, rc); });
  }
  static_for<0, NS>([&](auto vc) {
    constexpr int v = decltype(vc)::value, NM = dn_nm(v);
    FragD& cur = (v & 1) ? fb : fa; FragD& nxt = (v & 1) ? fa : fb;
    TS5(v);
    if constexpr (KN_PRI == 1) __builtin_amdgcn_s_setprio(3 - (v & 3));
    if constexpr (KN_PRI == 2) { if (wave >= 4) __builtin_amdgcn_s_setprio(v & 1); else __builtin_amdgcn_s_setprio(1 - (v & 1)); }
    if constexpr (KN_PRI == 3) { if constexpr (!(v & 1)) __builtin_amdgcn_s_setprio(3 - ((v >> 1) & 3)); }
    static_for<DT.first[v][0], DT.first[v][1]>(op);
    __builtin_amdgcn_sched_barrier(0);
    static_for<0, NM>([&](auto ic) {
      constexpr int I = decltype(ic)::value, n = NM == 16 ? I >> 1 : I, m = NM == 16 ? I & 1 : (v < 24 ? 1 : (v < BB ? 0 : I >> 2));
      if constexpr (I % (NM / 4) == 0) {
        constexpr int left = 12 - 2 * n + DT.rd[v][I];
        asm volatile("s_waitcnt lgkmcnt(%0)" :: "n"(left < 15 ? left : 15) : "memory");
        asm volatile("" : "+v"(cur.lo[n]), "+v"(cur.hi[n]), "+v"(cur.lo[n + 1]), "+v"(cur.hi[n + 1]));
        if constexpr (v == 0 && I == 0) {
#pragma unroll
          for (int i = 0; i < 3; ++i) asm volatile("" : "+v"(xr[i][0]), "+v"(xr[i][1]));
          STAMP(1);
        }
        __builtin_amdgcn_sched_barrier(0);
      }
      const bf16x8 w = __builtin_shufflevector(cur.lo[n], cur.hi[n], 0, 1, 2, 3, 4, 5, 6, 7);
      constexpr int an = v < DN_A ? n : (v < DN_B ? 4 : 0) + (n & 3), am = v < 24 ? m : 0;
      acc[an][am] = MFMA(w, __builtin_bit_cast(bf16x8, xr[dn_unit(v, n)][m]), acc[an][am]);
      __builtin_amdgcn_sched_barrier(0);
      static_for<DT.first[v][I + 1], DT.first[v][I + 2]>(op);
      static_for<DT.rd[v][I], DT.rd[v][I + 1]>([&](auto rc) { rdw(nxt, IC<v + 1>{}, rc); });
      __builtin_amdgcn_sched_barrier(0);
    });
    if constexpr (v == 23) STAMP(10);
  });
  STAMP(2);
  TS5(NS);
  const auto rF = RSRCN((const char*)a.sync + fp_off, got != 7u ? 24 : 0);
  u32x2 f0 = __builtin_amdgcn_raw_buffer_load_b64(rF, lane * 8, 0, 16);
  __builtin_amdgcn_sched_barrier(0);
  static_for<DT.first[NS][0], DN_N>(op);
  if (got != 7u) {
    if (!flags_up(f0, 3, tg, 0)) {
      f0 = __builtin_amdgcn_raw_buffer_load_b64(rF, lane * 8, 0, 16);
      asm volatile("s_sleep %0" :: "n"(KN_SLP) : "memory");
      for (unsigned polls = 1;; ++polls) {
        const u32x2 f1 = __builtin_amdgcn_raw_buffer_load_b64(rF, lane * 8, 0, 16);
        asm volatile("" ::: "memory");
        if (flags_up(f0, 3, tg, 0)) break;
        f0 = __builtin_amdgcn_raw_buffer_load_b64(rF, lane * 8, 0, 16);
        asm volatile("" ::: "memory");
        if (flags_up(f1, 3, tg, 0) || (!(polls & 255u) && !in_time(lds0))) break;
      }
    }
#pragma unroll
    for (int j = 5; j >= 0; --j) ldP(j, !((got >> (j >> 1)) & 1));
    __builtin_amdgcn_sched_barrier(0);
#pragma unroll
    for (int j = 0; j < 6; ++j) addP(j);
  }
  TS5(NS + 1);
  STAMP(5);
#define KN_OAUX 0
#pragma unroll
  for (int i = 0; i < 2; ++i) {
    const f32x4 u = acc[2 * i][0], w = acc[2 * i + 1][0];
    const auto s0 = __builtin_amdgcn_permlane16_swap(pk_bf16(u[0], u[1]), pk_bf16(w[0], w[1]), false, false);
    const auto s1 = __builtin_amdgcn_permlane16_swap(pk_bf16(u[2], u[3]), pk_bf16(w[2], w[3]), false, false);
    __builtin_amdgcn_raw_buffer_store_b128((u32x4){s0[0], s1[0], s0[1], s1[1]}, RSRC(a.O), orow + i * 64, 0, KN_OAUX);
  }
  if (bid == 0 && wave == 0) flag_set(a.sync, EPOCH, tg, 0);
  STAMP(7);
  STAMP(8);
  STAMP(3);
#endif
}

struct Ptrs { const bf16_t *x, *wqkv, *wo, *wg, *wu, *wd; bf16_t* out; char* ws; };
#define DBG(i)
#define WS_O ((bf16_t*)(p.ws + (8u << 20)))
#define WS_SL ((float*)(p.ws + (48u << 20)))
#define WS_H ((bf16_t*)(p.ws + (16u << 20)))
#define WS_ST ((float*)(p.ws + (24u << 20)))
#define WS_CS ((float*)(p.ws + (25u << 20)))
#define WS_SY ((unsigned*)(p.ws + (26u << 20)))
#define WS_A ((bf16_t*)(p.ws + (28u << 20)))
#if defined(__HIP_DEVICE_COMPILE__)
#define ARG_WARM(v) do { asm volatile("s_load_dword %0, %1, 0x0" : "=s"(v) : "s"(__builtin_amdgcn_kernarg_segment_ptr())); __builtin_amdgcn_sched_barrier(0); } while (0)
#else
#define ARG_WARM(v)
#endif
struct Args4 { GemmArgs q, b, c, d; };
extern "C" __global__ __launch_bounds__(512, 1) void k_all(Ptrs p) {
  unsigned warm = 0;
  ARG_WARM(warm);
#define PAD(k)
  const Args4 a{GemmArgs{p.x, p.wqkv, nullptr, nullptr, WS_O, (float*)WS_H, p.wg, p.wu, WS_CS, p.wd, p.wo, WS_SY, (float*)WS_A, warm DBG(0)},
    GemmArgs{WS_O, p.wo, nullptr, p.x, WS_H, WS_ST, nullptr, nullptr, nullptr, p.wd, nullptr, WS_SY, nullptr, 0u DBG(1)},
    GemmArgs{WS_H, p.wg, p.wu, p.out, WS_A, WS_ST, nullptr, nullptr, WS_CS, p.wd, nullptr, WS_SY, WS_SL, 0u DBG(2)},
    GemmArgs{WS_A, p.wd, nullptr, WS_H, p.out, nullptr, nullptr, nullptr, nullptr, nullptr, nullptr, WS_SY, WS_SL, 0u DBG(3)}};
  int pol = 0;
  k1_phase(a.q, [&](auto md, auto lo, auto hi) { wo_phase<decltype(md)::value, decltype(lo)::value, decltype(hi)::value>(a.b, a.c); }, pol);
  PAD(1);
  wo_phase<2, 0, 0>(a.b, a.c, pol);
  PAD(2);
  Late late;
  glu_phase(a.c, [&](auto tc, unsigned w) { dn_phase<1, decltype(tc)::value>(a.d, late, w); }, late, pol);
  PAD(3);
  dn_phase<2, 0>(a.d, late, 0u, pol);
  PAD(4);
}

extern "C" __global__ void k_zend() {}

extern "C" void kernel_launch(void* const* d_in, const int* in_sizes, int n_in, void* d_out, int out_size, void* d_ws,
                              size_t ws_size, hipStream_t stream) {
  const Ptrs p{(const bf16_t*)d_in[0], (const bf16_t*)d_in[1], (const bf16_t*)d_in[2], (const bf16_t*)d_in[4], (const bf16_t*)d_in[5], (const bf16_t*)d_in[6], (bf16_t*)d_out, (char*)d_ws};
  constexpr int L_all = SNAP + 64;
  static_assert(SNAP >= 3 * 256 * 128 + 4 * 14592 + 4096 + 1024 + 256 + 512 + 128 + 64 && L_all <= 163840, "LDS");
  (void)hipFuncSetAttribute((const void*)k_all, hipFuncAttributeMaxDynamicSharedMemorySize, L_all);
  k_all<<<256, 512, L_all, stream>>>(p);
}
```

```cpp
#include <hip/hip_runtime.h>
#include <hip/hip_bf16.h>
#include <stdint.h>
#include <type_traits>

typedef unsigned short bf16_t;
typedef short bf16x8 __attribute__((ext_vector_type(8)));
typedef short s16x4 __attribute__((ext_vector_type(4)));
typedef float f32x4 __attribute__((ext_vector_type(4)));
typedef float f32x2 __attribute__((ext_vector_type(2)));
typedef unsigned u32x2 __attribute__((ext_vector_type(2)));
typedef unsigned u32x4 __attribute__((ext_vector_type(4)));
typedef __bf16 bf16x2_t __attribute__((ext_vector_type(2)));
typedef __attribute__((address_space(3))) void* lds_void_ptr;
typedef __attribute__((address_space(3))) char* lds_char_ptr;

constexpr int S_ = 2048, D_ = 1024, DF_ = 3584;
constexpr int SH = 65536, SA = DF_ * 32;
__device__ __forceinline__ int rb_h(int rb, int pol) { return (rb & 63) * SH + ((((rb >> 6) ^ (rb >> 5) ^ pol) & 1) << 13); }
__device__ __forceinline__ int ko(int off) { return off + (off & ~8191); }
__device__ __forceinline__ int sl_at(int bid, int wave, int pol) { return ((bid >> 3) * 8 + (bid & 3) * 2 + (wave >> 2)) * 16384 + ((((bid >> 2) ^ (bid >> 7) ^ pol) & 1) << 13) + (wave & 3) * 2048; }

__device__ __forceinline__ float bflo(unsigned v) { return __uint_as_float(v << 16); }
__device__ __forceinline__ float bfhi(unsigned v) { return __uint_as_float(v & 0xffff0000u); }
__device__ __forceinline__ unsigned pk_bf16(float lo, float hi) {
  f32x2 v = {lo, hi};
  bf16x2_t b = __builtin_convertvector(v, bf16x2_t);
  return __builtin_bit_cast(unsigned, b);
}
__device__ __forceinline__ int wswz(int row) { return (((row >> 1) & 1) | (((row >> 3) & 1) << 1)) << 1; }

template <int OFF> __device__ __forceinline__ s16x4 ds_tr(unsigned addr) {
  s16x4 r; asm volatile("ds_read_b64_tr_b16 %0, %1 offset:%2" : "=v"(r) : "v"(addr), "n"(OFF)); return r;
}
template <int OFF> __device__ __forceinline__ bf16x8 ds_b128(unsigned addr) {
  bf16x8 r; asm volatile("ds_read_b128 %0, %1 offset:%2" : "=v"(r) : "v"(addr), "n"(OFF)); return r;
}
template <int OFF> __device__ __forceinline__ f32x2 ds_b64(unsigned addr) {
  f32x2 r; asm volatile("ds_read_b64 %0, %1 offset:%2" : "=v"(r) : "v"(addr), "n"(OFF)); return r;
}
__device__ __forceinline__ float sum8(float v) {
  v += __int_as_float(__builtin_amdgcn_update_dpp(0, __float_as_int(v), 0x128, 0xf, 0xf, false));
  auto a = __builtin_amdgcn_permlane16_swap(__float_as_uint(v), __float_as_uint(v), false, false);
  v = __uint_as_float(a[0]) + __uint_as_float(a[1]);
  auto b = __builtin_amdgcn_permlane32_swap(__float_as_uint(v), __float_as_uint(v), false, false);
  return __uint_as_float(b[0]) + __uint_as_float(b[1]);
}
__device__ __forceinline__ float sum8l(float v) {
  v += __int_as_float(__builtin_amdgcn_update_dpp(0, __float_as_int(v), 0xb1, 0xf, 0xf, false));
  v += __int_as_float(__builtin_amdgcn_update_dpp(0, __float_as_int(v), 0x4e, 0xf, 0xf, false));
  return v + __int_as_float(__builtin_amdgcn_update_dpp(0, __float_as_int(v), 0x141, 0xf, 0xf, false));
}
template <int OFF> __device__ __forceinline__ void ds_w128(unsigned addr, u32x4 v) { asm volatile("ds_write_b128 %0, %1 offset:%2" :: "v"(addr), "v"(v), "n"(OFF) : "memory"); }
__device__ __forceinline__ int ds_r32(unsigned addr) { int r; asm volatile("ds_read_b32 %0, %1" : "=v"(r) : "v"(addr)); return r; }
__device__ __forceinline__ void ds_w32(unsigned addr, int v) { asm volatile("ds_write_b32 %0, %1" :: "v"(addr), "v"(v) : "memory"); }
template <int N> __device__ __forceinline__ void wait_vm() { asm volatile("s_waitcnt vmcnt(%0)" ::"n"(N) : "memory"); }
__device__ __forceinline__ void raw_barrier() { asm volatile("" ::: "memory"); __builtin_amdgcn_s_barrier(); asm volatile("" ::: "memory"); }
__device__ __forceinline__ void lds_barrier() { asm volatile("s_waitcnt lgkmcnt(0)" ::: "memory"); __builtin_amdgcn_s_barrier(); asm volatile("" ::: "memory"); }

template <int I, int N, class F> __device__ __forceinline__ void static_for(F&& f) {
  if constexpr (I < N) { f(std::integral_constant<int, I>{}); static_for<I + 1, N>(f); }
}
template <int V> using IC = std::integral_constant<int, V>;
constexpr int first_at(int g, int r0, int num, int den, int n) { int i = 0; while (i < n && r0 + i * num / den < g) ++i; return i; }
#define RSRC(p) __builtin_amdgcn_make_buffer_rsrc((void*)(p), 0, 0x7fffffff, 0x00020000)
#define RSRCN(p, n) __builtin_amdgcn_make_buffer_rsrc((void*)(p), 0, (n), 0x00020000)
#define LDSP(a) ((lds_void_ptr)(size_t)(a))
#define MFMA(a, b, c) __builtin_amdgcn_mfma_f32_16x16x32_bf16(a, b, c, 0, 0, 0)
#define CODE_ADDR(v, sym) asm volatile("s_getpc_b64 s[40:41]\n\ts_add_u32 s40, s40, " sym "@rel32@lo+4\n\ts_addc_u32 s41, s41, " sym "@rel32@hi+12\n\ts_mov_b64 %0, s[40:41]" : "=s"(v) :: "s40", "s41", "scc")
#define MERGED 1
constexpr int SNAP = 162752;
constexpr int STAB1 = 786432;
constexpr int EPOCH = 0, F_POL = 128, PROBE = 49152;
#define F_SIDE(g) ((((g) ^ 1 ^ pol) & 1) << 13)
#define F_PAN(p) (((p) & 3) * 256 + F_SIDE((p) >> 2))
constexpr int F_MARK = 2048, F_H = 4096, F_ROW = 6144, F_COL = 16384;
#define F_O(t) (32768 + ((t) & 7) * 1024 + F_SIDE((t) >> 3))
#define WO_ROT (((xcd & 3) * 4) ^ (idx & 3))

#define F_ACT(p) (65536 + ((p) & 3) * 2048 + F_SIDE((p) >> 2))
#define F_PART(b) (131072 + ((b) >> 5) * 16384 + ((((b) >> 3) & 3) * 4 + ((b) & 3)) * 512 + F_SIDE((b) >> 2))
constexpr int TRIES = 6;
struct Tag { unsigned lo, hi; };
__device__ __forceinline__ Tag tag_get(unsigned lds0) {
  f32x2 v = ds_b64<0>(lds0 + SNAP);
  asm volatile("s_waitcnt lgkmcnt(0)" : "+v"(v) :: "memory");
  return Tag{(unsigned)__builtin_amdgcn_readfirstlane(__float_as_int(v[0])) + 8u, (unsigned)__builtin_amdgcn_readfirstlane(__float_as_int(v[1]))};
}
__device__ __forceinline__ bool in_time(unsigned lds0) {
  int v = ds_r32(lds0 + SNAP + 28);
  asm volatile("s_waitcnt lgkmcnt(0)" : "+v"(v) :: "memory");
  return (unsigned)__builtin_amdgcn_s_memrealtime() - (unsigned)__builtin_amdgcn_readfirstlane(v) < 200000000u;
}
__device__ __forceinline__ int lane_id() { int l; asm volatile("v_mbcnt_lo_u32_b32 %0, -1, 0\n\tv_mbcnt_hi_u32_b32 %0, -1, %0" : "=v"(l)); return l; }
#if defined(__HIP_DEVICE_COMPILE__)
__device__ __forceinline__ void flag_set(const unsigned* sync, int off, Tag t, unsigned state) {
  __builtin_amdgcn_raw_buffer_store_b64((u32x2){t.lo + state, t.hi}, RSRCN((const char*)sync + off, 8), lane_id() * 8u, 0, 16);
}
__device__ __forceinline__ u32x2 flag_get(const unsigned* sync, int off, int n) { return __builtin_amdgcn_raw_buffer_load_b64(RSRCN((const char*)sync + off, n * 8), lane_id() * 8u, 0, 16); }
__device__ __forceinline__ bool flags_up(u32x2 v, int n, Tag t, unsigned state) { return ((__builtin_amdgcn_ballot_w64(v[0] - t.lo - state > 7u - state) | __builtin_amdgcn_ballot_w64(v[1] != t.hi)) & (~0ull >> (64 - n))) == 0; }
__device__ __forceinline__ bool words_up(unsigned v, Tag t) { return ((__builtin_amdgcn_ballot_w64(v != t.lo) & 0x5555555555555555ull) | (__builtin_amdgcn_ballot_w64(v != t.hi) & 0xaaaaaaaaaaaaaaaaull)) == 0; }
__device__ __forceinline__ void wait_flags(unsigned lds0, const unsigned* sync, int off, int n, unsigned state) {
  const Tag t = tag_get(lds0);
  while (!flags_up(flag_get(sync, off, n), n, t, state) && in_time(lds0)) __builtin_amdgcn_s_sleep(8);
}
#endif
template <int N> __device__ __forceinline__ bool marked(unsigned addr) {
  bf16x8 v[N];
  static_for<0, N>([&](auto ic) { constexpr int i = decltype(ic)::value; v[i] = ds_b128<i * 4096>(addr); });
  asm volatile("s_waitcnt lgkmcnt(0)" ::: "memory");
  unsigned m = 0;
#pragma unroll
  for (int i = 0; i < N; ++i) {
    asm volatile("" : "+v"(v[i]));
    const u32x4 u = __builtin_bit_cast(u32x4, v[i]);
    m = max(max(m, u[0]), u[1]); m = max(max(m, u[2]), u[3]);
  }
  return __builtin_amdgcn_ballot_w64(m == 0xffffffffu) != 0;
}

struct GemmArgs {
  const bf16_t* X;
  const bf16_t* W0;
  const bf16_t* W1;
  const bf16_t* R;
  bf16_t* O;
  float* st;
  const bf16_t* CW0;
  const bf16_t* CW1;
  float* cs;
  const bf16_t* PF0;
  const bf16_t* PF1;
  unsigned* sync;
  float* slab;
  unsigned warm;
};
#define STAMP(i)
#define STAMPW(i, w)
#define TS6(i)
#define TS4(i)
__device__ __forceinline__ bf16x8 pick(bool on, int e) {
  const unsigned v = on ? ((e & 1) ? 0x3f800000u : 0x3f80u) : 0u; const int d = e >> 1;
  return __builtin_bit_cast(bf16x8, (u32x4){d == 0 ? v : 0u, d == 1 ? v : 0u, d == 2 ? v : 0u, d == 3 ? v : 0u});
}
template <int MF, int NF> struct Frags { bf16x8 x[MF]; s16x4 lo[NF], hi[NF]; };

__device__ __forceinline__ unsigned dn_wsrc(int lane, int wave) { return (unsigned)((lane >> 3) * (D_ * 2) + (((lane & 7) ^ wswz((wave & 3) * 8 + (lane >> 3))) << 4)); }
__device__ __forceinline__ int dn_wbase(int wave) { const int b = blockIdx.x; return ((b & 3) * 896 + (wave & 1) * 8 + ((wave >> 1) & 1) * 56) * (D_ * 2) + ((b >> 3) & 7) * 256 + ((b & 2) << 6); }
constexpr int FLP = 155648;
constexpr int OWNE = 0, OWNM = 98304;
constexpr int WO_A = 6, WO_E = 10;
constexpr int wo_pos(int s) { return s < 3 ? 18432 + s * 16384 : (s == 3 ? 129024 : (s == 4 ? 0 : 71424 + (s - 5) * 16384)); }
template <class HK>
__device__ __forceinline__ void k1_phase(const GemmArgs& a, HK hook, int& pol) {
#if defined(__HIP_DEVICE_COMPILE__)
  constexpr int K = D_, LDW = 3 * D_, WN = 4;
  constexpr int HR = 16, STRIDE = 128;
  constexpr int XT = 144 * 128, WU = 24576;
  constexpr int XBASE = 0, WBASE = 3 * XT, RING = WBASE + 4 * WU;
  constexpr int MF = 4, NF = 3, MFX = 5;
  constexpr int NT = 16, NXI = 18, NLW = 4, XI = 5, XIMIN = 4, WI = 6;
  extern __shared__ __attribute__((aligned(16))) char smem[];
  STAMP(0);
  const unsigned lds0 = (unsigned)(size_t)(lds_char_ptr)smem;
  const int tid = threadIdx.x, lane = tid & 63;
  const int wave = __builtin_amdgcn_readfirstlane(tid >> 6);
  const int wm = wave / WN, wn = wave % WN;
  const int bid = blockIdx.x, xcd = bid & 7, idx = bid >> 3;
  const int tm = (xcd / 4) * 8 + idx / 4, tn = (xcd % 4) * 4 + idx % 4;
  const int m0 = tm * 128;
  const int fr = lane & 15, fq = lane >> 4;
  const int wq = (lane >> 2) & 3, wp = lane & 3;
  constexpr int TCH = RING + 2304;
  auto touch = [&](const void* base, unsigned voff, unsigned soff, unsigned lim = 0x7fffffffu) {
    __builtin_amdgcn_raw_ptr_buffer_load_lds(RSRCN(base, lim), LDSP(lds0 + TCH), 4, voff, soff, 0, 0);
  };
  const auto rO = RSRC(a.O);

  unsigned xsrc[XI], wsrc[WI];
  {
    const int w4 = wave & 3, lr = lane >> 3;
    const unsigned xv = (unsigned)(lr * (K * 2) + (((lane & 7) ^ ((w4 * 4 + (lr >> 1)) & 7)) << 4));
    const int r0 = m0 - HR + w4 * 8;
#pragma unroll
    for (int i = 0; i < XI; ++i) xsrc[i] = xv + (unsigned)((i == 0 ? (r0 < 0 ? 0 : r0) : r0 + 32 * (i < 4 ? i : 4)) * (K * 2));
    const unsigned wv = (unsigned)((w4 * 8 + lr) * (LDW * 2) + tn * 128 + (((lane & 7) ^ wswz(w4 * 8 + lr)) << 4));
#pragma unroll
    for (int i = 0; i < WI; ++i) wsrc[i] = wv + (unsigned)((i & 1) * 32 * (LDW * 2) + (i >> 1) * 2048);
  }
  auto ldX = [&](int i, int tile, int slot) {
    const int j = wave + NLW * i;
    if (j >= NXI) return;
    __builtin_amdgcn_raw_ptr_buffer_load_lds(RSRC(a.X), LDSP(lds0 + XBASE + slot * XT + j * 1024), 16, xsrc[i], tile * 128, 0, 0);
  };
  auto ldW = [&](int i, int unit, int slot) {
    const int j = wave + NLW * i;
    __builtin_amdgcn_raw_ptr_buffer_load_lds(RSRC(a.W0), LDSP(lds0 + WBASE + slot * WU + j * 1024), 16, wsrc[i], unit * 64 * LDW * 2, 0, 0);
  };
  auto issueX = [&](int tile, int slot) {
    if (wave >= NLW) return;
#pragma unroll
    for (int i = 0; i < XI; ++i) ldX(i, tile, slot);
  };
  auto issueW = [&](int unit, int slot) {
    if (wave >= NLW) return;
#pragma unroll
    for (int i = 0; i < WI; ++i) ldW(i, unit, slot);
  };

  f32x4 acc[NF][MF];
#pragma unroll
  for (int n = 0; n < NF; ++n)
#pragma unroll
    for (int m = 0; m < MF; ++m) acc[n][m] = (f32x4){0.f, 0.f, 0.f, 0.f};
  f32x4 acch[NF];
#pragma unroll
  for (int n = 0; n < NF; ++n) acch[n] = (f32x4){0.f, 0.f, 0.f, 0.f};
  constexpr int ONES = RING;

  unsigned xa0, xa1, xh0, xh1;
  { const unsigned o = fr * 128 + ((fq ^ ((fr >> 1) & 7)) << 4), mo = (HR + wm * MF * 16) * 128;
    xa0 = lds0 + XBASE + mo + o; xa1 = lds0 + XBASE + mo + (o ^ 64); xh0 = lds0 + XBASE + o; xh1 = lds0 + XBASE + (o ^ 64); }
  unsigned wa;
  { const int row = 8 * fq + wq; wa = lds0 + WBASE + row * STRIDE + (((wn << 1) ^ wswz(row)) << 4) + wp * 8; }

  using I0 = IC<0>; using I1 = IC<1>;
  using T = std::true_type; using F = std::false_type;
  typedef Frags<MFX, NF> FR;
  struct RdCtx { unsigned xa, xh, wb; };
  auto mkctx = [&](bool ks1, unsigned xoffs, unsigned woffs) {
    RdCtx c; c.xa = (ks1 ? xa1 : xa0) + xoffs; c.xh = fr == 0 ? lds0 + ONES : (ks1 ? xh1 : xh0) + xoffs; c.wb = wa + woffs;
    return c;
  };
  auto rd_item = [&](FR& f, const RdCtx& c, auto KSc, auto kc) {
    constexpr int KS = decltype(KSc)::value, k = decltype(kc)::value;
    if constexpr (k < MF) f.x[k] = ds_b128<k * 2048>(c.xa);
    else if constexpr (k < MFX) f.x[k] = ds_b128<0>(c.xh);
    else {
      constexpr int n = k - MFX, o = n * 8192 + KS * 32 * STRIDE;
      f.lo[n] = ds_tr<o>(c.wb); f.hi[n] = ds_tr<o + 4 * STRIDE>(c.wb);
    }
  };
  auto wait_frags = [&](FR& f, auto Hc) {
    asm volatile("s_waitcnt lgkmcnt(0)" ::: "memory");
#pragma unroll
    for (int m = 0; m < MF + decltype(Hc)::value; ++m) asm volatile("" : "+v"(f.x[m]));
#pragma unroll
    for (int n = 0; n < NF; ++n) asm volatile("" : "+v"(f.lo[n]), "+v"(f.hi[n]));
  };
  auto step1 = [&](auto Hc, FR& cur, FR& nxt, const RdCtx& c, auto KSc, auto R0c, auto RLc, auto IBc, auto&& sync, auto NLc, auto&& ld) {
    constexpr int H = decltype(Hc)::value, NL = decltype(NLc)::value, R0 = decltype(R0c)::value;
    constexpr int MH = MF + H, NM = NF * MH, NR = decltype(RLc)::value == -100 ? 0 : MH + 2 * NF;
    constexpr int IB = decltype(IBc)::value < 0 && decltype(IBc)::value > -100 ? NM + decltype(IBc)::value : decltype(IBc)::value;
    constexpr int RL = NM + decltype(RLc)::value;
    static_for<0, NM>([&](auto ic) {
      constexpr int I = decltype(ic)::value, g = I / MH, q = I % MH;
      const bf16x8 w = __builtin_shufflevector(cur.lo[g], cur.hi[g], 0, 1, 2, 3, 4, 5, 6, 7);
      if constexpr (q < MF) acc[g][q] = MFMA(w, cur.x[q], acc[g][q]);
      else acch[g] = MFMA(w, cur.x[MF], acch[g]);
      __builtin_amdgcn_sched_barrier(0);
      if constexpr (I == IB) sync();
      static_for<first_at(I, R0, RL - R0, NR - 1, NR), first_at(I + 1, R0, RL - R0, NR - 1, NR)>([&](auto rc) {
        constexpr int r = decltype(rc)::value;
        if constexpr (r < MH) rd_item(nxt, c, KSc, rc);
        else {
          constexpr int n = (r - MH) >> 1, o = n * 8192 + decltype(KSc)::value * 32 * STRIDE;
          if constexpr ((r - MH) & 1) nxt.hi[n] = ds_tr<o + 4 * STRIDE>(c.wb); else nxt.lo[n] = ds_tr<o>(c.wb);
        }
      });
#pragma unroll
      for (int i = first_at(I, R0, NM - R0, NL, NL); i < first_at(I + 1, R0, NM - R0, NL, NL); ++i) ld(i);
      __builtin_amdgcn_sched_barrier(0);
    });
    if constexpr (NR > 0) wait_frags(nxt, Hc);
  };
  FR fa, fb;
  using IM1 = IC<-1>;
  unsigned tv6 = 0;

  asm volatile("s_waitcnt lgkmcnt(0)" :: "s"(a.warm), "v"(wsrc[WI - 1]), "v"(xsrc[XI - 1]));
  issueW(0, 0); issueX(0, 0);
  u32x4 sx[4];
  constexpr int TNEW = RING + 3072;
  auto ldT = [&](int dst) {
    int lx = lane; asm volatile("" : "+v"(lx));
    const auto rT = RSRCN((const char*)a.cs + STAB1, 2048 * 16);
    const unsigned to = (unsigned)((m0 - 16) * 16 + lx * 16);
    __builtin_amdgcn_raw_ptr_buffer_load_lds(rT, LDSP(lds0 + dst), 16, to, 0, 0, 16);
    __builtin_amdgcn_raw_ptr_buffer_load_lds(rT, LDSP(lds0 + dst + 1024), 16, to + 1024u, 0, 0, 16);
    if (lx < 16) __builtin_amdgcn_raw_ptr_buffer_load_lds(rT, LDSP(lds0 + dst + 2048), 16, to + 2048u, 0, 0, 16);
  };
  auto ldS = [&]() {
    int lx = lane; asm volatile("" : "+v"(lx));
    const unsigned so = (unsigned)((m0 + tn * 8 + (wave & 3) * 2 + (lx >> 5)) * (K * 2) + (lx & 31) * 16);
#pragma unroll
    for (int i = 0; i < 4; ++i) sx[i] = __builtin_amdgcn_raw_buffer_load_b128(RSRC(a.X), so, i * 512, 0);
  };
  float mu = 0.f, rsd = 0.f;
  auto stats = [&]() {
    float sm = 0.f, sq = 0.f;
    const bf16x2_t one2 = __builtin_bit_cast(bf16x2_t, 0x3f803f80u);
#pragma unroll
    for (int i = 0; i < 4; ++i)
#pragma unroll
      for (int e = 0; e < 4; ++e) { const unsigned u = sx[i][e]; const bf16x2_t v = __builtin_bit_cast(bf16x2_t, u); sm = __builtin_amdgcn_fdot2_f32_bf16(v, one2, sm, false); sq = __builtin_amdgcn_fdot2_f32_bf16(v, v, sq, false); }
    sm = sum8l(sm); sq = sum8l(sq);
    sm += __int_as_float(__builtin_amdgcn_update_dpp(0, __float_as_int(sm), 0x128, 0xf, 0xf, false));
    sq += __int_as_float(__builtin_amdgcn_update_dpp(0, __float_as_int(sq), 0x128, 0xf, 0xf, false));
    { auto p = __builtin_amdgcn_permlane16_swap(__float_as_uint(sm), __float_as_uint(sm), false, false); sm = __uint_as_float(p[0]) + __uint_as_float(p[1]); }
    { auto p = __builtin_amdgcn_permlane16_swap(__float_as_uint(sq), __float_as_uint(sq), false, false); sq = __uint_as_float(p[0]) + __uint_as_float(p[1]); }
    mu = sm * (1.f / 1024.f);
    float var = sq * (1.f / 1024.f) - mu * mu; var = var > 0.f ? var : 0.f;
    rsd = __builtin_amdgcn_rsqf(var + 1e-5f);
  };
  __builtin_amdgcn_sched_barrier(0);
  if (wave == 4) {
    if (lane < 8) *(unsigned*)(smem + ONES + lane * 4) = lane < 4 ? 0x3f803f80u : 0u;
  }
  auto pretouch = [&]() {
    touch(a.cs, lane < 8 ? (unsigned)((const char*)a.sync - (const char*)a.cs + bid * 1024 + lane * 128) : (lane == 8 ? (unsigned)(STAB1 + (m0 + tn * 8) * 16) : (lane < 23 ? (unsigned)(bid * 1792 + (lane - 9) * 128) : 0x80000000u)), 0);
  };
  if (wave == 5) {
    if (lane < 2) __builtin_amdgcn_raw_ptr_buffer_load_lds(RSRC(a.sync), LDSP(lds0 + SNAP), 4, lane * 4, EPOCH, 0, 16);
    ds_w32(lds0 + SNAP + 28, (int)__builtin_amdgcn_s_memrealtime());
  }
  constexpr int HQ = 5;
  const int slc = (3 - ((unsigned)(size_t)a.W0 >> 7) - (xcd & 3) * 4) & 7;
  const int hid = (idx >> 2) * 3 + (idx & 3) - ((idx & 3) > slc ? 1 : 0);
  const bool hlp = slc < 4 && (idx & 3) != slc;
  const int hj1 = hlp ? hid / 3 - HQ : -100, hj2 = hlp ? hid / 3 + 8 - HQ : -100;
  const unsigned htv = (unsigned)(lane * (LDW * 2) + (hid % 3) * 2048 + ((xcd & 3) * 4 + slc) * 128);
  if (wave == 6 && hlp && hj1 <= 0) touch(a.W0, htv, (hid / 3) * (64 * LDW * 2));
  auto code_touch = [&]() {
    unsigned long long c0, c1; CODE_ADDR(c0, "k_all"); CODE_ADDR(c1, "k_zend");
    const unsigned len = c1 - c0 < 262144ull ? (unsigned)(c1 - c0) : 0u;
    touch((const void*)c0, (unsigned)((lane * 32 + idx) * 128), 0, len);
  };
  auto rowt = [&](int t) { touch(a.W0, lane_id() * 128u, (t * 64 + (bid & 63)) * (LDW * 2), 0x7fffffffu); };
  auto batch = [&](int j) {
    const int t = j + 4;
    if (j == 0) { for (int u = 3; u <= 4; ++u) if ((u & 3) == (bid >> 6) && lane < 48) rowt(u); }
    else if (t < 16 && (t & 3) == (bid >> 6) && lane < 48) rowt(t);
    if (j == 5 + ((bid >> 6) + 3) % 4 + 4 * (idx & 1)) touch(a.PF1, lane_id() * 128u, bid * 8192);
  };
  if (wave == 7) code_touch();
  const int srow = m0 + wave * 16 + fr;
  const bool par = fr & 1;
  float bias[4];
  {
    const unsigned mk = 0x5555u & ((2u << (srow < 15 ? srow : 15)) - 1u), sft = (fr & 14) - fq * 4 + 14;
    const unsigned b0 = ~(mk >> sft), b1 = ~((mk << 16) >> sft);
#pragma unroll
    for (int j = 0; j < 4; ++j) bias[j] = __uint_as_float((unsigned)__builtin_amdgcn_sbfe((int)(j < 2 ? b0 : b1), 2 - 2 * (j & 1), 1) & 0xff61b1e6u);
  }
  unsigned pl = par ? 0x01000c0cu : 0x0c0c0100u, ph = par ? 0x03020c0cu : 0x0c0c0302u;
  asm volatile("" : "+v"(bias[0]), "+v"(bias[1]), "+v"(bias[2]), "+v"(bias[3]), "+v"(pl), "+v"(ph));
  constexpr int RS = 400, QT = WBASE + 3 * WU - 144 * RS;
  static_assert(wo_pos(0) >= XT && wo_pos(1) <= 2 * XT && wo_pos(2) + 16384 <= QT && wo_pos(3) >= QT + 144 * RS, "LDS");
  int qw = (16 + wm * 64 + fr) * RS + wn * 32 + fq * 8;
  unsigned ka = lds0 + QT + (wave * 16 + fr) * RS + fq * 16, va = lds0 + QT + (wave * 16 + fq * 4 + wq) * RS + 256 + wp * 16;
  asm volatile("" : "+v"(qw), "+v"(ka), "+v"(va));
  {
    asm volatile("" : "+v"(xa0), "+v"(xa1), "+v"(xh0), "+v"(xh1), "+v"(wa));
    __builtin_amdgcn_sched_barrier(0);
    issueW(1, 1); issueX(1, 1);
  }
  if (wave < NLW) wait_vm<WI + XIMIN>();
  lds_barrier();
  if (wave == 5) pretouch();
  if (wave == 4) batch(0);
  { const RdCtx c = mkctx(false, 0, 0); static_for<0, MFX + NF>([&](auto kc) { rd_item(fa, c, I0{}, kc); }); }
  wait_frags(fa, I1{});
  STAMP(1);
  f32x4 t4[MFX], csn[NF];
  int up = 0;
  const unsigned csa = lds0 + RING + 64 + wn * 192 + fq * 16, cfa = lds0 + ONES + 16 + wn * 4;
  auto ldC = [&]() { up = ds_r32(cfa); csn[0] = __builtin_bit_cast(f32x4, ds_b128<0>(csa)); csn[1] = __builtin_bit_cast(f32x4, ds_b128<64>(csa)); csn[2] = __builtin_bit_cast(f32x4, ds_b128<128>(csa)); };
  auto pinC = [&]() { asm volatile("s_waitcnt lgkmcnt(0)" : "+v"(up), "+v"(csn[0]), "+v"(csn[1]), "+v"(csn[2]) :: "memory"); };
  auto pinT = [&](bool h) { asm volatile("s_waitcnt lgkmcnt(0)" : "+v"(t4[0]), "+v"(t4[1]), "+v"(t4[2]), "+v"(t4[3]) :: "memory"); if (h) asm volatile("" : "+v"(t4[4])); };
  auto run = [&](auto ROLEc) __attribute__((always_inline)) {
    constexpr int ROLE = decltype(ROLEc)::value;
    constexpr bool LDR = ROLE < NLW;
    using HL = IC<LDR ? 1 : 0>;
    auto nos = [&]() {};
    TS6(0);
    if constexpr (!LDR) { ldS(); __builtin_amdgcn_sched_barrier(0); }
    { const RdCtx c = mkctx(true, 0, 0); step1(HL{}, fa, fb, c, I1{}, I0{}, IC<-4>{}, IC<-100>{}, nos, IC<LDR ? WI + XI : 0>{}, [&](int i) { if (i < WI) ldW(i, 2, 2); else ldX(i - WI, 2, 2); }); }
    const int jr = bid == 0 ? 4 : -1;
    int slx = 1;
    if constexpr (ROLE == 5) wait_vm<5>();
    auto iter = [&](int j, auto LXc, auto LWc, auto WAITc, auto TIc) {
      constexpr bool LX = decltype(LXc)::value && LDR, LW = decltype(LWc)::value && LDR;
      constexpr int WAIT = decltype(WAITc)::value, TI = decltype(TIc)::value;
      TS6(j);
      constexpr int TO = 2, OL = TI <= TO ? 0 : 2, OH = (LDR && TI >= TO) ? 2 : OL;
      constexpr int NLT = (LX ? XI : 0) + (LW ? WI : 0) + 2 * (OH - OL), N0 = (NLT + 1) / 2, N1 = NLT - N0;
      if constexpr (LDR) wait_vm<WAIT + (TI > 0 ? 2 * OL : 0)>();
      unsigned osrc = 0;
      if constexpr (OH > OL) { int lx = lane; asm volatile("" : "+v"(lx)); const int row = ROLE * 8 + (lx >> 3); osrc = (unsigned)(row * D_ + ((xcd & 1) * 8 + (idx & 7)) * 64 + ((lx & 7) ^ wswz(row)) * 8) * 2u; }
      if constexpr (ROLE == 6 && TI == 0) { if (j == hj1 || j == hj2) touch(a.W0, htv, (j + HQ) * (64 * LDW * 2)); }
      constexpr int S3 = 11;
      if constexpr (!LDR && TI == 0) {
        if (j == 2) {
          stats();
          int lx = lane; asm volatile("" : "+v"(lx));
          const Tag tg = tag_get(lds0);
          if constexpr (ROLE == 6) {
            const auto rP = RSRCN((const char*)a.sync + PROBE, bid == 0 ? 16384 : 0);
            const unsigned po = (unsigned)(((lx >> 3) & 1) * 8192 + (lx >> 4) * 2048 + (lx & 7) * 16);
            __builtin_amdgcn_raw_buffer_store_b128((u32x4){tg.lo, tg.lo, tg.lo, tg.lo}, rP, po, 0, 16);
            __builtin_amdgcn_raw_buffer_store_b128((u32x4){tg.lo, tg.lo, tg.lo, tg.lo}, rP, po + 1024u, 0, 16);
          }
          __builtin_amdgcn_raw_buffer_store_b128((u32x4){__float_as_uint(mu), tg.lo, __float_as_uint(rsd), tg.lo + tg.hi}, RSRC(a.cs),
                                                 (lx & 31) == 0 ? (unsigned)(STAB1 + (m0 + tn * 8 + (ROLE & 3) * 2 + (lx >> 5)) * 16) : 0x80000000u, 0, 16);
          __builtin_amdgcn_sched_barrier(0);
          asm volatile("s_nop 1" ::: "memory");
        }
      }
      if constexpr (ROLE == 6 && TI == 0) {
        if (j == jr) {
          wait_vm<0>();
          const Tag tg = tag_get(lds0);
          unsigned r0, r1, r2, r3, r4, r5, cnt;
#define RC1(x) "s_cmp_eq_u32 %" #x ", %8\n\ts_subb_u32 %6, %6, 0\n\t"
          asm volatile("s_not_b32 %1, %8\n\ts_not_b32 %2, %8\n\ts_not_b32 %3, %8\n\ts_not_b32 %4, %8\n\ts_not_b32 %5, %8\n\ts_mov_b32 %6, 3\n\t"
                       "s_load_dword %0, %7, 0\n\ts_load_dword %0, %7, 2048\n\ts_load_dword %0, %7, 5120\n\t"
                       "s_load_dword %1, %7, 8192\n\ts_load_dword %2, %7, 8192+2048\n\ts_load_dword %3, %7, 8192+4096\n\ts_load_dword %4, %7, 8192+6144\n\ts_load_dword %5, %7, 8192+1024\n\t"
                       "s_waitcnt lgkmcnt(5)\n\ts_nop 3\n\t" RC1(1) RC1(2) RC1(3) RC1(4) RC1(5) "s_waitcnt lgkmcnt(0)"
                       : "=&s"(r0), "=&s"(r1), "=&s"(r2), "=&s"(r3), "=&s"(r4), "=&s"(r5), "=&s"(cnt)
                       : "s"((const char*)a.sync + PROBE), "s"(tg.lo) : "scc", "memory");
          flag_set(a.sync, F_POL, tg, (int)cnt > 0 ? 1u : 0u);
        }
      }
      if constexpr (ROLE == 5 && TI == 0) {
        if (j == 7) { if (lane < 2) __builtin_amdgcn_raw_ptr_buffer_load_lds(RSRC(a.sync), LDSP(lds0 + SNAP + 8), 4, lane_id() * 4, F_POL, 0, 16); }
        if (j == 8) {
          wait_vm<0>();
          f32x2 pv = ds_b64<8>(lds0 + SNAP);
          const Tag tg = tag_get(lds0);
          asm volatile("" : "+v"(pv));
          u32x2 pw = __builtin_bit_cast(u32x2, pv);
          if (__builtin_expect(!flags_up(pw, 1, tg, 0), 0)) {
            do pw = flag_get(a.sync, F_POL, 1); while (!flags_up(pw, 1, tg, 0) && in_time(lds0));
            ds_w32(lds0 + SNAP + 8, __builtin_amdgcn_readfirstlane(pw[0]));
          }
          pol = (__builtin_amdgcn_readfirstlane(pw[0]) - tg.lo) & 1;
          const int mb = (xcd & 1) * 16 + (idx & 15);
          unsigned on = 0xffffffffu; asm volatile("" : "+v"(on));
          const unsigned mo = lane_id() * 16u;
#pragma unroll
          for (int i = 0; i < 3; ++i)
            __builtin_amdgcn_raw_buffer_store_b128((u32x4){on, on, on, on}, RSRC(a.st), mo + i * 1024, rb_h(((xcd >> 1) * 2 + (idx >> 4)) * 16 + (mb >> 1), pol) + ko((xcd >> 2) * 16384) + (mb & 1) * 3072, 16);
          __builtin_amdgcn_sched_barrier(0);
          asm volatile("s_nop 1" ::: "memory");
        }
        if (j == 10) { wait_vm<0>(); flag_set(a.sync, F_MARK + F_PAN((xcd >> 1) * 2 + (idx >> 4)) + ((xcd & 1) * 16 + (idx & 15)) * 8, tag_get(lds0), 0); }
      }
      if constexpr (ROLE == 7 && TI == 0) {
        if (j == S3) ldT(TNEW);
        if (j == S3 + 1) {
          for (;;) {
            wait_vm<0>();
            int lx = lane; asm volatile("" : "+v"(lx));
            const unsigned ad = lds0 + TNEW + lx * 16;
            u32x4 v[3];
            v[0] = __builtin_bit_cast(u32x4, ds_b128<0>(ad)); v[1] = __builtin_bit_cast(u32x4, ds_b128<1024>(ad)); v[2] = __builtin_bit_cast(u32x4, ds_b128<2048>(ad));
            const Tag tg = tag_get(lds0);
            asm volatile("" : "+v"(v[0]), "+v"(v[1]), "+v"(v[2]) :: "memory");
            auto good = [&](const u32x4& e) { return e[1] == tg.lo && e[3] == tg.lo + tg.hi; };
            const bool bad = (!good(v[0]) && (tm > 0 || lx >= 16)) || !good(v[1]) || (!good(v[2]) && lx < 16);
            if (__builtin_amdgcn_ballot_w64(bad) == 0 || !in_time(lds0)) break;
            ldT(TNEW);
          }
        }
      }
      if constexpr (ROLE == 7 && TI == 1) code_touch();
      if constexpr (ROLE == 4 && TI == 0) batch(j);
      if constexpr (ROLE >= 4 && TI == 2) {
        const bf16_t* cw = ROLE < 6 ? a.CW0 : a.CW1;
        touch(cw, lane < 56 ? (unsigned)(((ROLE & 1) * 512 + bid) * (DF_ * 2) + lane * 128) : 0x80000000u, 0);
      }
      int xp = slx + 2; if (xp >= 3) xp -= 3;
      auto ld = [&](int i) { if (LX && i < XI) ldX(i, j + 2, xp);
                             else if constexpr (LW) ldW(i - (LX ? XI : 0), j + 2, (j + 2) & 3);
                             else { const int q = i - (LX ? XI : 0), t = OL + (q >> 1), h = q & 1;
                               __builtin_amdgcn_raw_ptr_buffer_load_lds(RSRC(a.PF1), LDSP(lds0 + wo_pos(t >> 1) + (t & 1) * 8192 + (ROLE + 4 * h) * 1024), 16,
                                                                        osrc, ((((t & 1) * 8 + (t >> 1)) ^ WO_ROT) * 2 + h) * (32 * D_ * 2), 0, 0); } };
      { const RdCtx c = mkctx(false, slx * XT, (j & 3) * WU); step1(HL{}, fb, fa, c, I0{}, IC<LDR ? 1 : 0>{}, IC<-4>{}, IC<LDR ? 1 : -100>{}, [&]() { raw_barrier(); }, IC<N0>{}, ld); }
      { const RdCtx c = mkctx(true, slx * XT, (j & 3) * WU); step1(HL{}, fa, fb, c, I1{}, I0{}, IC<LDR ? -4 : -6>{},
          IC<LDR ? -100 : -3>{}, [&]() { if constexpr (TI != 3) { asm volatile("s_waitcnt lgkmcnt(0)" ::: "memory"); raw_barrier(); } }, IC<N1>{}, [&](int i) { ld(i + N0); }); }
      slx = slx + 1; if (slx >= 3) slx = 0;
    };
    using WS_ = IC<WI + XIMIN>;
    if constexpr (!LDR) raw_barrier();
    for (int j = 1; j <= NT - 4; ++j) iter(j, T{}, T{}, WS_{}, I0{});
    iter(NT - 3, T{}, T{}, WS_{}, I1{});
    iter(NT - 2, F{}, F{}, IC<WI + XIMIN>{}, IC<2>{});
    iter(NT - 1, F{}, F{}, I0{}, IC<3>{});
    TS6(16);
    { const RdCtx c = mkctx(false, 0, 0); const unsigned ta = lds0 + TNEW + fr * 16;
      step1(HL{}, fb, fa, c, I0{}, I0{}, IC<-100>{}, IC<-100>{}, nos, IC<MFX>{}, [&](int i) {
        if (i == 0) static_for<0, MF + (LDR ? 1 : 0)>([&](auto mc) { constexpr int m = decltype(mc)::value; t4[m] = __builtin_bit_cast(f32x4, ds_b128<(m < MF ? 16 + (ROLE / WN * MF + m) * 16 : 0) * 16>(ta)); });
        if constexpr (!LDR) { if (i == 2) ldC(); } });
      pinT(LDR); if constexpr (!LDR) pinC(); }
    TS6(17);
  };
  switch (wave) {
    case 0: run(IC<0>{}); break; case 1: run(IC<1>{}); break;
    case 2: run(IC<2>{}); break; case 3: run(IC<3>{}); break;
    case 4: run(IC<4>{}); break; case 5: run(IC<5>{}); break;
    case 6: run(IC<6>{}); break; default: run(IC<7>{}); break;
  }
  STAMP(2);
  Tag tg;
  {
    if (wave < 4) {
      if (fr == 0) {
#pragma unroll
        for (int n = 0; n < NF; ++n) *(f32x4*)(smem + RING + 64 + wn * 192 + fq * 16 + n * 64) = acch[n];
      }
      ds_w32(cfa, 1);
#pragma unroll
      for (int n = 0; n < NF; ++n)
#pragma unroll
        for (int e = 0; e < 4; ++e) { const float h = acch[n][e]; csn[n][e] = __int_as_float(__builtin_amdgcn_update_dpp(0, __float_as_int(h), 0x150, 0xf, 0xf, false)); }
    } else {
      unsigned polls = 0;
      while (__builtin_expect(__builtin_amdgcn_readfirstlane(up) == 0, 0) && ++polls < 40000000u) {
        ldC(); pinC();
      }
    }
    STAMP(9);
    TS6(18);
    TS6(19);
    STAMP(6);
    auto nrm = [&](const f32x4& v, const f32x4& cs, const f32x4& t) {
      const float mu = t[0], rs = t[2];
      return (u32x2){pk_bf16(rs * (v[0] - mu * cs[0]), rs * (v[1] - mu * cs[1])), pk_bf16(rs * (v[2] - mu * cs[2]), rs * (v[3] - mu * cs[3]))}; };
    u32x2 ov2[MF][NF];
#pragma unroll
    for (int m = 0; m < MF; ++m) {
#pragma unroll
      for (int n = 0; n < NF; ++n) { ov2[m][n] = nrm(acc[n][m], csn[n], t4[m]); asm volatile("" : "+v"(ov2[m][n])); }
      __builtin_amdgcn_sched_barrier(0);
#pragma unroll
      for (int n = 0; n < NF; ++n) *(u32x2*)(smem + QT + qw + m * 16 * RS + n * 128) = ov2[m][n];
      __builtin_amdgcn_sched_barrier(0);
    }
    if (wave < 4) {
#pragma unroll
      for (int n = 1; n < NF; ++n) *(u32x2*)(smem + QT + qw - 16 * RS + n * 128) = nrm(acch[n], csn[n], t4[MF]);
      __builtin_amdgcn_sched_barrier(0);
      hook(IC<3>{}, IC<2>{}, IC<WO_A>{});
    }
    STAMP(4);
    TS6(20);
    lds_barrier();
    TS6(21);
    STAMP(5);
    bf16x8 qf[2], kf[2][2]; s16x4 vf[2][2][2];
    qf[0] = ds_b128<16 * RS>(ka); kf[0][0] = ds_b128<128>(ka); kf[0][1] = ds_b128<16 * RS + 128>(ka);
    qf[1] = ds_b128<16 * RS + 64>(ka); kf[1][0] = ds_b128<192>(ka); kf[1][1] = ds_b128<16 * RS + 192>(ka);
    vf[0][0][0] = ds_tr<0>(va); vf[0][0][1] = ds_tr<16 * RS>(va); vf[0][1][0] = ds_tr<8>(va); vf[0][1][1] = ds_tr<16 * RS + 8>(va);
    vf[1][0][0] = ds_tr<64>(va); vf[1][0][1] = ds_tr<16 * RS + 64>(va); vf[1][1][0] = ds_tr<72>(va); vf[1][1][1] = ds_tr<16 * RS + 72>(va);
    u32x4 tp = __builtin_bit_cast(u32x4, ds_b128<0>(lds0 + SNAP));
    hook(IC<1>{}, IC<WO_A>{}, IC<WO_E>{});
    asm volatile("s_waitcnt lgkmcnt(9)" ::: "memory");
#pragma unroll
    for (int hh = 0; hh < 2; ++hh) asm volatile("" : "+v"(qf[hh]), "+v"(kf[hh][0]), "+v"(kf[hh][1]));
    const float sc2 = 0.17677669529663687f * 1.4426950408889634f;
    unsigned pm = (unsigned)__builtin_amdgcn_sbfe((int)pl, 24, 1); asm volatile("" : "+v"(pm));
    auto vmax = [](float x, float y) { float r; asm("v_max_f32 %0, %1, %2" : "=v"(r) : "v"(x), "v"(y)); return r; };
    u32x4 ov[2];
    const f32x4 z = {0.f, 0.f, 0.f, 0.f};
    f32x4 sc[2][2]; float lg[2][4], mx[2], sum[2];
    auto x16 = [](float v) { auto p = __builtin_amdgcn_permlane16_swap(__float_as_uint(v), __float_as_uint(v), false, false); return (f32x2){__uint_as_float(p[0]), __uint_as_float(p[1])}; };
    auto x32 = [](float v) { auto p = __builtin_amdgcn_permlane32_swap(__float_as_uint(v), __float_as_uint(v), false, false); return (f32x2){__uint_as_float(p[0]), __uint_as_float(p[1])}; };
#pragma unroll
    for (int hh = 0; hh < 2; ++hh) {
      sc[hh][0] = MFMA(kf[hh][0], qf[hh], z);
      sc[hh][1] = MFMA(kf[hh][1], qf[hh], z);
    }
    __builtin_amdgcn_sched_barrier(0);
#pragma unroll
    for (int hh = 0; hh < 2; ++hh) {
#pragma unroll
      for (int j = 0; j < 4; ++j) { const float e0 = sc[hh][j >> 1][(j & 1) * 2], e1 = sc[hh][j >> 1][(j & 1) * 2 + 1];
        lg[hh][j] = __builtin_fmaf(__uint_as_float((__float_as_uint(e1) & pm) | (__float_as_uint(e0) & ~pm)), sc2, bias[j]); }
      mx[hh] = fmaxf(fmaxf(lg[hh][0], lg[hh][1]), fmaxf(lg[hh][2], lg[hh][3]));
    }
    __builtin_amdgcn_sched_barrier(0);
    { auto p = __builtin_amdgcn_permlane16_swap(__float_as_uint(mx[0]), __float_as_uint(mx[1]), false, false);
      const f32x2 q = x32(vmax(__uint_as_float(p[0]), __uint_as_float(p[1]))), r = x16(vmax(q[0], q[1])); mx[0] = r[0]; mx[1] = r[1]; }
    __builtin_amdgcn_sched_barrier(0);
#pragma unroll
    for (int hh = 0; hh < 2; ++hh) {
#pragma unroll
      for (int j = 0; j < 4; ++j) lg[hh][j] = __builtin_amdgcn_exp2f(lg[hh][j] - mx[hh]);
      sum[hh] = (lg[hh][0] + lg[hh][1]) + (lg[hh][2] + lg[hh][3]);
    }
    __builtin_amdgcn_sched_barrier(0);
    asm volatile("s_waitcnt lgkmcnt(0)" ::: "memory");
#pragma unroll
    for (int hh = 0; hh < 2; ++hh) asm volatile("" : "+v"(vf[hh][0][0]), "+v"(vf[hh][0][1]), "+v"(vf[hh][1][0]), "+v"(vf[hh][1][1]));
    asm volatile("" : "+v"(tp));
    tg = Tag{(unsigned)__builtin_amdgcn_readfirstlane(tp[0]) + 8u, (unsigned)__builtin_amdgcn_readfirstlane(tp[1])};
    pol = (__builtin_amdgcn_readfirstlane(tp[2]) - tg.lo) & 1;
    { auto p = __builtin_amdgcn_permlane16_swap(__float_as_uint(sum[0]), __float_as_uint(sum[1]), false, false);
      const f32x2 q = x32(__uint_as_float(p[0]) + __uint_as_float(p[1])), r = x16(__builtin_amdgcn_rcpf(q[0] + q[1])); sum[0] = r[0]; sum[1] = r[1]; }
    __builtin_amdgcn_sched_barrier(0);
#pragma unroll
    for (int hh = 0; hh < 2; ++hh) {
      const float inv = sum[hh];
      const unsigned c0 = pk_bf16(lg[hh][0] * inv, lg[hh][1] * inv), c1 = pk_bf16(lg[hh][2] * inv, lg[hh][3] * inv);
      const u32x4 pp = {__builtin_amdgcn_perm(0u, c0, pl), __builtin_amdgcn_perm(0u, c0, ph), __builtin_amdgcn_perm(0u, c1, pl), __builtin_amdgcn_perm(0u, c1, ph)};
      const bf16x8 pf = __builtin_bit_cast(bf16x8, pp);
      sc[hh][0] = MFMA(__builtin_shufflevector(vf[hh][0][0], vf[hh][0][1], 0, 1, 2, 3, 4, 5, 6, 7), pf, z);
      sc[hh][1] = MFMA(__builtin_shufflevector(vf[hh][1][0], vf[hh][1][1], 0, 1, 2, 3, 4, 5, 6, 7), pf, z);
    }
    __builtin_amdgcn_sched_barrier(0);
#pragma unroll
    for (int hh = 0; hh < 2; ++hh) ov[hh] = (u32x4){pk_bf16(sc[hh][0][0], sc[hh][0][1]), pk_bf16(sc[hh][0][2], sc[hh][0][3]), pk_bf16(sc[hh][1][0], sc[hh][1][1]), pk_bf16(sc[hh][1][2], sc[hh][1][3])};
    STAMP(10);
#pragma unroll
    for (int hh = 0; hh < 2; ++hh) __builtin_amdgcn_raw_buffer_store_b128(ov[hh], rO, rb_h((m0 >> 4) + wave, pol) + ko(tn * 2048) + hh * 1024 + lane * 16, 0, 16);
    STAMP(11);
  }
  STAMP(7);
  STAMP(8);
  TS6(22);
  asm volatile("s_waitcnt vmcnt(0)" ::: "memory");
  flag_set(a.sync, F_O(tm) + (wave >> 1) * 256 + tn * 16 + (wave & 1) * 8, tg, 0);
  raw_barrier();
  hook(IC<1>{}, IC<WO_E>{}, IC<16>{});
  STAMP(3);
#endif
}

struct Late { u32x2 tag; };
template <class HK>
__device__ __forceinline__ void glu_phase(const GemmArgs& a, HK hook, Late& late, int pol) {
#if defined(__HIP_DEVICE_COMPILE__)
  constexpr int LDW = DF_, WN = 2;
  constexpr int UPOFF = 32 * 224 + 16;
  constexpr int XT = 32768, WU = 14592;
  constexpr int XBASE = 0, WBASE = 3 * XT, RING = WBASE + 4 * WU;
  constexpr int MF = 4, NF = 7;
  constexpr int NT = 16, NXI = 16, NWI = 14, NLW = 4, XI = 4, WI = 4;
  extern __shared__ __attribute__((aligned(16))) char smem[];
  STAMP(0);
  const unsigned lds0 = (unsigned)(size_t)(lds_char_ptr)smem;
  const int tid = threadIdx.x, lane = tid & 63;
  const int wave = __builtin_amdgcn_readfirstlane(tid >> 6);
  const int wm = wave / WN, wn = wave % WN;
  const int bid = blockIdx.x, xcd = bid & 7, idx = bid >> 3;
  const int tm = (xcd / 4) * 4 + idx / 8, tn = (xcd % 4) * 8 + idx % 8;
  const int m0 = tm * 256;
  const int krot = (tm >> 2) * 8;
  const int fr = lane & 15, fq = lane >> 4;
  const int wq = (lane >> 2) & 3, wp = lane & 3;
  const int panel = m0 >> 8;
  constexpr int TCH = RING + 5120;
  constexpr int DONE = TCH + 896;
  auto done_inc = [&]() { if (lane == 0) asm volatile("ds_add_u32 %0, %1" :: "v"(lds0 + DONE), "v"(1) : "memory"); };
  auto touch = [&](const void* base, unsigned voff, unsigned soff, unsigned lim = 0x7fffffffu) {
    __builtin_amdgcn_raw_ptr_buffer_load_lds(RSRCN(base, lim), LDSP(lds0 + TCH), 4, voff, soff, 0, 0);
  };
  const auto rO = RSRC(a.O);

  unsigned xsrc[XI], wsrc[WI];
#pragma unroll
  for (int i = 0; i < XI; ++i) {
    const int j = (wave % NLW) + NLW * i;
    xsrc[i] = (unsigned)(rb_h((m0 >> 4) + (j >> 1), pol) + (j & 1) * 1024 + lane * 16);
  }
#pragma unroll
  for (int i = 0; i < WI; ++i) {
    int j = (wave % NLW) + NLW * i; if (j >= NWI) j -= NLW;
    const int jj = j >= 7 ? j - 7 : j;
    const int b = jj * 1024 + lane * 16, p = b / 224, pos = (b % 224) >> 4;
    const int row = 8 * ((p & 15) >> 2) + (p & 3) + 4 * (p >> 4);
    wsrc[i] = (unsigned)(row * LDW + tn * 112 + pos * 8) * 2u;
  }
  auto ldX = [&](int i, int tile, int part, int slot) {
    tile = (tile + krot) & 15;
    const int j = wave + NLW * i;
    __builtin_amdgcn_raw_ptr_buffer_load_lds(RSRC(a.X), LDSP(lds0 + XBASE + slot * XT + (part * NXI + j) * 1024), 16, xsrc[i], ko(tile * 2048) + part * (NXI / 2) * SH, 0, 16);
  };
  auto ldW = [&](int i, int unit, int slot) {
    const auto rW0 = RSRC(a.W0);
    const auto rW1 = RSRC(a.W1);
    int j = wave + NLW * i; if (j >= NWI) j -= NLW;
    unit = (unit + 2 * krot) & 31;
    const bool up = j >= 7;
    __builtin_amdgcn_raw_ptr_buffer_load_lds(up ? rW1 : rW0, LDSP(lds0 + WBASE + slot * WU + (up ? UPOFF + (j - 7) * 1024 : j * 1024)), 16, wsrc[i], unit * 32 * LDW * 2, 0, 0);
  };
  auto issueX = [&](int tile, int part, int slot) {
    if (wave >= NLW) return;
#pragma unroll
    for (int i = 0; i < XI; ++i) ldX(i, tile, part, slot);
  };
  auto issueW = [&](int unit, int slot) {
    if (wave >= NLW) return;
#pragma unroll
    for (int i = 0; i < WI; ++i) ldW(i, unit, slot);
  };

  f32x4 acc[NF][MF];
#pragma unroll
  for (int n = 0; n < NF; ++n)
#pragma unroll
    for (int m = 0; m < MF; ++m) acc[n][m] = (f32x4){0.f, 0.f, 0.f, 0.f};
  f32x4 accs = {0.f, 0.f, 0.f, 0.f};

  const unsigned xa0 = lds0 + XBASE + wm * MF * 16 * 128 + lane * 16, xa1 = xa0 + 1024;
  unsigned wa[2];
#pragma unroll
  for (int c = 0; c < 2; ++c) wa[c] = lds0 + WBASE + (4 * fq + wq) * 224 + wn * 112 + (c == 0 ? wp * 8 : 96 + (wp & 1) * 8 + (wp >> 1) * UPOFF);

  using I0 = IC<0>; using I1 = IC<1>;
  using T = std::true_type; using F = std::false_type;
  typedef Frags<MF, NF> FR;
  struct RdCtx { unsigned xa, wb[2]; };
  auto mkctx = [&](bool ks1, unsigned xoffs, unsigned woffs) {
    RdCtx c; c.xa = (ks1 ? xa1 : xa0) + xoffs; c.wb[0] = wa[0] + woffs; c.wb[1] = wa[1] + woffs;
    return c;
  };
  auto rd_item = [&](FR& f, const RdCtx& c, auto kc) {
    constexpr int k = decltype(kc)::value;
    if constexpr (k < MF) f.x[k] = ds_b128<k * 2048>(c.xa);
    else {
      constexpr int n = k - MF, cc = n == 6, o = n == 6 ? 0 : (n >> 1) * 32 + (n & 1) * UPOFF;
      f.lo[n] = ds_tr<o>(c.wb[cc]); f.hi[n] = ds_tr<o + 16 * 224>(c.wb[cc]);
    }
  };
  auto wait_frags = [&](FR& f) {
    asm volatile("s_waitcnt lgkmcnt(0)" ::: "memory");
#pragma unroll
    for (int m = 0; m < MF; ++m) asm volatile("" : "+v"(f.x[m]));
#pragma unroll
    for (int n = 0; n < NF; ++n) asm volatile("" : "+v"(f.lo[n]), "+v"(f.hi[n]));
  };
  auto step = [&](FR& cur, FR& nxt, const RdCtx& c, auto PFc, auto NLc, auto&& ld) {
    constexpr bool PF = decltype(PFc)::value != 0; constexpr int NL = decltype(NLc)::value;
    constexpr int R = MF + NF, RG = NF - 1;
    static_for<0, NF>([&](auto gc) {
      constexpr int g = decltype(gc)::value;
      if constexpr (PF) static_for<0, R>([&](auto kc) { constexpr int k = decltype(kc)::value; if constexpr (k * RG / R == g) rd_item(nxt, c, kc); });
      static_for<0, NL>([&](auto ic) { constexpr int i = decltype(ic)::value; if constexpr (i * NF / NL == g) ld(i); });
      const bf16x8 w = __builtin_shufflevector(cur.lo[g], cur.hi[g], 0, 1, 2, 3, 4, 5, 6, 7);
#pragma unroll
      for (int m = 0; m < MF; ++m) acc[g][m] = MFMA(w, cur.x[m], acc[g][m]);
      __builtin_amdgcn_sched_barrier(0);
    });
    if constexpr (PF) wait_frags(nxt);
  };
  auto stepg = [&](auto CSc, FR& cur, FR& nxt, const RdCtx& c, auto Hc, auto R0c, auto IBc, auto&& sync, auto NLc, auto&& ld, auto&& gap) {
    constexpr bool CS = decltype(CSc)::value; constexpr int H = decltype(Hc)::value, R0 = decltype(R0c)::value, IB = decltype(IBc)::value, NL = decltype(NLc)::value;
    constexpr int NM = NF * MF, NR = MF + 2 * NF;
    static_for<0, NM>([&](auto ic) {
      constexpr int I = decltype(ic)::value, g = I / MF, m = I % MF;
      constexpr int RL = IB > 8 ? 20 : 22;
      const bf16x8 w = __builtin_shufflevector(cur.lo[g], cur.hi[g], 0, 1, 2, 3, 4, 5, 6, 7);
      acc[g][m] = MFMA(w, cur.x[m], acc[g][m]);
      __builtin_amdgcn_sched_barrier(0);
      if constexpr (I == IB) sync();
      static_for<first_at(I, R0, RL - R0, NR - 1, NR), first_at(I + 1, R0, RL - R0, NR - 1, NR)>([&](auto rc) {
        constexpr int r = decltype(rc)::value;
        if constexpr (r < MF) nxt.x[r] = ds_b128<r * 2048 + H * 1024>(c.xa);
        else {
          constexpr int n = (r - MF) >> 1, cc = n == 6, o = (n == 6 ? 0 : (n >> 1) * 32 + (n & 1) * UPOFF) + H * WU;
          if constexpr ((r - MF) & 1) nxt.hi[n] = ds_tr<o + 16 * 224>(c.wb[cc]); else nxt.lo[n] = ds_tr<o>(c.wb[cc]);
        }
      });
#pragma unroll
      for (int i = first_at(I, R0, NM - 1 - R0, NL, NL); i < first_at(I + 1, R0, NM - 1 - R0, NL, NL); ++i) ld(i);
      gap(ic);
      if constexpr (CS && m == MF - 1) {
        const unsigned sv = fr == g ? 0x3f803f80u : 0u;
        const u32x4 s4 = {sv, sv, sv, sv};
        accs = MFMA(w, __builtin_bit_cast(bf16x8, s4), accs);
      }
      __builtin_amdgcn_sched_barrier(0);
    });
    wait_frags(nxt);
  };
  FR fa, fb;
  unsigned tv4 = 0;

  f32x4* tb_row = (f32x4*)(smem + RING);
  float* tb_cs = (float*)(tb_row + 256);
  constexpr int RTAB = 524288;
  auto glu_stats_issue = [&]() {
    if (wave == 7) __builtin_amdgcn_raw_ptr_buffer_load_lds(RSRC(a.st), LDSP(lds0 + RING + 3072), 16, ((lane >> 2) * S_ + m0 + tn * 8 + (lane & 3) * 2) * 8, 0, 0, 16);
  };
  auto glu_stats = [&]() {
    int lx = lane; asm volatile("" : "+v"(lx));
    const unsigned sa = lds0 + RING + 3072 + lx * 8;
    f32x2 p0 = ds_b64<0>(sa), p1 = ds_b64<512>(sa);
    asm volatile("s_waitcnt lgkmcnt(0)" : "+v"(p0), "+v"(p1) :: "memory");
    const float mu = sum8(p0[0] + p1[0]) * (1.f / 16.f);
    const float v = sum8((p0[1] + p1[1]) + 64.f * ((p0[0] - mu) * (p0[0] - mu) + (p1[0] - mu) * (p1[0] - mu))) * (1.f / 1024.f) + 1e-5f;
    const float rs = __builtin_amdgcn_rsqf(v);
    const f32x4 rv = {mu, -0.10294324f * rs * rs * rs, -2.3022082f * rs, v};
    __builtin_amdgcn_raw_buffer_store_b128(__builtin_bit_cast(u32x4, rv), RSRC(a.cs), lx < 8 ? (unsigned)(RTAB + (m0 + tn * 8 + lx) * 16) : 0x80000000u, 0, 16);
  };
  f32x4 cg[4], cu[4], rw[MF], rw6[2]; f32x2 tgv; int dnv, go0; unsigned le, ta, t6;
  auto tabs = [&](int i) {
    if (i == 0) { const int lz = lane_id(), fr = lz & 15, fq = lz >> 4;
      ta = lds0 + RING + 4096 + wn * 224 + fq * 16; t6 = lds0 + RING + wm * 1024 + fr * 16;
      go0 = ((m0 >> 4) + wm * 4) * SA + fr * 16 + (fq & 1) * SA + (fq >> 1) * 256;
      le = lds0 + (wm * 4 + (fq & 1)) * 1024 + (wn * 2 + (fq >> 1)) * 256 + fr * 16;
      asm volatile("" : "+v"(ta), "+v"(t6), "+v"(go0), "+v"(le)); }
    if (i == 1) { rw[0] = __builtin_bit_cast(f32x4, ds_b128<0>(t6)); rw[1] = __builtin_bit_cast(f32x4, ds_b128<256>(t6)); cg[0] = __builtin_bit_cast(f32x4, ds_b128<0>(ta)); }
    if (i == 2) { cg[1] = __builtin_bit_cast(f32x4, ds_b128<64>(ta)); cu[0] = __builtin_bit_cast(f32x4, ds_b128<448>(ta)); cu[1] = __builtin_bit_cast(f32x4, ds_b128<448 + 64>(ta)); }
    if (i == 3) { rw[2] = __builtin_bit_cast(f32x4, ds_b128<512>(t6)); rw[3] = __builtin_bit_cast(f32x4, ds_b128<768>(t6)); tgv = ds_b64<0>(lds0 + SNAP); }
    if (i == 8) asm volatile("s_waitcnt lgkmcnt(0)" : "+v"(rw[0]), "+v"(rw[1]), "+v"(rw[2]), "+v"(rw[3]), "+v"(cg[0]), "+v"(cg[1]), "+v"(cu[0]), "+v"(cu[1]), "+v"(tgv) :: "memory");
    if (i == 9) { const int lz = lane_id(); const unsigned tc = ta - (lz & 32), tr = t6 + (lz & 32) * 8;
      cg[2] = __builtin_bit_cast(f32x4, ds_b128<128>(ta)); cu[2] = __builtin_bit_cast(f32x4, ds_b128<448 + 128>(ta)); cg[3] = __builtin_bit_cast(f32x4, ds_b128<192>(tc)); cu[3] = __builtin_bit_cast(f32x4, ds_b128<448 + 192>(tc));
      rw6[0] = __builtin_bit_cast(f32x4, ds_b128<0>(tr)); rw6[1] = __builtin_bit_cast(f32x4, ds_b128<512>(tr)); }
    if (i == 10) asm volatile("s_waitcnt lgkmcnt(0)" : "+v"(cg[2]), "+v"(cg[3]), "+v"(cu[2]), "+v"(cu[3]), "+v"(rw6[0]), "+v"(rw6[1]) :: "memory");
    if (i == 11) dnv = ds_r32(lds0 + DONE);
  };
  auto epilogue = [&]() __attribute__((always_inline)) {
    STAMP(2);
    auto prio = [&](auto gc) { __builtin_amdgcn_s_setprio(3 - decltype(gc)::value); };
    prio(I0{});
    STAMPW(9, 4);
    TS4(19);
    unsigned hv[MF][NF];
    auto keep = [&](unsigned a0, unsigned a1, unsigned b0, unsigned b1) {
      const auto s0 = __builtin_amdgcn_permlane16_swap(a0, b0, false, false), s1 = __builtin_amdgcn_permlane16_swap(a1, b1, false, false);
      return (u32x4){s0[0], s1[0], s0[1], s1[1]}; };
    auto put = [&](auto pc, auto hc) {
      constexpr int p = decltype(pc)::value, h = decltype(hc)::value;
      const u32x4 v = keep(hv[2 * h][2 * p], hv[2 * h][2 * p + 1], hv[2 * h + 1][2 * p], hv[2 * h + 1][2 * p + 1]);
      __builtin_amdgcn_raw_buffer_store_b128(v, rO, go0, (tn * 12 + p * 4 + wn * 2) * 256 + h * 2 * SA, 16);
      if constexpr (p < 2) ds_w128<OWNE + p * 16384 + h * 2048>(le, v); else ds_w128<h * 2048>(le + OWNM, v);
      __builtin_amdgcn_sched_barrier(0);
      asm volatile("s_nop 1" ::: "memory");
      __builtin_amdgcn_sched_barrier(0);
    };
#define STAGE(body) _Pragma("unroll") for (int i = 0; i < 4 * NN; ++i) { const int m = i / NN, n = N0 + i % NN; const f32x4 k = n == 6 ? rw6[m >> 1] : rw[m]; body; } __builtin_amdgcn_sched_barrier(0)
    auto grp = [&](auto N0c, auto NNc, auto&& at) {
      constexpr int N0 = decltype(N0c)::value, NN = decltype(NNc)::value;
      f32x2 G[4 * NN], U[4 * NN], P[4 * NN];
      __builtin_amdgcn_sched_barrier(0);
      STAGE({ const int e = (n == 6 ? m & 1 : n & 1) * 2; const f32x4 x = n == 6 ? acc[6][m & 2] : acc[n & 6][m]; G[i] = ((f32x2){x[e], x[e + 1]}) - k[0] * ((f32x2){cg[n >> 1][e], cg[n >> 1][e + 1]}); });
      STAGE(P[i] = G[i] * G[i]);
      at(I0{});
      STAGE(P[i] = P[i] * k[1] + k[2]);
      STAGE(P[i] = G[i] * P[i]);
      at(I1{});
      STAGE({ const int e = (n == 6 ? m & 1 : n & 1) * 2; const f32x4 x = n == 6 ? acc[6][m | 1] : acc[n | 1][m]; U[i] = ((f32x2){x[e], x[e + 1]}) - k[0] * ((f32x2){cu[n >> 1][e], cu[n >> 1][e + 1]}); });
      STAGE(P[i] = ((f32x2){__builtin_amdgcn_exp2f(P[i][0]), __builtin_amdgcn_exp2f(P[i][1])}));
      at(IC<2>{});
      __builtin_amdgcn_sched_barrier(0);
      STAGE(P[i] = P[i] * k[3] + k[3]);
      STAGE(P[i] = ((f32x2){__builtin_amdgcn_rcpf(P[i][0]), __builtin_amdgcn_rcpf(P[i][1])}));
      at(IC<3>{});
      STAGE(U[i] = G[i] * U[i]);
      STAGE(U[i] = U[i] * P[i]);
      STAGE({ hv[m][n] = pk_bf16(U[i][0], U[i][1]); asm volatile("" : "+v"(hv[m][n])); });
    };
#undef STAGE
    using I2 = IC<2>; using I4 = IC<4>;
    TS4(20);
    grp(I0{}, I2{}, [&](auto tc) { if constexpr (decltype(tc)::value == 3) tabs(11); });
    TS4(21);
    { int sd;
      asm volatile("1:\n\ts_waitcnt lgkmcnt(0)\n\tv_readfirstlane_b32 %1, %0\n\ts_cmp_ge_i32 %1, 8\n\ts_cbranch_scc1 2f\n\tds_read_b32 %0, %2\n\ts_branch 1b\n2:" : "+v"(dnv), "=&s"(sd) : "v"(lds0 + DONE) : "scc", "memory"); }
    put(I0{}, I0{}); put(I0{}, I1{});
    tabs(9);
    STAMP(4);
    STAMPW(11, 4);
    TS4(22);
    prio(I1{});
    const auto rFl = RSRCN((const char*)a.sync + F_ACT(panel) + (wm * 64 + tn * 2 + wn) * 8, 8);
    u32x2 fval = {__float_as_uint(tgv[0]) + 8u, __float_as_uint(tgv[1])}; unsigned foff = lane_id() * 8u;
    asm volatile("" : "+v"(fval), "+v"(foff));
    grp(I2{}, I2{}, [&](auto) {});
    TS4(23);
    { wait_vm<0>(); __builtin_amdgcn_raw_buffer_store_b64(fval, rFl, foff, 0, 16); STAMPW(6, 4); STAMP(10); }
    put(I1{}, I0{}); put(I1{}, I1{});
    TS4(24);
    prio(I2{});
    tabs(10);
    grp(I4{}, I2{}, [&](auto) {});
    TS4(25);
    auto poll = [&]() { if (foff < 256u) __builtin_amdgcn_raw_ptr_buffer_load_lds(RSRC(a.sync), LDSP(lds0 + FLP + wave * 128), 4, foff >> 1, F_ACT(panel) + wm * 512 + (xcd & 3) * 128, 0, 16); };
    put(I2{}, I0{}); put(I2{}, I1{});
    prio(IC<3>{});
#pragma unroll
    for (int q = 0; q < 2; ++q)
#pragma unroll
      for (int e = 0; e < 4; ++e) {
        const float x0 = acc[6][2 * q][e], x1 = acc[6][2 * q + 1][e];
        auto sw = __builtin_amdgcn_permlane32_swap(__float_as_uint(x0), __float_as_uint(x1), false, false);
        acc[6][2 * q][e] = __uint_as_float(sw[0]); acc[6][2 * q + 1][e] = __uint_as_float(sw[1]);
      }
    TS4(26);
    int go6 = go0 + (int)((foff >> 7) & 1u) * SA + (int)(foff >> 8) * (SA - 256) + (384 + tn * 2 + wn) * 256; asm volatile("" : "+v"(go6));
    grp(IC<6>{}, I1{}, [&](auto) {});
    poll();
    __builtin_amdgcn_raw_buffer_store_b128(keep(hv[0][6], hv[1][6], hv[2][6], hv[3][6]), rO, go6, 0, 16);
    __builtin_amdgcn_sched_barrier(0);
    asm volatile("s_nop 1" ::: "memory");
    late.tag = fval;
    asm volatile("" ::: "memory");
    __builtin_amdgcn_s_setprio(0);
  };

  TS4(0);
  constexpr int WR = 2;
  if (wave == 5) ds_w32(lds0 + DONE, 0);
  STAMP(9); STAMP(6);
  auto check_tile = [&](int t) {
    int lx = lane; asm volatile("" : "+v"(lx));
    const unsigned ad = lds0 + XBASE + t * XT + wave * 1024 + lx * 16;
    if (__builtin_expect(marked<8>(ad), 0)) {
      for (int tries = 0;; ++tries) {
        if (tries >= TRIES) wait_flags(lds0, a.sync, F_H + F_PAN(panel), 32, 0);
#pragma unroll 1
        for (int u = t; u < t + 1 + (t == 0); ++u) { issueX(u, 0, u); issueX(u, 1, u); }
        if (t == 0) wait_vm<2 * XI>(); else wait_vm<0>();
        if (tries >= TRIES || !marked<8>(ad)) break;
      }
    }
  };
  auto run = [&](auto LDRc) __attribute__((always_inline)) {
    constexpr bool LDR = decltype(LDRc)::value;
    if constexpr (LDR) {
      issueX(0, 0, 0); issueX(0, 1, 0); issueX(1, 0, 1); issueX(1, 1, 1);
      asm volatile("" : "+v"(wsrc[0]), "+v"(wsrc[1]), "+v"(wsrc[2]), "+v"(wsrc[3]));
      wait_vm<2 * XI>();
      check_tile(0);
    } else {
      wait_vm<0>();
    }
    unsigned long long tb = (unsigned long long)a.PF0; unsigned tv = 0, tl = 0x7fffffffu, ts = 7168u, to = 0, tmask = 0;
    if constexpr (!LDR) {
      unsigned long long c0, c1; CODE_ADDR(c0, "k_all"); CODE_ADDR(c1, "k_zend");
      if (wave == 4) { tb = c0; tl = c1 - c0 < 262144ull ? (unsigned)(c1 - c0) : 0u; tmask = 1u << 11; }
      if (wave == 5) { tmask = 0x55u << (6 + (idx & 1)); to = ((xcd & 3) * 32 + idx) * 57344 + (xcd >> 2) * 28672; }
      if (wave == 6) { tb = (unsigned long long)((idx & 16) ? a.W1 : a.W0); ts = 64 * LDW * 2; tmask = 0x1eu; to = (krot + 4) * ts; }
      const int l4 = lane / 14;
      tv = wave == 4 ? (unsigned)((lane * 32 + idx) * 128)
         : (lane >= 56 ? 0x80000000u : (wave == 5 ? (unsigned)(lane * 128) : (unsigned)(((idx & 15) * 4 + l4) * (LDW * 2) + (xcd & 3) * 1792 + (lane - l4 * 14) * 128)));
    }
    const int js = 2 * (tm & 3) + 1;
    const auto rC = RSRC(a.cs);
    constexpr int JR = 10, JS = 2;
    const int jf = wave == 4 ? NT - 4 : (wave == 7 ? JR : 64);
    const int fof = wave == 4 ? F_COL + tn * 256 : F_ROW + F_PAN(panel);
    unsigned evm = tmask | (8u << js) | (wave == 4 ? 3u << (NT - 4) : 0u) | (wave == 7 ? ((5u << JS) | 2u | (11u << JR)) : 0u);
    if constexpr (!LDR) asm volatile("" : "+v"(tv), "+s"(tb), "+s"(tl), "+s"(ts), "+s"(to), "+s"(tmask), "+s"(evm));
    lds_barrier();
    if constexpr (!LDR) { if (wave == 4) __builtin_amdgcn_raw_ptr_buffer_load_lds(RSRC(a.sync), LDSP(lds0 + TCH + 256), 4, lane * 4, F_H + F_PAN(panel), 0, 16); }
    { const RdCtx c = mkctx(false, 0, WR * WU); static_for<0, MF + NF>([&](auto kc) { rd_item(fa, c, kc); }); }
    wait_frags(fa);
    STAMP(1);
    TS4(1);
    { const RdCtx c = mkctx(true, 0, (WR + 1) * WU); step(fa, fb, c, T{}, IC<LDR ? 2 * XI : 0>{}, [&](int i) { ldX(i % XI, 2, i / XI, 2); }); }
    RdCtx cc = mkctx(false, XT, 0);
    int xs = 1, dX = XT, dW = 2 * WU;
    int wd = lds0 + WBASE + 2 * WU, wso = ((4 + 2 * krot) & 31) * (32 * LDW * 2), xd = lds0 + XBASE + wave * 1024, xso = ko(((3 + krot) & 15) * 2048);
    int wc[WI];
#pragma unroll
    for (int i = 0; i < WI; ++i) { int q = wave + NLW * i; if (q >= NWI) q -= NLW; wc[i] = q >= 7 ? UPOFF + (q - 7) * 1024 : q * 1024; }
    unsigned evs = evm, dw = 0, ca = 0, cw = 0; int lc = 0, dwb = 0;
    auto ldd = [&](int i) { static_for<0, 22>([&](auto kc) { if (i == decltype(kc)::value) hook(kc, dw, dwb); }); };
    constexpr int CFL = TCH + 256, CPB = XBASE + ((NT - 3) % 3) * XT;
    auto iter = [&](int j, auto LWc, auto LXc, auto WAITc, auto CAc, auto CBc) {
      constexpr bool LW = decltype(LWc)::value && LDR, LX = decltype(LXc)::value && LDR, LP = LW && !LX;
      TS4(j + 1);
      if constexpr (LDR) {
        wait_vm<decltype(WAITc)::value>();
        if constexpr (LX) { if (__builtin_expect(j <= 2, 0)) check_tile(j); }
      }
      if constexpr (!LDR) {
        if (__builtin_expect(evs & 2, 0)) {
          if (j == js + 3) {
            if ((tmask >> (j - 1)) & 1) wait_vm<1>(); else wait_vm<0>();
            flag_set(a.sync, F_COL + tn * 256 + (tm * 4 + wave - 4) * 8, tag_get(lds0), 0);
          }
          if (j == 1) glu_stats_issue();
          if (j == JS && wave == 7) { wait_vm<0>(); glu_stats(); }
          if (j == JS + 2 && wave == 7) {
            if (j == js + 2) wait_vm<1>(); else wait_vm<0>();
            flag_set(a.sync, fof + tn * 8, tag_get(lds0), 0);
          }
          {
            int lx = lane; asm volatile("" : "+v"(lx));
            if (j == jf) __builtin_amdgcn_raw_ptr_buffer_load_lds(RSRC(a.sync), LDSP(lds0 + CFL + (wave & 1) * 256), 4, lx * 4, fof, 0, 16);
            if (j == jf + 1) {
              wait_vm<0>();
              int fv = ds_r32(lds0 + CFL + (wave & 1) * 256 + lx * 4);
              const Tag tg = tag_get(lds0);
              asm volatile("" : "+v"(fv));
              while (!words_up((unsigned)fv, tg) && in_time(lds0))
                asm volatile("global_load_dword %0, %1, %2 sc1\n\ts_waitcnt vmcnt(0)" : "=v"(fv) : "v"(lx * 4), "s"((const char*)a.sync + fof) : "memory");
              if (wave == 7) {
#pragma unroll
                for (int i = 0; i < 4; ++i) __builtin_amdgcn_raw_ptr_buffer_load_lds(rC, LDSP(lds0 + RING + i * 1024), 16, lx * 16, RTAB + m0 * 16 + i * 1024, 0, 16);
              }
            }
            if (j == jf + 3) wait_vm<0>();
          }
          if ((tmask >> j) & 1) { touch((const void*)tb, tv, to, tl); to += ts; }
        }
        __builtin_amdgcn_sched_barrier(0);
      }
      constexpr bool LD = LDR && !LW;
      if constexpr (LD) {
        f32x4 p0 = __builtin_bit_cast(f32x4, ds_b128<0>(ca)), p1 = __builtin_bit_cast(f32x4, ds_b128<224>(ca)), p2 = __builtin_bit_cast(f32x4, ds_b128<448>(ca)), p3 = __builtin_bit_cast(f32x4, ds_b128<672>(ca));
        asm volatile("s_waitcnt lgkmcnt(0)" : "+v"(p0), "+v"(p1), "+v"(p2), "+v"(p3) :: "memory");
        const f32x4 cv = (p0 + p1) + (p2 + p3);
        const auto s0 = __builtin_amdgcn_permlane16_swap(__float_as_uint(cv[0]), __float_as_uint(cv[1]), false, false), s1 = __builtin_amdgcn_permlane16_swap(__float_as_uint(cv[2]), __float_as_uint(cv[3]), false, false);
        const auto s2 = __builtin_amdgcn_permlane32_swap(__float_as_uint(__uint_as_float(s0[0]) + __uint_as_float(s0[1])), __float_as_uint(__uint_as_float(s1[0]) + __uint_as_float(s1[1])), false, false);
        ds_w32(cw, __float_as_int(__uint_as_float(s2[0]) + __uint_as_float(s2[1])));
        __builtin_amdgcn_sched_barrier(0);
      }
      auto lda = [&](int i) { if constexpr (LD) ldd(i); else { const int k = i % WI;
          if constexpr (LP) { if (i < 4) __builtin_amdgcn_raw_ptr_buffer_load_lds(rC, LDSP(lds0 + CPB + wave * 4096 + i * 1024), 16, lane * 16, (tn * 4 + wn * 2 + (wave >> 1)) * 3584 + i * 1024, 0, 16); }
          if (k < 3 || wave < 2) __builtin_amdgcn_raw_ptr_buffer_load_lds((k >= 2 || (k == 1 && wave == 3)) ? RSRC(a.W1) : RSRC(a.W0), LDSP(wd + wc[k]), 16, wsrc[k], wso, 0, 0); } };
      auto ldb = [&](int i) {
          if constexpr (LX) __builtin_amdgcn_raw_ptr_buffer_load_lds(RSRC(a.X), LDSP(xd + i * 4096), 16, xsrc[i % XI], xso + (i / XI) * (NXI / 2) * SH, 0, 16);
          else if constexpr (LD) ldd(8 + i); };
      using NLA = IC<(LW || LD) ? 8 : 0>; using NLB = IC<(LX || LD) ? 8 : 0>;
      auto pin1 = [](int& v) { asm volatile("" : "+s"(v)); };
      stepg(CAc, fb, fa, cc, I0{}, IC<LDR ? 2 : 0>{}, IC<LDR ? 1 : -1>{}, [&]() { if constexpr (LD) lds_barrier(); else raw_barrier(); }, NLA{}, lda, [&](auto ic) { constexpr int I = decltype(ic)::value;
        if constexpr (LW && I == 12) { wd += WU; wso += 32 * LDW * 2; pin1(wd); pin1(wso); }
        if constexpr (LW && I == 24) { wd += dW > 0 ? -3 * WU : WU; pin1(wd); }
        if constexpr (LW && I == 25) { wso += 32 * LDW * 2; pin1(wso); }
        if constexpr (LW && I == 26) { wso = wso == 1024 * LDW * 2 ? 0 : wso; pin1(wso); }
        if constexpr (!LDR && I == 24) { evs >>= 1; asm volatile("" : "+s"(evs)); }
        if constexpr (I == 7) { dX = xs == 2 ? -2 * XT : XT; pin1(dX); }
        if constexpr (I == 10) { xs = xs == 2 ? 0 : xs + 1; pin1(xs); } });
      stepg(CBc, fa, fb, cc, I1{}, I0{}, IC<LDR ? -1 : 25>{},
          [&]() { asm volatile("s_waitcnt lgkmcnt(0)" ::: "memory"); if (decltype(WAITc)::value == 0 || j + 1 < NT) raw_barrier(); }, NLB{}, ldb, [&](auto ic) { constexpr int I = decltype(ic)::value;
        if constexpr (LP && I == 10) { lc = lane_id(); ca = (lc & 15) < 14 ? (lc & 15) * 16 : 0; asm volatile("" : "+v"(ca)); }
        if constexpr (LP && I == 12) { cw = lds0 + RING + 4096 + (wave >> 1) * 448 + wn * 224 + ca + (lc >> 4) * 4; asm volatile("" : "+v"(cw)); }
        if constexpr (LP && I == 14) { ca += lds0 + CPB + wave * 4096 + (lc >> 4) * 896; asm volatile("" : "+v"(ca)); }
        if constexpr (LP && I == 16) { dw = dn_wsrc(lc, wave); asm volatile("" : "+v"(dw)); }
        if constexpr (LP && I == 19) { dwb = dn_wbase(wave); pin1(dwb); }
        if constexpr (I == 8) { cc.xa += dX; asm volatile("" : "+v"(cc.xa)); }
        if constexpr (I == (LDR ? 24 : 22)) { cc.wb[0] += dW; cc.wb[1] += dW; asm volatile("" : "+v"(cc.wb[0]), "+v"(cc.wb[1])); }
        if constexpr (I == (LDR ? 25 : 23)) { dW = -dW; pin1(dW); }
        if constexpr (LX && I == 25) { xso = ((xso | 0x2000) + 2048) & 0xd800; pin1(xso); }
        if constexpr (LX && I == 26) { xd += xs == 1 ? -2 * XT : XT; pin1(xd); } });
    };
    using WX_ = IC<2 * XI>;
    if constexpr (LDR) {
      for (int j = 1; j < NT - 2; ++j) iter(j, T{}, T{}, WX_{}, F{}, F{});
      iter(NT - 2, T{}, F{}, WX_{}, F{}, F{});
      __builtin_amdgcn_s_setprio(3);
      iter(NT - 1, F{}, F{}, I0{}, F{}, F{});
    } else {
      int j = 1;
      if (wave == 4) {
        wait_vm<0>();
        int fv = ds_r32(lds0 + TCH + 256 + lane * 4);
        const Tag tg = tag_get(lds0);
        asm volatile("" : "+v"(fv));
        if (!words_up((unsigned)fv, tg)) wait_flags(lds0, a.sync, F_H + F_PAN(panel), 32, 0);
      }
      raw_barrier();
#pragma unroll 1
      for (; j < js; ++j) iter(j, F{}, F{}, I0{}, F{}, F{});
      const int ja = wm == 2 ? js + 2 : js;
#pragma unroll 1
      for (; j < ja; ++j) iter(j, F{}, F{}, I0{}, T{}, F{});
#pragma unroll 1
      for (; j < js + 2; ++j) iter(j, F{}, F{}, I0{}, F{}, T{});
      { int lx = lane; asm volatile("" : "+v"(lx));
        __builtin_amdgcn_raw_buffer_store_b128(__builtin_bit_cast(u32x4, accs), rC, (lx & 15) < NF ? (unsigned)((tn * 4 + wn * 2) * 3584 + (tm * 2 + (wm & 1)) * 224
                + ((lx & 15) < 6 ? (lx & 1) * 3584 + ((lx & 15) >> 1) * 64 + (lx >> 4) * 16 : (lx >> 5) * 3584 + 192 + (lx & 16))) : 0x80000000u, 0, 16); }
#pragma unroll 1
      for (; j < NT; ++j) iter(j, F{}, F{}, I1{}, F{}, F{});
    }
    done_inc();
    TS4(17);
    step(fb, fa, cc, F{}, IC<NF>{}, [&](int i) { tabs(i - 1); if constexpr (LDR) { if (i > 0) ldd(15 + i); } });
    tabs(8);
    TS4(18);
  };
  if (wave < NLW) run(T{}); else run(F{});
  epilogue();
  STAMP(7);
  STAMP(8);
  STAMPW(12, 4);
  STAMP(3);
#endif
}

struct FragW { s16x4 lo[4], hi[4]; };
template <int MODE, int LO, int HI>
__device__ __forceinline__ void wo_phase(const GemmArgs& a, const GemmArgs& nx, int pol = 0) {
#if defined(__HIP_DEVICE_COMPILE__)
  extern __shared__ __attribute__((aligned(16))) char smem[];
  if constexpr (MODE == 2) STAMP(0);
  const unsigned lds0 = (unsigned)(size_t)(lds_char_ptr)smem;
  const int tid = threadIdx.x, lane = tid & 63;
  const int wave = __builtin_amdgcn_readfirstlane(tid >> 6);
  const int bid = blockIdx.x, xcd = bid & 7, idx = bid >> 3;
  const int tm = (xcd >> 1) * 4 + (idx >> 3), tn = (xcd & 1) * 8 + (idx & 7);
  const int m0 = tm * 128, panel = m0 >> 8, rot = WO_ROT;
  if constexpr (MODE & 1) {
    const auto rW = __builtin_amdgcn_make_buffer_rsrc((void*)a.W0, 0, 0x7fffffff, 0x00020000);
    const int row = wave * 8 + (lane >> 3);
    const unsigned wsrc = (unsigned)(row * D_ + tn * 64 + ((lane & 7) ^ wswz(row)) * 8) * 2u;
    static_for<LO, HI>([&](auto ic) { constexpr int i = decltype(ic)::value;
      __builtin_amdgcn_raw_ptr_buffer_load_lds(rW, (lds_void_ptr)(size_t)(lds0 + wo_pos(i >> 1) + (i & 1) * 8192 + wave * 1024), 16, wsrc, (((i & 1) * 8 + (i >> 1)) ^ rot) * (64 * D_ * 2), 0, 0);
      if constexpr (MODE == 3) __builtin_amdgcn_raw_ptr_buffer_load_lds(rW, LDSP(lds0 + wo_pos(i >> 1) + (i & 1) * 8192 + 4096 + wave * 1024), 16, wsrc, (((i & 1) * 8 + (i >> 1)) ^ rot) * (64 * D_ * 2) + 32 * D_ * 2, 0, 0); });
  } else {
    constexpr int PAD = 149504;
    const int g = wave >> 2, rw = wave & 3;
    const int fr = lane & 15, fq = lane >> 4, wq = (lane >> 2) & 3, wp = lane & 3;
    unsigned wa[4];
#pragma unroll
    for (int c = 0; c < 4; ++c) { const int row = 8 * fq + wq; wa[c] = lds0 + g * 8192 + row * 128 + (((c << 1) ^ wswz(row)) << 4) + wp * 8; }
    FragW f[16];
    auto rd = [&](auto ic, auto rc) {
      constexpr int i = decltype(ic)::value, r = decltype(rc)::value, P = wo_pos(i >> 1) + (i & 1) * 4096, B = P & ~32767, o = P & 32767;
      if constexpr (r & 1) f[i].hi[r >> 1] = ds_tr<o + 512>(wa[r >> 1] + B); else f[i].lo[r >> 1] = ds_tr<o>(wa[r >> 1] + B);
    };
    auto pin = [&](FragW& q) {
#pragma unroll
      for (int n = 0; n < 4; ++n) asm volatile("" : "+v"(q.lo[n]), "+v"(q.hi[n]));
    };
    static_for<0, 16>([&](auto rc) { constexpr int r = decltype(rc)::value; rd(IC<r / 8>{}, IC<r % 8>{}); });
    const Tag tg = tag_get(lds0);
    pin(f[0]); pin(f[1]);
    if (wave == 2) {
      unsigned long long c0, c1; CODE_ADDR(c0, "k_all"); CODE_ADDR(c1, "k_zend");
      __builtin_amdgcn_raw_ptr_buffer_load_lds(RSRCN(c0, c1 - c0 < 262144ull ? (unsigned)(c1 - c0) : 0u), LDSP(lds0 + PAD), 4, (unsigned)((lane * 32 + idx) * 128), 0, 0, 0); }
    if (wave == 3) {
      const unsigned fo = (unsigned)(((lane & 32) ? F_PAN((xcd >> 2) * 4 + (idx >> 3)) : F_PAN(panel)) + (lane & 31) * 8);
      while (!flags_up(__builtin_amdgcn_raw_buffer_load_b64(RSRC(a.sync), fo, F_MARK, 16), 64, tg, 0) && in_time(lds0)) {}
    }
    const auto rX = __builtin_amdgcn_make_buffer_rsrc((void*)a.X, 0, 0x7fffffff, 0x00020000);
    const int rb = (m0 >> 4) + rw * 2 + g;
    unsigned xs[2] = {(unsigned)(rb_h(rb, pol) + lane * 16), (unsigned)(rb_h(rb ^ 1, pol) + lane * 16)};
    const int ck = (fq & 1) * 2 + (fq >> 1);
    bf16x8 s0 = pick(fq == (fr >> 3), fr & 7), s1 = pick(fq == 2 + (fr >> 3), fr & 7);
    asm volatile("" : "+v"(s0), "+v"(s1));
    unsigned orow = (unsigned)((rb * 16 + fr) * (D_ * 2) + tn * 128 + fq * 16), ofrg = (unsigned)(rb_h(rb, pol) + ko(tn * 2048) + ck * 256 + fr * 16);
    asm volatile("" : "+v"(xs[0]), "+v"(xs[1]), "+v"(orow), "+v"(ofrg));
    u32x4 res[2];
    STAMP(9);
    const int fo_off = F_O(tm) + rw * 256 + ((g ^ (rot >> 3)) & 1) * 128;
    const unsigned first = (rot & 4) ? 0xff00u : 0x00ffu;
    auto down = [&](u32x2 v) { return (unsigned)__builtin_amdgcn_ballot_w64(v[0] != tg.lo || v[1] != tg.hi) & 0xffffu; };
    unsigned dn, polls = 0;
    do { dn = down(flag_get(a.sync, fo_off, 16)); asm volatile("" ::: "memory"); } while ((dn & first) && ((++polls & 1023u) || in_time(lds0)));
    STAMP(6);
    u32x2 fl = {0u, 0u};
    u32x4 xr[16][2];
    auto ldX = [&](int i, int m) {
      if (i >= 0 && i < 16) xr[i][m] = __builtin_amdgcn_raw_buffer_load_b128(rX, xs[m] + (i & 1) * 1024, ko(((g * 8 + (i >> 1)) ^ rot) * 2048), 16);
      else if (m == 1 && (i == 16 || i == 17)) res[i - 16] = __builtin_amdgcn_raw_buffer_load_b128(RSRC(a.R), orow + (i - 16) * 64, 0, 0);
    };
    constexpr int XD = 4;
#pragma unroll
    for (int i = 0; i < XD; ++i) { ldX(i, 1); ldX(i, 0); __builtin_amdgcn_sched_barrier(0); }
    unsigned gsrc[2];
#pragma unroll
    for (int i = 0; i < 2; ++i) {
      const int j = wave + 8 * i, jj = j >= 7 ? j - 7 : j;
      const int b = jj * 1024 + lane * 16, q = b / 224, pos = (b % 224) >> 4;
      gsrc[i] = (unsigned)((8 * ((q & 15) >> 2) + (q & 3) + 4 * (q >> 4)) * DF_ + ((xcd & 3) * 8 + (idx & 7)) * 112 + pos * 8) * 2u;
      asm volatile("" : "+v"(gsrc[i]));
    }
    f32x4 acc[4][2];
#pragma unroll
    for (int n = 0; n < 4; ++n) { acc[n][0] = (f32x4){0.f, 0.f, 0.f, 0.f}; acc[n][1] = acc[n][0]; }
    __builtin_amdgcn_sched_barrier(0);
    f32x4 pa[4];
    auto slot = [&](int w) { return wo_pos(0) + w * 4096 + lane * 16; };
    static_for<0, 17>([&](auto ic) {
      constexpr int i = decltype(ic)::value;
      if constexpr (i < 16 && !(i & 1)) __builtin_amdgcn_s_setprio(3 - ((i >> 1) & 3));
      if constexpr (i >= 2 && i < 16) { asm volatile("s_waitcnt lgkmcnt(%0)" :: "n"(i < 15 ? 8 : 0) : "memory"); pin(f[i]); }
      if constexpr (i == WO_E - 2) raw_barrier();
      if constexpr (i == 8 - XD - 2) fl = flag_get(a.sync, fo_off, 16);
      if constexpr (i == 8 - XD) { if (__builtin_expect(dn && down(fl), 0)) wait_flags(lds0, a.sync, fo_off, 16, 0); }
      if constexpr (i == 16) {
        STAMP(2);
#pragma unroll
        for (int n = 0; n < 4; ++n) *(f32x4*)(smem + slot(wave) + n * 1024) = acc[n][1];
        __builtin_amdgcn_s_setprio(1);
        __builtin_amdgcn_sched_barrier(0);
        lds_barrier();
        STAMP(4);
        static_for<0, 4>([&](auto nc) { constexpr int n = decltype(nc)::value; pa[n] = __builtin_bit_cast(f32x4, ds_b128<n * 1024>(lds0 + slot(wave ^ 4))); });
        __builtin_amdgcn_sched_barrier(0);
      }
      static_for<0, 8>([&](auto jc) {
        constexpr int j = decltype(jc)::value, n = j & 3, m = 1 - (j >> 2), t = i;
        if constexpr (t >= 0 && t < 16) acc[n][m] = MFMA(__builtin_shufflevector(f[t].lo[n], f[t].hi[n], 0, 1, 2, 3, 4, 5, 6, 7), __builtin_bit_cast(bf16x8, xr[t][m]), acc[n][m]);
        if constexpr (m == 0 && t == 16) acc[n][0] = MFMA((n & 1) ? s1 : s0, __builtin_bit_cast(bf16x8, res[n >> 1]), acc[n][0]);
        if constexpr (i + 2 < 16) rd(IC<i + 2>{}, jc);
        if constexpr (j == 1) ldX(i + XD, 1);
        if constexpr (j == 5) ldX(i + XD, 0);
        __builtin_amdgcn_sched_barrier(0);
      });
      if constexpr (i == 0) STAMP(1);
    });
    auto ldG = [&](int i, int u) { const int j = wave + 8 * i;
      __builtin_amdgcn_raw_ptr_buffer_load_lds(RSRC(j >= 7 ? nx.W1 : nx.W0), LDSP(lds0 + 98304 + (u ^ 2) * 14592 + (j >= 7 ? 7184 + (j - 7) * 1024 : j * 1024)), 16, gsrc[i], (u + (xcd >> 2) * 16) * 32 * DF_ * 2, 0, 0); };
    auto ldG2 = [&](int u) { ldG(0, u); ldG(0, u + 1); if (wave < 6) { ldG(1, u); ldG(1, u + 1); } };
    asm volatile("s_waitcnt lgkmcnt(0)" : "+v"(pa[0]), "+v"(pa[1]), "+v"(pa[2]), "+v"(pa[3]) :: "memory");
    const auto rO = __builtin_amdgcn_make_buffer_rsrc((void*)a.O, 0, 0x7fffffff, 0x00020000);
#pragma unroll
    for (int i = 0; i < 2; ++i) {
      pa[2 * i] += acc[2 * i][0]; pa[2 * i + 1] += acc[2 * i + 1][0];
      const f32x4 u = pa[2 * i], v = pa[2 * i + 1];
      const auto t0 = __builtin_amdgcn_permlane16_swap(pk_bf16(u[0], u[1]), pk_bf16(v[0], v[1]), false, false);
      const auto t1 = __builtin_amdgcn_permlane16_swap(pk_bf16(u[2], u[3]), pk_bf16(v[2], v[3]), false, false);
      __builtin_amdgcn_raw_buffer_store_b128((u32x4){t0[0], t1[0], t0[1], t1[1]}, rO, ofrg + i * 1024, 0, 16);
    }
    __builtin_amdgcn_sched_barrier(0);
    __builtin_amdgcn_s_setprio(0);
    auto sum4 = [](float v) {
      auto p = __builtin_amdgcn_permlane16_swap(__float_as_uint(v), __float_as_uint(v), false, false); v = __uint_as_float(p[0]) + __uint_as_float(p[1]);
      auto q = __builtin_amdgcn_permlane32_swap(__float_as_uint(v), __float_as_uint(v), false, false); return __uint_as_float(q[0]) + __uint_as_float(q[1]); };
    f32x2 e[8], sm = {0.f, 0.f};
#pragma unroll
    for (int j = 0; j < 8; ++j) { e[j] = (f32x2){pa[j >> 1][(j & 1) * 2], pa[j >> 1][(j & 1) * 2 + 1]}; sm += e[j]; }
    const float mu = sum4(sm[0] + sm[1]) * (1.f / 64.f);
    f32x2 t2 = {0.f, 0.f};
#pragma unroll
    for (int j = 0; j < 8; ++j) { const f32x2 d = e[j] - mu; t2 += d * d; }
    const float q2 = sum4(t2[0] + t2[1]);
    __builtin_amdgcn_raw_buffer_store_b64((u32x2){__float_as_uint(mu), __float_as_uint(q2)}, __builtin_amdgcn_make_buffer_rsrc((void*)a.st, 0, 0x7fffffff, 0x00020000),
                                          lane < 16 ? (unsigned)((tn * S_ + rb * 16 + lane) * 8) : 0x80000000u, 0, 16);
    STAMP(7);
    STAMP(8);
    ldG2(0);
    ldG2(2);
    if (wave < 6) wait_vm<8>(); else wait_vm<4>();
    lds_barrier();
    if (wave == 0) flag_set(a.sync, F_H + F_PAN(panel) + ((tm & 1) * 16 + tn) * 8, tg, 0);
    STAMP(3);
  }
#endif
}

struct FragD { s16x4 lo[8], hi[8]; };
enum { O_X, O_W, O_SP, O_FA, O_FP, O_QC, O_QD, O_QP, O_RES, O_LP, O_CC, O_CD, O_CP, O_BAR, O_AR, O_AP };
constexpr int DN_A = 16, DN_E = 0, DN_PRE = 11;
constexpr unsigned char DN_OPS[][4] = {
 {0,10,O_CC,1}, {0,11,O_X,6}, {0,13,O_X,7}, {0,15,O_X,8}, {1,2,O_X,9}, {1,5,O_X,10}, {1,9,O_X,11}, {1,13,O_W,11}, {2,0,O_FA,2}, {2,2,O_X,12}, {2,5,O_X,13}, {2,9,O_X,14}, {2,13,O_W,12},
 {3,2,O_X,15}, {3,5,O_X,16}, {3,9,O_X,17}, {3,13,O_X,18}, {4,2,O_BAR,12}, {4,3,O_X,19}, {4,7,O_W,14}, {4,12,O_W,13}, {4,16,O_QC,2},
 {5,2,O_W,17}, {5,7,O_W,16}, {5,12,O_W,15}, {6,0,O_CC,2}, {6,2,O_X,20}, {6,5,O_X,21}, {6,9,O_X,22}, {6,13,O_W,18}, {7,2,O_X,23}, {7,5,O_X,24}, {7,9,O_X,25}, {7,13,O_W,19},
 {8,2,O_X,26}, {8,5,O_X,27}, {8,9,O_X,28}, {8,13,O_W,20}, {9,2,O_X,29}, {9,5,O_X,30}, {9,9,O_X,31}, {9,13,O_W,21}, {10,2,O_X,32}, {10,7,O_W,23}, {10,12,O_W,22},
 {11,2,O_X,33}, {11,5,O_X,34}, {11,9,O_X,35}, {11,13,O_X,36}, {11,16,O_QD,0}, {12,2,O_BAR,23}, {12,3,O_X,37}, {12,6,O_X,38}, {12,10,O_X,39}, {12,13,O_X,40},
 {13,2,O_X,41}, {13,5,O_X,42}, {13,9,O_X,43}, {13,13,O_W,24}, {14,0,O_CD,0}, {14,2,O_X,44}, {14,5,O_X,45}, {14,9,O_W,25}, {14,13,O_RES,0},
 {15,2,O_X,46}, {15,7,O_X,47}, {15,12,O_W,26}, {16,2,O_X,48}, {16,5,O_X,49}, {16,9,O_W,27}, {16,13,O_RES,1}, {17,2,O_X,50}, {17,5,O_X,51}, {17,9,O_X,52}, {17,13,O_W,28},
 {18,2,O_X,53}, {18,9,O_W,29}, {19,2,O_W,31}, {19,9,O_W,30}, {19,16,O_AR,0}, {20,2,O_SP,0}, {20,9,O_SP,1}, {21,2,O_X,54}, {21,5,O_X,55}, {21,9,O_SP,2}, {21,13,O_SP,3},
 {22,2,O_X,56}, {22,5,O_X,57}, {22,9,O_X,58}, {22,13,O_X,59}, {23,0,O_FP,0}, {23,2,O_BAR,31}, {23,3,O_X,60}, {23,6,O_X,61}, {23,10,O_X,62}, {23,13,O_X,63},
 {24,0,O_FP,1}, {24,1,O_SP,4}, {24,2,O_SP,5}, {25,8,O_QP,0}, {26,0,O_FP,2}, {27,0,O_CP,0}, {27,0,O_CP,1}, {27,2,O_LP,0}, {27,5,O_LP,1},
 {28,2,O_LP,2}, {28,5,O_LP,3}, {28,8,O_QP,2}, {30,0,O_CP,2}, {30,2,O_LP,4}, {30,5,O_LP,5}, {32,0,O_AP,0}, {32,0,O_AP,1}, {32,0,O_AP,2}, {32,0,O_AP,3}, {32,0,O_AP,4}, {32,0,O_AP,5},
};
constexpr int DN_N = sizeof(DN_OPS) / 4, DN_H = (24 - DN_A) / 2, DN_B = DN_A + DN_H, BB = 24 + DN_E, NS = BB + 8;
constexpr int dn_nm(int v) { return v < 24 - DN_E ? 16 : 8; }
constexpr int dn_unit(int v, int n) { return v < DN_A ? v : (v >= BB ? v - DN_E : DN_A + 2 * (((v < 24 ? v : v - DN_E) - DN_A) % DN_H) + (n >> 2)); }
constexpr int dn_wpos(int u, int h) { constexpr int P[19] = {8, 9, 10, 11, 16, 17, 18, 4, 5, 6, 7, 14, 15, 0, 1, 2, 3, 12, 13}; return P[u % 19] * 8192 + h * 4096; }
constexpr int dn_first(int v, int g) { int i = 0; while (i < DN_N && (DN_OPS[i][0] < v || (DN_OPS[i][0] == v && DN_OPS[i][1] < g))) ++i; return i; }
constexpr int dn_find(int kind, int arg) { for (int i = 0; i < DN_N; ++i) if (DN_OPS[i][2] == kind && DN_OPS[i][3] == arg) return i; return -1; }
constexpr int dn_vm(int lo, int hi) { int c = 0; for (int i = lo; i < hi; ++i) c += DN_OPS[i][2] <= O_LP; return c; }
constexpr bool dn_bar(int v) { for (int i = 0; i < DN_N; ++i) if (DN_OPS[i][0] == v && DN_OPS[i][2] == O_BAR) return true; return false; }
constexpr int dn_cnt(int w, int i) { int p = -1; for (int u = DN_PRE; u <= w; ++u) if (dn_find(O_W, u) > p) p = dn_find(O_W, u); return p >= 0 ? dn_vm(p + 1, i) : 63; }
constexpr bool dn_ok() {
  int have = DN_PRE - 1, bstep = 0;
  for (int v = 0; v + 1 < NS; ++v) {
    for (int i = dn_first(v, 0); i < dn_first(v + 1, 0); ++i) {
      if (DN_OPS[i][2] == O_BAR) { if (DN_OPS[i][3] > have) have = DN_OPS[i][3]; bstep = v; for (int u = DN_PRE; u <= have; ++u) if (dn_find(O_W, u) < 0 || dn_find(O_W, u) > i) return false; }
      if (DN_OPS[i][2] == O_W) {
        if (bstep < 1 && DN_OPS[i][3] > 12) return false;
        for (int t = bstep + 1; t < NS; ++t) for (int n = 0; n < 8; n += 4) if (dn_unit(t, n) < DN_OPS[i][3] && dn_wpos(dn_unit(t, n), 0) == dn_wpos(DN_OPS[i][3], 0)) return false;
      }
    }
    if (dn_unit(v + 1, 7) > have) return false;
  }
  for (int x = 6; x < 64; ++x) {
    int n = 0; for (int i = 0; i < dn_find(O_X, x); ++i) { if (DN_OPS[i][2] == O_CC) n = DN_OPS[i][3]; if (DN_OPS[i][2] == O_CD) n = 4; }
    if (n < (x < 20 ? 1 : (x < 48 ? 2 : 4))) return false;
  }
  if (dn_find(O_FA, 2) < 0) return false;
  return dn_find(O_AR, 0) < dn_find(O_SP, 0) && dn_find(O_AR, 0) > dn_find(O_RES, 1) && dn_find(O_SP, 0) >= dn_first(DN_B, 0) && dn_find(O_SP, 4) >= dn_first(24, 0);
}
static_assert(dn_ok(), "schedule");
constexpr int dn_rd(int v, int I) { int c = 0; const int r0 = dn_bar(v) ? 2 : 1; if (v + 1 < NS) for (int r = 0; r < 16; ++r) c += r0 + r * (dn_nm(v) - r0 + 1) / 16 <= I; return c; }
struct DnTab { short first[NS + 1][18], rd[NS][18], wait[DN_N]; };
constexpr DnTab dn_tab() {
  DnTab t = {};
  for (int v = 0; v <= NS; ++v) for (int g = 0; g < 18; ++g) { t.first[v][g] = dn_first(v, g); if (v < NS) t.rd[v][g] = dn_rd(v, g); }
  for (int i = 0; i < DN_N; ++i) {
    const int kind = DN_OPS[i][2], arg = DN_OPS[i][3];
    t.wait[i] = kind == O_CC ? 1 + dn_vm(0, i) : kind == O_FA ? dn_vm(0, i) : (kind == O_FP ? dn_vm(dn_find(O_SP, 2 * arg + 1) + 1, i) : (kind == O_BAR ? dn_cnt(arg, i) : 0));
  }
  return t;
}
constexpr DnTab DT = dn_tab();
constexpr int dn_uj(int v) { return v < 3 ? 0 : 1 + (v - 3) % 7; }
constexpr int dn_us(int v) { return v < 3 ? v : (v - 3) / 7; }
template <int U>
__device__ __forceinline__ void dn_first_w(const GemmArgs& a, unsigned wsrc, int wb) {
#if defined(__HIP_DEVICE_COMPILE__)
  extern __shared__ __attribute__((aligned(16))) char smem[];
  const unsigned lds0 = (unsigned)(size_t)(lds_char_ptr)smem;
  const int wave = __builtin_amdgcn_readfirstlane(threadIdx.x >> 6);
  __builtin_amdgcn_raw_ptr_buffer_load_lds(RSRC(a.W0), LDSP(lds0 + dn_wpos(U >> 1, U & 1) + wave * 1024), 16, wsrc,
                                           (wb + (((blockIdx.x >> 3) + dn_uj(U >> 1)) & 7) * (112 * D_ * 2) + dn_us(U >> 1) * (16 * D_ * 2)) ^ ((U & 1) << 7), 0, 0);
#endif
}
__device__ __forceinline__ void dn_phase(const GemmArgs& a, const Late& late, int pol) {
#if defined(__HIP_DEVICE_COMPILE__)
  extern __shared__ __attribute__((aligned(16))) char smem[];
  STAMP(0);
  const unsigned lds0 = (unsigned)(size_t)(lds_char_ptr)smem;
  const int lane = lane_id();
  const int wave = __builtin_amdgcn_readfirstlane(threadIdx.x >> 6);
  const int bid = blockIdx.x, xcd = bid & 7, idx = bid >> 3;
  const int panel = (xcd >> 2) * 4 + (idx >> 3), c = idx & 7, q = xcd & 3;
  const auto rW = RSRC(a.W0);
  const unsigned wsrc = dn_wsrc(lane, wave);
  int wb = dn_wbase(wave) ^ ((wave >> 2) << 7), wb2 = (wave * 56 + 48) * (D_ * 2) + c * 256 + ((q >> 1) << 7), xb = q * 8 * 3072;
  asm volatile("" : "+s"(wb), "+s"(wb2), "+s"(xb));
  auto ldW = [&](int v) {
    __builtin_amdgcn_raw_ptr_buffer_load_lds(rW, LDSP(lds0 + dn_wpos(v, 0) + wave * 1024), 16, wsrc,
                                             v < 24 ? wb + ((c + dn_uj(v)) & 7) * (112 * D_ * 2) + dn_us(v) * (16 * D_ * 2) : wb2 + ((v + c) & 7) * (448 * D_ * 2), 0, 0);
  };
  const int fa_off = F_ACT(panel) + (wave >> 1) * 512;
  const Tag tg = {(unsigned)__builtin_amdgcn_readfirstlane(late.tag[0]), (unsigned)__builtin_amdgcn_readfirstlane(late.tag[1])};
  const int fr = lane & 15, fq = lane >> 4, wq = (lane >> 2) & 3, wp = lane & 3;
  const int rb = wave * 2 + (q & 1);
  f32x4 acc[8][2];
#pragma unroll
  for (int n = 0; n < 8; ++n) { acc[n][0] = (f32x4){0.f, 0.f, 0.f, 0.f}; acc[n][1] = acc[n][0]; }
  unsigned wa[3][4];
#pragma unroll
  for (int i = 0; i < 4; ++i) { const int row = 8 * fq + wq; wa[0][i] = lds0 + row * 128 + (((i << 1) ^ wswz(row)) << 4) + wp * 8; wa[1][i] = wa[0][i] + 65536; wa[2][i] = wa[0][i] + 131072; }
  const auto rX = RSRC(a.X);
  const unsigned xs[2] = {(unsigned)((panel * 16 + rb) * SA + lane * 16), (unsigned)((panel * 16 + (rb ^ 1)) * SA + lane * 16)};
  FragD fa, fb;
  u32x4 xr[32][2];
  auto rdw = [&](FragD& f, auto vc, auto rc) {
    constexpr int v = decltype(vc)::value, r = decltype(rc)::value, n = r >> 1, u = dn_unit(v, n);
    constexpr int p = dn_wpos(u, (v < DN_A || v >= BB) ? n >> 2 : (v < DN_B ? 1 : 0)), o = (p & 65535) + (r & 1) * 512, b = p >> 16;
    if constexpr (r & 1) f.hi[n] = ds_tr<o>(wa[b][n & 3]); else f.lo[n] = ds_tr<o>(wa[b][n & 3]);
  };
  auto ldX = [&](int v, int m) {
    if (v < 24) xr[v][m] = __builtin_amdgcn_raw_buffer_load_b128(rX, xs[m] + dn_us(v) * 1024, xb + ((c + dn_uj(v)) & 7) * 3072, 16);
    else xr[v][m] = __builtin_amdgcn_raw_buffer_load_b128(rX, xs[0] + m * 1024, 384 * 256 + ((v + c) & 7) * 2048, 16);
  };
  const auto rS = RSRC(a.slab);
  const int ck = (fq & 1) * 2 + (fq >> 1);
  u32x4 pp[3][2], res[2];
  bf16x8 sp0, sp1;
  int sl0 = 0, fp_off = 0; unsigned v1 = 0, v3 = 0; u32x2 tv = late.tag;
  auto addP = [&](int j) { acc[2 * (j & 1)][0] = MFMA(sp0, __builtin_bit_cast(bf16x8, pp[j >> 1][j & 1]), acc[2 * (j & 1)][0]); acc[2 * (j & 1) + 1][0] = MFMA(sp1, __builtin_bit_cast(bf16x8, pp[j >> 1][j & 1]), acc[2 * (j & 1) + 1][0]); };
  auto ldP = [&](int j, bool ok) { pp[j >> 1][j & 1] = __builtin_amdgcn_raw_buffer_load_b128(RSRCN(a.slab, ok ? 0x7fffffff : 0), lane * 16 + (j & 1) * 1024, sl0 + (j >> 1) * (4 << 20), 16); };
  auto st128 = [&](u32x4 v, auto rs, unsigned vo, int so) {
    __builtin_amdgcn_raw_buffer_store_b128(v, rs, vo, so, 16);
    __builtin_amdgcn_sched_barrier(0);
    asm volatile("s_nop 1" ::: "memory");
    __builtin_amdgcn_sched_barrier(0); };
  auto stP = [&](int j) {
    const int k = 3 - (j >> 1), i = j & 1;
    const f32x4 s0 = acc[(k >> 1) * 4 + 2 * i][k & 1], s1 = acc[(k >> 1) * 4 + 2 * i + 1][k & 1];
    st128((u32x4){pk_bf16(s0[0], s0[1]), pk_bf16(s0[2], s0[3]), pk_bf16(s1[0], s1[1]), pk_bf16(s1[2], s1[3])}, rS, lane * 16 + i * 1024, (sl0 ^ (k << 15)) + (j >> 1) * (4 << 20)); };
  unsigned got = 0;
  unsigned orow = 0;
  u32x2 fl = {0u, 0u};
#define TS5(i)
  auto op = [&](auto ic) {
    constexpr int i = decltype(ic)::value, kind = DN_OPS[i][2], arg = DN_OPS[i][3];
    if constexpr (kind == O_X) ldX(arg >> 1, arg & 1);
    if constexpr (kind == O_W) ldW(arg);
    if constexpr (kind == O_SP) stP(arg);
    if constexpr (kind == O_FA) { wait_vm<DT.wait[i]>(); flag_set(a.sync, fa_off + ((q * 8 + c) * 2 + (wave & 1)) * 8 + (arg - 1) * 16384, tg, 0); }
    if constexpr (kind == O_FP) { wait_vm<DT.wait[i]>(); if constexpr (arg) STAMP(10 + arg); __builtin_amdgcn_raw_buffer_store_b64(tv, RSRC(a.sync), v1 + arg * 8, fp_off ^ ((3 - arg) << 9), 16); }
    if constexpr (kind == O_QC) fl = flag_get(a.sync, fa_off + q * 128 + (arg - 1) * 16384, 16);
    if constexpr (kind == O_QD) fl = flag_get(a.sync, fa_off + 16384, 64);
    if constexpr (kind == O_QP) fl = __builtin_amdgcn_raw_buffer_load_b64(RSRC(a.sync), v3, fp_off, 16);
    if constexpr (kind == O_RES) res[arg] = __builtin_amdgcn_raw_buffer_load_b128(RSRC(a.R), lane * 16 + arg * 1024, rb_h(panel * 16 + rb, pol) + ko(c * 4096 + (q >> 1) * 2048), 16);
    if constexpr (kind == O_AR) {
      const bf16x8 s0 = pick(fq == (fr >> 3), fr & 7), s1 = pick(fq == 2 + (fr >> 3), fr & 7);
#pragma unroll
      for (int n = 0; n < 4; ++n) acc[n][0] = MFMA((n & 1) ? s1 : s0, __builtin_bit_cast(bf16x8, res[n >> 1]), acc[n][0]);
      sp0 = pick(fq == (fr >> 2), fr & 3); sp1 = pick(fq == (fr >> 2), 4 + (fr & 3));
      orow = (unsigned)(((panel * 16 + rb) * 16 + fr) * (D_ * 2) + c * 256 + (q >> 1) * 128 + ck * 16);
      sl0 = sl_at(bid, wave, pol); fp_off = F_PART(bid) + wave * 64; v1 = lane == 0 ? 0u : 0x80000000u; v3 = lane < 3 ? lane * 8u : 0x80000000u;
      asm volatile("" : "+v"(sp0), "+v"(sp1), "+v"(orow), "+s"(sl0), "+s"(fp_off), "+v"(v1), "+v"(v3), "+v"(tv));
    }
    if constexpr (kind == O_AP) addP(arg);
    if constexpr (kind == O_LP) ldP(arg, (got >> (arg >> 1)) & 1);
    if constexpr (kind == O_CC && arg == 1) {
      wait_vm<DT.wait[i]>();
      f32x2 f2 = ds_b64<0>(lds0 + FLP + wave * 128 + (lane & 15) * 8);
      asm volatile("s_waitcnt lgkmcnt(0)" : "+v"(f2) :: "memory");
      fl = __builtin_bit_cast(u32x2, f2);
    }
    if constexpr (kind == O_CC) { if (__builtin_expect(!flags_up(fl, 16, tg, 0), 0)) wait_flags(lds0, a.sync, fa_off + q * 128 + (arg - 1) * 16384, 16, 0); if constexpr (arg == 1) STAMP(4); }
    if constexpr (kind == O_CD) { if (__builtin_expect(!flags_up(fl, 64, tg, 0), 0)) wait_flags(lds0, a.sync, fa_off + 16384, 64, 0); }
    if constexpr (kind == O_CP) got |= (unsigned)(__builtin_amdgcn_ballot_w64(fl[0] == tg.lo) & __builtin_amdgcn_ballot_w64(fl[1] == tg.hi)) & (1u << arg) & (got + 1u);
    if constexpr (kind == O_BAR) { wait_vm<DT.wait[i]>(); lds_barrier(); }
    __builtin_amdgcn_sched_barrier(0);
  };
  STAMP(9);
  lds_barrier();
  STAMP(6);
  {
    const unsigned e0 = lds0 + rb * 1024 + lane * 16, e1 = lds0 + (rb ^ 1) * 1024 + lane * 16;
    xr[0][0] = __builtin_bit_cast(u32x4, ds_b128<OWNE>(e0)); xr[0][1] = __builtin_bit_cast(u32x4, ds_b128<OWNE>(e1));
    xr[1][0] = __builtin_bit_cast(u32x4, ds_b128<OWNE + 16384>(e0)); xr[1][1] = __builtin_bit_cast(u32x4, ds_b128<OWNE + 16384>(e1));
    xr[2][0] = __builtin_bit_cast(u32x4, ds_b128<0>(e0 + OWNM)); xr[2][1] = __builtin_bit_cast(u32x4, ds_b128<0>(e1 + OWNM));
    static_for<0, 16>([&](auto rc) { rdw(fa, IC<0>{}, rc); });
  }
  static_for<0, NS>([&](auto vc) {
    constexpr int v = decltype(vc)::value, NM = dn_nm(v);
    FragD& cur = (v & 1) ? fb : fa; FragD& nxt = (v & 1) ? fa : fb;
    TS5(v);
    __builtin_amdgcn_s_setprio(3 - (v & 3));
    static_for<DT.first[v][0], DT.first[v][1]>(op);
    __builtin_amdgcn_sched_barrier(0);
    static_for<0, NM>([&](auto ic) {
      constexpr int I = decltype(ic)::value, n = NM == 16 ? I >> 1 : I, m = NM == 16 ? I & 1 : (v < 24 ? 1 : (v < BB ? 0 : I >> 2));
      if constexpr (I % (NM / 4) == 0) {
        constexpr int left = 12 - 2 * n + DT.rd[v][I];
        asm volatile("s_waitcnt lgkmcnt(%0)" :: "n"(left < 15 ? left : 15) : "memory");
        asm volatile("" : "+v"(cur.lo[n]), "+v"(cur.hi[n]), "+v"(cur.lo[n + 1]), "+v"(cur.hi[n + 1]));
        if constexpr (v == 0 && I == 0) {
#pragma unroll
          for (int i = 0; i < 3; ++i) asm volatile("" : "+v"(xr[i][0]), "+v"(xr[i][1]));
          STAMP(1);
        }
        __builtin_amdgcn_sched_barrier(0);
      }
      const bf16x8 w = __builtin_shufflevector(cur.lo[n], cur.hi[n], 0, 1, 2, 3, 4, 5, 6, 7);
      constexpr int an = v < DN_A ? n : (v < DN_B ? 4 : 0) + (n & 3), am = v < 24 ? m : 0;
      acc[an][am] = MFMA(w, __builtin_bit_cast(bf16x8, xr[dn_unit(v, n)][m]), acc[an][am]);
      __builtin_amdgcn_sched_barrier(0);
      static_for<DT.first[v][I + 1], DT.first[v][I + 2]>(op);
      static_for<DT.rd[v][I], DT.rd[v][I + 1]>([&](auto rc) { rdw(nxt, IC<v + 1>{}, rc); });
      __builtin_amdgcn_sched_barrier(0);
    });
    if constexpr (v == 23) STAMP(10);
  });
  STAMP(2);
  TS5(NS);
  const auto rF = RSRCN(a.sync, got != 7u ? 0x7fffffff : 0);
  u32x2 f0 = __builtin_amdgcn_raw_buffer_load_b64(rF, v3, fp_off, 16);
  __builtin_amdgcn_sched_barrier(0);
  static_for<DT.first[NS][0], DN_N>(op);
  if (got != 7u) {
    if (!flags_up(f0, 3, tg, 0)) {
      f0 = __builtin_amdgcn_raw_buffer_load_b64(rF, v3, fp_off, 16);
      asm volatile("s_sleep 5" ::: "memory");
      for (unsigned polls = 1;; ++polls) {
        const u32x2 f1 = __builtin_amdgcn_raw_buffer_load_b64(rF, v3, fp_off, 16);
        asm volatile("" ::: "memory");
        if (flags_up(f0, 3, tg, 0)) break;
        f0 = __builtin_amdgcn_raw_buffer_load_b64(rF, v3, fp_off, 16);
        asm volatile("" ::: "memory");
        if (flags_up(f1, 3, tg, 0) || (!(polls & 255u) && !in_time(lds0))) break;
      }
    }
#pragma unroll
    for (int j = 5; j >= 0; --j) ldP(j, !((got >> (j >> 1)) & 1));
    __builtin_amdgcn_sched_barrier(0);
#pragma unroll
    for (int j = 0; j < 6; ++j) addP(j);
  }
  TS5(NS + 1);
  STAMP(5);
#pragma unroll
  for (int i = 0; i < 2; ++i) {
    const f32x4 u = acc[2 * i][0], w = acc[2 * i + 1][0];
    const auto s0 = __builtin_amdgcn_permlane16_swap(pk_bf16(u[0], u[1]), pk_bf16(w[0], w[1]), false, false);
    const auto s1 = __builtin_amdgcn_permlane16_swap(pk_bf16(u[2], u[3]), pk_bf16(w[2], w[3]), false, false);
    __builtin_amdgcn_raw_buffer_store_b128((u32x4){s0[0], s1[0], s0[1], s1[1]}, RSRC(a.O), orow + i * 64, 0, 0);
  }
  if (bid == 0 && wave == 0) flag_set(a.sync, EPOCH, tg, 0);
  STAMP(7);
  STAMP(8);
  STAMP(3);
#endif
}

struct Ptrs { const bf16_t *x, *wqkv, *wo, *wg, *wu, *wd; bf16_t* out; char* ws; };
#define DBG(i)
#define WS_O ((bf16_t*)(p.ws + (8u << 20)))
#define WS_SL ((float*)(p.ws + (48u << 20)))
#define WS_H ((bf16_t*)(p.ws + (16u << 20)))
#define WS_ST ((float*)(p.ws + (24u << 20)))
#define WS_CS ((float*)(p.ws + (25u << 20)))
#define WS_SY ((unsigned*)(p.ws + (26u << 20)))
#define WS_A ((bf16_t*)(p.ws + (28u << 20)))
#if defined(__HIP_DEVICE_COMPILE__)
#define ARG_WARM(v) do { asm volatile("s_load_dword %0, %1, 0x0" : "=s"(v) : "s"(__builtin_amdgcn_kernarg_segment_ptr())); __builtin_amdgcn_sched_barrier(0); } while (0)
#else
#define ARG_WARM(v)
#endif
struct Args4 { GemmArgs q, b, c, d; };
extern "C" __global__ __launch_bounds__(512, 1) void k_all(Ptrs p) {
  unsigned warm = 0;
  ARG_WARM(warm);
  p.ws = (char*)(((size_t)p.ws + 0x1fffff) & ~(size_t)0x1fffff);
#define PAD(k)
  const Args4 a{GemmArgs{p.x, p.wqkv, nullptr, nullptr, WS_O, (float*)WS_H, p.wg, p.wu, WS_CS, p.wd, p.wo, WS_SY, (float*)WS_A, warm DBG(0)},
    GemmArgs{WS_O, p.wo, nullptr, p.x, WS_H, WS_ST, nullptr, nullptr, nullptr, p.wd, nullptr, WS_SY, nullptr, 0u DBG(1)},
    GemmArgs{WS_H, p.wg, p.wu, p.out, WS_A, WS_ST, nullptr, nullptr, WS_CS, p.wd, nullptr, WS_SY, WS_SL, 0u DBG(2)},
    GemmArgs{WS_A, p.wd, nullptr, WS_H, p.out, nullptr, nullptr, nullptr, nullptr, nullptr, nullptr, WS_SY, WS_SL, 0u DBG(3)}};
  int pol = 0;
  k1_phase(a.q, [&](auto md, auto lo, auto hi) { wo_phase<decltype(md)::value, decltype(lo)::value, decltype(hi)::value>(a.b, a.c); }, pol);
  PAD(1);
  wo_phase<2, 0, 0>(a.b, a.c, pol);
  PAD(2);
  Late late;
  glu_phase(a.c, [&](auto tc, unsigned w, int wb) { dn_first_w<decltype(tc)::value>(a.d, w, wb); }, late, pol);
  PAD(3);
  dn_phase(a.d, late, pol);
  PAD(4);
}

extern "C" __global__ void k_zend() {}

extern "C" void kernel_launch(void* const* d_in, const int* in_sizes, int n_in, void* d_out, int out_size, void* d_ws,
                              size_t ws_size, hipStream_t stream) {
  const Ptrs p{(const bf16_t*)d_in[0], (const bf16_t*)d_in[1], (const bf16_t*)d_in[2], (const bf16_t*)d_in[4], (const bf16_t*)d_in[5], (const bf16_t*)d_in[6], (bf16_t*)d_out, (char*)d_ws};
  constexpr int L_all = SNAP + 64;
  static_assert(SNAP >= 3 * 256 * 128 + 4 * 14592 + 4096 + 1024 + 256 + 512 + 128 + 64 && L_all <= 163840, "LDS");
  (void)hipFuncSetAttribute((const void*)k_all, hipFuncAttributeMaxDynamicSharedMemorySize, L_all);
  k_all<<<256, 512, L_all, stream>>>(p);
}
```

```cpp
#include <hip/hip_runtime.h>
#include <hip/hip_bf16.h>
#include <stdint.h>
#include <type_traits>

typedef unsigned short bf16_t;
typedef short bf16x8 __attribute__((ext_vector_type(8)));
typedef short s16x4 __attribute__((ext_vector_type(4)));
typedef float f32x4 __attribute__((ext_vector_type(4)));
typedef float f32x2 __attribute__((ext_vector_type(2)));
typedef unsigned u32x2 __attribute__((ext_vector_type(2)));
typedef unsigned u32x4 __attribute__((ext_vector_type(4)));
typedef __bf16 bf16x2_t __attribute__((ext_vector_type(2)));
typedef __attribute__((address_space(3))) void* lds_void_ptr;
typedef __attribute__((address_space(3))) char* lds_char_ptr;

constexpr int S_ = 2048, D_ = 1024, DF_ = 3584;
constexpr int SH = 65536, SA = DF_ * 32;
__device__ __forceinline__ int rb_h(int rb, int pol) { return (rb & 63) * SH + ((((rb >> 6) ^ (rb >> 5) ^ pol) & 1) << 13); }
__device__ __forceinline__ int ko(int off) { return off + (off & ~8191); }
__device__ __forceinline__ int sl_at(int bid, int wave, int pol) { return ((bid >> 3) * 8 + (bid & 3) * 2 + (wave >> 2)) * 16384 + ((((bid >> 2) ^ (bid >> 7) ^ pol) & 1) << 13) + (wave & 3) * 2048; }

__device__ __forceinline__ float bflo(unsigned v) { return __uint_as_float(v << 16); }
__device__ __forceinline__ float bfhi(unsigned v) { return __uint_as_float(v & 0xffff0000u); }
__device__ __forceinline__ unsigned pk_bf16(float lo, float hi) {
  f32x2 v = {lo, hi};
  bf16x2_t b = __builtin_convertvector(v, bf16x2_t);
  return __builtin_bit_cast(unsigned, b);
}
__device__ __forceinline__ int wswz(int row) { return (((row >> 1) & 1) | (((row >> 3) & 1) << 1)) << 1; }

template <int OFF> __device__ __forceinline__ s16x4 ds_tr(unsigned addr) {
  s16x4 r; asm volatile("ds_read_b64_tr_b16 %0, %1 offset:%2" : "=v"(r) : "v"(addr), "n"(OFF)); return r;
}
template <int OFF> __device__ __forceinline__ bf16x8 ds_b128(unsigned addr) {
  bf16x8 r; asm volatile("ds_read_b128 %0, %1 offset:%2" : "=v"(r) : "v"(addr), "n"(OFF)); return r;
}
template <int OFF> __device__ __forceinline__ f32x2 ds_b64(unsigned addr) {
  f32x2 r; asm volatile("ds_read_b64 %0, %1 offset:%2" : "=v"(r) : "v"(addr), "n"(OFF)); return r;
}
__device__ __forceinline__ float sum8(float v) {
  v += __int_as_float(__builtin_amdgcn_update_dpp(0, __float_as_int(v), 0x128, 0xf, 0xf, false));
  auto a = __builtin_amdgcn_permlane16_swap(__float_as_uint(v), __float_as_uint(v), false, false);
  v = __uint_as_float(a[0]) + __uint_as_float(a[1]);
  auto b = __builtin_amdgcn_permlane32_swap(__float_as_uint(v), __float_as_uint(v), false, false);
  return __uint_as_float(b[0]) + __uint_as_float(b[1]);
}
__device__ __forceinline__ float sum8l(float v) {
  v += __int_as_float(__builtin_amdgcn_update_dpp(0, __float_as_int(v), 0xb1, 0xf, 0xf, false));
  v += __int_as_float(__builtin_amdgcn_update_dpp(0, __float_as_int(v), 0x4e, 0xf, 0xf, false));
  return v + __int_as_float(__builtin_amdgcn_update_dpp(0, __float_as_int(v), 0x141, 0xf, 0xf, false));
}
template <int OFF> __device__ __forceinline__ void ds_w128(unsigned addr, u32x4 v) { asm volatile("ds_write_b128 %0, %1 offset:%2" :: "v"(addr), "v"(v), "n"(OFF) : "memory"); }
__device__ __forceinline__ int ds_r32(unsigned addr) { int r; asm volatile("ds_read_b32 %0, %1" : "=v"(r) : "v"(addr)); return r; }
__device__ __forceinline__ void ds_w32(unsigned addr, int v) { asm volatile("ds_write_b32 %0, %1" :: "v"(addr), "v"(v) : "memory"); }
template <int N> __device__ __forceinline__ void wait_vm() { asm volatile("s_waitcnt vmcnt(%0)" ::"n"(N) : "memory"); }
__device__ __forceinline__ void raw_barrier() { asm volatile("" ::: "memory"); __builtin_amdgcn_s_barrier(); asm volatile("" ::: "memory"); }
__device__ __forceinline__ void lds_barrier() { asm volatile("s_waitcnt lgkmcnt(0)" ::: "memory"); __builtin_amdgcn_s_barrier(); asm volatile("" ::: "memory"); }

template <int I, int N, class F> __device__ __forceinline__ void static_for(F&& f) {
  if constexpr (I < N) { f(std::integral_constant<int, I>{}); static_for<I + 1, N>(f); }
}
template <int V> using IC = std::integral_constant<int, V>;
constexpr int first_at(int g, int r0, int num, int den, int n) { int i = 0; while (i < n && r0 + i * num / den < g) ++i; return i; }
#define RSRC(p) __builtin_amdgcn_make_buffer_rsrc((void*)(p), 0, 0x7fffffff, 0x00020000)
#define RSRCN(p, n) __builtin_amdgcn_make_buffer_rsrc((void*)(p), 0, (n), 0x00020000)
#define LDSP(a) ((lds_void_ptr)(size_t)(a))
#define MFMA(a, b, c) __builtin_amdgcn_mfma_f32_16x16x32_bf16(a, b, c, 0, 0, 0)
#define CODE_ADDR(v, sym) asm volatile("s_getpc_b64 s[40:41]\n\ts_add_u32 s40, s40, " sym "@rel32@lo+4\n\ts_addc_u32 s41, s41, " sym "@rel32@hi+12\n\ts_mov_b64 %0, s[40:41]" : "=s"(v) :: "s40", "s41", "scc")
#define MERGED 1
constexpr int SNAP = 162752;
constexpr int STAB1 = 786432;
constexpr int EPOCH = 0, F_POL = 128, PROBE = 49152;
#define F_SIDE(g) ((((g) ^ 1 ^ pol) & 1) << 13)
#define F_PAN(p) (((p) & 3) * 256 + F_SIDE((p) >> 2))
constexpr int F_MARK = 2048, F_H = 4096, F_ROW = 6144, F_COL = 16384;
#define F_O(t) (32768 + ((t) & 7) * 1024 + F_SIDE((t) >> 3))
#define WO_ROT (((xcd & 3) * 4) ^ (idx & 3))

#define F_ACT(p) (65536 + ((p) & 3) * 2048 + F_SIDE((p) >> 2))
#define F_PART(b) (131072 + ((b) >> 5) * 16384 + ((((b) >> 3) & 3) * 4 + ((b) & 3)) * 512 + F_SIDE((b) >> 2))
constexpr int TRIES = 6;
struct Tag { unsigned lo, hi; };
__device__ __forceinline__ Tag tag_get(unsigned lds0) {
  f32x2 v = ds_b64<0>(lds0 + SNAP);
  asm volatile("s_waitcnt lgkmcnt(0)" : "+v"(v) :: "memory");
  return Tag{(unsigned)__builtin_amdgcn_readfirstlane(__float_as_int(v[0])) + 8u, (unsigned)__builtin_amdgcn_readfirstlane(__float_as_int(v[1]))};
}
__device__ __forceinline__ bool in_time(unsigned lds0) {
  int v = ds_r32(lds0 + SNAP + 28);
  asm volatile("s_waitcnt lgkmcnt(0)" : "+v"(v) :: "memory");
  return (unsigned)__builtin_amdgcn_s_memrealtime() - (unsigned)__builtin_amdgcn_readfirstlane(v) < 200000000u;
}
__device__ __forceinline__ int lane_id() { int l; asm volatile("v_mbcnt_lo_u32_b32 %0, -1, 0\n\tv_mbcnt_hi_u32_b32 %0, -1, %0" : "=v"(l)); return l; }
#if defined(__HIP_DEVICE_COMPILE__)
__device__ __forceinline__ void flag_set(const unsigned* sync, int off, Tag t, unsigned state) {
  __builtin_amdgcn_raw_buffer_store_b64((u32x2){t.lo + state, t.hi}, RSRCN((const char*)sync + off, 8), lane_id() * 8u, 0, 16);
}
__device__ __forceinline__ u32x2 flag_get(const unsigned* sync, int off, int n) { return __builtin_amdgcn_raw_buffer_load_b64(RSRCN((const char*)sync + off, n * 8), lane_id() * 8u, 0, 16); }
__device__ __forceinline__ bool flags_up(u32x2 v, int n, Tag t, unsigned state) { return ((__builtin_amdgcn_ballot_w64(v[0] - t.lo - state > 7u - state) | __builtin_amdgcn_ballot_w64(v[1] != t.hi)) & (~0ull >> (64 - n))) == 0; }
__device__ __forceinline__ bool words_up(unsigned v, Tag t) { return ((__builtin_amdgcn_ballot_w64(v != t.lo) & 0x5555555555555555ull) | (__builtin_amdgcn_ballot_w64(v != t.hi) & 0xaaaaaaaaaaaaaaaaull)) == 0; }
__device__ __forceinline__ void wait_flags(unsigned lds0, const unsigned* sync, int off, int n, unsigned state) {
  const Tag t = tag_get(lds0);
  while (!flags_up(flag_get(sync, off, n), n, t, state) && in_time(lds0)) __builtin_amdgcn_s_sleep(8);
}
#endif
template <int N> __device__ __forceinline__ bool marked(unsigned addr) {
  bf16x8 v[N];
  static_for<0, N>([&](auto ic) { constexpr int i = decltype(ic)::value; v[i] = ds_b128<i * 4096>(addr); });
  asm volatile("s_waitcnt lgkmcnt(0)" ::: "memory");
  unsigned m = 0;
#pragma unroll
  for (int i = 0; i < N; ++i) {
    asm volatile("" : "+v"(v[i]));
    const u32x4 u = __builtin_bit_cast(u32x4, v[i]);
    m = max(max(m, u[0]), u[1]); m = max(max(m, u[2]), u[3]);
  }
  return __builtin_amdgcn_ballot_w64(m == 0xffffffffu) != 0;
}

struct GemmArgs {
  const bf16_t* X;
  const bf16_t* W0;
  const bf16_t* W1;
  const bf16_t* R;
  bf16_t* O;
  float* st;
  const bf16_t* CW0;
  const bf16_t* CW1;
  float* cs;
  const bf16_t* PF0;
  const bf16_t* PF1;
  unsigned* sync;
  float* slab;
  unsigned warm;
};
#define STAMP(i)
#define STAMPW(i, w)
#define TS6(i)
#define TS4(i)
__device__ __forceinline__ bf16x8 pick(bool on, int e) {
  const unsigned v = on ? ((e & 1) ? 0x3f800000u : 0x3f80u) : 0u; const int d = e >> 1;
  return __builtin_bit_cast(bf16x8, (u32x4){d == 0 ? v : 0u, d == 1 ? v : 0u, d == 2 ? v : 0u, d == 3 ? v : 0u});
}
template <int MF, int NF> struct Frags { bf16x8 x[MF]; s16x4 lo[NF], hi[NF]; };

__device__ __forceinline__ unsigned dn_wsrc(int lane, int wave) { return (unsigned)((lane >> 3) * (D_ * 2) + (((lane & 7) ^ wswz((wave & 3) * 8 + (lane >> 3))) << 4)); }
__device__ __forceinline__ int dn_wbase(int wave) { const int b = blockIdx.x; return ((b & 3) * 896 + (wave & 1) * 8 + ((wave >> 1) & 1) * 56) * (D_ * 2) + ((b >> 3) & 7) * 256 + ((b & 2) << 6); }
constexpr int FLP = 155648;
constexpr int OWNE = 0, OWNM = 98304;
constexpr int WO_A = 6, WO_E = 10;
constexpr int wo_pos(int s) { return s < 3 ? 18432 + s * 16384 : (s == 3 ? 129024 : (s == 4 ? 0 : 71424 + (s - 5) * 16384)); }
template <class HK>
__device__ __forceinline__ void k1_phase(const GemmArgs& a, HK hook, int& pol) {
#if defined(__HIP_DEVICE_COMPILE__)
  constexpr int K = D_, LDW = 3 * D_, WN = 4;
  constexpr int HR = 16, STRIDE = 128;
  constexpr int XT = 144 * 128, WU = 24576;
  constexpr int XBASE = 0, WBASE = 3 * XT, RING = WBASE + 4 * WU;
  constexpr int MF = 4, NF = 3, MFX = 5;
  constexpr int NT = 16, NXI = 18, NLW = 4, XI = 5, XIMIN = 4, WI = 6;
  extern __shared__ __attribute__((aligned(16))) char smem[];
  STAMP(0);
  const unsigned lds0 = (unsigned)(size_t)(lds_char_ptr)smem;
  const int tid = threadIdx.x, lane = tid & 63;
  const int wave = __builtin_amdgcn_readfirstlane(tid >> 6);
  const int wm = wave / WN, wn = wave % WN;
  const int bid = blockIdx.x, xcd = bid & 7, idx = bid >> 3;
  const int tm = (xcd / 4) * 8 + idx / 4, tn = (xcd % 4) * 4 + idx % 4;
  const int m0 = tm * 128;
  const int fr = lane & 15, fq = lane >> 4;
  const int wq = (lane >> 2) & 3, wp = lane & 3;
  constexpr int TCH = RING + 2304;
  auto touch = [&](const void* base, unsigned voff, unsigned soff, unsigned lim = 0x7fffffffu) {
    __builtin_amdgcn_raw_ptr_buffer_load_lds(RSRCN(base, lim), LDSP(lds0 + TCH), 4, voff, soff, 0, 0);
  };
  const auto rO = RSRC(a.O);

  unsigned xsrc[XI], wsrc[WI];
  {
    const int w4 = wave & 3, lr = lane >> 3;
    const unsigned xv = (unsigned)(lr * (K * 2) + (((lane & 7) ^ ((w4 * 4 + (lr >> 1)) & 7)) << 4));
    const int r0 = m0 - HR + w4 * 8;
#pragma unroll
    for (int i = 0; i < XI; ++i) xsrc[i] = xv + (unsigned)((i == 0 ? (r0 < 0 ? 0 : r0) : r0 + 32 * (i < 4 ? i : 4)) * (K * 2));
    const unsigned wv = (unsigned)((w4 * 8 + lr) * (LDW * 2) + tn * 128 + (((lane & 7) ^ wswz(w4 * 8 + lr)) << 4));
#pragma unroll
    for (int i = 0; i < WI; ++i) wsrc[i] = wv + (unsigned)((i & 1) * 32 * (LDW * 2) + (i >> 1) * 2048);
  }
  auto ldX = [&](int i, int tile, int slot) {
    const int j = wave + NLW * i;
    if (j >= NXI) return;
    __builtin_amdgcn_raw_ptr_buffer_load_lds(RSRC(a.X), LDSP(lds0 + XBASE + slot * XT + j * 1024), 16, xsrc[i], tile * 128, 0, 0);
  };
  auto ldW = [&](int i, int unit, int slot) {
    const int j = wave + NLW * i;
    __builtin_amdgcn_raw_ptr_buffer_load_lds(RSRC(a.W0), LDSP(lds0 + WBASE + slot * WU + j * 1024), 16, wsrc[i], unit * 64 * LDW * 2, 0, 0);
  };
  auto issueX = [&](int tile, int slot) {
    if (wave >= NLW) return;
#pragma unroll
    for (int i = 0; i < XI; ++i) ldX(i, tile, slot);
  };
  auto issueW = [&](int unit, int slot) {
    if (wave >= NLW) return;
#pragma unroll
    for (int i = 0; i < WI; ++i) ldW(i, unit, slot);
  };

  f32x4 acc[NF][MF];
#pragma unroll
  for (int n = 0; n < NF; ++n)
#pragma unroll
    for (int m = 0; m < MF; ++m) acc[n][m] = (f32x4){0.f, 0.f, 0.f, 0.f};
  f32x4 acch[NF];
#pragma unroll
  for (int n = 0; n < NF; ++n) acch[n] = (f32x4){0.f, 0.f, 0.f, 0.f};
  constexpr int ONES = RING;

  unsigned xa0, xa1, xh0, xh1;
  { const unsigned o = fr * 128 + ((fq ^ ((fr >> 1) & 7)) << 4), mo = (HR + wm * MF * 16) * 128;
    xa0 = lds0 + XBASE + mo + o; xa1 = lds0 + XBASE + mo + (o ^ 64); xh0 = lds0 + XBASE + o; xh1 = lds0 + XBASE + (o ^ 64); }
  unsigned wa;
  { const int row = 8 * fq + wq; wa = lds0 + WBASE + row * STRIDE + (((wn << 1) ^ wswz(row)) << 4) + wp * 8; }

  using I0 = IC<0>; using I1 = IC<1>;
  using T = std::true_type; using F = std::false_type;
  typedef Frags<MFX, NF> FR;
  struct RdCtx { unsigned xa, xh, wb; };
  auto mkctx = [&](bool ks1, unsigned xoffs, unsigned woffs) {
    RdCtx c; c.xa = (ks1 ? xa1 : xa0) + xoffs; c.xh = fr == 0 ? lds0 + ONES : (ks1 ? xh1 : xh0) + xoffs; c.wb = wa + woffs;
    return c;
  };
  auto rd_item = [&](FR& f, const RdCtx& c, auto KSc, auto kc) {
    constexpr int KS = decltype(KSc)::value, k = decltype(kc)::value;
    if constexpr (k < MF) f.x[k] = ds_b128<k * 2048>(c.xa);
    else if constexpr (k < MFX) f.x[k] = ds_b128<0>(c.xh);
    else {
      constexpr int n = k - MFX, o = n * 8192 + KS * 32 * STRIDE;
      f.lo[n] = ds_tr<o>(c.wb); f.hi[n] = ds_tr<o + 4 * STRIDE>(c.wb);
    }
  };
  auto wait_frags = [&](FR& f, auto Hc) {
    asm volatile("s_waitcnt lgkmcnt(0)" ::: "memory");
#pragma unroll
    for (int m = 0; m < MF + decltype(Hc)::value; ++m) asm volatile("" : "+v"(f.x[m]));
#pragma unroll
    for (int n = 0; n < NF; ++n) asm volatile("" : "+v"(f.lo[n]), "+v"(f.hi[n]));
  };
  auto step1 = [&](auto Hc, FR& cur, FR& nxt, const RdCtx& c, auto KSc, auto R0c, auto RLc, auto IBc, auto&& sync, auto NLc, auto&& ld) {
    constexpr int H = decltype(Hc)::value, NL = decltype(NLc)::value, R0 = decltype(R0c)::value;
    constexpr int MH = MF + H, NM = NF * MH, NR = decltype(RLc)::value == -100 ? 0 : MH + 2 * NF;
    constexpr int IB = decltype(IBc)::value < 0 && decltype(IBc)::value > -100 ? NM + decltype(IBc)::value : decltype(IBc)::value;
    constexpr int RL = NM + decltype(RLc)::value;
    static_for<0, NM>([&](auto ic) {
      constexpr int I = decltype(ic)::value, g = I / MH, q = I % MH;
      const bf16x8 w = __builtin_shufflevector(cur.lo[g], cur.hi[g], 0, 1, 2, 3, 4, 5, 6, 7);
      if constexpr (q < MF) acc[g][q] = MFMA(w, cur.x[q], acc[g][q]);
      else acch[g] = MFMA(w, cur.x[MF], acch[g]);
      __builtin_amdgcn_sched_barrier(0);
      if constexpr (I == IB) sync();
      static_for<first_at(I, R0, RL - R0, NR - 1, NR), first_at(I + 1, R0, RL - R0, NR - 1, NR)>([&](auto rc) {
        constexpr int r = decltype(rc)::value;
        if constexpr (r < MH) rd_item(nxt, c, KSc, rc);
        else {
          constexpr int n = (r - MH) >> 1, o = n * 8192 + decltype(KSc)::value * 32 * STRIDE;
          if constexpr ((r - MH) & 1) nxt.hi[n] = ds_tr<o + 4 * STRIDE>(c.wb); else nxt.lo[n] = ds_tr<o>(c.wb);
        }
      });
#pragma unroll
      for (int i = first_at(I, R0, NM - R0, NL, NL); i < first_at(I + 1, R0, NM - R0, NL, NL); ++i) ld(i);
      __builtin_amdgcn_sched_barrier(0);
    });
    if constexpr (NR > 0) wait_frags(nxt, Hc);
  };
  FR fa, fb;
  using IM1 = IC<-1>;
  unsigned tv6 = 0;

  asm volatile("s_waitcnt lgkmcnt(0)" :: "s"(a.warm), "v"(wsrc[WI - 1]), "v"(xsrc[XI - 1]));
  issueW(0, 0); issueX(0, 0);
  u32x4 sx[4];
  constexpr int TNEW = RING + 3072;
  auto ldT = [&](int dst) {
    int lx = lane; asm volatile("" : "+v"(lx));
    const auto rT = RSRCN((const char*)a.cs + STAB1, 2048 * 16);
    const unsigned to = (unsigned)((m0 - 16) * 16 + lx * 16);
    __builtin_amdgcn_raw_ptr_buffer_load_lds(rT, LDSP(lds0 + dst), 16, to, 0, 0, 16);
    __builtin_amdgcn_raw_ptr_buffer_load_lds(rT, LDSP(lds0 + dst + 1024), 16, to + 1024u, 0, 0, 16);
    if (lx < 16) __builtin_amdgcn_raw_ptr_buffer_load_lds(rT, LDSP(lds0 + dst + 2048), 16, to + 2048u, 0, 0, 16);
  };
  auto ldS = [&]() {
    int lx = lane; asm volatile("" : "+v"(lx));
    const unsigned so = (unsigned)((m0 + tn * 8 + (wave & 3) * 2 + (lx >> 5)) * (K * 2) + (lx & 31) * 16);
#pragma unroll
    for (int i = 0; i < 4; ++i) sx[i] = __builtin_amdgcn_raw_buffer_load_b128(RSRC(a.X), so, i * 512, 0);
  };
  float mu = 0.f, rsd = 0.f;
  auto stats = [&]() {
    float sm = 0.f, sq = 0.f;
    const bf16x2_t one2 = __builtin_bit_cast(bf16x2_t, 0x3f803f80u);
#pragma unroll
    for (int i = 0; i < 4; ++i)
#pragma unroll
      for (int e = 0; e < 4; ++e) { const unsigned u = sx[i][e]; const bf16x2_t v = __builtin_bit_cast(bf16x2_t, u); sm = __builtin_amdgcn_fdot2_f32_bf16(v, one2, sm, false); sq = __builtin_amdgcn_fdot2_f32_bf16(v, v, sq, false); }
    sm = sum8l(sm); sq = sum8l(sq);
    sm += __int_as_float(__builtin_amdgcn_update_dpp(0, __float_as_int(sm), 0x128, 0xf, 0xf, false));
    sq += __int_as_float(__builtin_amdgcn_update_dpp(0, __float_as_int(sq), 0x128, 0xf, 0xf, false));
    { auto p = __builtin_amdgcn_permlane16_swap(__float_as_uint(sm), __float_as_uint(sm), false, false); sm = __uint_as_float(p[0]) + __uint_as_float(p[1]); }
    { auto p = __builtin_amdgcn_permlane16_swap(__float_as_uint(sq), __float_as_uint(sq), false, false); sq = __uint_as_float(p[0]) + __uint_as_float(p[1]); }
    mu = sm * (1.f / 1024.f);
    float var = sq * (1.f / 1024.f) - mu * mu; var = var > 0.f ? var : 0.f;
    rsd = __builtin_amdgcn_rsqf(var + 1e-5f);
  };
  __builtin_amdgcn_sched_barrier(0);
  if (wave == 4) {
    if (lane < 8) *(unsigned*)(smem + ONES + lane * 4) = lane < 4 ? 0x3f803f80u : 0u;
  }
  auto pretouch = [&]() {
    touch(a.cs, lane < 8 ? (unsigned)((const char*)a.sync - (const char*)a.cs + bid * 1024 + lane * 128) : (lane == 8 ? (unsigned)(STAB1 + (m0 + tn * 8) * 16) : (lane < 23 ? (unsigned)(bid * 1792 + (lane - 9) * 128) : 0x80000000u)), 0);
  };
  if (wave == 5) {
    if (lane < 2) __builtin_amdgcn_raw_ptr_buffer_load_lds(RSRC(a.sync), LDSP(lds0 + SNAP), 4, lane * 4, EPOCH, 0, 16);
    ds_w32(lds0 + SNAP + 28, (int)__builtin_amdgcn_s_memrealtime());
  }
  constexpr int HQ = 5;
  const int slc = (3 - ((unsigned)(size_t)a.W0 >> 7) - (xcd & 3) * 4) & 7;
  const int hid = (idx >> 2) * 3 + (idx & 3) - ((idx & 3) > slc ? 1 : 0);
  const bool hlp = slc < 4 && (idx & 3) != slc;
  const int hj1 = hlp ? hid / 3 - HQ : -100, hj2 = hlp ? hid / 3 + 8 - HQ : -100;
  const unsigned htv = (unsigned)(lane * (LDW * 2) + (hid % 3) * 2048 + ((xcd & 3) * 4 + slc) * 128);
  if (wave == 6 && hlp && hj1 <= 0) touch(a.W0, htv, (hid / 3) * (64 * LDW * 2));
  auto code_touch = [&]() {
    unsigned long long c0, c1; CODE_ADDR(c0, "k_all"); CODE_ADDR(c1, "k_zend");
    const unsigned len = c1 - c0 < 262144ull ? (unsigned)(c1 - c0) : 0u;
    touch((const void*)c0, (unsigned)((lane * 32 + idx) * 128), 0, len);
  };
  auto rowt = [&](int t) { touch(a.W0, lane_id() * 128u, (t * 64 + (bid & 63)) * (LDW * 2), 0x7fffffffu); };
  auto batch = [&](int j) {
    const int t = j + 4;
    if (j == 0) { for (int u = 3; u <= 4; ++u) if ((u & 3) == (bid >> 6) && lane < 48) rowt(u); }
    else if (t < 16 && (t & 3) == (bid >> 6) && lane < 48) rowt(t);
    if (j == 5 + ((bid >> 6) + 3) % 4 + 4 * (idx & 1)) touch(a.PF1, lane_id() * 128u, bid * 8192);
  };
  if (wave == 7) code_touch();
  const int srow = m0 + wave * 16 + fr;
  const bool par = fr & 1;
  float bias[4];
  {
    const unsigned mk = 0x5555u & ((2u << (srow < 15 ? srow : 15)) - 1u), sft = (fr & 14) - fq * 4 + 14;
    const unsigned b0 = ~(mk >> sft), b1 = ~((mk << 16) >> sft);
#pragma unroll
    for (int j = 0; j < 4; ++j) bias[j] = __uint_as_float((unsigned)__builtin_amdgcn_sbfe((int)(j < 2 ? b0 : b1), 2 - 2 * (j & 1), 1) & 0xff61b1e6u);
  }
  unsigned pl = par ? 0x01000c0cu : 0x0c0c0100u, ph = par ? 0x03020c0cu : 0x0c0c0302u;
  asm volatile("" : "+v"(bias[0]), "+v"(bias[1]), "+v"(bias[2]), "+v"(bias[3]), "+v"(pl), "+v"(ph));
  constexpr int RS = 400, QT = WBASE + 3 * WU - 144 * RS;
  static_assert(wo_pos(0) >= XT && wo_pos(1) <= 2 * XT && wo_pos(2) + 16384 <= QT && wo_pos(3) >= QT + 144 * RS, "LDS");
  int qw = (16 + wm * 64 + fr) * RS + wn * 32 + fq * 8;
  unsigned ka = lds0 + QT + (wave * 16 + fr) * RS + fq * 16, va = lds0 + QT + (wave * 16 + fq * 4 + wq) * RS + 256 + wp * 16;
  asm volatile("" : "+v"(qw), "+v"(ka), "+v"(va));
  {
    asm volatile("" : "+v"(xa0), "+v"(xa1), "+v"(xh0), "+v"(xh1), "+v"(wa));
    __builtin_amdgcn_sched_barrier(0);
    issueW(1, 1); issueX(1, 1);
  }
  if (wave < NLW) wait_vm<WI + XIMIN>();
  lds_barrier();
  if (wave == 5) pretouch();
  if (wave == 4) batch(0);
  { const RdCtx c = mkctx(false, 0, 0); static_for<0, MFX + NF>([&](auto kc) { rd_item(fa, c, I0{}, kc); }); }
  wait_frags(fa, I1{});
  STAMP(1);
  f32x4 t4[MFX], csn[NF];
  int up = 0;
  const unsigned csa = lds0 + RING + 64 + wn * 192 + fq * 16, cfa = lds0 + ONES + 16 + wn * 4;
  auto ldC = [&]() { up = ds_r32(cfa); csn[0] = __builtin_bit_cast(f32x4, ds_b128<0>(csa)); csn[1] = __builtin_bit_cast(f32x4, ds_b128<64>(csa)); csn[2] = __builtin_bit_cast(f32x4, ds_b128<128>(csa)); };
  auto pinC = [&]() { asm volatile("s_waitcnt lgkmcnt(0)" : "+v"(up), "+v"(csn[0]), "+v"(csn[1]), "+v"(csn[2]) :: "memory"); };
  auto pinT = [&](bool h) { asm volatile("s_waitcnt lgkmcnt(0)" : "+v"(t4[0]), "+v"(t4[1]), "+v"(t4[2]), "+v"(t4[3]) :: "memory"); if (h) asm volatile("" : "+v"(t4[4])); };
  auto run = [&](auto ROLEc) __attribute__((always_inline)) {
    constexpr int ROLE = decltype(ROLEc)::value;
    constexpr bool LDR = ROLE < NLW;
    using HL = IC<LDR ? 1 : 0>;
    auto nos = [&]() {};
    TS6(0);
    if constexpr (!LDR) { ldS(); __builtin_amdgcn_sched_barrier(0); }
    { const RdCtx c = mkctx(true, 0, 0); step1(HL{}, fa, fb, c, I1{}, I0{}, IC<-4>{}, IC<-100>{}, nos, IC<LDR ? WI + XI : 0>{}, [&](int i) { if (i < WI) ldW(i, 2, 2); else ldX(i - WI, 2, 2); }); }
    const int jr = bid == 0 ? 4 : -1;
    int slx = 1;
    if constexpr (ROLE == 5) wait_vm<5>();
    auto iter = [&](int j, auto LXc, auto LWc, auto WAITc, auto TIc) {
      constexpr bool LX = decltype(LXc)::value && LDR, LW = decltype(LWc)::value && LDR;
      constexpr int WAIT = decltype(WAITc)::value, TI = decltype(TIc)::value;
      TS6(j);
      constexpr int TO = 2, OL = TI <= TO ? 0 : 2, OH = (LDR && TI >= TO) ? 2 : OL;
      constexpr int NLT = (LX ? XI : 0) + (LW ? WI : 0) + 2 * (OH - OL), N0 = (NLT + 1) / 2, N1 = NLT - N0;
      if constexpr (LDR) wait_vm<WAIT + (TI > 0 ? 2 * OL : 0)>();
      unsigned osrc = 0;
      if constexpr (OH > OL) { int lx = lane; asm volatile("" : "+v"(lx)); const int row = ROLE * 8 + (lx >> 3); osrc = (unsigned)(row * D_ + ((xcd & 1) * 8 + (idx & 7)) * 64 + ((lx & 7) ^ wswz(row)) * 8) * 2u; }
      if constexpr (ROLE == 6 && TI == 0) { if (j == hj1 || j == hj2) touch(a.W0, htv, (j + HQ) * (64 * LDW * 2)); }
      constexpr int S3 = 11;
      if constexpr (!LDR && TI == 0) {
        if (j == 2) {
          stats();
          int lx = lane; asm volatile("" : "+v"(lx));
          const Tag tg = tag_get(lds0);
          if constexpr (ROLE == 6) {
            const auto rP = RSRCN((const char*)a.sync + PROBE, bid == 0 ? 16384 : 0);
            const unsigned po = (unsigned)(((lx >> 3) & 1) * 8192 + (lx >> 4) * 2048 + (lx & 7) * 16);
            __builtin_amdgcn_raw_buffer_store_b128((u32x4){tg.lo, tg.lo, tg.lo, tg.lo}, rP, po, 0, 16);
            __builtin_amdgcn_raw_buffer_store_b128((u32x4){tg.lo, tg.lo, tg.lo, tg.lo}, rP, po + 1024u, 0, 16);
          }
          __builtin_amdgcn_raw_buffer_store_b128((u32x4){__float_as_uint(mu), tg.lo, __float_as_uint(rsd), tg.lo + tg.hi}, RSRC(a.cs),
                                                 (lx & 31) == 0 ? (unsigned)(STAB1 + (m0 + tn * 8 + (ROLE & 3) * 2 + (lx >> 5)) * 16) : 0x80000000u, 0, 16);
          __builtin_amdgcn_sched_barrier(0);
          asm volatile("s_nop 1" ::: "memory");
        }
      }
      if constexpr (ROLE == 6 && TI == 0) {
        if (j == jr) {
          wait_vm<0>();
          const Tag tg = tag_get(lds0);
          unsigned r0, r1, r2, r3, r4, r5, cnt;
#define RC1(x) "s_cmp_eq_u32 %" #x ", %8\n\ts_subb_u32 %6, %6, 0\n\t"
          asm volatile("s_not_b32 %1, %8\n\ts_not_b32 %2, %8\n\ts_not_b32 %3, %8\n\ts_not_b32 %4, %8\n\ts_not_b32 %5, %8\n\ts_mov_b32 %6, 3\n\t"
                       "s_load_dword %0, %7, 0\n\ts_load_dword %0, %7, 2048\n\ts_load_dword %0, %7, 5120\n\t"
                       "s_load_dword %1, %7, 8192\n\ts_load_dword %2, %7, 8192+2048\n\ts_load_dword %3, %7, 8192+4096\n\ts_load_dword %4, %7, 8192+6144\n\ts_load_dword %5, %7, 8192+1024\n\t"
                       "s_waitcnt lgkmcnt(5)\n\ts_nop 3\n\t" RC1(1) RC1(2) RC1(3) RC1(4) RC1(5) "s_waitcnt lgkmcnt(0)"
                       : "=&s"(r0), "=&s"(r1), "=&s"(r2), "=&s"(r3), "=&s"(r4), "=&s"(r5), "=&s"(cnt)
                       : "s"((const char*)a.sync + PROBE), "s"(tg.lo) : "scc", "memory");
          flag_set(a.sync, F_POL, tg, (int)cnt > 0 ? 1u : 0u);
        }
      }
      if constexpr (ROLE == 5 && TI == 0) {
        if (j == 7) { if (lane < 2) __builtin_amdgcn_raw_ptr_buffer_load_lds(RSRC(a.sync), LDSP(lds0 + SNAP + 8), 4, lane_id() * 4, F_POL, 0, 16); }
        if (j == 8) {
          wait_vm<0>();
          f32x2 pv = ds_b64<8>(lds0 + SNAP);
          const Tag tg = tag_get(lds0);
          asm volatile("" : "+v"(pv));
          u32x2 pw = __builtin_bit_cast(u32x2, pv);
          if (__builtin_expect(!flags_up(pw, 1, tg, 0), 0)) {
            do pw = flag_get(a.sync, F_POL, 1); while (!flags_up(pw, 1, tg, 0) && in_time(lds0));
            ds_w32(lds0 + SNAP + 8, __builtin_amdgcn_readfirstlane(pw[0]));
          }
          pol = (__builtin_amdgcn_readfirstlane(pw[0]) - tg.lo) & 1;
          const int mb = (xcd & 1) * 16 + (idx & 15);
          unsigned on = 0xffffffffu; asm volatile("" : "+v"(on));
          const unsigned mo = lane_id() * 16u;
#pragma unroll
          for (int i = 0; i < 3; ++i)
            __builtin_amdgcn_raw_buffer_store_b128((u32x4){on, on, on, on}, RSRC(a.st), mo + i * 1024, rb_h(((xcd >> 1) * 2 + (idx >> 4)) * 16 + (mb >> 1), pol) + ko((xcd >> 2) * 16384) + (mb & 1) * 3072, 16);
          __builtin_amdgcn_sched_barrier(0);
          asm volatile("s_nop 1" ::: "memory");
        }
        if (j == 10) { wait_vm<0>(); flag_set(a.sync, F_MARK + F_PAN((xcd >> 1) * 2 + (idx >> 4)) + ((xcd & 1) * 16 + (idx & 15)) * 8, tag_get(lds0), 0); }
      }
      if constexpr (ROLE == 7 && TI == 0) {
        if (j == S3) ldT(TNEW);
        if (j == S3 + 1) {
          for (;;) {
            wait_vm<0>();
            int lx = lane; asm volatile("" : "+v"(lx));
            const unsigned ad = lds0 + TNEW + lx * 16;
            u32x4 v[3];
            v[0] = __builtin_bit_cast(u32x4, ds_b128<0>(ad)); v[1] = __builtin_bit_cast(u32x4, ds_b128<1024>(ad)); v[2] = __builtin_bit_cast(u32x4, ds_b128<2048>(ad));
            const Tag tg = tag_get(lds0);
            asm volatile("" : "+v"(v[0]), "+v"(v[1]), "+v"(v[2]) :: "memory");
            auto good = [&](const u32x4& e) { return e[1] == tg.lo && e[3] == tg.lo + tg.hi; };
            const bool bad = (!good(v[0]) && (tm > 0 || lx >= 16)) || !good(v[1]) || (!good(v[2]) && lx < 16);
            if (__builtin_amdgcn_ballot_w64(bad) == 0 || !in_time(lds0)) break;
            ldT(TNEW);
          }
        }
      }
      if constexpr (ROLE == 7 && TI == 1) code_touch();
      if constexpr (ROLE == 4 && TI == 0) batch(j);
      if constexpr (ROLE >= 4 && TI == 2) {
        const bf16_t* cw = ROLE < 6 ? a.CW0 : a.CW1;
        touch(cw, lane < 56 ? (unsigned)(((ROLE & 1) * 512 + bid) * (DF_ * 2) + lane * 128) : 0x80000000u, 0);
      }
      int xp = slx + 2; if (xp >= 3) xp -= 3;
      auto ld = [&](int i) { if (LX && i < XI) ldX(i, j + 2, xp);
                             else if constexpr (LW) ldW(i - (LX ? XI : 0), j + 2, (j + 2) & 3);
                             else { const int q = i - (LX ? XI : 0), t = OL + (q >> 1), h = q & 1;
                               __builtin_amdgcn_raw_ptr_buffer_load_lds(RSRC(a.PF1), LDSP(lds0 + wo_pos(t >> 1) + (t & 1) * 8192 + (ROLE + 4 * h) * 1024), 16,
                                                                        osrc, ((((t & 1) * 8 + (t >> 1)) ^ WO_ROT) * 2 + h) * (32 * D_ * 2), 0, 0); } };
      { const RdCtx c = mkctx(false, slx * XT, (j & 3) * WU); step1(HL{}, fb, fa, c, I0{}, IC<LDR ? 1 : 0>{}, IC<-4>{}, IC<LDR ? 1 : -100>{}, [&]() { raw_barrier(); }, IC<N0>{}, ld); }
      { const RdCtx c = mkctx(true, slx * XT, (j & 3) * WU); step1(HL{}, fa, fb, c, I1{}, I0{}, IC<LDR ? -4 : -6>{},
          IC<LDR ? -100 : -3>{}, [&]() { if constexpr (TI != 3) { asm volatile("s_waitcnt lgkmcnt(0)" ::: "memory"); raw_barrier(); } }, IC<N1>{}, [&](int i) { ld(i + N0); }); }
      slx = slx + 1; if (slx >= 3) slx = 0;
    };
    using WS_ = IC<WI + XIMIN>;
    if constexpr (!LDR) raw_barrier();
    for (int j = 1; j <= NT - 4; ++j) iter(j, T{}, T{}, WS_{}, I0{});
    iter(NT - 3, T{}, T{}, WS_{}, I1{});
    iter(NT - 2, F{}, F{}, IC<WI + XIMIN>{}, IC<2>{});
    iter(NT - 1, F{}, F{}, I0{}, IC<3>{});
    TS6(16);
    { const RdCtx c = mkctx(false, 0, 0); const unsigned ta = lds0 + TNEW + fr * 16;
      step1(HL{}, fb, fa, c, I0{}, I0{}, IC<-100>{}, IC<-100>{}, nos, IC<MFX>{}, [&](int i) {
        if (i == 0) static_for<0, MF + (LDR ? 1 : 0)>([&](auto mc) { constexpr int m = decltype(mc)::value; t4[m] = __builtin_bit_cast(f32x4, ds_b128<(m < MF ? 16 + (ROLE / WN * MF + m) * 16 : 0) * 16>(ta)); });
        if constexpr (!LDR) { if (i == 2) ldC(); } });
      pinT(LDR); if constexpr (!LDR) pinC(); }
    TS6(17);
  };
  switch (wave) {
    case 0: run(IC<0>{}); break; case 1: run(IC<1>{}); break;
    case 2: run(IC<2>{}); break; case 3: run(IC<3>{}); break;
    case 4: run(IC<4>{}); break; case 5: run(IC<5>{}); break;
    case 6: run(IC<6>{}); break; default: run(IC<7>{}); break;
  }
  STAMP(2);
  Tag tg;
  {
    if (wave < 4) {
      if (fr == 0) {
#pragma unroll
        for (int n = 0; n < NF; ++n) *(f32x4*)(smem + RING + 64 + wn * 192 + fq * 16 + n * 64) = acch[n];
      }
      ds_w32(cfa, 1);
#pragma unroll
      for (int n = 0; n < NF; ++n)
#pragma unroll
        for (int e = 0; e < 4; ++e) { const float h = acch[n][e]; csn[n][e] = __int_as_float(__builtin_amdgcn_update_dpp(0, __float_as_int(h), 0x150, 0xf, 0xf, false)); }
    } else {
      unsigned polls = 0;
      while (__builtin_expect(__builtin_amdgcn_readfirstlane(up) == 0, 0) && ++polls < 40000000u) {
        ldC(); pinC();
      }
    }
    STAMP(9);
    TS6(18);
    TS6(19);
    STAMP(6);
    auto nrm = [&](const f32x4& v, const f32x4& cs, const f32x4& t) {
      const float mu = t[0], rs = t[2];
      return (u32x2){pk_bf16(rs * (v[0] - mu * cs[0]), rs * (v[1] - mu * cs[1])), pk_bf16(rs * (v[2] - mu * cs[2]), rs * (v[3] - mu * cs[3]))}; };
    u32x2 ov2[MF][NF];
#pragma unroll
    for (int m = 0; m < MF; ++m) {
#pragma unroll
      for (int n = 0; n < NF; ++n) { ov2[m][n] = nrm(acc[n][m], csn[n], t4[m]); asm volatile("" : "+v"(ov2[m][n])); }
      __builtin_amdgcn_sched_barrier(0);
#pragma unroll
      for (int n = 0; n < NF; ++n) *(u32x2*)(smem + QT + qw + m * 16 * RS + n * 128) = ov2[m][n];
      __builtin_amdgcn_sched_barrier(0);
    }
    if (wave < 4) {
#pragma unroll
      for (int n = 1; n < NF; ++n) *(u32x2*)(smem + QT + qw - 16 * RS + n * 128) = nrm(acch[n], csn[n], t4[MF]);
      __builtin_amdgcn_sched_barrier(0);
      hook(IC<3>{}, IC<2>{}, IC<WO_A>{});
    }
    STAMP(4);
    TS6(20);
    lds_barrier();
    TS6(21);
    STAMP(5);
    bf16x8 qf[2], kf[2][2]; s16x4 vf[2][2][2];
    qf[0] = ds_b128<16 * RS>(ka); kf[0][0] = ds_b128<128>(ka); kf[0][1] = ds_b128<16 * RS + 128>(ka);
    qf[1] = ds_b128<16 * RS + 64>(ka); kf[1][0] = ds_b128<192>(ka); kf[1][1] = ds_b128<16 * RS + 192>(ka);
    vf[0][0][0] = ds_tr<0>(va); vf[0][0][1] = ds_tr<16 * RS>(va); vf[0][1][0] = ds_tr<8>(va); vf[0][1][1] = ds_tr<16 * RS + 8>(va);
    vf[1][0][0] = ds_tr<64>(va); vf[1][0][1] = ds_tr<16 * RS + 64>(va); vf[1][1][0] = ds_tr<72>(va); vf[1][1][1] = ds_tr<16 * RS + 72>(va);
    u32x4 tp = __builtin_bit_cast(u32x4, ds_b128<0>(lds0 + SNAP));
    hook(IC<1>{}, IC<WO_A>{}, IC<WO_E>{});
    asm volatile("s_waitcnt lgkmcnt(9)" ::: "memory");
#pragma unroll
    for (int hh = 0; hh < 2; ++hh) asm volatile("" : "+v"(qf[hh]), "+v"(kf[hh][0]), "+v"(kf[hh][1]));
    const float sc2 = 0.17677669529663687f * 1.4426950408889634f;
    unsigned pm = (unsigned)__builtin_amdgcn_sbfe((int)pl, 24, 1); asm volatile("" : "+v"(pm));
    auto vmax = [](float x, float y) { float r; asm("v_max_f32 %0, %1, %2" : "=v"(r) : "v"(x), "v"(y)); return r; };
    u32x4 ov[2];
    const f32x4 z = {0.f, 0.f, 0.f, 0.f};
    f32x4 sc[2][2]; float lg[2][4], mx[2], sum[2];
    auto x16 = [](float v) { auto p = __builtin_amdgcn_permlane16_swap(__float_as_uint(v), __float_as_uint(v), false, false); return (f32x2){__uint_as_float(p[0]), __uint_as_float(p[1])}; };
    auto x32 = [](float v) { auto p = __builtin_amdgcn_permlane32_swap(__float_as_uint(v), __float_as_uint(v), false, false); return (f32x2){__uint_as_float(p[0]), __uint_as_float(p[1])}; };
#pragma unroll
    for (int hh = 0; hh < 2; ++hh) {
      sc[hh][0] = MFMA(kf[hh][0], qf[hh], z);
      sc[hh][1] = MFMA(kf[hh][1], qf[hh], z);
    }
    __builtin_amdgcn_sched_barrier(0);
#pragma unroll
    for (int hh = 0; hh < 2; ++hh) {
#pragma unroll
      for (int j = 0; j < 4; ++j) { const float e0 = sc[hh][j >> 1][(j & 1) * 2], e1 = sc[hh][j >> 1][(j & 1) * 2 + 1];
        lg[hh][j] = __builtin_fmaf(__uint_as_float((__float_as_uint(e1) & pm) | (__float_as_uint(e0) & ~pm)), sc2, bias[j]); }
      mx[hh] = fmaxf(fmaxf(lg[hh][0], lg[hh][1]), fmaxf(lg[hh][2], lg[hh][3]));
    }
    __builtin_amdgcn_sched_barrier(0);
    { auto p = __builtin_amdgcn_permlane16_swap(__float_as_uint(mx[0]), __float_as_uint(mx[1]), false, false);
      const f32x2 q = x32(vmax(__uint_as_float(p[0]), __uint_as_float(p[1]))), r = x16(vmax(q[0], q[1])); mx[0] = r[0]; mx[1] = r[1]; }
    __builtin_amdgcn_sched_barrier(0);
#pragma unroll
    for (int hh = 0; hh < 2; ++hh) {
#pragma unroll
      for (int j = 0; j < 4; ++j) lg[hh][j] = __builtin_amdgcn_exp2f(lg[hh][j] - mx[hh]);
      sum[hh] = (lg[hh][0] + lg[hh][1]) + (lg[hh][2] + lg[hh][3]);
    }
    __builtin_amdgcn_sched_barrier(0);
    asm volatile("s_waitcnt lgkmcnt(0)" ::: "memory");
#pragma unroll
    for (int hh = 0; hh < 2; ++hh) asm volatile("" : "+v"(vf[hh][0][0]), "+v"(vf[hh][0][1]), "+v"(vf[hh][1][0]), "+v"(vf[hh][1][1]));
    asm volatile("" : "+v"(tp));
    tg = Tag{(unsigned)__builtin_amdgcn_readfirstlane(tp[0]) + 8u, (unsigned)__builtin_amdgcn_readfirstlane(tp[1])};
    pol = (__builtin_amdgcn_readfirstlane(tp[2]) - tg.lo) & 1;
    { auto p = __builtin_amdgcn_permlane16_swap(__float_as_uint(sum[0]), __float_as_uint(sum[1]), false, false);
      const f32x2 q = x32(__uint_as_float(p[0]) + __uint_as_float(p[1])), r = x16(__builtin_amdgcn_rcpf(q[0] + q[1])); sum[0] = r[0]; sum[1] = r[1]; }
    __builtin_amdgcn_sched_barrier(0);
#pragma unroll
    for (int hh = 0; hh < 2; ++hh) {
      const float inv = sum[hh];
      const unsigned c0 = pk_bf16(lg[hh][0] * inv, lg[hh][1] * inv), c1 = pk_bf16(lg[hh][2] * inv, lg[hh][3] * inv);
      const u32x4 pp = {__builtin_amdgcn_perm(0u, c0, pl), __builtin_amdgcn_perm(0u, c0, ph), __builtin_amdgcn_perm(0u, c1, pl), __builtin_amdgcn_perm(0u, c1, ph)};
      const bf16x8 pf = __builtin_bit_cast(bf16x8, pp);
      sc[hh][0] = MFMA(__builtin_shufflevector(vf[hh][0][0], vf[hh][0][1], 0, 1, 2, 3, 4, 5, 6, 7), pf, z);
      sc[hh][1] = MFMA(__builtin_shufflevector(vf[hh][1][0], vf[hh][1][1], 0, 1, 2, 3, 4, 5, 6, 7), pf, z);
    }
    __builtin_amdgcn_sched_barrier(0);
#pragma unroll
    for (int hh = 0; hh < 2; ++hh) ov[hh] = (u32x4){pk_bf16(sc[hh][0][0], sc[hh][0][1]), pk_bf16(sc[hh][0][2], sc[hh][0][3]), pk_bf16(sc[hh][1][0], sc[hh][1][1]), pk_bf16(sc[hh][1][2], sc[hh][1][3])};
    STAMP(10);
#pragma unroll
    for (int hh = 0; hh < 2; ++hh) __builtin_amdgcn_raw_buffer_store_b128(ov[hh], rO, rb_h((m0 >> 4) + wave, pol) + ko(tn * 2048) + hh * 1024 + lane * 16, 0, 16);
    STAMP(11);
  }
  STAMP(7);
  STAMP(8);
  TS6(22);
  asm volatile("s_waitcnt vmcnt(0)" ::: "memory");
  flag_set(a.sync, F_O(tm) + (wave >> 1) * 256 + tn * 16 + (wave & 1) * 8, tg, 0);
  raw_barrier();
  hook(IC<1>{}, IC<WO_E>{}, IC<16>{});
  STAMP(3);
#endif
}

struct Late { u32x2 tag; };
template <class HK>
__device__ __forceinline__ void glu_phase(const GemmArgs& a, HK hook, Late& late, int pol) {
#if defined(__HIP_DEVICE_COMPILE__)
  constexpr int LDW = DF_, WN = 2;
  constexpr int UPOFF = 32 * 224 + 16;
  constexpr int XT = 32768, WU = 14592;
  constexpr int XBASE = 0, WBASE = 3 * XT, RING = WBASE + 4 * WU;
  constexpr int MF = 4, NF = 7;
  constexpr int NT = 16, NXI = 16, NWI = 14, NLW = 4, XI = 4, WI = 4;
  extern __shared__ __attribute__((aligned(16))) char smem[];
  STAMP(0);
  const unsigned lds0 = (unsigned)(size_t)(lds_char_ptr)smem;
  const int tid = threadIdx.x, lane = tid & 63;
  const int wave = __builtin_amdgcn_readfirstlane(tid >> 6);
  const int wm = wave / WN, wn = wave % WN;
  const int bid = blockIdx.x, xcd = bid & 7, idx = bid >> 3;
  const int tm = (xcd / 4) * 4 + idx / 8, tn = (xcd % 4) * 8 + idx % 8;
  const int m0 = tm * 256;
  const int krot = (tm >> 2) * 8;
  const int fr = lane & 15, fq = lane >> 4;
  const int wq = (lane >> 2) & 3, wp = lane & 3;
  const int panel = m0 >> 8;
  constexpr int TCH = RING + 5120;
  constexpr int DONE = TCH + 896;
  auto done_inc = [&]() { if (lane == 0) asm volatile("ds_add_u32 %0, %1" :: "v"(lds0 + DONE), "v"(1) : "memory"); };
  auto touch = [&](const void* base, unsigned voff, unsigned soff, unsigned lim = 0x7fffffffu) {
    __builtin_amdgcn_raw_ptr_buffer_load_lds(RSRCN(base, lim), LDSP(lds0 + TCH), 4, voff, soff, 0, 0);
  };
  const auto rO = RSRC(a.O);

  unsigned xsrc[XI], wsrc[WI];
#pragma unroll
  for (int i = 0; i < XI; ++i) {
    const int j = (wave % NLW) + NLW * i;
    xsrc[i] = (unsigned)(rb_h((m0 >> 4) + (j >> 1), pol) + (j & 1) * 1024 + lane * 16);
  }
#pragma unroll
  for (int i = 0; i < WI; ++i) {
    int j = (wave % NLW) + NLW * i; if (j >= NWI) j -= NLW;
    const int jj = j >= 7 ? j - 7 : j;
    const int b = jj * 1024 + lane * 16, p = b / 224, pos = (b % 224) >> 4;
    const int row = 8 * ((p & 15) >> 2) + (p & 3) + 4 * (p >> 4);
    wsrc[i] = (unsigned)(row * LDW + tn * 112 + pos * 8) * 2u;
  }
  auto ldX = [&](int i, int tile, int part, int slot) {
    tile = (tile + krot) & 15;
    const int j = wave + NLW * i;
    __builtin_amdgcn_raw_ptr_buffer_load_lds(RSRC(a.X), LDSP(lds0 + XBASE + slot * XT + (part * NXI + j) * 1024), 16, xsrc[i], ko(tile * 2048) + part * (NXI / 2) * SH, 0, 16);
  };
  auto ldW = [&](int i, int unit, int slot) {
    const auto rW0 = RSRC(a.W0);
    const auto rW1 = RSRC(a.W1);
    int j = wave + NLW * i; if (j >= NWI) j -= NLW;
    unit = (unit + 2 * krot) & 31;
    const bool up = j >= 7;
    __builtin_amdgcn_raw_ptr_buffer_load_lds(up ? rW1 : rW0, LDSP(lds0 + WBASE + slot * WU + (up ? UPOFF + (j - 7) * 1024 : j * 1024)), 16, wsrc[i], unit * 32 * LDW * 2, 0, 0);
  };
  auto issueX = [&](int tile, int part, int slot) {
    if (wave >= NLW) return;
#pragma unroll
    for (int i = 0; i < XI; ++i) ldX(i, tile, part, slot);
  };
  auto issueW = [&](int unit, int slot) {
    if (wave >= NLW) return;
#pragma unroll
    for (int i = 0; i < WI; ++i) ldW(i, unit, slot);
  };

  f32x4 acc[NF][MF];
#pragma unroll
  for (int n = 0; n < NF; ++n)
#pragma unroll
    for (int m = 0; m < MF; ++m) acc[n][m] = (f32x4){0.f, 0.f, 0.f, 0.f};
  f32x4 accs = {0.f, 0.f, 0.f, 0.f};

  const unsigned xa0 = lds0 + XBASE + wm * MF * 16 * 128 + lane * 16, xa1 = xa0 + 1024;
  unsigned wa[2];
#pragma unroll
  for (int c = 0; c < 2; ++c) wa[c] = lds0 + WBASE + (4 * fq + wq) * 224 + wn * 112 + (c == 0 ? wp * 8 : 96 + (wp & 1) * 8 + (wp >> 1) * UPOFF);

  using I0 = IC<0>; using I1 = IC<1>;
  using T = std::true_type; using F = std::false_type;
  typedef Frags<MF, NF> FR;
  struct RdCtx { unsigned xa, wb[2]; };
  auto mkctx = [&](bool ks1, unsigned xoffs, unsigned woffs) {
    RdCtx c; c.xa = (ks1 ? xa1 : xa0) + xoffs; c.wb[0] = wa[0] + woffs; c.wb[1] = wa[1] + woffs;
    return c;
  };
  auto rd_item = [&](FR& f, const RdCtx& c, auto kc) {
    constexpr int k = decltype(kc)::value;
    if constexpr (k < MF) f.x[k] = ds_b128<k * 2048>(c.xa);
    else {
      constexpr int n = k - MF, cc = n == 6, o = n == 6 ? 0 : (n >> 1) * 32 + (n & 1) * UPOFF;
      f.lo[n] = ds_tr<o>(c.wb[cc]); f.hi[n] = ds_tr<o + 16 * 224>(c.wb[cc]);
    }
  };
  auto wait_frags = [&](FR& f) {
    asm volatile("s_waitcnt lgkmcnt(0)" ::: "memory");
#pragma unroll
    for (int m = 0; m < MF; ++m) asm volatile("" : "+v"(f.x[m]));
#pragma unroll
    for (int n = 0; n < NF; ++n) asm volatile("" : "+v"(f.lo[n]), "+v"(f.hi[n]));
  };
  auto step = [&](FR& cur, FR& nxt, const RdCtx& c, auto PFc, auto NLc, auto&& ld) {
    constexpr bool PF = decltype(PFc)::value != 0; constexpr int NL = decltype(NLc)::value;
    constexpr int R = MF + NF, RG = NF - 1;
    static_for<0, NF>([&](auto gc) {
      constexpr int g = decltype(gc)::value;
      if constexpr (PF) static_for<0, R>([&](auto kc) { constexpr int k = decltype(kc)::value; if constexpr (k * RG / R == g) rd_item(nxt, c, kc); });
      static_for<0, NL>([&](auto ic) { constexpr int i = decltype(ic)::value; if constexpr (i * NF / NL == g) ld(i); });
      const bf16x8 w = __builtin_shufflevector(cur.lo[g], cur.hi[g], 0, 1, 2, 3, 4, 5, 6, 7);
#pragma unroll
      for (int m = 0; m < MF; ++m) acc[g][m] = MFMA(w, cur.x[m], acc[g][m]);
      __builtin_amdgcn_sched_barrier(0);
    });
    if constexpr (PF) wait_frags(nxt);
  };
  auto stepg = [&](auto CSc, FR& cur, FR& nxt, const RdCtx& c, auto Hc, auto R0c, auto IBc, auto&& sync, auto NLc, auto&& ld, auto&& gap) {
    constexpr bool CS = decltype(CSc)::value; constexpr int H = decltype(Hc)::value, R0 = decltype(R0c)::value, IB = decltype(IBc)::value, NL = decltype(NLc)::value;
    constexpr int NM = NF * MF, NR = MF + 2 * NF;
    static_for<0, NM>([&](auto ic) {
      constexpr int I = decltype(ic)::value, g = I / MF, m = I % MF;
      constexpr int RL = IB > 8 ? 20 : 22;
      const bf16x8 w = __builtin_shufflevector(cur.lo[g], cur.hi[g], 0, 1, 2, 3, 4, 5, 6, 7);
      acc[g][m] = MFMA(w, cur.x[m], acc[g][m]);
      __builtin_amdgcn_sched_barrier(0);
      if constexpr (I == IB) sync();
      static_for<first_at(I, R0, RL - R0, NR - 1, NR), first_at(I + 1, R0, RL - R0, NR - 1, NR)>([&](auto rc) {
        constexpr int r = decltype(rc)::value;
        if constexpr (r < MF) nxt.x[r] = ds_b128<r * 2048 + H * 1024>(c.xa);
        else {
          constexpr int n = (r - MF) >> 1, cc = n == 6, o = (n == 6 ? 0 : (n >> 1) * 32 + (n & 1) * UPOFF) + H * WU;
          if constexpr ((r - MF) & 1) nxt.hi[n] = ds_tr<o + 16 * 224>(c.wb[cc]); else nxt.lo[n] = ds_tr<o>(c.wb[cc]);
        }
      });
#pragma unroll
      for (int i = first_at(I, R0, NM - 1 - R0, NL, NL); i < first_at(I + 1, R0, NM - 1 - R0, NL, NL); ++i) ld(i);
      gap(ic);
      if constexpr (CS && m == MF - 1) {
        const unsigned sv = fr == g ? 0x3f803f80u : 0u;
        const u32x4 s4 = {sv, sv, sv, sv};
        accs = MFMA(w, __builtin_bit_cast(bf16x8, s4), accs);
      }
      __builtin_amdgcn_sched_barrier(0);
    });
    wait_frags(nxt);
  };
  FR fa, fb;
  unsigned tv4 = 0;

  f32x4* tb_row = (f32x4*)(smem + RING);
  float* tb_cs = (float*)(tb_row + 256);
  constexpr int RTAB = 524288;
  auto glu_stats_issue = [&]() {
    if (wave == 7) __builtin_amdgcn_raw_ptr_buffer_load_lds(RSRC(a.st), LDSP(lds0 + RING + 3072), 16, ((lane >> 2) * S_ + m0 + tn * 8 + (lane & 3) * 2) * 8, 0, 0, 16);
  };
  auto glu_stats = [&]() {
    int lx = lane; asm volatile("" : "+v"(lx));
    const unsigned sa = lds0 + RING + 3072 + lx * 8;
    f32x2 p0 = ds_b64<0>(sa), p1 = ds_b64<512>(sa);
    asm volatile("s_waitcnt lgkmcnt(0)" : "+v"(p0), "+v"(p1) :: "memory");
    const float mu = sum8(p0[0] + p1[0]) * (1.f / 16.f);
    const float v = sum8((p0[1] + p1[1]) + 64.f * ((p0[0] - mu) * (p0[0] - mu) + (p1[0] - mu) * (p1[0] - mu))) * (1.f / 1024.f) + 1e-5f;
    const float rs = __builtin_amdgcn_rsqf(v);
    const f32x4 rv = {mu, -0.10294324f * rs * rs * rs, -2.3022082f * rs, v};
    __builtin_amdgcn_raw_buffer_store_b128(__builtin_bit_cast(u32x4, rv), RSRC(a.cs), lx < 8 ? (unsigned)(RTAB + (m0 + tn * 8 + lx) * 16) : 0x80000000u, 0, 16);
  };
  f32x4 cg[4], cu[4], rw[MF], rw6[2]; f32x2 tgv; int dnv, go0; unsigned le, ta, t6;
  auto tabs = [&](int i) {
    if (i == 0) { const int lz = lane_id(), fr = lz & 15, fq = lz >> 4;
      ta = lds0 + RING + 4096 + wn * 224 + fq * 16; t6 = lds0 + RING + wm * 1024 + fr * 16;
      go0 = ((m0 >> 4) + wm * 4) * SA + fr * 16 + (fq & 1) * SA + (fq >> 1) * 256;
      le = lds0 + (wm * 4 + (fq & 1)) * 1024 + (wn * 2 + (fq >> 1)) * 256 + fr * 16;
      asm volatile("" : "+v"(ta), "+v"(t6), "+v"(go0), "+v"(le)); }
    if (i == 1) { rw[0] = __builtin_bit_cast(f32x4, ds_b128<0>(t6)); rw[1] = __builtin_bit_cast(f32x4, ds_b128<256>(t6)); cg[0] = __builtin_bit_cast(f32x4, ds_b128<0>(ta)); }
    if (i == 2) { cg[1] = __builtin_bit_cast(f32x4, ds_b128<64>(ta)); cu[0] = __builtin_bit_cast(f32x4, ds_b128<448>(ta)); cu[1] = __builtin_bit_cast(f32x4, ds_b128<448 + 64>(ta)); }
    if (i == 3) { rw[2] = __builtin_bit_cast(f32x4, ds_b128<512>(t6)); rw[3] = __builtin_bit_cast(f32x4, ds_b128<768>(t6)); tgv = ds_b64<0>(lds0 + SNAP); }
    if (i == 8) asm volatile("s_waitcnt lgkmcnt(0)" : "+v"(rw[0]), "+v"(rw[1]), "+v"(rw[2]), "+v"(rw[3]), "+v"(cg[0]), "+v"(cg[1]), "+v"(cu[0]), "+v"(cu[1]), "+v"(tgv) :: "memory");
    if (i == 9) { const int lz = lane_id(); const unsigned tc = ta - (lz & 32), tr = t6 + (lz & 32) * 8;
      cg[2] = __builtin_bit_cast(f32x4, ds_b128<128>(ta)); cu[2] = __builtin_bit_cast(f32x4, ds_b128<448 + 128>(ta)); cg[3] = __builtin_bit_cast(f32x4, ds_b128<192>(tc)); cu[3] = __builtin_bit_cast(f32x4, ds_b128<448 + 192>(tc));
      rw6[0] = __builtin_bit_cast(f32x4, ds_b128<0>(tr)); rw6[1] = __builtin_bit_cast(f32x4, ds_b128<512>(tr)); }
    if (i == 10) asm volatile("s_waitcnt lgkmcnt(0)" : "+v"(cg[2]), "+v"(cg[3]), "+v"(cu[2]), "+v"(cu[3]), "+v"(rw6[0]), "+v"(rw6[1]) :: "memory");
    if (i == 11) dnv = ds_r32(lds0 + DONE);
  };
  auto epilogue = [&]() __attribute__((always_inline)) {
    STAMP(2);
    auto prio = [&](auto gc) { __builtin_amdgcn_s_setprio(3 - decltype(gc)::value); };
    prio(I0{});
    STAMPW(9, 4);
    TS4(19);
    unsigned hv[MF][NF];
    auto keep = [&](unsigned a0, unsigned a1, unsigned b0, unsigned b1) {
      const auto s0 = __builtin_amdgcn_permlane16_swap(a0, b0, false, false), s1 = __builtin_amdgcn_permlane16_swap(a1, b1, false, false);
      return (u32x4){s0[0], s1[0], s0[1], s1[1]}; };
    auto put = [&](auto pc, auto hc) {
      constexpr int p = decltype(pc)::value, h = decltype(hc)::value;
      const u32x4 v = keep(hv[2 * h][2 * p], hv[2 * h][2 * p + 1], hv[2 * h + 1][2 * p], hv[2 * h + 1][2 * p + 1]);
      __builtin_amdgcn_raw_buffer_store_b128(v, rO, go0, (tn * 12 + p * 4 + wn * 2) * 256 + h * 2 * SA, 16);
      if constexpr (p < 2) ds_w128<OWNE + p * 16384 + h * 2048>(le, v); else ds_w128<h * 2048>(le + OWNM, v);
      __builtin_amdgcn_sched_barrier(0);
      asm volatile("s_nop 1" ::: "memory");
      __builtin_amdgcn_sched_barrier(0);
    };
#define STAGE(body) _Pragma("unroll") for (int i = 0; i < 4 * NN; ++i) { const int m = i / NN, n = N0 + i % NN; const f32x4 k = n == 6 ? rw6[m >> 1] : rw[m]; body; } __builtin_amdgcn_sched_barrier(0)
    auto grp = [&](auto N0c, auto NNc, auto&& at) {
      constexpr int N0 = decltype(N0c)::value, NN = decltype(NNc)::value;
      f32x2 G[4 * NN], U[4 * NN], P[4 * NN];
      __builtin_amdgcn_sched_barrier(0);
      STAGE({ const int e = (n == 6 ? m & 1 : n & 1) * 2; const f32x4 x = n == 6 ? acc[6][m & 2] : acc[n & 6][m]; G[i] = ((f32x2){x[e], x[e + 1]}) - k[0] * ((f32x2){cg[n >> 1][e], cg[n >> 1][e + 1]}); });
      STAGE(P[i] = G[i] * G[i]);
      at(I0{});
      STAGE(P[i] = P[i] * k[1] + k[2]);
      STAGE(P[i] = G[i] * P[i]);
      at(I1{});
      STAGE({ const int e = (n == 6 ? m & 1 : n & 1) * 2; const f32x4 x = n == 6 ? acc[6][m | 1] : acc[n | 1][m]; U[i] = ((f32x2){x[e], x[e + 1]}) - k[0] * ((f32x2){cu[n >> 1][e], cu[n >> 1][e + 1]}); });
      STAGE(P[i] = ((f32x2){__builtin_amdgcn_exp2f(P[i][0]), __builtin_amdgcn_exp2f(P[i][1])}));
      at(IC<2>{});
      __builtin_amdgcn_sched_barrier(0);
      STAGE(P[i] = P[i] * k[3] + k[3]);
      STAGE(P[i] = ((f32x2){__builtin_amdgcn_rcpf(P[i][0]), __builtin_amdgcn_rcpf(P[i][1])}));
      at(IC<3>{});
      STAGE(U[i] = G[i] * U[i]);
      STAGE(U[i] = U[i] * P[i]);
      STAGE({ hv[m][n] = pk_bf16(U[i][0], U[i][1]); asm volatile("" : "+v"(hv[m][n])); });
    };
#undef STAGE
    using I2 = IC<2>; using I4 = IC<4>;
    TS4(20);
    grp(I0{}, I2{}, [&](auto tc) { if constexpr (decltype(tc)::value == 3) tabs(11); });
    TS4(21);
    { int sd;
      asm volatile("1:\n\ts_waitcnt lgkmcnt(0)\n\tv_readfirstlane_b32 %1, %0\n\ts_cmp_ge_i32 %1, 8\n\ts_cbranch_scc1 2f\n\tds_read_b32 %0, %2\n\ts_branch 1b\n2:" : "+v"(dnv), "=&s"(sd) : "v"(lds0 + DONE) : "scc", "memory"); }
    put(I0{}, I0{}); put(I0{}, I1{});
    tabs(9);
    STAMP(4);
    STAMPW(11, 4);
    TS4(22);
    prio(I1{});
    const auto rFl = RSRCN((const char*)a.sync + F_ACT(panel) + (wm * 64 + tn * 2 + wn) * 8, 8);
    u32x2 fval = {__float_as_uint(tgv[0]) + 8u, __float_as_uint(tgv[1])}; unsigned foff = lane_id() * 8u;
    asm volatile("" : "+v"(fval), "+v"(foff));
    grp(I2{}, I2{}, [&](auto) {});
    TS4(23);
    { wait_vm<0>(); __builtin_amdgcn_raw_buffer_store_b64(fval, rFl, foff, 0, 16); STAMPW(6, 4); STAMP(10); }
    put(I1{}, I0{}); put(I1{}, I1{});
    TS4(24);
    prio(I2{});
    tabs(10);
    grp(I4{}, I2{}, [&](auto) {});
    TS4(25);
    auto poll = [&]() { if (foff < 256u) __builtin_amdgcn_raw_ptr_buffer_load_lds(RSRC(a.sync), LDSP(lds0 + FLP + wave * 128), 4, foff >> 1, F_ACT(panel) + wm * 512 + (xcd & 3) * 128, 0, 16); };
    put(I2{}, I0{}); put(I2{}, I1{});
    prio(IC<3>{});
#pragma unroll
    for (int q = 0; q < 2; ++q)
#pragma unroll
      for (int e = 0; e < 4; ++e) {
        const float x0 = acc[6][2 * q][e], x1 = acc[6][2 * q + 1][e];
        auto sw = __builtin_amdgcn_permlane32_swap(__float_as_uint(x0), __float_as_uint(x1), false, false);
        acc[6][2 * q][e] = __uint_as_float(sw[0]); acc[6][2 * q + 1][e] = __uint_as_float(sw[1]);
      }
    TS4(26);
    int go6 = go0 + (int)((foff >> 7) & 1u) * SA + (int)(foff >> 8) * (SA - 256) + (384 + tn * 2 + wn) * 256; asm volatile("" : "+v"(go6));
    grp(IC<6>{}, I1{}, [&](auto) {});
    poll();
    __builtin_amdgcn_raw_buffer_store_b128(keep(hv[0][6], hv[1][6], hv[2][6], hv[3][6]), rO, go6, 0, 16);
    __builtin_amdgcn_sched_barrier(0);
    asm volatile("s_nop 1" ::: "memory");
    late.tag = fval;
    asm volatile("" ::: "memory");
    __builtin_amdgcn_s_setprio(0);
  };

  TS4(0);
  constexpr int WR = 2;
  if (wave == 5) ds_w32(lds0 + DONE, 0);
  STAMP(9); STAMP(6);
  auto check_tile = [&](int t) {
    int lx = lane; asm volatile("" : "+v"(lx));
    const unsigned ad = lds0 + XBASE + t * XT + wave * 1024 + lx * 16;
    if (__builtin_expect(marked<8>(ad), 0)) {
      for (int tries = 0;; ++tries) {
        if (tries >= TRIES) wait_flags(lds0, a.sync, F_H + F_PAN(panel), 32, 0);
#pragma unroll 1
        for (int u = t; u < t + 1 + (t == 0); ++u) { issueX(u, 0, u); issueX(u, 1, u); }
        if (t == 0) wait_vm<2 * XI>(); else wait_vm<0>();
        if (tries >= TRIES || !marked<8>(ad)) break;
      }
    }
  };
  auto run = [&](auto LDRc) __attribute__((always_inline)) {
    constexpr bool LDR = decltype(LDRc)::value;
    if constexpr (LDR) {
      issueX(0, 0, 0); issueX(0, 1, 0); issueX(1, 0, 1); issueX(1, 1, 1);
      asm volatile("" : "+v"(wsrc[0]), "+v"(wsrc[1]), "+v"(wsrc[2]), "+v"(wsrc[3]));
      wait_vm<2 * XI>();
      check_tile(0);
    } else {
      wait_vm<0>();
    }
    unsigned long long tb = (unsigned long long)a.PF0; unsigned tv = 0, tl = 0x7fffffffu, ts = 7168u, to = 0, tmask = 0;
    if constexpr (!LDR) {
      unsigned long long c0, c1; CODE_ADDR(c0, "k_all"); CODE_ADDR(c1, "k_zend");
      if (wave == 4) { tb = c0; tl = c1 - c0 < 262144ull ? (unsigned)(c1 - c0) : 0u; tmask = 1u << 11; }
      if (wave == 5) { tmask = 0x55u << (6 + (idx & 1)); to = ((xcd & 3) * 32 + idx) * 57344 + (xcd >> 2) * 28672; }
      if (wave == 6) { tb = (unsigned long long)((idx & 16) ? a.W1 : a.W0); ts = 64 * LDW * 2; tmask = 0x1eu; to = (krot + 4) * ts; }
      const int l4 = lane / 14;
      tv = wave == 4 ? (unsigned)((lane * 32 + idx) * 128)
         : (lane >= 56 ? 0x80000000u : (wave == 5 ? (unsigned)(lane * 128) : (unsigned)(((idx & 15) * 4 + l4) * (LDW * 2) + (xcd & 3) * 1792 + (lane - l4 * 14) * 128)));
    }
    const int js = 2 * (tm & 3) + 1;
    const auto rC = RSRC(a.cs);
    constexpr int JR = 10, JS = 2;
    const int jf = wave == 4 ? NT - 4 : (wave == 7 ? JR : 64);
    const int fof = wave == 4 ? F_COL + tn * 256 : F_ROW + F_PAN(panel);
    unsigned evm = tmask | (8u << js) | (wave == 4 ? 3u << (NT - 4) : 0u) | (wave == 7 ? ((5u << JS) | 2u | (11u << JR)) : 0u);
    if constexpr (!LDR) asm volatile("" : "+v"(tv), "+s"(tb), "+s"(tl), "+s"(ts), "+s"(to), "+s"(tmask), "+s"(evm));
    lds_barrier();
    if constexpr (!LDR) { if (wave == 4) __builtin_amdgcn_raw_ptr_buffer_load_lds(RSRC(a.sync), LDSP(lds0 + TCH + 256), 4, lane * 4, F_H + F_PAN(panel), 0, 16); }
    { const RdCtx c = mkctx(false, 0, WR * WU); static_for<0, MF + NF>([&](auto kc) { rd_item(fa, c, kc); }); }
    wait_frags(fa);
    STAMP(1);
    TS4(1);
    { const RdCtx c = mkctx(true, 0, (WR + 1) * WU); step(fa, fb, c, T{}, IC<LDR ? 2 * XI : 0>{}, [&](int i) { ldX(i % XI, 2, i / XI, 2); }); }
    RdCtx cc = mkctx(false, XT, 0);
    int xs = 1, dX = XT, dW = 2 * WU;
    int wd = lds0 + WBASE + 2 * WU, wso = ((4 + 2 * krot) & 31) * (32 * LDW * 2), xd = lds0 + XBASE + wave * 1024, xso = ko(((3 + krot) & 15) * 2048);
    int wc[WI];
#pragma unroll
    for (int i = 0; i < WI; ++i) { int q = wave + NLW * i; if (q >= NWI) q -= NLW; wc[i] = q >= 7 ? UPOFF + (q - 7) * 1024 : q * 1024; }
    unsigned evs = evm, dw = 0, ca = 0, cw = 0; int lc = 0, dwb = 0;
    auto ldd = [&](int i) { static_for<0, 22>([&](auto kc) { if (i == decltype(kc)::value) hook(kc, dw, dwb); }); };
    constexpr int CFL = TCH + 256, CPB = XBASE + ((NT - 3) % 3) * XT;
    auto iter = [&](int j, auto LWc, auto LXc, auto WAITc, auto CAc, auto CBc) {
      constexpr bool LW = decltype(LWc)::value && LDR, LX = decltype(LXc)::value && LDR, LP = LW && !LX;
      TS4(j + 1);
      if constexpr (LDR) {
        wait_vm<decltype(WAITc)::value>();
        if constexpr (LX) { if (__builtin_expect(j <= 2, 0)) check_tile(j); }
      }
      if constexpr (!LDR) {
        if (__builtin_expect(evs & 2, 0)) {
          if (j == js + 3) {
            if ((tmask >> (j - 1)) & 1) wait_vm<1>(); else wait_vm<0>();
            flag_set(a.sync, F_COL + tn * 256 + (tm * 4 + wave - 4) * 8, tag_get(lds0), 0);
          }
          if (j == 1) glu_stats_issue();
          if (j == JS && wave == 7) { wait_vm<0>(); glu_stats(); }
          if (j == JS + 2 && wave == 7) {
            if (j == js + 2) wait_vm<1>(); else wait_vm<0>();
            flag_set(a.sync, fof + tn * 8, tag_get(lds0), 0);
          }
          {
            int lx = lane; asm volatile("" : "+v"(lx));
            if (j == jf) __builtin_amdgcn_raw_ptr_buffer_load_lds(RSRC(a.sync), LDSP(lds0 + CFL + (wave & 1) * 256), 4, lx * 4, fof, 0, 16);
            if (j == jf + 1) {
              wait_vm<0>();
              int fv = ds_r32(lds0 + CFL + (wave & 1) * 256 + lx * 4);
              const Tag tg = tag_get(lds0);
              asm volatile("" : "+v"(fv));
              while (!words_up((unsigned)fv, tg) && in_time(lds0))
                asm volatile("global_load_dword %0, %1, %2 sc1\n\ts_waitcnt vmcnt(0)" : "=v"(fv) : "v"(lx * 4), "s"((const char*)a.sync + fof) : "memory");
              if (wave == 7) {
#pragma unroll
                for (int i = 0; i < 4; ++i) __builtin_amdgcn_raw_ptr_buffer_load_lds(rC, LDSP(lds0 + RING + i * 1024), 16, lx * 16, RTAB + m0 * 16 + i * 1024, 0, 16);
              }
            }
            if (j == jf + 3) wait_vm<0>();
          }
          if ((tmask >> j) & 1) { touch((const void*)tb, tv, to, tl); to += ts; }
        }
        __builtin_amdgcn_sched_barrier(0);
      }
      constexpr bool LD = LDR && !LW;
      if constexpr (LD) {
        f32x4 p0 = __builtin_bit_cast(f32x4, ds_b128<0>(ca)), p1 = __builtin_bit_cast(f32x4, ds_b128<224>(ca)), p2 = __builtin_bit_cast(f32x4, ds_b128<448>(ca)), p3 = __builtin_bit_cast(f32x4, ds_b128<672>(ca));
        asm volatile("s_waitcnt lgkmcnt(0)" : "+v"(p0), "+v"(p1), "+v"(p2), "+v"(p3) :: "memory");
        const f32x4 cv = (p0 + p1) + (p2 + p3);
        const auto s0 = __builtin_amdgcn_permlane16_swap(__float_as_uint(cv[0]), __float_as_uint(cv[1]), false, false), s1 = __builtin_amdgcn_permlane16_swap(__float_as_uint(cv[2]), __float_as_uint(cv[3]), false, false);
        const auto s2 = __builtin_amdgcn_permlane32_swap(__float_as_uint(__uint_as_float(s0[0]) + __uint_as_float(s0[1])), __float_as_uint(__uint_as_float(s1[0]) + __uint_as_float(s1[1])), false, false);
        ds_w32(cw, __float_as_int(__uint_as_float(s2[0]) + __uint_as_float(s2[1])));
        __builtin_amdgcn_sched_barrier(0);
      }
      auto lda = [&](int i) { if constexpr (LD) ldd(i); else { const int k = i % WI;
          if constexpr (LP) { if (i < 4) __builtin_amdgcn_raw_ptr_buffer_load_lds(rC, LDSP(lds0 + CPB + wave * 4096 + i * 1024), 16, lane * 16, (tn * 4 + wn * 2 + (wave >> 1)) * 3584 + i * 1024, 0, 16); }
          if (k < 3 || wave < 2) __builtin_amdgcn_raw_ptr_buffer_load_lds((k >= 2 || (k == 1 && wave == 3)) ? RSRC(a.W1) : RSRC(a.W0), LDSP(wd + wc[k]), 16, wsrc[k], wso, 0, 0); } };
      auto ldb = [&](int i) {
          if constexpr (LX) __builtin_amdgcn_raw_ptr_buffer_load_lds(RSRC(a.X), LDSP(xd + i * 4096), 16, xsrc[i % XI], xso + (i / XI) * (NXI / 2) * SH, 0, 16);
          else if constexpr (LD) ldd(8 + i); };
      using NLA = IC<(LW || LD) ? 8 : 0>; using NLB = IC<(LX || LD) ? 8 : 0>;
      auto pin1 = [](int& v) { asm volatile("" : "+s"(v)); };
      stepg(CAc, fb, fa, cc, I0{}, IC<LDR ? 2 : 0>{}, IC<LDR ? 1 : -1>{}, [&]() { if constexpr (LD) lds_barrier(); else raw_barrier(); }, NLA{}, lda, [&](auto ic) { constexpr int I = decltype(ic)::value;
        if constexpr (LW && I == 12) { wd += WU; wso += 32 * LDW * 2; pin1(wd); pin1(wso); }
        if constexpr (LW && I == 24) { wd += dW > 0 ? -3 * WU : WU; pin1(wd); }
        if constexpr (LW && I == 25) { wso += 32 * LDW * 2; pin1(wso); }
        if constexpr (LW && I == 26) { wso = wso == 1024 * LDW * 2 ? 0 : wso; pin1(wso); }
        if constexpr (!LDR && I == 24) { evs >>= 1; asm volatile("" : "+s"(evs)); }
        if constexpr (I == 7) { dX = xs == 2 ? -2 * XT : XT; pin1(dX); }
        if constexpr (I == 10) { xs = xs == 2 ? 0 : xs + 1; pin1(xs); } });
      stepg(CBc, fa, fb, cc, I1{}, I0{}, IC<LDR ? -1 : 25>{},
          [&]() { asm volatile("s_waitcnt lgkmcnt(0)" ::: "memory"); if (decltype(WAITc)::value == 0 || j + 1 < NT) raw_barrier(); }, NLB{}, ldb, [&](auto ic) { constexpr int I = decltype(ic)::value;
        if constexpr (LP && I == 10) { lc = lane_id(); ca = (lc & 15) < 14 ? (lc & 15) * 16 : 0; asm volatile("" : "+v"(ca)); }
        if constexpr (LP && I == 12) { cw = lds0 + RING + 4096 + (wave >> 1) * 448 + wn * 224 + ca + (lc >> 4) * 4; asm volatile("" : "+v"(cw)); }
        if constexpr (LP && I == 14) { ca += lds0 + CPB + wave * 4096 + (lc >> 4) * 896; asm volatile("" : "+v"(ca)); }
        if constexpr (LP && I == 16) { dw = dn_wsrc(lc, wave); asm volatile("" : "+v"(dw)); }
        if constexpr (LP && I == 19) { dwb = dn_wbase(wave); pin1(dwb); }
        if constexpr (I == 8) { cc.xa += dX; asm volatile("" : "+v"(cc.xa)); }
        if constexpr (I == (LDR ? 24 : 22)) { cc.wb[0] += dW; cc.wb[1] += dW; asm volatile("" : "+v"(cc.wb[0]), "+v"(cc.wb[1])); }
        if constexpr (I == (LDR ? 25 : 23)) { dW = -dW; pin1(dW); }
        if constexpr (LX && I == 25) { xso = ((xso | 0x2000) + 2048) & 0xd800; pin1(xso); }
        if constexpr (LX && I == 26) { xd += xs == 1 ? -2 * XT : XT; pin1(xd); } });
    };
    using WX_ = IC<2 * XI>;
    if constexpr (LDR) {
      for (int j = 1; j < NT - 2; ++j) iter(j, T{}, T{}, WX_{}, F{}, F{});
      iter(NT - 2, T{}, F{}, WX_{}, F{}, F{});
      __builtin_amdgcn_s_setprio(3);
      iter(NT - 1, F{}, F{}, I0{}, F{}, F{});
    } else {
      int j = 1;
      if (wave == 4) {
        wait_vm<0>();
        int fv = ds_r32(lds0 + TCH + 256 + lane * 4);
        const Tag tg = tag_get(lds0);
        asm volatile("" : "+v"(fv));
        if (!words_up((unsigned)fv, tg)) wait_flags(lds0, a.sync, F_H + F_PAN(panel), 32, 0);
      }
      raw_barrier();
#pragma unroll 1
      for (; j < js; ++j) iter(j, F{}, F{}, I0{}, F{}, F{});
      const int ja = wm == 2 ? js + 2 : js;
#pragma unroll 1
      for (; j < ja; ++j) iter(j, F{}, F{}, I0{}, T{}, F{});
#pragma unroll 1
      for (; j < js + 2; ++j) iter(j, F{}, F{}, I0{}, F{}, T{});
      { int lx = lane; asm volatile("" : "+v"(lx));
        __builtin_amdgcn_raw_buffer_store_b128(__builtin_bit_cast(u32x4, accs), rC, (lx & 15) < NF ? (unsigned)((tn * 4 + wn * 2) * 3584 + (tm * 2 + (wm & 1)) * 224
                + ((lx & 15) < 6 ? (lx & 1) * 3584 + ((lx & 15) >> 1) * 64 + (lx >> 4) * 16 : (lx >> 5) * 3584 + 192 + (lx & 16))) : 0x80000000u, 0, 16); }
#pragma unroll 1
      for (; j < NT; ++j) iter(j, F{}, F{}, I1{}, F{}, F{});
    }
    done_inc();
    TS4(17);
    step(fb, fa, cc, F{}, IC<NF>{}, [&](int i) { tabs(i - 1); if constexpr (LDR) { if (i > 0) ldd(15 + i); } });
    tabs(8);
    TS4(18);
  };
  if (wave < NLW) run(T{}); else run(F{});
  epilogue();
  STAMP(7);
  STAMP(8);
  STAMPW(12, 4);
  STAMP(3);
#endif
}

struct FragW { s16x4 lo[4], hi[4]; };
template <int MODE, int LO, int HI>
__device__ __forceinline__ void wo_phase(const GemmArgs& a, const GemmArgs& nx, int pol = 0) {
#if defined(__HIP_DEVICE_COMPILE__)
  extern __shared__ __attribute__((aligned(16))) char smem[];
  if constexpr (MODE == 2) STAMP(0);
  const unsigned lds0 = (unsigned)(size_t)(lds_char_ptr)smem;
  const int tid = threadIdx.x, lane = tid & 63;
  const int wave = __builtin_amdgcn_readfirstlane(tid >> 6);
  const int bid = blockIdx.x, xcd = bid & 7, idx = bid >> 3;
  const int tm = (xcd >> 1) * 4 + (idx >> 3), tn = (xcd & 1) * 8 + (idx & 7);
  const int m0 = tm * 128, panel = m0 >> 8, rot = WO_ROT;
  if constexpr (MODE & 1) {
    const auto rW = __builtin_amdgcn_make_buffer_rsrc((void*)a.W0, 0, 0x7fffffff, 0x00020000);
    const int row = wave * 8 + (lane >> 3);
    const unsigned wsrc = (unsigned)(row * D_ + tn * 64 + ((lane & 7) ^ wswz(row)) * 8) * 2u;
    static_for<LO, HI>([&](auto ic) { constexpr int i = decltype(ic)::value;
      __builtin_amdgcn_raw_ptr_buffer_load_lds(rW, (lds_void_ptr)(size_t)(lds0 + wo_pos(i >> 1) + (i & 1) * 8192 + wave * 1024), 16, wsrc, (((i & 1) * 8 + (i >> 1)) ^ rot) * (64 * D_ * 2), 0, 0);
      if constexpr (MODE == 3) __builtin_amdgcn_raw_ptr_buffer_load_lds(rW, LDSP(lds0 + wo_pos(i >> 1) + (i & 1) * 8192 + 4096 + wave * 1024), 16, wsrc, (((i & 1) * 8 + (i >> 1)) ^ rot) * (64 * D_ * 2) + 32 * D_ * 2, 0, 0); });
  } else {
    constexpr int PAD = 149504;
    const int g = wave >> 2, rw = wave & 3;
    const int fr = lane & 15, fq = lane >> 4, wq = (lane >> 2) & 3, wp = lane & 3;
    unsigned wa[4];
#pragma unroll
    for (int c = 0; c < 4; ++c) { const int row = 8 * fq + wq; wa[c] = lds0 + g * 8192 + row * 128 + (((c << 1) ^ wswz(row)) << 4) + wp * 8; }
    FragW f[16];
    auto rd = [&](auto ic, auto rc) {
      constexpr int i = decltype(ic)::value, r = decltype(rc)::value, P = wo_pos(i >> 1) + (i & 1) * 4096, B = P & ~32767, o = P & 32767;
      if constexpr (r & 1) f[i].hi[r >> 1] = ds_tr<o + 512>(wa[r >> 1] + B); else f[i].lo[r >> 1] = ds_tr<o>(wa[r >> 1] + B);
    };
    auto pin = [&](FragW& q) {
#pragma unroll
      for (int n = 0; n < 4; ++n) asm volatile("" : "+v"(q.lo[n]), "+v"(q.hi[n]));
    };
    static_for<0, 16>([&](auto rc) { constexpr int r = decltype(rc)::value; rd(IC<r / 8>{}, IC<r % 8>{}); });
    const Tag tg = tag_get(lds0);
    pin(f[0]); pin(f[1]);
    if (wave == 2) {
      unsigned long long c0, c1; CODE_ADDR(c0, "k_all"); CODE_ADDR(c1, "k_zend");
      __builtin_amdgcn_raw_ptr_buffer_load_lds(RSRCN(c0, c1 - c0 < 262144ull ? (unsigned)(c1 - c0) : 0u), LDSP(lds0 + PAD), 4, (unsigned)((lane * 32 + idx) * 128), 0, 0, 0); }
    if (wave == 3) {
      const unsigned fo = (unsigned)(((lane & 32) ? F_PAN((xcd >> 2) * 4 + (idx >> 3)) : F_PAN(panel)) + (lane & 31) * 8);
      while (!flags_up(__builtin_amdgcn_raw_buffer_load_b64(RSRC(a.sync), fo, F_MARK, 16), 64, tg, 0) && in_time(lds0)) {}
    }
    const auto rX = __builtin_amdgcn_make_buffer_rsrc((void*)a.X, 0, 0x7fffffff, 0x00020000);
    const int rb = (m0 >> 4) + rw * 2 + g;
    unsigned xs[2] = {(unsigned)(rb_h(rb, pol) + lane * 16), (unsigned)(rb_h(rb ^ 1, pol) + lane * 16)};
    const int ck = (fq & 1) * 2 + (fq >> 1);
    bf16x8 s0 = pick(fq == (fr >> 3), fr & 7), s1 = pick(fq == 2 + (fr >> 3), fr & 7);
    asm volatile("" : "+v"(s0), "+v"(s1));
    unsigned orow = (unsigned)((rb * 16 + fr) * (D_ * 2) + tn * 128 + fq * 16), ofrg = (unsigned)(rb_h(rb, pol) + ko(tn * 2048) + ck * 256 + fr * 16);
    asm volatile("" : "+v"(xs[0]), "+v"(xs[1]), "+v"(orow), "+v"(ofrg));
    u32x4 res[2];
    STAMP(9);
    const int fo_off = F_O(tm) + rw * 256 + ((g ^ (rot >> 3)) & 1) * 128;
    const unsigned first = (rot & 4) ? 0xff00u : 0x00ffu;
    auto down = [&](u32x2 v) { return (unsigned)__builtin_amdgcn_ballot_w64(v[0] != tg.lo || v[1] != tg.hi) & 0xffffu; };
    unsigned dn, polls = 0;
    do { dn = down(flag_get(a.sync, fo_off, 16)); asm volatile("" ::: "memory"); } while ((dn & first) && ((++polls & 1023u) || in_time(lds0)));
    STAMP(6);
    u32x2 fl = {0u, 0u};
    u32x4 xr[16][2];
    auto ldX = [&](int i, int m) {
      if (i >= 0 && i < 16) xr[i][m] = __builtin_amdgcn_raw_buffer_load_b128(rX, xs[m] + (i & 1) * 1024, ko(((g * 8 + (i >> 1)) ^ rot) * 2048), 16);
      else if (m == 1 && (i == 16 || i == 17)) res[i - 16] = __builtin_amdgcn_raw_buffer_load_b128(RSRC(a.R), orow + (i - 16) * 64, 0, 0);
    };
    constexpr int XD = 3;
#pragma unroll
    for (int i = 0; i < XD; ++i) { ldX(i, 1); ldX(i, 0); __builtin_amdgcn_sched_barrier(0); }
    unsigned gsrc[2];
#pragma unroll
    for (int i = 0; i < 2; ++i) {
      const int j = wave + 8 * i, jj = j >= 7 ? j - 7 : j;
      const int b = jj * 1024 + lane * 16, q = b / 224, pos = (b % 224) >> 4;
      gsrc[i] = (unsigned)((8 * ((q & 15) >> 2) + (q & 3) + 4 * (q >> 4)) * DF_ + ((xcd & 3) * 8 + (idx & 7)) * 112 + pos * 8) * 2u;
      asm volatile("" : "+v"(gsrc[i]));
    }
    f32x4 acc[4][2];
#pragma unroll
    for (int n = 0; n < 4; ++n) { acc[n][0] = (f32x4){0.f, 0.f, 0.f, 0.f}; acc[n][1] = acc[n][0]; }
    __builtin_amdgcn_sched_barrier(0);
    f32x4 pa[4];
    auto slot = [&](int w) { return wo_pos(0) + w * 4096 + lane * 16; };
    static_for<0, 17>([&](auto ic) {
      constexpr int i = decltype(ic)::value;
      if constexpr (i < 16 && !(i & 1)) __builtin_amdgcn_s_setprio(3 - ((i >> 1) & 3));
      if constexpr (i >= 2 && i < 16) { asm volatile("s_waitcnt lgkmcnt(%0)" :: "n"(i < 15 ? 8 : 0) : "memory"); pin(f[i]); }
      if constexpr (i == WO_E - 2) raw_barrier();
      if constexpr (i == 8 - XD - 2) fl = flag_get(a.sync, fo_off, 16);
      if constexpr (i == 8 - XD) { if (__builtin_expect(dn && down(fl), 0)) wait_flags(lds0, a.sync, fo_off, 16, 0); }
      if constexpr (i == 16) {
        STAMP(2);
#pragma unroll
        for (int n = 0; n < 4; ++n) *(f32x4*)(smem + slot(wave) + n * 1024) = acc[n][1];
        __builtin_amdgcn_s_setprio(1);
        __builtin_amdgcn_sched_barrier(0);
        lds_barrier();
        STAMP(4);
        static_for<0, 4>([&](auto nc) { constexpr int n = decltype(nc)::value; pa[n] = __builtin_bit_cast(f32x4, ds_b128<n * 1024>(lds0 + slot(wave ^ 4))); });
        __builtin_amdgcn_sched_barrier(0);
      }
      static_for<0, 8>([&](auto jc) {
        constexpr int j = decltype(jc)::value, n = j & 3, m = 1 - (j >> 2), t = i;
        if constexpr (t >= 0 && t < 16) acc[n][m] = MFMA(__builtin_shufflevector(f[t].lo[n], f[t].hi[n], 0, 1, 2, 3, 4, 5, 6, 7), __builtin_bit_cast(bf16x8, xr[t][m]), acc[n][m]);
        if constexpr (m == 0 && t == 16) acc[n][0] = MFMA((n & 1) ? s1 : s0, __builtin_bit_cast(bf16x8, res[n >> 1]), acc[n][0]);
        if constexpr (i + 2 < 16) rd(IC<i + 2>{}, jc);
        if constexpr (j == 1) ldX(i + XD, 1);
        if constexpr (j == 5) ldX(i + XD, 0);
        __builtin_amdgcn_sched_barrier(0);
      });
      if constexpr (i == 0) STAMP(1);
    });
    auto ldG = [&](int i, int u) { const int j = wave + 8 * i;
      __builtin_amdgcn_raw_ptr_buffer_load_lds(RSRC(j >= 7 ? nx.W1 : nx.W0), LDSP(lds0 + 98304 + (u ^ 2) * 14592 + (j >= 7 ? 7184 + (j - 7) * 1024 : j * 1024)), 16, gsrc[i], (u + (xcd >> 2) * 16) * 32 * DF_ * 2, 0, 0); };
    auto ldG2 = [&](int u) { ldG(0, u); ldG(0, u + 1); if (wave < 6) { ldG(1, u); ldG(1, u + 1); } };
    asm volatile("s_waitcnt lgkmcnt(0)" : "+v"(pa[0]), "+v"(pa[1]), "+v"(pa[2]), "+v"(pa[3]) :: "memory");
    const auto rO = __builtin_amdgcn_make_buffer_rsrc((void*)a.O, 0, 0x7fffffff, 0x00020000);
#pragma unroll
    for (int i = 0; i < 2; ++i) {
      pa[2 * i] += acc[2 * i][0]; pa[2 * i + 1] += acc[2 * i + 1][0];
      const f32x4 u = pa[2 * i], v = pa[2 * i + 1];
      const auto t0 = __builtin_amdgcn_permlane16_swap(pk_bf16(u[0], u[1]), pk_bf16(v[0], v[1]), false, false);
      const auto t1 = __builtin_amdgcn_permlane16_swap(pk_bf16(u[2], u[3]), pk_bf16(v[2], v[3]), false, false);
      __builtin_amdgcn_raw_buffer_store_b128((u32x4){t0[0], t1[0], t0[1], t1[1]}, rO, ofrg + i * 1024, 0, 16);
    }
    __builtin_amdgcn_sched_barrier(0);
    __builtin_amdgcn_s_setprio(0);
    auto sum4 = [](float v) {
      auto p = __builtin_amdgcn_permlane16_swap(__float_as_uint(v), __float_as_uint(v), false, false); v = __uint_as_float(p[0]) + __uint_as_float(p[1]);
      auto q = __builtin_amdgcn_permlane32_swap(__float_as_uint(v), __float_as_uint(v), false, false); return __uint_as_float(q[0]) + __uint_as_float(q[1]); };
    f32x2 e[8], sm = {0.f, 0.f};
#pragma unroll
    for (int j = 0; j < 8; ++j) { e[j] = (f32x2){pa[j >> 1][(j & 1) * 2], pa[j >> 1][(j & 1) * 2 + 1]}; sm += e[j]; }
    const float mu = sum4(sm[0] + sm[1]) * (1.f / 64.f);
    f32x2 t2 = {0.f, 0.f};
#pragma unroll
    for (int j = 0; j < 8; ++j) { const f32x2 d = e[j] - mu; t2 += d * d; }
    const float q2 = sum4(t2[0] + t2[1]);
    __builtin_amdgcn_raw_buffer_store_b64((u32x2){__float_as_uint(mu), __float_as_uint(q2)}, __builtin_amdgcn_make_buffer_rsrc((void*)a.st, 0, 0x7fffffff, 0x00020000),
                                          lane < 16 ? (unsigned)((tn * S_ + rb * 16 + lane) * 8) : 0x80000000u, 0, 16);
    STAMP(7);
    STAMP(8);
    ldG2(0);
    ldG2(2);
    if (wave < 6) wait_vm<8>(); else wait_vm<4>();
    lds_barrier();
    if (wave == 0) flag_set(a.sync, F_H + F_PAN(panel) + ((tm & 1) * 16 + tn) * 8, tg, 0);
    STAMP(3);
  }
#endif
}

struct FragD { s16x4 lo[8], hi[8]; };
enum { O_X, O_W, O_SP, O_FA, O_FP, O_QC, O_QD, O_QP, O_RES, O_LP, O_CC, O_CD, O_CP, O_BAR, O_AR, O_AP };
constexpr int DN_A = 16, DN_E = 0, DN_PRE = 11;
constexpr unsigned char DN_OPS[][4] = {
 {0,10,O_CC,1}, {0,11,O_X,6}, {0,13,O_X,7}, {0,15,O_X,8}, {1,2,O_X,9}, {1,5,O_X,10}, {1,9,O_X,11}, {1,13,O_W,11}, {2,0,O_FA,2}, {2,2,O_X,12}, {2,5,O_X,13}, {2,9,O_X,14}, {2,13,O_W,12},
 {3,2,O_X,15}, {3,5,O_X,16}, {3,9,O_X,17}, {3,13,O_X,18}, {4,2,O_BAR,12}, {4,3,O_X,19}, {4,7,O_W,14}, {4,12,O_W,13}, {4,16,O_QC,2},
 {5,2,O_W,17}, {5,7,O_W,16}, {5,12,O_W,15}, {6,0,O_CC,2}, {6,2,O_X,20}, {6,5,O_X,21}, {6,9,O_X,22}, {6,13,O_W,18}, {7,2,O_X,23}, {7,5,O_X,24}, {7,9,O_X,25}, {7,13,O_W,19},
 {8,2,O_X,26}, {8,5,O_X,27}, {8,9,O_X,28}, {8,13,O_W,20}, {9,2,O_X,29}, {9,5,O_X,30}, {9,9,O_X,31}, {9,13,O_W,21}, {10,2,O_X,32}, {10,7,O_W,23}, {10,12,O_W,22},
 {11,2,O_X,33}, {11,5,O_X,34}, {11,9,O_X,35}, {11,13,O_X,36}, {11,16,O_QD,0}, {12,2,O_BAR,23}, {12,3,O_X,37}, {12,6,O_X,38}, {12,10,O_X,39}, {12,13,O_X,40},
 {13,2,O_X,41}, {13,5,O_X,42}, {13,9,O_X,43}, {13,13,O_W,24}, {14,0,O_CD,0}, {14,2,O_X,44}, {14,5,O_X,45}, {14,9,O_W,25}, {14,13,O_RES,0},
 {15,2,O_X,46}, {15,7,O_X,47}, {15,12,O_W,26}, {16,2,O_X,48}, {16,5,O_X,49}, {16,9,O_W,27}, {16,13,O_RES,1}, {17,2,O_X,50}, {17,5,O_X,51}, {17,9,O_X,52}, {17,13,O_W,28},
 {18,2,O_X,53}, {18,9,O_W,29}, {19,2,O_W,31}, {19,9,O_W,30}, {19,16,O_AR,0}, {20,2,O_SP,0}, {20,9,O_SP,1}, {21,2,O_X,54}, {21,5,O_X,55}, {21,9,O_SP,2}, {21,13,O_SP,3},
 {22,2,O_X,56}, {22,5,O_X,57}, {22,9,O_X,58}, {22,13,O_X,59}, {23,0,O_FP,0}, {23,2,O_BAR,31}, {23,3,O_X,60}, {23,6,O_X,61}, {23,10,O_X,62}, {23,13,O_X,63},
 {24,0,O_FP,1}, {24,1,O_SP,4}, {24,2,O_SP,5}, {25,8,O_QP,0}, {26,0,O_FP,2}, {27,0,O_CP,0}, {27,0,O_CP,1}, {27,2,O_LP,0}, {27,5,O_LP,1},
 {28,2,O_LP,2}, {28,5,O_LP,3}, {28,8,O_QP,2}, {30,0,O_CP,2}, {30,2,O_LP,4}, {30,5,O_LP,5}, {32,0,O_AP,0}, {32,0,O_AP,1}, {32,0,O_AP,2}, {32,0,O_AP,3}, {32,0,O_AP,4}, {32,0,O_AP,5},
};
constexpr int DN_N = sizeof(DN_OPS) / 4, DN_H = (24 - DN_A) / 2, DN_B = DN_A + DN_H, BB = 24 + DN_E, NS = BB + 8;
constexpr int dn_nm(int v) { return v < 24 - DN_E ? 16 : 8; }
constexpr int dn_unit(int v, int n) { return v < DN_A ? v : (v >= BB ? v - DN_E : DN_A + 2 * (((v < 24 ? v : v - DN_E) - DN_A) % DN_H) + (n >> 2)); }
constexpr int dn_wpos(int u, int h) { constexpr int P[19] = {8, 9, 10, 11, 16, 17, 18, 4, 5, 6, 7, 14, 15, 0, 1, 2, 3, 12, 13}; return P[u % 19] * 8192 + h * 4096; }
constexpr int dn_first(int v, int g) { int i = 0; while (i < DN_N && (DN_OPS[i][0] < v || (DN_OPS[i][0] == v && DN_OPS[i][1] < g))) ++i; return i; }
constexpr int dn_find(int kind, int arg) { for (int i = 0; i < DN_N; ++i) if (DN_OPS[i][2] == kind && DN_OPS[i][3] == arg) return i; return -1; }
constexpr int dn_vm(int lo, int hi) { int c = 0; for (int i = lo; i < hi; ++i) c += DN_OPS[i][2] <= O_LP; return c; }
constexpr bool dn_bar(int v) { for (int i = 0; i < DN_N; ++i) if (DN_OPS[i][0] == v && DN_OPS[i][2] == O_BAR) return true; return false; }
constexpr int dn_cnt(int w, int i) { int p = -1; for (int u = DN_PRE; u <= w; ++u) if (dn_find(O_W, u) > p) p = dn_find(O_W, u); return p >= 0 ? dn_vm(p + 1, i) : 63; }
constexpr bool dn_ok() {
  int have = DN_PRE - 1, bstep = 0;
  for (int v = 0; v + 1 < NS; ++v) {
    for (int i = dn_first(v, 0); i < dn_first(v + 1, 0); ++i) {
      if (DN_OPS[i][2] == O_BAR) { if (DN_OPS[i][3] > have) have = DN_OPS[i][3]; bstep = v; for (int u = DN_PRE; u <= have; ++u) if (dn_find(O_W, u) < 0 || dn_find(O_W, u) > i) return false; }
      if (DN_OPS[i][2] == O_W) {
        if (bstep < 1 && DN_OPS[i][3] > 12) return false;
        for (int t = bstep + 1; t < NS; ++t) for (int n = 0; n < 8; n += 4) if (dn_unit(t, n) < DN_OPS[i][3] && dn_wpos(dn_unit(t, n), 0) == dn_wpos(DN_OPS[i][3], 0)) return false;
      }
    }
    if (dn_unit(v + 1, 7) > have) return false;
  }
  for (int x = 6; x < 64; ++x) {
    int n = 0; for (int i = 0; i < dn_find(O_X, x); ++i) { if (DN_OPS[i][2] == O_CC) n = DN_OPS[i][3]; if (DN_OPS[i][2] == O_CD) n = 4; }
    if (n < (x < 20 ? 1 : (x < 48 ? 2 : 4))) return false;
  }
  if (dn_find(O_FA, 2) < 0) return false;
  return dn_find(O_AR, 0) < dn_find(O_SP, 0) && dn_find(O_AR, 0) > dn_find(O_RES, 1) && dn_find(O_SP, 0) >= dn_first(DN_B, 0) && dn_find(O_SP, 4) >= dn_first(24, 0);
}
static_assert(dn_ok(), "schedule");
constexpr int dn_rd(int v, int I) { int c = 0; const int r0 = dn_bar(v) ? 2 : 1; if (v + 1 < NS) for (int r = 0; r < 16; ++r) c += r0 + r * (dn_nm(v) - r0 + 1) / 16 <= I; return c; }
struct DnTab { short first[NS + 1][18], rd[NS][18], wait[DN_N]; };
constexpr DnTab dn_tab() {
  DnTab t = {};
  for (int v = 0; v <= NS; ++v) for (int g = 0; g < 18; ++g) { t.first[v][g] = dn_first(v, g); if (v < NS) t.rd[v][g] = dn_rd(v, g); }
  for (int i = 0; i < DN_N; ++i) {
    const int kind = DN_OPS[i][2], arg = DN_OPS[i][3];
    t.wait[i] = kind == O_CC ? 1 + dn_vm(0, i) : kind == O_FA ? dn_vm(0, i) : (kind == O_FP ? dn_vm(dn_find(O_SP, 2 * arg + 1) + 1, i) : (kind == O_BAR ? dn_cnt(arg, i) : 0));
  }
  return t;
}
constexpr DnTab DT = dn_tab();
constexpr int dn_uj(int v) { return v < 3 ? 0 : 1 + (v - 3) % 7; }
constexpr int dn_us(int v) { return v < 3 ? v : (v - 3) / 7; }
template <int U>
__device__ __forceinline__ void dn_first_w(const GemmArgs& a, unsigned wsrc, int wb) {
#if defined(__HIP_DEVICE_COMPILE__)
  extern __shared__ __attribute__((aligned(16))) char smem[];
  const unsigned lds0 = (unsigned)(size_t)(lds_char_ptr)smem;
  const int wave = __builtin_amdgcn_readfirstlane(threadIdx.x >> 6);
  __builtin_amdgcn_raw_ptr_buffer_load_lds(RSRC(a.W0), LDSP(lds0 + dn_wpos(U >> 1, U & 1) + wave * 1024), 16, wsrc,
                                           (wb + (((blockIdx.x >> 3) + dn_uj(U >> 1)) & 7) * (112 * D_ * 2) + dn_us(U >> 1) * (16 * D_ * 2)) ^ ((U & 1) << 7), 0, 0);
#endif
}
__device__ __forceinline__ void dn_phase(const GemmArgs& a, const Late& late, int pol) {
#if defined(__HIP_DEVICE_COMPILE__)
  extern __shared__ __attribute__((aligned(16))) char smem[];
  STAMP(0);
  const unsigned lds0 = (unsigned)(size_t)(lds_char_ptr)smem;
  const int lane = lane_id();
  const int wave = __builtin_amdgcn_readfirstlane(threadIdx.x >> 6);
  const int bid = blockIdx.x, xcd = bid & 7, idx = bid >> 3;
  const int panel = (xcd >> 2) * 4 + (idx >> 3), c = idx & 7, q = xcd & 3;
  const auto rW = RSRC(a.W0);
  const unsigned wsrc = dn_wsrc(lane, wave);
  int wb = dn_wbase(wave) ^ ((wave >> 2) << 7), wb2 = (wave * 56 + 48) * (D_ * 2) + c * 256 + ((q >> 1) << 7), xb = q * 8 * 3072;
  asm volatile("" : "+s"(wb), "+s"(wb2), "+s"(xb));
  auto ldW = [&](int v) {
    __builtin_amdgcn_raw_ptr_buffer_load_lds(rW, LDSP(lds0 + dn_wpos(v, 0) + wave * 1024), 16, wsrc,
                                             v < 24 ? wb + ((c + dn_uj(v)) & 7) * (112 * D_ * 2) + dn_us(v) * (16 * D_ * 2) : wb2 + ((v + c) & 7) * (448 * D_ * 2), 0, 0);
  };
  const int fa_off = F_ACT(panel) + (wave >> 1) * 512;
  const Tag tg = {(unsigned)__builtin_amdgcn_readfirstlane(late.tag[0]), (unsigned)__builtin_amdgcn_readfirstlane(late.tag[1])};
  const int fr = lane & 15, fq = lane >> 4, wq = (lane >> 2) & 3, wp = lane & 3;
  const int rb = wave * 2 + (q & 1);
  f32x4 acc[8][2];
#pragma unroll
  for (int n = 0; n < 8; ++n) { acc[n][0] = (f32x4){0.f, 0.f, 0.f, 0.f}; acc[n][1] = acc[n][0]; }
  unsigned wa[3][4];
#pragma unroll
  for (int i = 0; i < 4; ++i) { const int row = 8 * fq + wq; wa[0][i] = lds0 + row * 128 + (((i << 1) ^ wswz(row)) << 4) + wp * 8; wa[1][i] = wa[0][i] + 65536; wa[2][i] = wa[0][i] + 131072; }
  const auto rX = RSRC(a.X);
  const unsigned xs[2] = {(unsigned)((panel * 16 + rb) * SA + lane * 16), (unsigned)((panel * 16 + (rb ^ 1)) * SA + lane * 16)};
  FragD fa, fb;
  u32x4 xr[32][2];
  auto rdw = [&](FragD& f, auto vc, auto rc) {
    constexpr int v = decltype(vc)::value, r = decltype(rc)::value, n = r >> 1, u = dn_unit(v, n);
    constexpr int p = dn_wpos(u, (v < DN_A || v >= BB) ? n >> 2 : (v < DN_B ? 1 : 0)), o = (p & 65535) + (r & 1) * 512, b = p >> 16;
    if constexpr (r & 1) f.hi[n] = ds_tr<o>(wa[b][n & 3]); else f.lo[n] = ds_tr<o>(wa[b][n & 3]);
  };
  auto ldX = [&](int v, int m) {
    if (v < 24) xr[v][m] = __builtin_amdgcn_raw_buffer_load_b128(rX, xs[m] + dn_us(v) * 1024, xb + ((c + dn_uj(v)) & 7) * 3072, 16);
    else xr[v][m] = __builtin_amdgcn_raw_buffer_load_b128(rX, xs[0] + m * 1024, 384 * 256 + ((v + c) & 7) * 2048, 16);
  };
  const auto rS = RSRC(a.slab);
  const int ck = (fq & 1) * 2 + (fq >> 1);
  u32x4 pp[3][2], res[2];
  bf16x8 sp0, sp1;
  int sl0 = 0, fp_off = 0; unsigned v1 = 0, v3 = 0; u32x2 tv = late.tag;
  auto addP = [&](int j) { acc[2 * (j & 1)][0] = MFMA(sp0, __builtin_bit_cast(bf16x8, pp[j >> 1][j & 1]), acc[2 * (j & 1)][0]); acc[2 * (j & 1) + 1][0] = MFMA(sp1, __builtin_bit_cast(bf16x8, pp[j >> 1][j & 1]), acc[2 * (j & 1) + 1][0]); };
  auto ldP = [&](int j, bool ok) { pp[j >> 1][j & 1] = __builtin_amdgcn_raw_buffer_load_b128(RSRCN(a.slab, ok ? 0x7fffffff : 0), lane * 16 + (j & 1) * 1024, sl0 + (j >> 1) * (4 << 20), 16); };
  auto st128 = [&](u32x4 v, auto rs, unsigned vo, int so) {
    __builtin_amdgcn_raw_buffer_store_b128(v, rs, vo, so, 16);
    __builtin_amdgcn_sched_barrier(0);
    asm volatile("s_nop 1" ::: "memory");
    __builtin_amdgcn_sched_barrier(0); };
  auto stP = [&](int j) {
    const int k = 3 - (j >> 1), i = j & 1;
    const f32x4 s0 = acc[(k >> 1) * 4 + 2 * i][k & 1], s1 = acc[(k >> 1) * 4 + 2 * i + 1][k & 1];
    st128((u32x4){pk_bf16(s0[0], s0[1]), pk_bf16(s0[2], s0[3]), pk_bf16(s1[0], s1[1]), pk_bf16(s1[2], s1[3])}, rS, lane * 16 + i * 1024, (sl0 ^ (k << 15)) + (j >> 1) * (4 << 20)); };
  unsigned got = 0;
  unsigned orow = 0;
  u32x2 fl = {0u, 0u};
#define TS5(i)
  auto op = [&](auto ic) {
    constexpr int i = decltype(ic)::value, kind = DN_OPS[i][2], arg = DN_OPS[i][3];
    if constexpr (kind == O_X) ldX(arg >> 1, arg & 1);
    if constexpr (kind == O_W) ldW(arg);
    if constexpr (kind == O_SP) stP(arg);
    if constexpr (kind == O_FA) { wait_vm<DT.wait[i]>(); flag_set(a.sync, fa_off + ((q * 8 + c) * 2 + (wave & 1)) * 8 + (arg - 1) * 16384, tg, 0); }
    if constexpr (kind == O_FP) { wait_vm<DT.wait[i]>(); if constexpr (arg) STAMP(10 + arg); __builtin_amdgcn_raw_buffer_store_b64(tv, RSRC(a.sync), v1 + arg * 8, fp_off ^ ((3 - arg) << 9), 16); }
    if constexpr (kind == O_QC) fl = flag_get(a.sync, fa_off + q * 128 + (arg - 1) * 16384, 16);
    if constexpr (kind == O_QD) fl = flag_get(a.sync, fa_off + 16384, 64);
    if constexpr (kind == O_QP) fl = __builtin_amdgcn_raw_buffer_load_b64(RSRC(a.sync), v3, fp_off, 16);
    if constexpr (kind == O_RES) res[arg] = __builtin_amdgcn_raw_buffer_load_b128(RSRC(a.R), lane * 16 + arg * 1024, rb_h(panel * 16 + rb, pol) + ko(c * 4096 + (q >> 1) * 2048), 16);
    if constexpr (kind == O_AR) {
      const bf16x8 s0 = pick(fq == (fr >> 3), fr & 7), s1 = pick(fq == 2 + (fr >> 3), fr & 7);
#pragma unroll
      for (int n = 0; n < 4; ++n) acc[n][0] = MFMA((n & 1) ? s1 : s0, __builtin_bit_cast(bf16x8, res[n >> 1]), acc[n][0]);
      sp0 = pick(fq == (fr >> 2), fr & 3); sp1 = pick(fq == (fr >> 2), 4 + (fr & 3));
      orow = (unsigned)(((panel * 16 + rb) * 16 + (fr & 7)) * (D_ * 2) + c * 256 + (q >> 1) * 128 + ck * 16 + (fr & 8) * 8);
      sl0 = sl_at(bid, wave, pol); fp_off = F_PART(bid) + wave * 64; v1 = lane == 0 ? 0u : 0x80000000u; v3 = lane < 3 ? lane * 8u : 0x80000000u;
      asm volatile("" : "+v"(sp0), "+v"(sp1), "+v"(orow), "+s"(sl0), "+s"(fp_off), "+v"(v1), "+v"(v3), "+v"(tv));
    }
    if constexpr (kind == O_AP) addP(arg);
    if constexpr (kind == O_LP) ldP(arg, (got >> (arg >> 1)) & 1);
    if constexpr (kind == O_CC && arg == 1) {
      wait_vm<DT.wait[i]>();
      f32x2 f2 = ds_b64<0>(lds0 + FLP + wave * 128 + (lane & 15) * 8);
      asm volatile("s_waitcnt lgkmcnt(0)" : "+v"(f2) :: "memory");
      fl = __builtin_bit_cast(u32x2, f2);
    }
    if constexpr (kind == O_CC) { if (__builtin_expect(!flags_up(fl, 16, tg, 0), 0)) wait_flags(lds0, a.sync, fa_off + q * 128 + (arg - 1) * 16384, 16, 0); if constexpr (arg == 1) STAMP(4); }
    if constexpr (kind == O_CD) { if (__builtin_expect(!flags_up(fl, 64, tg, 0), 0)) wait_flags(lds0, a.sync, fa_off + 16384, 64, 0); }
    if constexpr (kind == O_CP) got |= (unsigned)(__builtin_amdgcn_ballot_w64(fl[0] == tg.lo) & __builtin_amdgcn_ballot_w64(fl[1] == tg.hi)) & (1u << arg) & (got + 1u);
    if constexpr (kind == O_BAR) { wait_vm<DT.wait[i]>(); lds_barrier(); }
    __builtin_amdgcn_sched_barrier(0);
  };
  STAMP(9);
  lds_barrier();
  STAMP(6);
  {
    const unsigned e0 = lds0 + rb * 1024 + lane * 16, e1 = lds0 + (rb ^ 1) * 1024 + lane * 16;
    xr[0][0] = __builtin_bit_cast(u32x4, ds_b128<OWNE>(e0)); xr[0][1] = __builtin_bit_cast(u32x4, ds_b128<OWNE>(e1));
    xr[1][0] = __builtin_bit_cast(u32x4, ds_b128<OWNE + 16384>(e0)); xr[1][1] = __builtin_bit_cast(u32x4, ds_b128<OWNE + 16384>(e1));
    xr[2][0] = __builtin_bit_cast(u32x4, ds_b128<0>(e0 + OWNM)); xr[2][1] = __builtin_bit_cast(u32x4, ds_b128<0>(e1 + OWNM));
    static_for<0, 16>([&](auto rc) { rdw(fa, IC<0>{}, rc); });
  }
  static_for<0, NS>([&](auto vc) {
    constexpr int v = decltype(vc)::value, NM = dn_nm(v);
    FragD& cur = (v & 1) ? fb : fa; FragD& nxt = (v & 1) ? fa : fb;
    TS5(v);
    __builtin_amdgcn_s_setprio(3 - (v & 3));
    static_for<DT.first[v][0], DT.first[v][1]>(op);
    __builtin_amdgcn_sched_barrier(0);
    static_for<0, NM>([&](auto ic) {
      constexpr int I = decltype(ic)::value, n = NM == 16 ? I >> 1 : I, m = NM == 16 ? I & 1 : (v < 24 ? 1 : (v < BB ? 0 : I >> 2));
      if constexpr (I % (NM / 4) == 0) {
        constexpr int left = 12 - 2 * n + DT.rd[v][I];
        asm volatile("s_waitcnt lgkmcnt(%0)" :: "n"(left < 15 ? left : 15) : "memory");
        asm volatile("" : "+v"(cur.lo[n]), "+v"(cur.hi[n]), "+v"(cur.lo[n + 1]), "+v"(cur.hi[n + 1]));
        if constexpr (v == 0 && I == 0) {
#pragma unroll
          for (int i = 0; i < 3; ++i) asm volatile("" : "+v"(xr[i][0]), "+v"(xr[i][1]));
          STAMP(1);
        }
        __builtin_amdgcn_sched_barrier(0);
      }
      const bf16x8 w = __builtin_shufflevector(cur.lo[n], cur.hi[n], 0, 1, 2, 3, 4, 5, 6, 7);
      constexpr int an = v < DN_A ? n : (v < DN_B ? 4 : 0) + (n & 3), am = v < 24 ? m : 0;
      acc[an][am] = MFMA(w, __builtin_bit_cast(bf16x8, xr[dn_unit(v, n)][m]), acc[an][am]);
      __builtin_amdgcn_sched_barrier(0);
      static_for<DT.first[v][I + 1], DT.first[v][I + 2]>(op);
      static_for<DT.rd[v][I], DT.rd[v][I + 1]>([&](auto rc) { rdw(nxt, IC<v + 1>{}, rc); });
      __builtin_amdgcn_sched_barrier(0);
    });
    if constexpr (v == 23) STAMP(10);
  });
  STAMP(2);
  TS5(NS);
  const auto rF = RSRCN(a.sync, got != 7u ? 0x7fffffff : 0);
  u32x2 f0 = __builtin_amdgcn_raw_buffer_load_b64(rF, v3, fp_off, 16);
  __builtin_amdgcn_sched_barrier(0);
  static_for<DT.first[NS][0], DN_N>(op);
  if (got != 7u) {
    if (!flags_up(f0, 3, tg, 0)) {
      f0 = __builtin_amdgcn_raw_buffer_load_b64(rF, v3, fp_off, 16);
      asm volatile("s_sleep 5" ::: "memory");
      for (unsigned polls = 1;; ++polls) {
        const u32x2 f1 = __builtin_amdgcn_raw_buffer_load_b64(rF, v3, fp_off, 16);
        asm volatile("" ::: "memory");
        if (flags_up(f0, 3, tg, 0)) break;
        f0 = __builtin_amdgcn_raw_buffer_load_b64(rF, v3, fp_off, 16);
        asm volatile("" ::: "memory");
        if (flags_up(f1, 3, tg, 0) || (!(polls & 255u) && !in_time(lds0))) break;
      }
    }
#pragma unroll
    for (int j = 5; j >= 0; --j) ldP(j, !((got >> (j >> 1)) & 1));
    __builtin_amdgcn_sched_barrier(0);
#pragma unroll
    for (int j = 0; j < 6; ++j) addP(j);
  }
  TS5(NS + 1);
  STAMP(5);
  u32x4 o[2];
#pragma unroll
  for (int i = 0; i < 2; ++i) {
    const f32x4 u = acc[2 * i][0], w = acc[2 * i + 1][0];
    const auto s0 = __builtin_amdgcn_permlane16_swap(pk_bf16(u[0], u[1]), pk_bf16(w[0], w[1]), false, false);
    const auto s1 = __builtin_amdgcn_permlane16_swap(pk_bf16(u[2], u[3]), pk_bf16(w[2], w[3]), false, false);
    o[i] = (u32x4){s0[0], s1[0], s0[1], s1[1]};
  }
  {
#pragma unroll
    for (int e = 0; e < 4; ++e) {
      const int x0 = o[0][e], x1 = o[1][e];
      o[0][e] = __builtin_amdgcn_update_dpp(x0, x1, 0x128, 0xf, 0xc, false);
      o[1][e] = __builtin_amdgcn_update_dpp(x1, x0, 0x128, 0xf, 0x3, false);
    }
  }
#pragma unroll
  for (int i = 0; i < 2; ++i) __builtin_amdgcn_raw_buffer_store_b128(o[i], RSRC(a.O), orow, i * 8 * D_ * 2, 18);
  __builtin_amdgcn_sched_barrier(0);
  asm volatile("s_nop 1" ::: "memory");
  if (bid == 0 && wave == 0) flag_set(a.sync, EPOCH, tg, 0);
  STAMP(7);
  STAMP(8);
  STAMP(3);
#endif
}

struct Ptrs { const bf16_t *x, *wqkv, *wo, *wg, *wu, *wd; bf16_t* out; char* ws; };
#define DBG(i)
#define WS_O ((bf16_t*)(p.ws + (8u << 20)))
#define WS_SL ((float*)(p.ws + (48u << 20)))
#define WS_H ((bf16_t*)(p.ws + (16u << 20)))
#define WS_ST ((float*)(p.ws + (24u << 20)))
#define WS_CS ((float*)(p.ws + (25u << 20)))
#define WS_SY ((unsigned*)(p.ws + (26u << 20)))
#define WS_A ((bf16_t*)(p.ws + (28u << 20)))
#if defined(__HIP_DEVICE_COMPILE__)
#define ARG_WARM(v) do { asm volatile("s_load_dword %0, %1, 0x0" : "=s"(v) : "s"(__builtin_amdgcn_kernarg_segment_ptr())); __builtin_amdgcn_sched_barrier(0); } while (0)
#else
#define ARG_WARM(v)
#endif
struct Args4 { GemmArgs q, b, c, d; };
extern "C" __global__ __launch_bounds__(512, 1) void k_all(Ptrs p) {
  unsigned warm = 0;
  ARG_WARM(warm);
  p.ws = (char*)(((size_t)p.ws + 0x1fffff) & ~(size_t)0x1fffff);
#define PAD(k)
  const Args4 a{GemmArgs{p.x, p.wqkv, nullptr, nullptr, WS_O, (float*)WS_H, p.wg, p.wu, WS_CS, p.wd, p.wo, WS_SY, (float*)WS_A, warm DBG(0)},
    GemmArgs{WS_O, p.wo, nullptr, p.x, WS_H, WS_ST, nullptr, nullptr, nullptr, p.wd, nullptr, WS_SY, nullptr, 0u DBG(1)},
    GemmArgs{WS_H, p.wg, p.wu, p.out, WS_A, WS_ST, nullptr, nullptr, WS_CS, p.wd, nullptr, WS_SY, WS_SL, 0u DBG(2)},
    GemmArgs{WS_A, p.wd, nullptr, WS_H, p.out, nullptr, nullptr, nullptr, nullptr, nullptr, nullptr, WS_SY, WS_SL, 0u DBG(3)}};
  int pol = 0;
  k1_phase(a.q, [&](auto md, auto lo, auto hi) { wo_phase<decltype(md)::value, decltype(lo)::value, decltype(hi)::value>(a.b, a.c); }, pol);
  PAD(1);
  wo_phase<2, 0, 0>(a.b, a.c, pol);
  PAD(2);
  Late late;
  glu_phase(a.c, [&](auto tc, unsigned w, int wb) { dn_first_w<decltype(tc)::value>(a.d, w, wb); }, late, pol);
  PAD(3);
  dn_phase(a.d, late, pol);
  PAD(4);
}

extern "C" __global__ void k_zend() {}

extern "C" void kernel_launch(void* const* d_in, const int* in_sizes, int n_in, void* d_out, int out_size, void* d_ws,
                              size_t ws_size, hipStream_t stream) {
  const Ptrs p{(const bf16_t*)d_in[0], (const bf16_t*)d_in[1], (const bf16_t*)d_in[2], (const bf16_t*)d_in[4], (const bf16_t*)d_in[5], (const bf16_t*)d_in[6], (bf16_t*)d_out, (char*)d_ws};
  constexpr int L_all = SNAP + 64;
  static_assert(SNAP >= 3 * 256 * 128 + 4 * 14592 + 4096 + 1024 + 256 + 512 + 128 + 64 && L_all <= 163840, "LDS");
  (void)hipFuncSetAttribute((const void*)k_all, hipFuncAttributeMaxDynamicSharedMemorySize, L_all);
  k_all<<<256, 512, L_all, stream>>>(p);
}
```
